```python
import torch

def _softmax(x, axis=-1):
    return torch.softmax(x, dim=axis)
def _gelu(x):
    return torch.nn.functional.gelu(x, approximate="tanh")
def _silu(x):
    return torch.nn.functional.silu(x)
def _sigmoid(x):
    return torch.sigmoid(x)
def _layernorm(x, axis=-1, eps=1e-5):
    m = x.mean(dim=axis, keepdim=True)
    v = x.var(dim=axis, keepdim=True, unbiased=False)
    return (x - m) / torch.sqrt(v + eps)
def _scatter_add(tgt, idx, src, axis=0):
    return tgt.clone().index_add_(axis, idx, src)
def _segment_sum(src, seg, n):
    out = torch.zeros((n,) + src.shape[1:], dtype=src.dtype)
    return out.index_add_(0, seg, src)
def _pad(x, pw):
    flat = tuple(v for pair in reversed(pw) for v in pair)
    return torch.nn.functional.pad(x, flat)

kernel_name = 'sequential_preLN__mha_block_cached__moe_arch_mod__decode'

def setup_inputs():

    _w = (lambda a: (lambda t: t.to(torch.bfloat16) if t.is_floating_point() else t)(torch.as_tensor(a)))
    def _A_setup():
        B, Sq, D = 4, 1, 640
        H, Sk, Dh = 32, 8192, 192
        return {
            "x": _w((lambda *s: torch.randn(*s, dtype=torch.bfloat16) * 0.05)(B, Sq, D)),
            "k_cache": _w((lambda *s: torch.randn(*s, dtype=torch.bfloat16) * 3.2)(B, H, Sk, Dh)),
            "v_cache": _w((lambda *s: torch.randn(*s, dtype=torch.bfloat16) * 0.4)(B, H, Sk, Dh)),
            "wq": _w((lambda *s: torch.randn(*s, dtype=torch.bfloat16) * 3.2)(D, H * Dh) * (D ** -0.5)),
            "wo": _w((lambda *s: torch.randn(*s, dtype=torch.bfloat16) * 0.4)(H * Dh, D) * ((H * Dh) ** -0.5)),
        }
    _A_in = _A_setup()

    def _B_setup():
        B, S, D, F, C = 4, 1, 640, 1536, 1
        return {
            "x": _w((lambda *s: torch.randn(*s, dtype=torch.bfloat16) * 0.05)(B, S, D)),
            "w_r": _w((lambda *s: torch.randn(*s, dtype=torch.bfloat16) * 0.05)(D)),
            "w_gate": _w((lambda *s: torch.randn(*s, dtype=torch.bfloat16) * 0.05)(D, F)),
            "w_up": _w((lambda *s: torch.randn(*s, dtype=torch.bfloat16) * 0.05)(D, F)),
            "w_down": _w((lambda *s: torch.randn(*s, dtype=torch.bfloat16) * 0.05)(F, D)),
        }
    _B_in = _B_setup()

    return {
        "A_x": _A_in["x"],
        "A_k_cache": _A_in["k_cache"],
        "A_v_cache": _A_in["v_cache"],
        "A_wq": _A_in["wq"],
        "A_wo": _A_in["wo"],
        "B_x": _B_in["x"],
        "B_w_r": _B_in["w_r"],
        "B_w_gate": _B_in["w_gate"],
        "B_w_up": _B_in["w_up"],
        "B_w_down": _B_in["w_down"],
    }

def reference(A_x, A_k_cache, A_v_cache, A_wq, A_wo, B_x, B_w_r, B_w_gate, B_w_up, B_w_down):
    def _A_ref(x, k_cache, v_cache, wq, wo):
        B, Sq, D = x.shape
        H, Dh = 32, 192
        q = (lambda x, *d: x.permute(*d))((x @ wq).reshape(B, Sq, H, Dh), 0, 2, 1, 3)
        scores = torch.einsum("bhqd,bhkd->bhqk", q, k_cache) * (Dh ** -0.5)
        p = _softmax(scores, axis=-1)
        o = torch.einsum("bhqk,bhkd->bhqd", p, v_cache)
        return (lambda x, *d: x.permute(*d))(o, 0, 2, 1, 3).reshape(B, Sq, H * Dh) @ wo

    def _B_ref(x, w_r, w_gate, w_up, w_down):
        B, S, D = x.shape
        C = 1
        r = x @ w_r
        idx = torch.argsort(-r, axis=-1)[:, :C]
        idx_e = (lambda x, n, axis: torch.repeat_interleave(x, n, dim=axis))(idx.reshape(B, C, 1), D, axis=2)
        x_sel = (lambda x, i, axis: torch.take_along_dim(x, i, dim=axis))(x, idx_e, axis=1)
        g = _silu(x_sel @ w_gate)
        u = x_sel @ w_up
        y = (g * u) @ w_down
        r_sel = _sigmoid((lambda x, i, axis: torch.take_along_dim(x, i, dim=axis))(r, idx, axis=1))
        y = y * r_sel.reshape(B, C, 1)
        b_off = (torch.arange(B) * S).reshape(B, 1)
        flat_idx = (idx + b_off).reshape(B * C)
        zero = (x * 0.0).reshape(B * S, D)
        upd = _scatter_add(zero, flat_idx, y.reshape(B * C, D), axis=0)
        return x + upd.reshape(B, S, D)

    x = A_x
    h = x + _A_ref(_layernorm(x, axis=-1), A_k_cache, A_v_cache, A_wq, A_wo)
    h2 = h + _B_ref(_layernorm(h, axis=-1), B_w_r, B_w_gate, B_w_up, B_w_down)
    return h2
```

```cpp
#include <hip/hip_runtime.h>
#include <hip/hip_bf16.h>
#include <stdint.h>

typedef __hip_bfloat16 bf16;
using u32x2 = __attribute__((ext_vector_type(2))) unsigned;
using u32x4 = __attribute__((ext_vector_type(4))) unsigned;
using f32x4 = __attribute__((ext_vector_type(4))) float;
using f32x2 = __attribute__((ext_vector_type(2))) float;
using bf16x8 = __attribute__((ext_vector_type(8))) __bf16;

constexpr int NB = 4, DM = 640, NH = 32, SK = 8192, DH = 192, HD = NH * DH, FF = 1536;
constexpr float LN_EPS = 1e-5f;
constexpr float QSCALE = 0.07216878364870322f * 1.4426950408889634f;

constexpr int NWG = 256;
constexpr int GS = 8, NG = NWG / GS, NI = GS / 2;
constexpr int HPG = NI / NB;
constexpr int PPI = 512 / GS;
constexpr int NPAIR = NI * PPI;

constexpr int TAUK = 1320, TAUV = 1338;
constexpr int TSTART = 200;
constexpr int WGAP = 285, WP0 = 64, WPN = 176;
constexpr int VGAP = 90;
constexpr int SPRINT = 4;
constexpr int JUMP = 145;
constexpr int HST = 2800, RT0 = 3900, RTS = 1000;
constexpr unsigned NRD = 5, RMIN = 50, FBP = 60, FBL = 150;
constexpr int W0START = 1700, W0SPAN = 1000, W1START = 2700, W1SPAN = 2600;
constexpr int ROT = 16, ROWS = 0xC9;
constexpr int FBM = 100, FBX = 15, FB0 = 170, FB1 = 55, FBS = 10;
constexpr int FBD = 20, HOLD = 400, GB = 24, GBS = 2;
constexpr unsigned PRD = 3; constexpr int PRB = 24;
constexpr int TLEAD = 40, PFD = 6, WFD = 3;
constexpr int QWAIT = 750, QBACK = 100;
constexpr int WDELAY = 100, WDELAY2 = 460, G2LATE = 780, GLATE = 150, DLATE = 220, DLATE2 = 120;

constexpr int KS = 8;
constexpr int WS_PML = 0;
constexpr int WS_PO = WS_PML + NWG * 64;
constexpr int WS_PQ = WS_PO + NWG * DH;
constexpr int WS_OC = WS_PQ + NWG * 5 * (DH / 2);
constexpr int WS_SLAB = WS_OC + (NB - 1) * NH * DH;
constexpr int WS_ACT = WS_SLAB + KS * NB * DM;
constexpr int WS_ST = WS_ACT + NB * FF;
constexpr int WS_HN = WS_ST + 32;
constexpr int WS_HR = WS_HN + NWG;
constexpr int WS_HA = WS_HR + 2 * NWG;
constexpr int QFS = 2 * GS * 32;
constexpr int WS_QF = WS_HA + 32 * NWG;
constexpr int WS_QP = WS_QF + NG * QFS;
constexpr int WS_END = WS_QP + NH * 2 * NB * DH;
constexpr int NPROD = HPG * 6;
static_assert(WS_PO % 32 == 0 && WS_SLAB % 32 == 0 && WS_ACT % 32 == 0 && WS_ST % 32 == 0 && WS_HN % 32 == 0 && WS_QF % 32 == 0 && WS_QP % 32 == 0, "line alignment");


__device__ __forceinline__ int row_of(int b) { return (ROWS >> (2 * b)) & 3; }
__device__ __forceinline__ float bf_lo(unsigned u) { return __uint_as_float(u << 16); }
__device__ __forceinline__ float bf_hi(unsigned u) { return __uint_as_float(u & 0xffff0000u); }
__device__ __forceinline__ f32x2 bf_pair(unsigned u) { return f32x2{bf_lo(u), bf_hi(u)}; }
__device__ __forceinline__ f32x2 fma2(float a, f32x2 b, f32x2 c) { return __builtin_elementwise_fma(f32x2{a, a}, b, c); }
__device__ __forceinline__ f32x2 fma2(f32x2 a, f32x2 b, f32x2 c) { return __builtin_elementwise_fma(a, b, c); }

template <int CTRL> __device__ __forceinline__ float dpp(float x) {
  return __builtin_bit_cast(float, __builtin_amdgcn_mov_dpp(__builtin_bit_cast(int, x), CTRL, 0xf, 0xf, true));
}
constexpr int DPP_XOR1 = 0xB1, DPP_XOR2 = 0x4E, DPP_HMIRROR = 0x141, DPP_MIRROR = 0x140, DPP_ROR8 = 0x128;

__device__ __forceinline__ float sum_x16_32(float x) {
  auto s = __builtin_amdgcn_permlane16_swap(__float_as_uint(x), __float_as_uint(x), false, false);
  x = __uint_as_float(s[0]) + __uint_as_float(s[1]);
  auto t = __builtin_amdgcn_permlane32_swap(__float_as_uint(x), __float_as_uint(x), false, false);
  return __uint_as_float(t[0]) + __uint_as_float(t[1]);
}
__device__ __forceinline__ float max_x16_32(float x) {
  auto s = __builtin_amdgcn_permlane16_swap(__float_as_uint(x), __float_as_uint(x), false, false);
  x = fmaxf(__uint_as_float(s[0]), __uint_as_float(s[1]));
  auto t = __builtin_amdgcn_permlane32_swap(__float_as_uint(x), __float_as_uint(x), false, false);
  return fmaxf(__uint_as_float(t[0]), __uint_as_float(t[1]));
}
__device__ __forceinline__ float tsum32(float a, float b) {
  auto t = __builtin_amdgcn_permlane32_swap(__float_as_uint(a), __float_as_uint(b), false, false);
  return __uint_as_float(t[0]) + __uint_as_float(t[1]);
}
__device__ __forceinline__ float tsum16(float a, float b) {
  auto t = __builtin_amdgcn_permlane16_swap(__float_as_uint(a), __float_as_uint(b), false, false);
  return __uint_as_float(t[0]) + __uint_as_float(t[1]);
}
__device__ __forceinline__ float sum_r(float v) { v += dpp<DPP_ROR8>(v); return sum_x16_32(v); }
__device__ __forceinline__ float row_sum(float v) {
  v += dpp<DPP_XOR1>(v); v += dpp<DPP_XOR2>(v); v += dpp<DPP_HMIRROR>(v);
  return v + dpp<DPP_MIRROR>(v);
}
__device__ __forceinline__ float row_max(float v) {
  v = fmaxf(v, dpp<DPP_XOR1>(v)); v = fmaxf(v, dpp<DPP_XOR2>(v)); v = fmaxf(v, dpp<DPP_HMIRROR>(v));
  return fmaxf(v, dpp<DPP_MIRROR>(v));
}
__device__ __forceinline__ float wave_sum(float v) {
  v += dpp<DPP_XOR1>(v); v += dpp<DPP_XOR2>(v); v += dpp<DPP_HMIRROR>(v);
  return sum_r(v);
}

__device__ __forceinline__ u32x4 ld_nt(const char* p) { return __builtin_nontemporal_load(reinterpret_cast<const u32x4*>(p)); }

struct Tile { u32x4 d[3]; };

#define PIN4(a, o) asm volatile("" : "+v"((a)[(o) + 0]), "+v"((a)[(o) + 1]), "+v"((a)[(o) + 2]), "+v"((a)[(o) + 3]))
#define MEMFENCE() do { asm volatile("" ::: "memory"); __builtin_amdgcn_sched_barrier(0); } while (0)

__device__ __forceinline__ void glds16_nt(const void* gsrc, unsigned lds_dst) {
  unsigned keep;
  asm volatile("s_mov_b32 %0, m0\n\ts_mov_b32 m0, %2\n\ts_nop 0\n\tglobal_load_lds_dwordx4 %1, off nt\n\ts_mov_b32 m0, %0"
               : "=&s"(keep) : "v"(gsrc), "s"(lds_dst) : "memory");
}

__device__ __forceinline__ float tile_score(const Tile& k, const f32x2 (&q)[12]) {
  f32x2 s0 = {0.f, 0.f}, s1 = s0;
#pragma unroll
  for (int i = 0; i < 3; ++i) {
    s0 = fma2(bf_pair(k.d[i][0]), q[i * 4], s0);     s1 = fma2(bf_pair(k.d[i][1]), q[i * 4 + 1], s1);
    s0 = fma2(bf_pair(k.d[i][2]), q[i * 4 + 2], s0); s1 = fma2(bf_pair(k.d[i][3]), q[i * 4 + 3], s1);
  }
  s0 += s1;
  return s0[0] + s0[1];
}
__device__ __forceinline__ void score_put(float s, float* dst, int j) {
  s += dpp<DPP_XOR1>(s);
  s += dpp<DPP_XOR2>(s);
  s += dpp<DPP_HMIRROR>(s);
  if (j == 0) *dst = s;
}
__device__ __forceinline__ void tile_load(Tile& t, const char* base, unsigned off, int half) {
#pragma unroll
  for (int i = 0; i < 3; ++i) t.d[i] = ld_nt(base + off + half * 3072 + i * 128);
}
__device__ __forceinline__ float score_next(Tile& k, const f32x2 (&q)[12], const char* base, unsigned off, int half) {
  f32x2 s0 = {0.f, 0.f}, s1 = s0;
#pragma unroll
  for (int i = 0; i < 3; ++i) {
    s0 = fma2(bf_pair(k.d[i][0]), q[i * 4], s0);     s1 = fma2(bf_pair(k.d[i][1]), q[i * 4 + 1], s1);
    s0 = fma2(bf_pair(k.d[i][2]), q[i * 4 + 2], s0); s1 = fma2(bf_pair(k.d[i][3]), q[i * 4 + 3], s1);
    asm volatile("" : "+v"(s0), "+v"(s1));
    MEMFENCE();
    k.d[i] = ld_nt(base + off + half * 3072 + i * 128);
    MEMFENCE();
  }
  s0 += s1;
  return s0[0] + s0[1];
}
__device__ __forceinline__ void pv_next(float p, Tile& v, f32x2 (&acc)[12], const char* base, unsigned off, int half) {
#pragma unroll
  for (int i = 0; i < 3; ++i) {
    asm volatile("" : "+v"(v.d[i]), "+v"(p));
#pragma unroll
    for (int d = 0; d < 4; ++d) acc[i * 4 + d] = fma2(p, bf_pair(v.d[i][d]), acc[i * 4 + d]);
    PIN4(acc, i * 4);
    MEMFENCE();
    v.d[i] = ld_nt(base + off + half * 3072 + i * 128);
    MEMFENCE();
  }
}
__device__ __forceinline__ void pv_last(float p, Tile& v, f32x2 (&acc)[12]) {
#pragma unroll
  for (int i = 0; i < 3; ++i) {
    asm volatile("" : "+v"(v.d[i]), "+v"(p));
#pragma unroll
    for (int d = 0; d < 4; ++d) acc[i * 4 + d] = fma2(p, bf_pair(v.d[i][d]), acc[i * 4 + d]);
    PIN4(acc, i * 4);
  }
}
__device__ __forceinline__ void q_load(f32x2 (&q)[12], const float* src, int j) {
#pragma unroll
  for (int i = 0; i < 6; ++i) {
    const f32x4 a = *reinterpret_cast<const f32x4*>(src + (i >> 1) * 64 + j * 8 + (i & 1) * 4);
    q[i * 2] = f32x2{a[0], a[1]}; q[i * 2 + 1] = f32x2{a[2], a[3]};
  }
}
#define VMCNT(n) asm volatile("s_waitcnt vmcnt(%0)" :: "n"(n) : "memory")

constexpr int AUX_SC1 = 16, AUX_EARLY = 18;
constexpr unsigned SPIN_CAP = 1u << 17;
__device__ __forceinline__ unsigned nz_bits(float v) { return max(__float_as_uint(v), 1u); }
__device__ __forceinline__ bool all_nz(u32x4 v) { return v[0] != 0u && v[1] != 0u && v[2] != 0u && v[3] != 0u; }
__device__ __forceinline__ unsigned min4(u32x4 v) { return min(min(v[0], v[1]), min(v[2], v[3])); }
__device__ __forceinline__ unsigned ld32(__amdgpu_buffer_rsrc_t rs, int word) { return __builtin_amdgcn_raw_buffer_load_b32(rs, word * 4, 0, AUX_SC1); }
__device__ __forceinline__ u32x2 ld64(__amdgpu_buffer_rsrc_t rs, int word) { return __builtin_amdgcn_raw_buffer_load_b64(rs, word * 4, 0, AUX_SC1); }
__device__ __forceinline__ u32x4 ld128(__amdgpu_buffer_rsrc_t rs, int word) { return __builtin_amdgcn_raw_buffer_load_b128(rs, word * 4, 0, AUX_SC1); }
__device__ __forceinline__ void st32(__amdgpu_buffer_rsrc_t rs, int word, unsigned v) { __builtin_amdgcn_raw_buffer_store_b32(v, rs, word * 4, 0, AUX_SC1); }
__device__ __forceinline__ void st64(__amdgpu_buffer_rsrc_t rs, int word, u32x2 v) { __builtin_amdgcn_raw_buffer_store_b64(v, rs, word * 4, 0, AUX_SC1); }
__device__ __forceinline__ void st128(__amdgpu_buffer_rsrc_t rs, int word, u32x4 v) { __builtin_amdgcn_raw_buffer_store_b128(v, rs, word * 4, 0, AUX_SC1); }

__device__ __forceinline__ bool leader_release(__amdgpu_buffer_rsrc_t rs, int lane, unsigned t0, unsigned rd) {
  u32x4 a[4];
#pragma unroll
  for (int k = 0; k < 4; ++k) a[k] = ld128(rs, WS_HA + (lane * 4 + k) * 32);
  const u32x4 n = ld128(rs, WS_HN + lane * 4);
  if (rd == 0u && !__all(a[0][0] == n[0] && a[1][0] == n[1] && a[2][0] == n[2] && a[3][0] == n[3])) return false;
  const float e = fmaxf(fmaxf((float)(int)(t0 - a[0][2]), (float)(int)(t0 - a[1][2])), fmaxf((float)(int)(t0 - a[2][2]), (float)(int)(t0 - a[3][2])));
  unsigned first = t0 - (unsigned)(int)max_x16_32(row_max(e));
  float ls = 0.f, lx = 0.f;
#pragma unroll
  for (int k = 0; k < 4; ++k) {
    const float l = a[k][0] == n[k] + rd ? (float)min(a[k][3] + (rd ? 0u : min(a[k][2] - first, 300u)), 2000u) : 0.f;
    ls += l; lx = fmaxf(lx, l);
  }
  ls = wave_sum(ls) * (1.0f / NWG); lx = max_x16_32(row_max(lx));
  const int extra = min(max((int)(ls * (FBM / 100.0f) + lx * (FBX / 100.0f)) - (rd ? FB1 + (int)(NRD - rd) * FBD : FB0), 0), 600);
  first = (first & 0xffffu) | ((unsigned)extra << 16);
  if (rd != 0u && ls * NWG < HOLD) return true;
  st128(rs, WS_HR + lane * 8, u32x4{a[0][1] + rd, first, a[1][1] + rd, first});
  st128(rs, WS_HR + lane * 8 + 4, u32x4{a[2][1] + rd, first, a[3][1] + rd, first});
  return true;
}

typedef __attribute__((address_space(3))) unsigned lds_u32;
__device__ __forceinline__ unsigned lds_add(lds_u32* p, int lane) {
  unsigned k = 0;
  if (lane == 0) k = __hip_atomic_fetch_add(p, 1u, __ATOMIC_RELAXED, __HIP_MEMORY_SCOPE_WORKGROUP);
  return __builtin_amdgcn_readfirstlane(k);
}
#define CFENCE() asm volatile("" ::: "memory")
__device__ __forceinline__ int arrive(lds_u32* p) {
  int v;
  unsigned long long keep;
  asm volatile("s_mov_b64 %1, exec\n\ts_mov_b64 exec, 1\n\tds_add_rtn_u32 %0, %2, %3\n\ts_mov_b64 exec, %1"
               : "=&v"(v), "=&s"(keep) : "v"((unsigned)(size_t)p), "v"(1u) : "memory");
  return v;
}
__device__ __forceinline__ int arrived(int v) {
  asm volatile("s_waitcnt lgkmcnt(0)" : "+v"(v) :: "memory");
  return __builtin_amdgcn_readfirstlane(v);
}
__device__ __forceinline__ unsigned lds_get(lds_u32* p) {
  return __builtin_amdgcn_readfirstlane(__hip_atomic_load(p, __ATOMIC_RELAXED, __HIP_MEMORY_SCOPE_WORKGROUP));
}
template <int SLEEP> __device__ __forceinline__ void lds_wait(lds_u32* p, unsigned n) {
#pragma unroll 1
  for (unsigned spin = 0; spin < (SPIN_CAP << 4); ++spin) {
    if (lds_get(p) >= n) break;
    __builtin_amdgcn_s_sleep(SLEEP);
  }
  asm volatile("" ::: "memory");
}
__device__ __forceinline__ void pause(unsigned ticks) {
  const unsigned long long t0 = __builtin_amdgcn_s_memrealtime();
#pragma unroll 1
  for (unsigned spin = 0; spin < 4096u && __builtin_amdgcn_s_memrealtime() - t0 < ticks; ++spin) __builtin_amdgcn_s_sleep(16);
}
__device__ __forceinline__ unsigned clk() { return (unsigned)__builtin_amdgcn_s_memrealtime(); }
struct Table {
  unsigned t0; int was, moved, slowed;
  __device__ __forceinline__ void gate(int due, lds_u32* mine, lds_u32* slow) {
    const int left = (int)lds_get(slow) - slowed, d = max(min(left, FBS), -GBS);
    t0 += d; was -= d; slowed += d;
    int late = (int)(clk() - t0) - due;
    if (late - was > JUMP) { t0 += late - was; moved += late - was; late = was; *mine = moved; }
    mine[4] = max(late - max(left - d, 0), 0);
    was = late;
#pragma unroll 1
    for (int spin = 0; spin < 65536 && late < 0; ++spin) { __builtin_amdgcn_s_sleep(1); late = (int)(clk() - t0) - due; }
  }
  __device__ __forceinline__ void agree(lds_u32* all, lds_u32* mine) {
    if (moved == 0) return;
    const int m = (int)min(min(lds_get(all), lds_get(all + 1)), min(lds_get(all + 2), lds_get(all + 3))) + 30;
    if (m < moved) { t0 -= moved - m; moved = m; was = 1 << 20; *mine = moved; }
  }
};
__device__ __forceinline__ void until(unsigned t0, int due) {
#pragma unroll 1
  for (int spin = 0; spin < 65536 && (int)(clk() - t0) < due; ++spin) __builtin_amdgcn_s_sleep(1);
}
#define LGKM0() asm volatile("s_waitcnt lgkmcnt(0)" ::: "memory")

__device__ __forceinline__ void split4(f32x4 v, u32x2& hi, u32x2& lo) {
  unsigned h[4], r[4];
#pragma unroll
  for (int i = 0; i < 4; ++i) { h[i] = __float_as_uint(v[i]) & 0xffff0000u; r[i] = __float_as_uint(v[i] - __uint_as_float(h[i])); }
  hi = u32x2{__builtin_amdgcn_perm(h[1], h[0], 0x07060302u), __builtin_amdgcn_perm(h[3], h[2], 0x07060302u)};
  lo = u32x2{__builtin_amdgcn_perm(r[1], r[0], 0x07060302u), __builtin_amdgcn_perm(r[3], r[2], 0x07060302u)};
}
__device__ __forceinline__ f32x4 mfma16(u32x4 a, u32x4 b, f32x4 c) {
  return __builtin_amdgcn_mfma_f32_16x16x32_bf16(__builtin_bit_cast(bf16x8, a), __builtin_bit_cast(bf16x8, b), c, 0, 0, 0);
}

constexpr int NW = 8, NMW = 4;
constexpr int NLP = 5, NRP = 7;
constexpr int NTP = 4 * (NLP + NRP);
constexpr int REG = NLP * 6 * 1024;
constexpr int NRING = 4;
constexpr int NS = 6, NA = NS - 1;
__device__ __forceinline__ int seg_beg(int s) { return s >= NS ? NPAIR : ((0xC87640 >> (4 * s)) & 15) * 16; }
__device__ __forceinline__ int seg_k(int t) { return (int)(0x5433222211110000ull >> (4 * (t >> 4))) & 15; }
__device__ __forceinline__ int seg_of_k(int k) { return (0x321540 >> (4 * k)) & 15; }
__device__ __forceinline__ int k_of_seg(int s) { return (0x215430 >> (4 * s)) & 15; }
__device__ __forceinline__ int pair_of(int t) { return t < 64 ? t : (t < 192 ? t + 64 : t - 128); }
constexpr int OFF_CNT = 0, OFF_REG = 256,
              OFF_SC = OFF_REG + 4 * REG, OFF_BUF = OFF_SC + NPAIR * 16 * 4, OFF_Q = OFF_BUF + NRING * NMW * DH * 4, OFF_ML = OFF_Q + NS * DH * 4,
              OFF_RED = OFF_ML + 128, OFF_RS = OFF_RED + 4 * 256 * 4, OFF_END = OFF_RS + 5 * 16;
enum { F_REL = 0, F_DONE, C_T = 3, F_OUT, C_P, C_K, F_P, F_Q = 8, F_FREE, S_FIRST, F_Q2, C_W = 12, C_SC = 16, C_PART = 24, S_ECHO = 32, S_MOVED = 48, S_SLOW = 56 };
__device__ __forceinline__ int due_k(int p) { return TSTART + ((p * TAUK + min(max(p - WP0, 0), WPN) * (WGAP * 64 / WPN)) >> 6); }
__device__ __forceinline__ int due_v(int p) { return TSTART + WGAP + ((NPAIR * TAUK + p * TAUV + max(p - NPAIR / 2, 0) * (VGAP * 64 / (NPAIR / 2))) >> 6); }
constexpr int AW_LD = DH * 2 + 16, AG_LD = 160 * 2 + 16, AD_LD = 384 * 2 + 16;

__global__ void __launch_bounds__(NW * 64) fused_kernel(const bf16* __restrict__ x, const bf16* __restrict__ kc,
                                                        const bf16* __restrict__ vc, const bf16* __restrict__ wq,
                                                        const bf16* __restrict__ wo, const bf16* __restrict__ wr,
                                                        const bf16* __restrict__ wg, const bf16* __restrict__ wu,
                                                        const bf16* __restrict__ wd, float* ws, bf16* __restrict__ out) {
  __shared__ __attribute__((aligned(16))) unsigned char smem[OFF_END];
  float* s_sc = reinterpret_cast<float*>(smem + OFF_SC);
  float* s_buf = reinterpret_cast<float*>(smem + OFF_BUF);
  float* s_q = reinterpret_cast<float*>(smem + OFF_Q);
  u32x4* s_ml = reinterpret_cast<u32x4*>(smem + OFF_ML);
  lds_u32* s_c = (lds_u32*)(smem + OFF_CNT);

  const int tid = threadIdx.x, lane = tid & 63, r = lane >> 3, j = lane & 7;
  const int wave = __builtin_amdgcn_readfirstlane(tid >> 6);
  const int tw = wave - NMW;
  const int bid = blockIdx.x, xl = bid & 7, idx = bid >> 3;
  const int g = (xl * 32 + idx) / GS, mm = idx % GS;
  const __amdgpu_buffer_rsrc_t rs = __builtin_amdgcn_make_buffer_rsrc(ws, 0, WS_END * 4, 0x00020000);
  const int ro = ((idx & 3) << 3) | (idx >> 2);
  const bool isW = ro < 10, isG = ro >= 10 && ro < 13;
  const int w_ks = xl, w_ct = ro;
  const int g_cg = xl * 3 + (ro - 10);
  const int d_ct = (xl + ((ro - 13) >> 1) * 8) * 2 + ((ro - 13) & 1);
  const bool isD = ro >= 13 && ro < 17 && d_ct < DM / 32;
  const bool isC = (mm & 7) == 3 || (mm & 7) >= 6;
  const int c_it = (mm >> 3) * 3 + max((mm & 7) - 5, 0);
  const int qf = WS_QF + g * QFS;
  const char* kb = reinterpret_cast<const char*>(kc);
  const char* vb = reinterpret_cast<const char*>(vc);
  const unsigned lo = (r * 24 + j) * 16;
  const int xb = g * GS + mm, c0 = mm * 8 + g * ROT;
  const int b2 = (2 * NH + (xb >> 4)) * 1024 + (xb & 15) * 8, b3 = (2 * NH + 16 + (xb >> 5)) * 1024 + (xb & 31) * 8;
  auto poff = [&](int p) -> unsigned {
    const int turn = (p & 63) >> 2;
    int t = (row_of(p >> 6) * NH + g) * 1024 + ((turn * 64 + (p & 3) * 2 + c0) & 1023);
    if ((p >> 6) == 1) t = (turn < 8 ? b2 + turn * 128 : b3 + (turn & 4) * 2048 + (turn & 3) * 256) + (p & 3) * 2;
    return (unsigned)t * 3072u;
  };
  const unsigned lds_base = (unsigned)(size_t)(__attribute__((address_space(3))) unsigned char*)smem + OFF_REG + tw * REG;
  auto merged = [&](int it, int ln) -> u32x4 {
    const float* src = s_buf + (it % NRING) * (NMW * DH) + ((ln >> 1) & 7) * 24 + (ln >> 4) * 8 + (ln & 1) * 4;
    const f32x4 o = (*reinterpret_cast<const f32x4*>(src) + *reinterpret_cast<const f32x4*>(src + DH)) +
                    (*reinterpret_cast<const f32x4*>(src + 2 * DH) + *reinterpret_cast<const f32x4*>(src + 3 * DH));
    return u32x4{nz_bits(o[0]), nz_bits(o[1]), nz_bits(o[2]), nz_bits(o[3])};
  };
  auto publish = [&](int it) -> bool {
    const int k = arrive(s_c + C_PART + it);
    u32x4 o = merged(it, lane < DH / 4 ? lane : 0);
    asm volatile("" : "+v"(o));
    if (arrived(k) != NMW - 1) return false;
    if (lane < DH / 4) st128(rs, WS_PO + xb * DH + lane * 4, o);
    if (lane == 0) __hip_atomic_fetch_add(s_c + F_DONE, 1u, __ATOMIC_RELAXED, __HIP_MEMORY_SCOPE_WORKGROUP);
    return true;
  };
#define NONCE() do { unsigned n_ = __builtin_amdgcn_readfirstlane(f) + 1u; while (__any(f == n_)) ++n_; \
    if (lane < 32) st32(rs, qf + (GS + mm) * 32 + lane, n_); f = n_; } while (0)
  unsigned r0 = 0u;
  unsigned t0 = clk();
  if (wave < NMW) {
    Tile ta, tb;
    const int c = lane & (GS - 1);
    unsigned f = 0u;
    if (wave == 3) f = ld32(rs, qf + c * 32 + mm);
    if (mm < NPROD) {
      const int hp = g + (mm / 6) * NG, ck = (mm % 6) >> 1, hf = mm & 1, jp = hf * 4 + wave;
      const int bb = lane >> 4, t = lane & 15;
      const u32x4* xp = reinterpret_cast<const u32x4*>(x + row_of(bb) * DM);
      u32x4 xv[5], xs = {0u, 0u, 0u, 0u};
#pragma unroll
      for (int k = 0; k < 5; ++k) xv[k] = xp[t + 16 * k];
      if (t < 10) xs = xp[jp * 10 + t];
      const u32x4* wp = reinterpret_cast<const u32x4*>(wq + (size_t)(jp * 80 + r) * HD + hp * DH + ck * 64) + j;
      u32x4 wv[10];
#pragma unroll
      for (int k = 0; k < 10; ++k) wv[k] = __builtin_nontemporal_load(wp + (size_t)k * 8 * (HD / 8));
      if (wave == 0) s_c[lane] = lane == C_K ? NTP + NMW : 0u;
      MEMFENCE(); LGKM0(); __builtin_amdgcn_s_barrier(); MEMFENCE();
      if (wave == 3) { NONCE(); }
      float sm = 0.f;
#pragma unroll
      for (int k = 0; k < 5; ++k)
#pragma unroll
        for (int d = 0; d < 4; ++d) sm += bf_lo(xv[k][d]) + bf_hi(xv[k][d]);
      const float mean = row_sum(sm) * (1.0f / DM);
      float sv = 0.f;
#pragma unroll
      for (int k = 0; k < 5; ++k)
#pragma unroll
        for (int d = 0; d < 4; ++d) { const float a = bf_lo(xv[k][d]) - mean, c2 = bf_hi(xv[k][d]) - mean; sv += a * a + c2 * c2; }
      const float rstd = 1.0f / sqrtf(row_sum(sv) * (1.0f / DM) + LN_EPS);
      float* s_xn = s_buf + wave * 320;
      if (t < 10) {
#pragma unroll
        for (int d = 0; d < 4; ++d) {
          s_xn[(t * 8 + 2 * d) * 4 + bb] = (bf_lo(xs[d]) - mean) * rstd;
          s_xn[(t * 8 + 2 * d + 1) * 4 + bb] = (bf_hi(xs[d]) - mean) * rstd;
        }
      }
      LGKM0();
      f32x2 qa[16];
#pragma unroll
      for (int i = 0; i < 16; ++i) qa[i] = f32x2{0.f, 0.f};
#pragma unroll
      for (int k = 0; k < 10; ++k) {
        const f32x4 xn = *reinterpret_cast<const f32x4*>(&s_xn[(k * 8 + r) * 4]);
#pragma unroll
        for (int d = 0; d < 4; ++d) {
          const f32x2 w2 = bf_pair(wv[k][d]);
#pragma unroll
          for (int e = 0; e < 4; ++e) qa[e * 4 + d] = fma2(xn[e], w2, qa[e * 4 + d]);
        }
      }
      float t16[16], t8[8];
#pragma unroll
      for (int i = 0; i < 16; ++i) t16[i] = tsum32(qa[i >> 1][i & 1], qa[8 + (i >> 1)][i & 1]);
#pragma unroll
      for (int i = 0; i < 8; ++i) t8[i] = tsum16(t16[i], t16[i + 8]);
#pragma unroll
      for (int i = 0; i < 8; ++i) t8[i] += dpp<DPP_ROR8>(t8[i]);
      const bool up = (lane & 8) != 0;
      f32x4* r4 = reinterpret_cast<f32x4*>(smem + OFF_RED);
      r4[wave * 64 + lane] = f32x4{up ? t8[4] : t8[0], up ? t8[5] : t8[1], up ? t8[6] : t8[2], up ? t8[7] : t8[3]};
      LGKM0();
      if (lds_add(s_c + C_P, lane) == NMW - 1) {
        asm volatile("" ::: "memory");
        const f32x4 o4 = (r4[lane] + r4[64 + lane]) + (r4[128 + lane] + r4[192 + lane]);
        st128(rs, WS_QP + ((hp * 2 + hf) * NB + bb) * DH + ck * 64 + j * 8 + (up ? 4 : 0), __builtin_bit_cast(u32x4, o4));
        VMCNT(0);
        if (lane == 0) s_c[F_OUT] = 1u;
      }
    } else {
      if (wave == 0) s_c[lane] = lane == C_K ? NTP + NMW : 0u;
      MEMFENCE(); LGKM0(); __builtin_amdgcn_s_barrier(); MEMFENCE();
      if (wave == 3) { NONCE(); }
    }
    MEMFENCE();
    if (wave == 3) {
      __builtin_amdgcn_s_setprio(3);
      const unsigned nonce = f;
      unsigned echoed = 0u;
      if (mm < NPROD) {
        echoed = ld32(rs, qf + (GS + c) * 32);
        asm volatile("" : "+v"(echoed) :: "memory");
        lds_wait<1>(s_c + F_OUT, 1u);
        if (lane < 32) st32(rs, qf + mm * 32 + lane, echoed);
#pragma unroll 1
        while ((int)(clk() - t0) < QWAIT - QBACK) {
          const unsigned nv = ld32(rs, qf + (GS + c) * 32);
          if (__any(nv != echoed)) {
            if (lane < 32) st32(rs, qf + mm * 32 + lane, nv);
            echoed = nv;
          }
          __builtin_amdgcn_s_sleep(4);
        }
      }
      until(t0, QWAIT);
#pragma unroll 1
      for (unsigned spin = 0; spin < SPIN_CAP; ++spin) {
        const unsigned nv = ld32(rs, qf + (GS + c) * 32);
        f = ld32(rs, qf + c * 32 + mm);
        const bool done = __all(c == mm || c >= NPROD || f == nonce);
        if (mm < NPROD && __any(nv != echoed)) {
          if (lane < 32) st32(rs, qf + mm * 32 + lane, nv);
          echoed = nv;
        }
        if (done) break;
        __builtin_amdgcn_s_sleep(8);
      }
      if (lane < GS) s_c[S_ECHO + lane] = echoed;
      if (lane < DH / 4) {
        u32x4 p[3][2];
#pragma unroll
        for (int it = 0; it < 3; ++it)
#pragma unroll
          for (int k = 0; k < 2; ++k) p[it][k] = ld128(rs, WS_QP + ((g * 2 + k) * NB + (it ? it + 1 : 0)) * DH + lane * 4);
#pragma unroll
        for (int it = 0; it < 3; ++it) {
          *reinterpret_cast<f32x4*>(&s_q[(it ? it + 3 : 0) * DH + lane * 4]) = (__builtin_bit_cast(f32x4, p[it][0]) + __builtin_bit_cast(f32x4, p[it][1])) * QSCALE;
          if (it != 1) { LGKM0(); if (lane == 0) s_c[F_Q] = it + 1; MEMFENCE(); }
        }
      }
    }
    __builtin_amdgcn_s_setprio(3);
    until(t0, due_k(NTP + wave));
    Table tt = {t0, 1 << 20, 0, 0};
    bool common = false;
    tile_load(ta, kb, poff(NTP + wave) + lo, 0);
    tile_load(tb, kb, poff(NTP + wave) + lo, 1);
    MEMFENCE();
    {
      f32x2 q[12];
      int cur = NTP + wave, nxt = lds_add(s_c + C_K, lane), passed = 0, ci = -1;
      for (;;) {
        const bool last = nxt >= NPAIR;
        const unsigned off = poff(last ? wave : pair_of(nxt)) + lo;
        const char* base = last ? vb : kb;
        const int it = seg_k(cur), cp = pair_of(cur);
        if (it != ci) {
          LGKM0();
          for (; passed < it; ++passed) lds_add(s_c + C_SC + passed, lane);
          lds_wait<1>(s_c + (it < 3 ? F_Q : F_Q2), min(it + 1, 3));
          q_load(q, s_q + seg_of_k(it) * DH, j);
          if (!common) { const unsigned d = lds_get(s_c + S_FIRST); if (d) { tt.t0 -= d - 1u; common = true; tt.was = 1 << 20; } }
          tt.agree(s_c + S_MOVED, s_c + S_MOVED + wave);
          ci = it;
        }
        unsigned tk = 0u;
        if (lane == 0) tk = __hip_atomic_fetch_add(s_c + C_K, 1u, __ATOMIC_RELAXED, __HIP_MEMORY_SCOPE_WORKGROUP);
        MEMFENCE();
        tt.gate(due_k(last ? NPAIR + wave : nxt), s_c + S_MOVED + wave, s_c + S_SLOW);
        MEMFENCE();
        const float sa = score_next(ta, q, base, off, 0);
        score_put(sa, s_sc + cp * 16 + r, j);
        MEMFENCE();
        const float sb = score_next(tb, q, base, off, 1);
        score_put(sb, s_sc + cp * 16 + 8 + r, j);
        MEMFENCE();
        if (last) break;
        cur = nxt; nxt = __builtin_amdgcn_readfirstlane(tk);
      }
      LGKM0();
      for (; passed < NS; ++passed) lds_add(s_c + C_SC + passed, lane);
    }
#pragma unroll 1
    for (int it = 0; it < NS; ++it) {
      lds_wait<1>(s_c + F_P, k_of_seg(it) + 1);
      if (!common) { const unsigned d = lds_get(s_c + S_FIRST); if (d) { tt.t0 -= d - 1u; common = true; tt.was = 1 << 20; } }
      tt.agree(s_c + S_MOVED, s_c + S_MOVED + wave);
      f32x2 acc[12];
#pragma unroll
      for (int i = 0; i < 12; ++i) acc[i] = f32x2{0.f, 0.f};
#pragma unroll 1
      for (int p = seg_beg(it) + wave; p < min(seg_beg(it + 1), NPAIR - NMW); p += NMW) {
        const unsigned off = poff(p + NMW) + lo;
        const float pa = s_sc[p * 16 + r], pb = s_sc[p * 16 + 8 + r];
        MEMFENCE();
        if (p + NMW < NPAIR - SPRINT * NMW) tt.gate(due_v(p + NMW), s_c + S_MOVED + wave, s_c + S_SLOW);
        MEMFENCE();
        pv_next(pa, ta, acc, vb, off, 0);
        pv_next(pb, tb, acc, vb, off, 1);
      }
      if (it == NS - 1) {
        const int p = NPAIR - NMW + wave;
        const float pa = s_sc[p * 16 + r], pb = s_sc[p * 16 + 8 + r];
        pv_last(pa, ta, acc);
        pv_last(pb, tb, acc);
      }
      float t12[12], t6[6];
#pragma unroll
      for (int i = 0; i < 12; ++i) t12[i] = tsum32(acc[i >> 1][i & 1], acc[6 + (i >> 1)][i & 1]);
#pragma unroll
      for (int i = 0; i < 6; ++i) t6[i] = tsum16(t12[i], t12[i + 6]);
#pragma unroll
      for (int i = 0; i < 6; ++i) t6[i] += dpp<DPP_ROR8>(t6[i]);
      if (it >= NRING) lds_wait<1>(s_c + F_FREE, it - NRING + 1);
      if ((lane & 8) == 0) {
        f32x2* dst = reinterpret_cast<f32x2*>(&s_buf[(it % NRING) * (NMW * DH) + wave * DH + j * 24 + (lane >> 4) * 6]);
        dst[0] = f32x2{t6[0], t6[1]}; dst[1] = f32x2{t6[2], t6[3]}; dst[2] = f32x2{t6[4], t6[5]};
      }
      CFENCE();
      if (it < NA) { if (lane == 0) __hip_atomic_fetch_add(s_c + C_PART + it, 1u, __ATOMIC_RELAXED, __HIP_MEMORY_SCOPE_WORKGROUP); }
    }
    publish(NS - 1);
    return;
  }

  {
    Tile pk[NRP * 2];
    __builtin_amdgcn_s_setprio(3);
#pragma unroll
    for (int e = 0; e < NLP * 2; ++e) {
      const char* src = kb + (poff((e >> 1) * 4 + tw) + lo + (e & 1) * 3072);
      if (e >= 2) until(t0, due_k(e * 2 + tw) - TLEAD);
#pragma unroll
      for (int i = 0; i < 3; ++i) glds16_nt(src + i * 128, lds_base + (e * 3 + i) * 1024);
      if (e == 1) __builtin_amdgcn_s_barrier();
      if (e * 3 + 3 > PFD) VMCNT(PFD);
    }
#pragma unroll
    for (int e = 0; e < NRP * 2; ++e) {
      until(t0, due_k((NLP * 2 + e) * 2 + tw) - TLEAD);
      tile_load(pk[e], kb, poff((NLP + (e >> 1)) * 4 + tw) + lo, e & 1);
      MEMFENCE();
      if (e + 1 < NRP * 2) VMCNT(PFD);
    }
    if (tw == 0) {
      r0 = ld32(rs, WS_HR + bid * 2);
      if (bid == 0) {
        u32x4 n;
#pragma unroll
        for (int k = 0; k < 4; ++k) n[k] = ld32(rs, WS_HA + (lane * 4 + k) * 32) + 1u;
        st128(rs, WS_HN + lane * 4, n);
      }
      const u32x4 z = {0u, 0u, 0u, 0u};
      if (lane < 48) {
#pragma unroll
        for (int it = NA; it < NS; ++it) st128(rs, WS_PO + xb * DH + lane * 4, z);
      }
      else st128(rs, WS_PML + (g * GS + mm) * 64 + (lane - 48) * 4, z);
      if (isG && g_cg == 0 && lane < 8) st128(rs, WS_ST + lane * 4, z);
      if (isW) st128(rs, WS_SLAB + (w_ks * NB + (lane >> 4)) * DM + w_ct * 64 + (lane & 15) * 4, z);
      else if (isG) st128(rs, WS_ACT + (lane >> 4) * FF + g_cg * 64 + (lane & 15) * 4, z);
      else if (isC && lane < 48) st128(rs, WS_OC + (c_it * NG + g) * DH + lane * 4, z);
    }
    __builtin_amdgcn_s_setprio(0);
    lds_wait<1>(s_c + F_Q, HPG);
    f32x2 q[12];
    q_load(q, s_q, j);
    const u32x4* s_kv = reinterpret_cast<const u32x4*>(smem + OFF_REG + tw * REG) + lane;
    static_assert(NLP * 4 <= PPI && NTP <= HPG * PPI, "tail tiles: batch row 0 only");
#pragma unroll 1
    for (int e = 0; e < NLP * 2; ++e) {
      Tile tmp;
#pragma unroll
      for (int i = 0; i < 3; ++i) tmp.d[i] = s_kv[(e * 3 + i) * 64];
      score_put(tile_score(tmp, q), s_sc + ((e >> 1) * 4 + tw) * 16 + (e & 1) * 8 + r, j);
    }
#pragma unroll
    for (int e = 0; e < NRP * 2; ++e) {
      if ((NLP * 2 + e) * 2 == PPI) q_load(q, s_q + DH, j);
      score_put(tile_score(pk[e], q), s_sc + ((NLP + (e >> 1)) * 4 + tw) * 16 + (e & 1) * 8 + r, j);
      __builtin_amdgcn_sched_barrier(0);
    }
    asm volatile("s_waitcnt vmcnt(0) lgkmcnt(0)" ::: "memory");
#pragma unroll
    for (int it = 0; it * PPI < NTP; ++it) lds_add(s_c + C_SC + it, lane);
  }

  const int mr = lane & 15, qd = lane >> 4;
  unsigned char* reg = smem + OFF_REG + tw * REG;
  float* red = reinterpret_cast<float*>(smem + OFF_RED);
#define PERM_LO(hi_, lo_) __builtin_amdgcn_perm(hi_, lo_, 0x05040100u)
#define PERM_HI(hi_, lo_) __builtin_amdgcn_perm(hi_, lo_, 0x07060302u)
  int served = 0;
  auto done = [&](int n) {
    if (tw == 3 && served < min(n, NA)) {
      const int from = served;
      int ln = lane;
      asm volatile("" : "+v"(ln));
#pragma unroll 1
      for (; served < min(n, NA); ++served) {
        lds_wait<8>(s_c + C_PART + served, NMW);
        const f32x4 o = __builtin_bit_cast(f32x4, merged(served, ln < DH / 4 ? ln : 0));
        const float am = max_x16_32(row_max(fmaxf(fmaxf(fabsf(o[0]), fabsf(o[1])), fmaxf(fabsf(o[2]), fabsf(o[3])))));
        const float inv = am > 1e-30f ? 32767.0f / am : 0.f;
        const int q0 = __float2int_rn(o[0] * inv), q1 = __float2int_rn(o[1] * inv), q2 = __float2int_rn(o[2] * inv), q3 = __float2int_rn(o[3] * inv);
        if (ln < DH / 4) reinterpret_cast<u32x2*>(s_sc + served * (DH / 2))[ln] = u32x2{PERM_LO((unsigned)q1, (unsigned)q0), PERM_LO((unsigned)q3, (unsigned)q2)};
        const bool nan = __any((o[0] + o[1]) + (o[2] + o[3]) != (o[0] + o[1]) + (o[2] + o[3]));
        if (ln == 0) s_ml[served][2] = nan ? 0x7fc00000u : nz_bits(am * (1.0f / 32767.0f));
        LGKM0();
        if (ln == 0) s_c[F_FREE] = served + 1;
      }
#pragma unroll 1
      for (int c = from * 24 + ln; c < served * 24; c += 64) st128(rs, WS_PQ + xb * 5 * (DH / 2) + c * 4, reinterpret_cast<const u32x4*>(s_sc)[c]);
      VMCNT(0);
      if (ln < 8) st128(rs, WS_PML + xb * 64 + (from ? 32 : 0) + ln * 4, s_ml[ln]);
      if (ln == 0) __hip_atomic_fetch_add(s_c + F_DONE, (unsigned)(served - from), __ATOMIC_RELAXED, __HIP_MEMORY_SCOPE_WORKGROUP);
    }
    if (n > NA) lds_wait<1>(s_c + F_DONE, n); else lds_wait<8>(s_c + F_DONE, n);
  };
  auto done_all = [&]() {
#pragma unroll 1
    for (int n = 4; n <= NA; ++n) done(n);
  };
  const int lp = (lane < DH / 4 ? lane : 0) * 4;
  auto weight = [&](int it, int x0, int cnt) -> float {
    const int mlw = WS_PML + (x0 + (lane & (cnt - 1))) * 64 + (it < 4 ? 0 : 32) + it * 4;
    u32x4 ml;
#pragma unroll 1
    for (unsigned spin = 0; spin < SPIN_CAP; ++spin) {
      ml = ld128(rs, mlw);
      if (__all(min4(ml) != 0u)) break;
      __builtin_amdgcn_s_sleep(16);
    }
    const float mk = __uint_as_float(ml[0]);
    const float e = __builtin_amdgcn_exp2f(mk - max_x16_32(row_max(mk)));
    return e / (wave_sum(e * __uint_as_float(ml[1])) * ((float)cnt * (1.0f / 64.0f))) * __uint_as_float(ml[2]);
  };
  auto prep = [&]() {
    if (tw == 0) {
      const unsigned tok = __builtin_amdgcn_readfirstlane(r0) + 1u;
      const int c = lane & (GS - 1);
      unsigned echoed = 0u, qe = s_c[S_ECHO + c];
      bool first = true, released = false, sent = false;
#pragma unroll 1
      while ((int)(clk() - t0) < HST) {
        const unsigned qn = ld32(rs, qf + (GS + c) * 32);
        if (mm < NPROD && __any(qn != qe)) {
          if (lane < 32) st32(rs, qf + mm * 32 + lane, qn);
          qe = qn;
        }
        __builtin_amdgcn_s_sleep(64);
      }
      int slow = 0;
      auto lag = [&]() { return (lds_get(s_c + S_MOVED + 4) + lds_get(s_c + S_MOVED + 5) + lds_get(s_c + S_MOVED + 6) + lds_get(s_c + S_MOVED + 7)) >> 2; };
      const unsigned behind = lag();
#pragma unroll 1
      for (unsigned spin = 0; spin < SPIN_CAP && !released; ++spin) {
        const unsigned qn = ld32(rs, qf + (GS + c) * 32);
        const u32x2 rv = ld64(rs, WS_HR + bid * 2);
        const unsigned nv = __builtin_amdgcn_readfirstlane(ld32(rs, WS_HN + bid));
        if (__builtin_amdgcn_readfirstlane(rv[0]) - tok <= NRD) {
          released = true;
          slow = __builtin_amdgcn_readfirstlane(rv[1]) >> 16;
          if (lane == 0) { s_c[S_FIRST] = min((t0 - rv[1]) & 0xffffu, 300u) + 1u; s_c[S_SLOW] = slow; }
        } else {
          if (mm < NPROD && __any(qn != qe)) {
            if (lane < 32) st32(rs, qf + mm * 32 + lane, qn);
            qe = qn;
          }
          if (first || nv != echoed) {
            echoed = nv; first = false;
            if (lane < 8) st128(rs, WS_HA + bid * 32 + lane * 4, u32x4{echoed, tok, t0, behind});
          }
          if (bid == 0 && !sent) sent = leader_release(rs, lane, t0, 0u);
          __builtin_amdgcn_s_sleep(32);
        }
      }
      if (lane == 0) s_c[F_REL] = 1u;
      if (lane < DH / 4) {
        u32x4 p[3][2];
#pragma unroll
        for (int e = 0; e < 3; ++e)
#pragma unroll
          for (int k = 0; k < 2; ++k) p[e][k] = ld128(rs, WS_QP + (((e ? 8 + e * 8 + (xb >> 5) : xb >> 4) * 2 + k) * NB + 1) * DH + lane * 4);
#pragma unroll
        for (int e = 0; e < 3; ++e) *reinterpret_cast<f32x4*>(&s_q[(1 + e) * DH + lane * 4]) = (__builtin_bit_cast(f32x4, p[e][0]) + __builtin_bit_cast(f32x4, p[e][1])) * QSCALE;
      }
      LGKM0();
      if (lane == 0) s_c[F_Q2] = 3u;
      unsigned prev = slow;
#pragma unroll 1
      for (unsigned rd = 1; rd <= NRD; ++rd) {
        const int tr = RT0 + (int)(rd - 1) * RTS;
        until(t0, tr);
        const unsigned bh = lag();
        if (bh > RMIN && lane < 8) st128(rs, WS_HA + bid * 32 + lane * 4, u32x4{echoed + rd, tok, t0, bh});
        if (bid == 0) { until(t0, tr + 220); leader_release(rs, lane, t0, rd); }
        until(t0, tr + 500);
        const u32x2 rv = ld64(rs, WS_HR + bid * 2);
        if (__builtin_amdgcn_readfirstlane(rv[0]) != tok + rd) {
          prev = 0u;
          const int fl = -(int)min(min(lds_get(s_c + S_MOVED), lds_get(s_c + S_MOVED + 1)), min(lds_get(s_c + S_MOVED + 2), lds_get(s_c + S_MOVED + 3)));
          slow = slow > fl ? max(slow - GB, fl) : slow - (rd <= PRD ? PRB : 0);
          if (lane == 0) s_c[S_SLOW] = slow;
        } else {
          const unsigned ex = __builtin_amdgcn_readfirstlane(rv[1]) >> 16;
          slow += ex + min(ex, prev) * (rd == NRD ? FBL : FBP) / 100u;
          prev = ex;
          if (lane == 0) s_c[S_SLOW] = slow;
        }
      }
    }
    if (tw == 3) {
#pragma unroll 1
      for (int ik = 0; ik < NS; ++ik) {
        const int it = seg_of_k(ik), nv = (seg_beg(it + 1) - seg_beg(it)) >> 4;
        lds_wait<4>(s_c + C_SC + ik, ik == 0 ? NW : NMW);
        f32x4* sp = reinterpret_cast<f32x4*>(s_sc + seg_beg(it) * 16) + lane;
        f32x4 v[4];
#pragma unroll
        for (int k = 0; k < 4; ++k) v[k] = sp[(k & (nv - 1)) * 64];
        const f32x4 mx = __builtin_elementwise_max(__builtin_elementwise_max(v[0], v[1]), __builtin_elementwise_max(v[2], v[3]));
        const float m = max_x16_32(row_max(fmaxf(fmaxf(mx[0], mx[1]), fmaxf(mx[2], mx[3]))));
        float l = 0.f;
#pragma unroll
        for (int k = 0; k < 4; ++k) {
#pragma unroll
          for (int e = 0; e < 4; ++e) v[k][e] = __builtin_amdgcn_exp2f(v[k][e] - m);
          if (k < nv) {
            l += (v[k][0] + v[k][1]) + (v[k][2] + v[k][3]);
            sp[k * 64] = v[k];
          }
        }
        l = wave_sum(l);
        if (lane == 0) s_ml[it] = u32x4{nz_bits(m), nz_bits(l), 0x3f800000u, 0x3f800000u};
        LGKM0();
        if (lane == 0) s_c[F_P] = ik + 1;
      }
    }
  };
  if (isW) {
    u32x4 bx[24];
    const char* p = reinterpret_cast<const char*>(wo + (size_t)((w_ks * 4 + tw) * DH + r) * DM + w_ct * 64 + j * 8);
#pragma unroll 1
    for (int i = 0; i < 24; ++i) { until(t0, tw == 0 ? W0START + i * (W0SPAN / 24) : W1START + i * (W1SPAN / 24)); glds16_nt(p, lds_base + i * 1024); p += 8 * DM * 2; VMCNT(WFD); }
    asm volatile("s_waitcnt vmcnt(0)" ::: "memory");
#pragma unroll
    for (int s = 0; s < 6; ++s) {
      u32x2 e[8];
#pragma unroll
      for (int k = 0; k < 8; ++k) e[k] = *reinterpret_cast<const u32x2*>(reg + (s * 32 + qd * 8 + k) * 128 + mr * 8);
#pragma unroll
      for (int k = 0; k < 4; ++k) {
        bx[s * 4 + 0][k] = PERM_LO(e[2 * k + 1][0], e[2 * k][0]); bx[s * 4 + 1][k] = PERM_HI(e[2 * k + 1][0], e[2 * k][0]);
        bx[s * 4 + 2][k] = PERM_LO(e[2 * k + 1][1], e[2 * k][1]); bx[s * 4 + 3][k] = PERM_HI(e[2 * k + 1][1], e[2 * k][1]);
      }
    }
#pragma unroll
    for (int s = 0; s < 24; s += 4) asm volatile("" : "+v"(bx[s]), "+v"(bx[s + 1]), "+v"(bx[s + 2]), "+v"(bx[s + 3]));
    LGKM0();
    prep();
    lds_wait<8>(s_c + F_REL, 1u);
    const int hh = w_ks * 4 + tw;
#pragma unroll 1
    for (int pass = 0; pass < 3; ++pass) {
      if (pass < 2) {
        done(pass ? NA : 4);
        pause(pass ? WDELAY2 : WDELAY2 + 300);
        const int b0 = pass * 2, b1 = pass + 1;
        u32x4 v0, v1;
#pragma unroll 1
        for (unsigned spin = 0; spin < SPIN_CAP; ++spin) {
          v0 = ld128(rs, WS_OC + (b0 * NH + hh) * DH + lp);
          v1 = ld128(rs, WS_OC + (b1 * NH + hh) * DH + lp);
          if (__all(min(min4(v0), min4(v1)) != 0u)) break;
          __builtin_amdgcn_s_sleep(16);
        }
        if (lane < DH / 4) {
          *reinterpret_cast<u32x2*>(reg + (b0 * 4) * AW_LD + lane * 8) = u32x2{v0[0], v0[1]};
          *reinterpret_cast<u32x2*>(reg + (b0 * 4 + 1) * AW_LD + lane * 8) = u32x2{v0[2], v0[3]};
          *reinterpret_cast<u32x2*>(reg + (b1 * 4) * AW_LD + lane * 8) = u32x2{v1[0], v1[1]};
          *reinterpret_cast<u32x2*>(reg + (b1 * 4 + 1) * AW_LD + lane * 8) = u32x2{v1[2], v1[3]};
        }
      } else {
        const float wk = weight(NS - 1, hh * GS, GS);
        float w[GS];
#pragma unroll
        for (int k = 0; k < GS; ++k) { w[k] = __uint_as_float(__builtin_amdgcn_readlane(__float_as_uint(wk), k)); asm volatile("" : "+s"(w[k])); }
        done(NS);
        __builtin_amdgcn_s_setprio(3);
        const int po = WS_PO + hh * GS * DH;
        u32x4 pv[GS];
#pragma unroll 1
        for (unsigned spin = 0; spin < SPIN_CAP; ++spin) {
#pragma unroll
          for (int k = 0; k < GS; ++k) pv[k] = __builtin_amdgcn_raw_buffer_load_b128(rs, lp * 4, (po + k * DH) * 4, AUX_SC1);
          unsigned mn = min4(pv[0]);
#pragma unroll
          for (int k = 1; k < GS; ++k) mn = min(mn, min4(pv[k]));
          if (__all(mn != 0u)) break;
          __builtin_amdgcn_s_sleep(1);
        }
        f32x4 o = w[0] * __builtin_bit_cast(f32x4, pv[0]), o2 = w[1] * __builtin_bit_cast(f32x4, pv[1]);
#pragma unroll
        for (int k = 2; k < GS; k += 2) { o += w[k] * __builtin_bit_cast(f32x4, pv[k]); o2 += w[k + 1] * __builtin_bit_cast(f32x4, pv[k + 1]); }
        u32x2 hi, lw;
        split4(o + o2, hi, lw);
        if (lane < DH / 4) {
          *reinterpret_cast<u32x2*>(reg + ((NB - 1) * 4) * AW_LD + lane * 8) = hi;
          *reinterpret_cast<u32x2*>(reg + ((NB - 1) * 4 + 1) * AW_LD + lane * 8) = lw;
        }
      }
      CFENCE();
      u32x4 a[6];
#pragma unroll
      for (int s = 0; s < 6; ++s) a[s] = *reinterpret_cast<const u32x4*>(reg + mr * AW_LD + s * 64 + qd * 16);
      __builtin_amdgcn_sched_barrier(0);
      f32x4 c[4];
#pragma unroll
      for (int t = 0; t < 4; ++t) c[t] = f32x4{0.f, 0.f, 0.f, 0.f};
#pragma unroll
      for (int s = 0; s < 6; ++s)
#pragma unroll
        for (int t = 0; t < 4; ++t) c[t] = mfma16(a[s], bx[s * 4 + t], c[t]);
      const bool mine = max(qd - 1, 0) == pass;
      f32x4* r4 = reinterpret_cast<f32x4*>(red);
      if (mine) r4[tw * 64 + lane] = f32x4{c[0][0] + c[0][1], c[1][0] + c[1][1], c[2][0] + c[2][1], c[3][0] + c[3][1]};
      const int k = arrive(s_c + C_W + pass);
      f32x4 sum = (r4[lane] + r4[64 + lane]) + (r4[128 + lane] + r4[192 + lane]);
      asm volatile("" : "+v"(sum));
      if (arrived(k) == 3) {
        if (mine) st128(rs, WS_SLAB + (w_ks * NB + qd) * DM + w_ct * 64 + mr * 4, u32x4{nz_bits(sum[0]), nz_bits(sum[1]), nz_bits(sum[2]), nz_bits(sum[3])});
      }
    }
  } else if (isG) {
    u32x4 bx[40];
    const size_t off = (size_t)(tw * 160 + r) * FF + g_cg * 64 + j * 8;
    const char* pg = reinterpret_cast<const char*>(wg + off);
    const char* pu = reinterpret_cast<const char*>(wu + off);
#pragma unroll
    for (int half = 0; half < 2; ++half) {
      const int nstep = half ? 2 : 3;
#pragma unroll 1
      for (int i = 0; i < nstep * 4; ++i) { until(t0, tw == 0 ? W0START + (half * 12 + i) * (W0SPAN / 20) : W1START + (half * 12 + i) * (W1SPAN / 20)); glds16_nt(pg, lds_base + i * 1024); glds16_nt(pu, lds_base + 12288 + i * 1024); pg += 8 * FF * 2; pu += 8 * FF * 2; VMCNT(WFD); }
      asm volatile("s_waitcnt vmcnt(0)" ::: "memory");
#pragma unroll
      for (int s = 0; s < nstep; ++s) {
        u32x4 e[8];
#pragma unroll
        for (int k = 0; k < 8; ++k) e[k] = *reinterpret_cast<const u32x4*>(reg + (mr >> 3) * 12288 + (s * 32 + qd * 8 + k) * 128 + (mr & 7) * 16);
#pragma unroll
        for (int c = 0; c < 4; ++c)
#pragma unroll
          for (int k = 0; k < 4; ++k) {
            bx[(half * 3 + s) * 8 + 2 * c][k] = PERM_LO(e[2 * k + 1][c], e[2 * k][c]);
            bx[(half * 3 + s) * 8 + 2 * c + 1][k] = PERM_HI(e[2 * k + 1][c], e[2 * k][c]);
          }
#pragma unroll
        for (int t = 0; t < 8; t += 4) asm volatile("" : "+v"(bx[(half * 3 + s) * 8 + t]), "+v"(bx[(half * 3 + s) * 8 + t + 1]), "+v"(bx[(half * 3 + s) * 8 + t + 2]), "+v"(bx[(half * 3 + s) * 8 + t + 3]));
      }
      LGKM0();
    }
    float cg[4], cu[4];
    {
      const u32x4 ones = {0x3f803f80u, 0x3f803f80u, 0x3f803f80u, 0x3f803f80u};
      f32x4 c[8];
#pragma unroll
      for (int t = 0; t < 8; ++t) c[t] = f32x4{0.f, 0.f, 0.f, 0.f};
#pragma unroll
      for (int s = 0; s < 5; ++s)
#pragma unroll
        for (int t = 0; t < 8; ++t) c[t] = mfma16(ones, bx[s * 8 + t], c[t]);
      f32x4* r4 = reinterpret_cast<f32x4*>(red);
      if (lane < 16) {
        r4[tw * 64 + 32 + lane * 2] = f32x4{c[0][0], c[1][0], c[2][0], c[3][0]};
        r4[tw * 64 + 32 + lane * 2 + 1] = f32x4{c[4][0], c[5][0], c[6][0], c[7][0]};
      }
    }
    uint2 wr4 = make_uint2(0u, 0u);
    if (lane < 40) {
      wr4 = *reinterpret_cast<const uint2*>(wr + tw * 160 + lane * 4);
#pragma unroll
      for (int b = 0; b < NB; ++b) *reinterpret_cast<uint2*>(reg + 8192 + b * 320 + lane * 8) = *reinterpret_cast<const uint2*>(x + row_of(b) * DM + tw * 160 + lane * 4);
    }
    {
      const float wsum = wave_sum((bf_lo(wr4.x) + bf_hi(wr4.x)) + (bf_lo(wr4.y) + bf_hi(wr4.y)));
      if (lane == 0) reinterpret_cast<float*>(smem + OFF_RS)[16 + tw] = wsum;
    }
    LGKM0();
    lds_add(s_c + C_W + 1, lane);
    prep();
    done_all();
    lds_wait<8>(s_c + C_W + 1, 4u);
    const unsigned tb = clk();
#pragma unroll
    for (int w = 0; w < 4; ++w) { cg[w] = red[w * 256 + 128 + lane]; cu[w] = red[w * 256 + 192 + lane]; }
    const f32x4 ws4 = reinterpret_cast<const f32x4*>(smem + OFF_RS)[4];
    float wsum[4];
#pragma unroll
    for (int w = 0; w < 4; ++w) wsum[w] = __uint_as_float(__builtin_amdgcn_readfirstlane(__float_as_uint(ws4[w])));
    lds_wait<8>(s_c + F_REL, 1u);
#pragma unroll 1
    for (int g_b = 0; g_b < NB; ++g_b) {
      int ln = lane;
      asm volatile("" : "+v"(ln));
      lds_wait<1>(s_c + C_W, g_b);
      if (g_b == 2) until(tb, G2LATE);
      if (g_b == NB - 1) {
        done(NS);
        until(clk(), GLATE);
      }
      f32x4 hv = {0.f, 0.f, 0.f, 0.f};
      if (ln < 40) {
        const int off = (WS_SLAB + g_b * DM + tw * 160 + ln * 4) * 4;
        uint2 xg = *reinterpret_cast<const uint2*>(reg + 8192 + g_b * 320 + ln * 8);
        asm volatile("" : "+v"(xg.x), "+v"(xg.y));
        u32x4 sl[KS];
#pragma unroll 1
        for (unsigned spin = 0; spin < SPIN_CAP; ++spin) {
#pragma unroll
          for (int ks = 0; ks < KS; ++ks) sl[ks] = __builtin_amdgcn_raw_buffer_load_b128(rs, off, ks * (NB * DM * 4), AUX_SC1);
          unsigned mn = min4(sl[0]);
#pragma unroll
          for (int ks = 1; ks < KS; ++ks) mn = min(mn, min4(sl[ks]));
          if (!__any(mn == 0u)) break;
          if (g_b == NB - 1) __builtin_amdgcn_s_sleep(1); else __builtin_amdgcn_s_sleep(16);
        }
#define SL(k) __builtin_bit_cast(f32x4, sl[k])
        hv = f32x4{bf_lo(xg.x), bf_hi(xg.x), bf_lo(xg.y), bf_hi(xg.y)} + (((SL(0) + SL(1)) + (SL(2) + SL(3))) + ((SL(4) + SL(5)) + (SL(6) + SL(7))));
#undef SL
      }
      const float cw = __uint_as_float(__builtin_amdgcn_readfirstlane(__float_as_uint(hv[0])));
      if (ln < 40) {
        hv -= cw;
        u32x2 hi, lw;
        split4(hv, hi, lw);
        *reinterpret_cast<u32x2*>(reg + ln * 8) = hi;
        *reinterpret_cast<u32x2*>(reg + AG_LD + ln * 8) = lw;
      }
      CFENCE();
      u32x4 a[5];
#pragma unroll
      for (int s = 0; s < 5; ++s) a[s] = *reinterpret_cast<const u32x4*>(reg + (ln & 1) * AG_LD + s * 64 + (ln >> 4) * 16);
      __builtin_amdgcn_sched_barrier(0);
      f32x4 c[8];
#pragma unroll
      for (int t = 0; t < 8; ++t) c[t] = f32x4{0.f, 0.f, 0.f, 0.f};
#pragma unroll
      for (int s = 0; s < 5; ++s)
#pragma unroll
        for (int t = 0; t < 8; ++t) c[t] = mfma16(a[s], bx[s * 8 + t], c[t]);
      const float s1 = wave_sum((hv[0] + hv[1]) + (hv[2] + hv[3]));
      const float s2 = wave_sum((hv[0] * hv[0] + hv[1] * hv[1]) + (hv[2] * hv[2] + hv[3] * hv[3]));
      const float dot = wave_sum((hv[0] * bf_lo(wr4.x) + hv[1] * bf_hi(wr4.x)) + (hv[2] * bf_lo(wr4.y) + hv[3] * bf_hi(wr4.y)));
      f32x4* r4 = reinterpret_cast<f32x4*>(red);
      f32x4* rst = reinterpret_cast<f32x4*>(smem + OFF_RS);
      if (ln < 16) {
        r4[tw * 64 + ln * 2] = f32x4{c[0][0] + c[0][1], c[1][0] + c[1][1], c[2][0] + c[2][1], c[3][0] + c[3][1]};
        r4[tw * 64 + ln * 2 + 1] = f32x4{c[4][0] + c[4][1], c[5][0] + c[5][1], c[6][0] + c[6][1], c[7][0] + c[7][1]};
        if (ln == 0) rst[tw] = f32x4{cw, s1, s2, dot};
      }
      const int k = arrive(s_c + C_T);
      f32x4 t0 = rst[0], t1 = rst[1], t2 = rst[2], t3 = rst[3];
      float pg[4], pu[4];
#pragma unroll
      for (int w = 0; w < 4; ++w) { pg[w] = red[w * 256 + ln]; pu[w] = red[w * 256 + 64 + ln]; }
      asm volatile("" : "+v"(t0), "+v"(t1), "+v"(t2), "+v"(t3));
      PIN4(pg, 0); PIN4(pu, 0);
      if (arrived(k) == g_b * 4 + 3) {
        const float mean = ((t0[0] + t1[0]) + (t2[0] + t3[0])) * 0.25f + ((t0[1] + t1[1]) + (t2[1] + t3[1])) * (1.0f / DM);
        const float d0 = t0[0] - mean, d1 = t1[0] - mean, d2 = t2[0] - mean, d3 = t3[0] - mean;
        const float M2 = ((t0[2] + d0 * (2.0f * t0[1] + 160.0f * d0)) + (t1[2] + d1 * (2.0f * t1[1] + 160.0f * d1))) +
                         ((t2[2] + d2 * (2.0f * t2[1] + 160.0f * d2)) + (t3[2] + d3 * (2.0f * t3[1] + 160.0f * d3)));
        const float rstd = __builtin_amdgcn_rsqf(fmaxf(M2, 0.f) * (1.0f / DM) + LN_EPS);
        const float gg = rstd * (((pg[0] + pg[1]) + (pg[2] + pg[3])) + ((d0 * cg[0] + d1 * cg[1]) + (d2 * cg[2] + d3 * cg[3])));
        const float uu = rstd * (((pu[0] + pu[1]) + (pu[2] + pu[3])) + ((d0 * cu[0] + d1 * cu[1]) + (d2 * cu[2] + d3 * cu[3])));
        const float act = __uint_as_float(nz_bits(gg * __builtin_amdgcn_rcpf(1.0f + __expf(-gg)) * uu));
        const u32x4 av = {__float_as_uint(act), __float_as_uint(dpp<0x55>(act)), __float_as_uint(dpp<0xAA>(act)), __float_as_uint(dpp<0xFF>(act))};
        const float rr = rstd * (((t0[3] + t1[3]) + (t2[3] + t3[3])) + ((d0 * wsum[0] + d1 * wsum[1]) + (d2 * wsum[2] + d3 * wsum[3])));
        const u32x4 sv = {nz_bits(mean), nz_bits(rstd), nz_bits(__builtin_amdgcn_rcpf(1.0f + __expf(-rr))), 0x3f800000u};
        const bool stat = g_cg == 0 && ln == 1;
        if ((ln & 3) == 0 || stat) st128(rs, stat ? WS_ST + g_b * 4 : WS_ACT + g_b * FF + g_cg * 64 + ln, stat ? sv : av);
        LGKM0();
        if (ln == 0) s_c[C_W] = g_b + 1;
      }
    }
  } else if (isD) {
    u32x4 bd[24];
    unsigned xd;
    const char* p = reinterpret_cast<const char*>(wd + (size_t)(tw * 384 + (lane >> 2)) * DM + d_ct * 32 + (lane & 3) * 8);
#pragma unroll 1
    for (int i = 0; i < 24; ++i) { until(t0, tw == 0 ? W0START + i * (W0SPAN / 24) : W1START + i * (W1SPAN / 24)); glds16_nt(p, lds_base + i * 1024); p += 16 * DM * 2; VMCNT(WFD); }
    xd = *reinterpret_cast<const unsigned*>(x + row_of(qd) * DM + d_ct * 32 + mr * 2);
    asm volatile("s_waitcnt vmcnt(0)" ::: "memory");
#pragma unroll
    for (int s = 0; s < 12; ++s) {
      unsigned e[8];
#pragma unroll
      for (int k = 0; k < 8; ++k) e[k] = *reinterpret_cast<const unsigned*>(reg + (s * 32 + qd * 8 + k) * 64 + mr * 4);
#pragma unroll
      for (int k = 0; k < 4; ++k) { bd[s * 2][k] = PERM_LO(e[2 * k + 1], e[2 * k]); bd[s * 2 + 1][k] = PERM_HI(e[2 * k + 1], e[2 * k]); }
    }
#pragma unroll
    for (int s = 0; s < 24; s += 4) asm volatile("" : "+v"(bd[s]), "+v"(bd[s + 1]), "+v"(bd[s + 2]), "+v"(bd[s + 3]));
    LGKM0();
    prep();
    lds_wait<8>(s_c + F_REL, 1u);
    done_all();
    done(NS);
    const unsigned te = clk();
    auto put = [&](int id, u32x4 v) {
      const int bi = id / 96, pos = id - bi * 96;
      u32x2 hi, lw;
      split4(__builtin_bit_cast(f32x4, v), hi, lw);
      *reinterpret_cast<u32x2*>(reg + (bi * 4) * AD_LD + pos * 8) = hi;
      *reinterpret_cast<u32x2*>(reg + (bi * 4 + 1) * AD_LD + pos * 8) = lw;
    };
    auto word = [&](int id) -> int { const int bi = id / 96; return WS_ACT + bi * FF + tw * 384 + (id - bi * 96) * 4; };
    {
      u32x4 av[5];
#pragma unroll 1
      for (unsigned spin = 0; spin < SPIN_CAP; ++spin) {
        bool ok = true;
#pragma unroll
        for (int i = 0; i < 5; ++i) av[i] = ld128(rs, word(min(i * 64 + lane, 287)));
#pragma unroll
        for (int i = 0; i < 5; ++i) ok = ok && all_nz(av[i]);
        if (__all(ok)) break;
        __builtin_amdgcn_s_sleep(8);
      }
#pragma unroll
      for (int i = 0; i < 5; ++i) put(min(i * 64 + lane, 287), av[i]);
    }
    until(te, DLATE);
    f32x2 hv;
    {
      const int off = (WS_SLAB + qd * DM + d_ct * 32 + mr * 2) * 4;
      u32x2 sl[KS];
#pragma unroll 1
      for (unsigned spin = 0; spin < SPIN_CAP; ++spin) {
#pragma unroll
        for (int ks = 0; ks < KS; ++ks) sl[ks] = __builtin_amdgcn_raw_buffer_load_b64(rs, off, ks * (NB * DM * 4), AUX_SC1);
        unsigned mn = min(sl[0][0], sl[0][1]);
#pragma unroll
        for (int ks = 1; ks < KS; ++ks) mn = min(mn, min(sl[ks][0], sl[ks][1]));
        if (__all(mn != 0u)) break;
        __builtin_amdgcn_s_sleep(4);
      }
#define SL(k) __builtin_bit_cast(f32x2, sl[k])
      hv = bf_pair(xd) + (((SL(0) + SL(1)) + (SL(2) + SL(3))) + ((SL(4) + SL(5)) + (SL(6) + SL(7))));
#undef SL
      asm volatile("" : "+v"(hv));
    }
    until(clk(), DLATE2);
    u32x4 a0, a1, st;
    const int id1 = 352 + (lane & 31);
#pragma unroll 1
    for (unsigned spin = 0; spin < SPIN_CAP; ++spin) {
      a0 = ld128(rs, word(288 + lane));
      a1 = ld128(rs, word(id1));
      st = ld128(rs, WS_ST + qd * 4);
      if (__all(min(min(min4(a0), min4(a1)), min4(st)) != 0u)) break;
      __builtin_amdgcn_s_sleep(1);
    }
    put(288 + lane, a0);
    put(id1, a1);
    CFENCE();
    u32x4 a[12];
#pragma unroll
    for (int s = 0; s < 12; ++s) a[s] = *reinterpret_cast<const u32x4*>(reg + mr * AD_LD + s * 64 + qd * 16);
    __builtin_amdgcn_sched_barrier(0);
    f32x4 c0 = {0.f, 0.f, 0.f, 0.f}, c1 = c0;
#pragma unroll
    for (int s = 0; s < 12; ++s) {
      c0 = mfma16(a[s], bd[2 * s], c0);
      c1 = mfma16(a[s], bd[2 * s + 1], c1);
    }
    f32x2* r2 = reinterpret_cast<f32x2*>(red);
    r2[tw * 64 + lane] = f32x2{c0[0] + c0[1], c1[0] + c1[1]};
    const int k = arrive(s_c + C_T);
    f32x2 y = (r2[lane] + r2[64 + lane]) + (r2[128 + lane] + r2[192 + lane]);
    asm volatile("" : "+v"(y));
    if (arrived(k) == 3) {
      until(t0, 14000);
      const f32x4 sf = __builtin_bit_cast(f32x4, st);
      const __hip_bfloat162 o2 = __halves2bfloat162(__float2bfloat16(hv[0] + ((hv[0] - sf[0]) * sf[1] + y[0] * sf[2])),
                                                    __float2bfloat16(hv[1] + ((hv[1] - sf[0]) * sf[1] + y[1] * sf[2])));
      __hip_atomic_store(reinterpret_cast<unsigned*>(out + row_of(qd) * DM + d_ct * 32 + mr * 2), *reinterpret_cast<const unsigned*>(&o2), __ATOMIC_RELAXED, __HIP_MEMORY_SCOPE_AGENT);
    }
  } else {
    prep();
    if (isC && tw == 1) {
      lds_wait<8>(s_c + F_REL, 1u);
      const int cs = c_it != 1 ? c_it * 2 : max(g >> 3, 1), cnt = c_it != 1 ? 8 : (g < 16 ? 16 : 32), x0 = c_it != 1 ? g * 8 : (g < 16 ? g * 16 : (g & 7) * 32);
      done(c_it < 2 ? 4 : NA);
      pause(WDELAY);
      const float wk = weight(cs, x0, cnt);
      f32x4 o = {0.f, 0.f, 0.f, 0.f};
#pragma unroll 1
      for (int k0 = 0; k0 < cnt; k0 += 16) {
        u32x2 pv[16];
#pragma unroll
        for (int k = 0; k < 16; ++k) pv[k] = __builtin_amdgcn_raw_buffer_load_b64(rs, lp * 2, (WS_PQ + ((x0 + ((k0 + k) & (cnt - 1))) * 5 + cs) * (DH / 2)) * 4, AUX_EARLY);
#pragma unroll
        for (int k = 0; k < 16; ++k) {
          const float w = k0 + k < cnt ? __uint_as_float(__builtin_amdgcn_readlane(__float_as_uint(wk), k0 + k)) : 0.f;
          o += w * f32x4{(float)(short)pv[k][0], (float)((int)pv[k][0] >> 16), (float)(short)pv[k][1], (float)((int)pv[k][1] >> 16)};
        }
      }
      u32x2 hi, lw;
      split4(o, hi, lw);
      if (lane < DH / 4) st128(rs, WS_OC + (c_it * NG + g) * DH + lane * 4, u32x4{max(hi[0], 1u), max(hi[1], 1u), max(lw[0], 1u), max(lw[1], 1u)});
    }
  }
  if (tw == 3) done_all();
}

extern "C" void kernel_launch(void* const* d_in, const int* in_sizes, int n_in, void* d_out, int out_size, void* d_ws,
                              size_t ws_size, hipStream_t stream) {
  const bf16* x = (const bf16*)d_in[0];
  const bf16* kc = (const bf16*)d_in[1];
  const bf16* vc = (const bf16*)d_in[2];
  const bf16* wq = (const bf16*)d_in[3];
  const bf16* wo = (const bf16*)d_in[4];
  const bf16* wr = (const bf16*)d_in[6];
  const bf16* wg = (const bf16*)d_in[7];
  const bf16* wu = (const bf16*)d_in[8];
  const bf16* wd = (const bf16*)d_in[9];
  fused_kernel<<<NWG, NW * 64, 0, stream>>>(x, kc, vc, wq, wo, wr, wg, wu, wd, (float*)d_ws, (bf16*)d_out);
}
```

```cpp
#include <hip/hip_runtime.h>
#include <hip/hip_bf16.h>
#include <stdint.h>

typedef __hip_bfloat16 bf16;
using u32x2 = __attribute__((ext_vector_type(2))) unsigned;
using u32x4 = __attribute__((ext_vector_type(4))) unsigned;
using f32x4 = __attribute__((ext_vector_type(4))) float;
using f32x2 = __attribute__((ext_vector_type(2))) float;
using bf16x8 = __attribute__((ext_vector_type(8))) __bf16;

constexpr int NB = 4, DM = 640, NH = 32, SK = 8192, DH = 192, HD = NH * DH, FF = 1536;
constexpr float LN_EPS = 1e-5f;
constexpr float QSCALE = 0.07216878364870322f * 1.4426950408889634f;

constexpr int NWG = 256;
constexpr int GS = 8, NG = NWG / GS, NI = GS / 2;
constexpr int HPG = NI / NB;
constexpr int PPI = 512 / GS;
constexpr int NPAIR = NI * PPI;

constexpr int TAUK = 1320, TAUV = 1338;
constexpr int TSTART = 200;
constexpr int WGAP = 165, WP0 = 64, WPN = 176;
constexpr int VGAP = 90;
constexpr int SPRINT = 4;
constexpr int JUMP = 145;
constexpr int HST = 2800, RT0 = 3900, RTS = 1000;
constexpr unsigned NRD = 5, RMIN = 50, FBP = 60, FBL = 150;
constexpr int W0START = 1700, W0SPAN = 1000, W1START = 2700, W1SPAN = 2600;
constexpr int ROT = 16, ROWS = 0xC9;
constexpr int FBM = 100, FBX = 15, FB0 = 170, FB1 = 55, FBS = 10;
constexpr int FBD = 20, HOLD = 400, GB = 24, GBS = 2;
constexpr unsigned PRD = 3; constexpr int PRB = 24;
constexpr int TLEAD = 40, PFD = 6, WFD = 3;
constexpr int QWAIT = 750, QBACK = 100;
constexpr int WDELAY = 100, WDELAY2 = 460, GLATE = 150, DLATE = 220, DLATE2 = 60;
constexpr int GLEAD = 1, GCNT = 4, DP = 0, DPW = 60, PW = 0, HDELAY = 1150, HDELAY2 = 780, GW = 30, DW = 50;

constexpr int KS = 8;
constexpr int WS_PML = 0;
constexpr int WS_PO = WS_PML + NWG * 64;
constexpr int WS_PQ = WS_PO + NWG * DH;
constexpr int WS_OC = WS_PQ + NWG * 5 * (DH / 2);
constexpr int WS_SLAB = WS_OC + (NB - 1) * NH * DH;
constexpr int WS_ACT = WS_SLAB + KS * NB * DM;
constexpr int WS_H = WS_ACT + NB * FF;
constexpr int WS_ST = WS_H + (NB - 1) * 4 * 192;
constexpr int WS_HN = WS_ST + 32;
constexpr int WS_HR = WS_HN + NWG;
constexpr int WS_HA = WS_HR + 2 * NWG;
constexpr int QFS = 2 * GS * 32;
constexpr int WS_QF = WS_HA + 32 * NWG;
constexpr int WS_QP = WS_QF + NG * QFS;
constexpr int WS_END = WS_QP + NH * 2 * NB * DH;
constexpr int NPROD = HPG * 6;
static_assert(WS_PO % 32 == 0 && WS_SLAB % 32 == 0 && WS_ACT % 32 == 0 && WS_ST % 32 == 0 && WS_HN % 32 == 0 && WS_QF % 32 == 0 && WS_QP % 32 == 0, "line alignment");


__device__ __forceinline__ int row_of(int b) { return (ROWS >> (2 * b)) & 3; }
__device__ __forceinline__ float bf_lo(unsigned u) { return __uint_as_float(u << 16); }
__device__ __forceinline__ float bf_hi(unsigned u) { return __uint_as_float(u & 0xffff0000u); }
__device__ __forceinline__ f32x2 bf_pair(unsigned u) { return f32x2{bf_lo(u), bf_hi(u)}; }
__device__ __forceinline__ f32x2 fma2(float a, f32x2 b, f32x2 c) { return __builtin_elementwise_fma(f32x2{a, a}, b, c); }
__device__ __forceinline__ f32x2 fma2(f32x2 a, f32x2 b, f32x2 c) { return __builtin_elementwise_fma(a, b, c); }

template <int CTRL> __device__ __forceinline__ float dpp(float x) {
  return __builtin_bit_cast(float, __builtin_amdgcn_mov_dpp(__builtin_bit_cast(int, x), CTRL, 0xf, 0xf, true));
}
constexpr int DPP_XOR1 = 0xB1, DPP_XOR2 = 0x4E, DPP_HMIRROR = 0x141, DPP_MIRROR = 0x140, DPP_ROR8 = 0x128;

__device__ __forceinline__ float sum_x16_32(float x) {
  auto s = __builtin_amdgcn_permlane16_swap(__float_as_uint(x), __float_as_uint(x), false, false);
  x = __uint_as_float(s[0]) + __uint_as_float(s[1]);
  auto t = __builtin_amdgcn_permlane32_swap(__float_as_uint(x), __float_as_uint(x), false, false);
  return __uint_as_float(t[0]) + __uint_as_float(t[1]);
}
__device__ __forceinline__ float max_x16_32(float x) {
  auto s = __builtin_amdgcn_permlane16_swap(__float_as_uint(x), __float_as_uint(x), false, false);
  x = fmaxf(__uint_as_float(s[0]), __uint_as_float(s[1]));
  auto t = __builtin_amdgcn_permlane32_swap(__float_as_uint(x), __float_as_uint(x), false, false);
  return fmaxf(__uint_as_float(t[0]), __uint_as_float(t[1]));
}
__device__ __forceinline__ float tsum32(float a, float b) {
  auto t = __builtin_amdgcn_permlane32_swap(__float_as_uint(a), __float_as_uint(b), false, false);
  return __uint_as_float(t[0]) + __uint_as_float(t[1]);
}
__device__ __forceinline__ float tsum16(float a, float b) {
  auto t = __builtin_amdgcn_permlane16_swap(__float_as_uint(a), __float_as_uint(b), false, false);
  return __uint_as_float(t[0]) + __uint_as_float(t[1]);
}
__device__ __forceinline__ float sum_r(float v) { v += dpp<DPP_ROR8>(v); return sum_x16_32(v); }
__device__ __forceinline__ float row_sum(float v) {
  v += dpp<DPP_XOR1>(v); v += dpp<DPP_XOR2>(v); v += dpp<DPP_HMIRROR>(v);
  return v + dpp<DPP_MIRROR>(v);
}
__device__ __forceinline__ float row_max(float v) {
  v = fmaxf(v, dpp<DPP_XOR1>(v)); v = fmaxf(v, dpp<DPP_XOR2>(v)); v = fmaxf(v, dpp<DPP_HMIRROR>(v));
  return fmaxf(v, dpp<DPP_MIRROR>(v));
}
__device__ __forceinline__ float wave_sum(float v) {
  v += dpp<DPP_XOR1>(v); v += dpp<DPP_XOR2>(v); v += dpp<DPP_HMIRROR>(v);
  return sum_r(v);
}

__device__ __forceinline__ u32x4 ld_nt(const char* p) { return __builtin_nontemporal_load(reinterpret_cast<const u32x4*>(p)); }

struct Tile { u32x4 d[3]; };

#define PIN4(a, o) asm volatile("" : "+v"((a)[(o) + 0]), "+v"((a)[(o) + 1]), "+v"((a)[(o) + 2]), "+v"((a)[(o) + 3]))
#define MEMFENCE() do { asm volatile("" ::: "memory"); __builtin_amdgcn_sched_barrier(0); } while (0)

__device__ __forceinline__ void glds16_nt(const void* gsrc, unsigned lds_dst) {
  unsigned keep;
  asm volatile("s_mov_b32 %0, m0\n\ts_mov_b32 m0, %2\n\ts_nop 0\n\tglobal_load_lds_dwordx4 %1, off nt\n\ts_mov_b32 m0, %0"
               : "=&s"(keep) : "v"(gsrc), "s"(lds_dst) : "memory");
}

__device__ __forceinline__ float tile_score(const Tile& k, const f32x2 (&q)[12]) {
  f32x2 s0 = {0.f, 0.f}, s1 = s0;
#pragma unroll
  for (int i = 0; i < 3; ++i) {
    s0 = fma2(bf_pair(k.d[i][0]), q[i * 4], s0);     s1 = fma2(bf_pair(k.d[i][1]), q[i * 4 + 1], s1);
    s0 = fma2(bf_pair(k.d[i][2]), q[i * 4 + 2], s0); s1 = fma2(bf_pair(k.d[i][3]), q[i * 4 + 3], s1);
  }
  s0 += s1;
  return s0[0] + s0[1];
}
__device__ __forceinline__ void score_put(float s, float* dst, int j) {
  s += dpp<DPP_XOR1>(s);
  s += dpp<DPP_XOR2>(s);
  s += dpp<DPP_HMIRROR>(s);
  if (j == 0) *dst = s;
}
__device__ __forceinline__ void tile_load(Tile& t, const char* base, unsigned off, int half) {
#pragma unroll
  for (int i = 0; i < 3; ++i) t.d[i] = ld_nt(base + off + half * 3072 + i * 128);
}
__device__ __forceinline__ float score_next(Tile& k, const f32x2 (&q)[12], const char* base, unsigned off, int half) {
  f32x2 s0 = {0.f, 0.f}, s1 = s0;
#pragma unroll
  for (int i = 0; i < 3; ++i) {
    s0 = fma2(bf_pair(k.d[i][0]), q[i * 4], s0);     s1 = fma2(bf_pair(k.d[i][1]), q[i * 4 + 1], s1);
    s0 = fma2(bf_pair(k.d[i][2]), q[i * 4 + 2], s0); s1 = fma2(bf_pair(k.d[i][3]), q[i * 4 + 3], s1);
    asm volatile("" : "+v"(s0), "+v"(s1));
    MEMFENCE();
    k.d[i] = ld_nt(base + off + half * 3072 + i * 128);
    MEMFENCE();
  }
  s0 += s1;
  return s0[0] + s0[1];
}
__device__ __forceinline__ void pv_next(float p, Tile& v, f32x2 (&acc)[12], const char* base, unsigned off, int half) {
#pragma unroll
  for (int i = 0; i < 3; ++i) {
    asm volatile("" : "+v"(v.d[i]), "+v"(p));
#pragma unroll
    for (int d = 0; d < 4; ++d) acc[i * 4 + d] = fma2(p, bf_pair(v.d[i][d]), acc[i * 4 + d]);
    PIN4(acc, i * 4);
    MEMFENCE();
    v.d[i] = ld_nt(base + off + half * 3072 + i * 128);
    MEMFENCE();
  }
}
__device__ __forceinline__ void pv_last(float p, Tile& v, f32x2 (&acc)[12]) {
#pragma unroll
  for (int i = 0; i < 3; ++i) {
    asm volatile("" : "+v"(v.d[i]), "+v"(p));
#pragma unroll
    for (int d = 0; d < 4; ++d) acc[i * 4 + d] = fma2(p, bf_pair(v.d[i][d]), acc[i * 4 + d]);
    PIN4(acc, i * 4);
  }
}
__device__ __forceinline__ void q_load(f32x2 (&q)[12], const float* src, int j) {
#pragma unroll
  for (int i = 0; i < 6; ++i) {
    const f32x4 a = *reinterpret_cast<const f32x4*>(src + (i >> 1) * 64 + j * 8 + (i & 1) * 4);
    q[i * 2] = f32x2{a[0], a[1]}; q[i * 2 + 1] = f32x2{a[2], a[3]};
  }
}
#define VMCNT(n) asm volatile("s_waitcnt vmcnt(%0)" :: "n"(n) : "memory")

constexpr int AUX_SC1 = 16, AUX_EARLY = 18;
constexpr unsigned SPIN_CAP = 1u << 17;
__device__ __forceinline__ unsigned nz_bits(float v) { return max(__float_as_uint(v), 1u); }
__device__ __forceinline__ bool all_nz(u32x4 v) { return v[0] != 0u && v[1] != 0u && v[2] != 0u && v[3] != 0u; }
__device__ __forceinline__ unsigned min4(u32x4 v) { return min(min(v[0], v[1]), min(v[2], v[3])); }
__device__ __forceinline__ unsigned ld32(__amdgpu_buffer_rsrc_t rs, int word) { return __builtin_amdgcn_raw_buffer_load_b32(rs, word * 4, 0, AUX_SC1); }
__device__ __forceinline__ u32x2 ld64(__amdgpu_buffer_rsrc_t rs, int word) { return __builtin_amdgcn_raw_buffer_load_b64(rs, word * 4, 0, AUX_SC1); }
__device__ __forceinline__ u32x4 ld128(__amdgpu_buffer_rsrc_t rs, int word) { return __builtin_amdgcn_raw_buffer_load_b128(rs, word * 4, 0, AUX_SC1); }
__device__ __forceinline__ void st32(__amdgpu_buffer_rsrc_t rs, int word, unsigned v) { __builtin_amdgcn_raw_buffer_store_b32(v, rs, word * 4, 0, AUX_SC1); }
__device__ __forceinline__ void st64(__amdgpu_buffer_rsrc_t rs, int word, u32x2 v) { __builtin_amdgcn_raw_buffer_store_b64(v, rs, word * 4, 0, AUX_SC1); }
__device__ __forceinline__ void st128(__amdgpu_buffer_rsrc_t rs, int word, u32x4 v) { __builtin_amdgcn_raw_buffer_store_b128(v, rs, word * 4, 0, AUX_SC1); }

__device__ __forceinline__ bool leader_release(__amdgpu_buffer_rsrc_t rs, int lane, unsigned t0, unsigned rd) {
  u32x4 a[4];
#pragma unroll
  for (int k = 0; k < 4; ++k) a[k] = ld128(rs, WS_HA + (lane * 4 + k) * 32);
  const u32x4 n = ld128(rs, WS_HN + lane * 4);
  if (rd == 0u && !__all(a[0][0] == n[0] && a[1][0] == n[1] && a[2][0] == n[2] && a[3][0] == n[3])) return false;
  const float e = fmaxf(fmaxf((float)(int)(t0 - a[0][2]), (float)(int)(t0 - a[1][2])), fmaxf((float)(int)(t0 - a[2][2]), (float)(int)(t0 - a[3][2])));
  unsigned first = t0 - (unsigned)(int)max_x16_32(row_max(e));
  float ls = 0.f, lx = 0.f;
#pragma unroll
  for (int k = 0; k < 4; ++k) {
    const float l = a[k][0] == n[k] + rd ? (float)min(a[k][3] + (rd ? 0u : min(a[k][2] - first, 300u)), 2000u) : 0.f;
    ls += l; lx = fmaxf(lx, l);
  }
  ls = wave_sum(ls) * (1.0f / NWG); lx = max_x16_32(row_max(lx));
  const int extra = min(max((int)(ls * (FBM / 100.0f) + lx * (FBX / 100.0f)) - (rd ? FB1 + (int)(NRD - rd) * FBD : FB0), 0), 600);
  first = (first & 0xffffu) | ((unsigned)extra << 16);
  if (rd != 0u && ls * NWG < HOLD) return true;
  st128(rs, WS_HR + lane * 8, u32x4{a[0][1] + rd, first, a[1][1] + rd, first});
  st128(rs, WS_HR + lane * 8 + 4, u32x4{a[2][1] + rd, first, a[3][1] + rd, first});
  return true;
}

typedef __attribute__((address_space(3))) unsigned lds_u32;
__device__ __forceinline__ unsigned lds_add(lds_u32* p, int lane) {
  unsigned k = 0;
  if (lane == 0) k = __hip_atomic_fetch_add(p, 1u, __ATOMIC_RELAXED, __HIP_MEMORY_SCOPE_WORKGROUP);
  return __builtin_amdgcn_readfirstlane(k);
}
#define CFENCE() asm volatile("" ::: "memory")
__device__ __forceinline__ int arrive(lds_u32* p) {
  int v;
  unsigned long long keep;
  asm volatile("s_mov_b64 %1, exec\n\ts_mov_b64 exec, 1\n\tds_add_rtn_u32 %0, %2, %3\n\ts_mov_b64 exec, %1"
               : "=&v"(v), "=&s"(keep) : "v"((unsigned)(size_t)p), "v"(1u) : "memory");
  return v;
}
__device__ __forceinline__ int arrived(int v) {
  asm volatile("s_waitcnt lgkmcnt(0)" : "+v"(v) :: "memory");
  return __builtin_amdgcn_readfirstlane(v);
}
__device__ __forceinline__ unsigned lds_get(lds_u32* p) {
  return __builtin_amdgcn_readfirstlane(__hip_atomic_load(p, __ATOMIC_RELAXED, __HIP_MEMORY_SCOPE_WORKGROUP));
}
template <int SLEEP> __device__ __forceinline__ void lds_wait(lds_u32* p, unsigned n) {
#pragma unroll 1
  for (unsigned spin = 0; spin < (SPIN_CAP << 4); ++spin) {
    if (lds_get(p) >= n) break;
    __builtin_amdgcn_s_sleep(SLEEP);
  }
  asm volatile("" ::: "memory");
}
__device__ __forceinline__ void pause(unsigned ticks) {
  const unsigned long long t0 = __builtin_amdgcn_s_memrealtime();
#pragma unroll 1
  for (unsigned spin = 0; spin < 4096u && __builtin_amdgcn_s_memrealtime() - t0 < ticks; ++spin) __builtin_amdgcn_s_sleep(16);
}
__device__ __forceinline__ unsigned clk() { return (unsigned)__builtin_amdgcn_s_memrealtime(); }
struct Table {
  unsigned t0; int was, moved, slowed;
  __device__ __forceinline__ void gate(int due, lds_u32* mine, lds_u32* slow) {
    const int left = (int)lds_get(slow) - slowed, d = max(min(left, FBS), -GBS);
    t0 += d; was -= d; slowed += d;
    int late = (int)(clk() - t0) - due;
    if (late - was > JUMP) { t0 += late - was; moved += late - was; late = was; *mine = moved; }
    mine[4] = max(late - max(left - d, 0), 0);
    was = late;
#pragma unroll 1
    for (int spin = 0; spin < 65536 && late < 0; ++spin) { __builtin_amdgcn_s_sleep(1); late = (int)(clk() - t0) - due; }
  }
  __device__ __forceinline__ void agree(lds_u32* all, lds_u32* mine) {
    if (moved == 0) return;
    const int m = (int)min(min(lds_get(all), lds_get(all + 1)), min(lds_get(all + 2), lds_get(all + 3))) + 30;
    if (m < moved) { t0 -= moved - m; moved = m; was = 1 << 20; *mine = moved; }
  }
};
__device__ __forceinline__ void until(unsigned t0, int due) {
#pragma unroll 1
  for (int spin = 0; spin < 65536 && (int)(clk() - t0) < due; ++spin) __builtin_amdgcn_s_sleep(1);
}
#define LGKM0() asm volatile("s_waitcnt lgkmcnt(0)" ::: "memory")

__device__ __forceinline__ void split4(f32x4 v, u32x2& hi, u32x2& lo) {
  unsigned h[4], r[4];
#pragma unroll
  for (int i = 0; i < 4; ++i) { h[i] = __float_as_uint(v[i]) & 0xffff0000u; r[i] = __float_as_uint(v[i] - __uint_as_float(h[i])); }
  hi = u32x2{__builtin_amdgcn_perm(h[1], h[0], 0x07060302u), __builtin_amdgcn_perm(h[3], h[2], 0x07060302u)};
  lo = u32x2{__builtin_amdgcn_perm(r[1], r[0], 0x07060302u), __builtin_amdgcn_perm(r[3], r[2], 0x07060302u)};
}
__device__ __forceinline__ f32x4 mfma16(u32x4 a, u32x4 b, f32x4 c) {
  return __builtin_amdgcn_mfma_f32_16x16x32_bf16(__builtin_bit_cast(bf16x8, a), __builtin_bit_cast(bf16x8, b), c, 0, 0, 0);
}

constexpr int NW = 8, NMW = 4;
constexpr int NLP = 5, NRP = 7;
constexpr int NTP = 4 * (NLP + NRP);
constexpr int REG = NLP * 6 * 1024;
constexpr int NRING = 4;
constexpr int NS = 6, NA = NS - 1;
__device__ __forceinline__ int seg_beg(int s) { return s >= NS ? NPAIR : ((0xC87640 >> (4 * s)) & 15) * 16; }
__device__ __forceinline__ int seg_k(int t) { return (int)(0x5433222211110000ull >> (4 * (t >> 4))) & 15; }
__device__ __forceinline__ int seg_of_k(int k) { return (0x321540 >> (4 * k)) & 15; }
__device__ __forceinline__ int k_of_seg(int s) { return (0x215430 >> (4 * s)) & 15; }
__device__ __forceinline__ int pair_of(int t) { return t < 64 ? t : (t < 192 ? t + 64 : t - 128); }
constexpr int OFF_CNT = 0, OFF_REG = 256,
              OFF_SC = OFF_REG + 4 * REG, OFF_BUF = OFF_SC + NPAIR * 16 * 4, OFF_Q = OFF_BUF + NRING * NMW * DH * 4, OFF_ML = OFF_Q + NS * DH * 4,
              OFF_RED = OFF_ML + 128, OFF_RS = OFF_RED + 4 * 256 * 4, OFF_END = OFF_RS + 5 * 16;
enum { F_REL = 0, F_DONE, C_LAST, C_T = 3, F_OUT, C_P, C_K, F_P, F_Q = 8, F_FREE, S_FIRST, F_Q2, C_W = 12, C_SC = 16, C_PART = 24, S_ECHO = 32, S_MOVED = 48, S_SLOW = 56 };
__device__ __forceinline__ int due_k(int p) { return TSTART + ((p * TAUK + min(max(p - WP0, 0), WPN) * (WGAP * 64 / WPN)) >> 6); }
__device__ __forceinline__ int due_v(int p) { return TSTART + WGAP + ((NPAIR * TAUK + p * TAUV + max(p - NPAIR / 2, 0) * (VGAP * 64 / (NPAIR / 2))) >> 6); }
constexpr int AW_LD = DH * 2 + 16, AG_LD = 160 * 2 + 16;

__global__ void __launch_bounds__(NW * 64) fused_kernel(const bf16* __restrict__ x, const bf16* __restrict__ kc,
                                                        const bf16* __restrict__ vc, const bf16* __restrict__ wq,
                                                        const bf16* __restrict__ wo, const bf16* __restrict__ wr,
                                                        const bf16* __restrict__ wg, const bf16* __restrict__ wu,
                                                        const bf16* __restrict__ wd, float* ws, bf16* __restrict__ out) {
  __shared__ __attribute__((aligned(16))) unsigned char smem[OFF_END];
  float* s_sc = reinterpret_cast<float*>(smem + OFF_SC);
  float* s_buf = reinterpret_cast<float*>(smem + OFF_BUF);
  float* s_q = reinterpret_cast<float*>(smem + OFF_Q);
  u32x4* s_ml = reinterpret_cast<u32x4*>(smem + OFF_ML);
  lds_u32* s_c = (lds_u32*)(smem + OFF_CNT);

  const int tid = threadIdx.x, lane = tid & 63, r = lane >> 3, j = lane & 7;
  const int wave = __builtin_amdgcn_readfirstlane(tid >> 6);
  const int tw = wave - NMW;
  const int bid = blockIdx.x, xl = bid & 7, idx = bid >> 3;
  const int g = (xl * 32 + idx) / GS, mm = idx % GS;
  const __amdgpu_buffer_rsrc_t rs = __builtin_amdgcn_make_buffer_rsrc(ws, 0, WS_END * 4, 0x00020000);
  const int ro = ((idx & 3) << 3) | (idx >> 2);
  const bool isW = ro < 10, isG = ro >= 10 && ro < 16;
  const int w_ks = xl, w_ct = ro;
  const int g_cg = xl * 6 + (ro - 10);
  const int d_ct = (xl + ((ro - 16) >> 2) * 8) * 2 + (((ro - 16) >> 1) & 1);
  const bool isD = ro >= 16 && ro < 24 && (ro & 1) == 0 && d_ct < DM / 32;
  const bool isH = ro == 22 && xl >= 2 && xl < 5;
  const bool isC = (mm & 7) == 3 || (mm & 7) >= 6;
  const int c_it = (mm >> 3) * 3 + max((mm & 7) - 5, 0);
  const int qf = WS_QF + g * QFS;
  const char* kb = reinterpret_cast<const char*>(kc);
  const char* vb = reinterpret_cast<const char*>(vc);
  const unsigned lo = (r * 24 + j) * 16;
  const int xb = g * GS + mm, c0 = mm * 8 + g * ROT;
  const int b2 = (2 * NH + (xb >> 4)) * 1024 + (xb & 15) * 8, b3 = (2 * NH + 16 + (xb >> 5)) * 1024 + (xb & 31) * 8;
  auto poff = [&](int p) -> unsigned {
    const int turn = (p & 63) >> 2;
    int t = (row_of(p >> 6) * NH + g) * 1024 + ((turn * 64 + (p & 3) * 2 + c0) & 1023);
    if ((p >> 6) == 1) t = (turn < 8 ? b2 + turn * 128 : b3 + (turn & 4) * 2048 + (turn & 3) * 256) + (p & 3) * 2;
    return (unsigned)t * 3072u;
  };
  const unsigned lds_base = (unsigned)(size_t)(__attribute__((address_space(3))) unsigned char*)smem + OFF_REG + tw * REG;
  auto merged = [&](int it, int ln) -> u32x4 {
    const float* src = s_buf + (it % NRING) * (NMW * DH) + ((ln >> 1) & 7) * 24 + (ln >> 4) * 8 + (ln & 1) * 4;
    const f32x4 o = (*reinterpret_cast<const f32x4*>(src) + *reinterpret_cast<const f32x4*>(src + DH)) +
                    (*reinterpret_cast<const f32x4*>(src + 2 * DH) + *reinterpret_cast<const f32x4*>(src + 3 * DH));
    return u32x4{nz_bits(o[0]), nz_bits(o[1]), nz_bits(o[2]), nz_bits(o[3])};
  };
  auto publish = [&](int it) -> bool {
    const int k = arrive(s_c + C_PART + it);
    u32x4 o = merged(it, lane < DH / 4 ? lane : 0);
    asm volatile("" : "+v"(o));
    if (arrived(k) != NMW - 1) return false;
    if (lane < DH / 4) st128(rs, WS_PO + xb * DH + lane * 4, o);
    if (lane == 0) __hip_atomic_fetch_add(s_c + F_DONE, 1u, __ATOMIC_RELAXED, __HIP_MEMORY_SCOPE_WORKGROUP);
    return true;
  };
#define NONCE() do { unsigned n_ = __builtin_amdgcn_readfirstlane(f) + 1u; while (__any(f == n_)) ++n_; \
    if (lane < 32) st32(rs, qf + (GS + mm) * 32 + lane, n_); f = n_; } while (0)
  unsigned r0 = 0u;
  unsigned t0 = clk();
  if (wave < NMW) {
    Tile ta, tb;
    const int c = lane & (GS - 1);
    unsigned f = 0u;
    if (wave == 3) f = ld32(rs, qf + c * 32 + mm);
    if (mm < NPROD) {
      const int hp = g + (mm / 6) * NG, ck = (mm % 6) >> 1, hf = mm & 1, jp = hf * 4 + wave;
      const int bb = lane >> 4, t = lane & 15;
      const u32x4* xp = reinterpret_cast<const u32x4*>(x + row_of(bb) * DM);
      u32x4 xv[5], xs = {0u, 0u, 0u, 0u};
#pragma unroll
      for (int k = 0; k < 5; ++k) xv[k] = xp[t + 16 * k];
      if (t < 10) xs = xp[jp * 10 + t];
      const u32x4* wp = reinterpret_cast<const u32x4*>(wq + (size_t)(jp * 80 + r) * HD + hp * DH + ck * 64) + j;
      u32x4 wv[10];
#pragma unroll
      for (int k = 0; k < 10; ++k) wv[k] = __builtin_nontemporal_load(wp + (size_t)k * 8 * (HD / 8));
      if (wave == 0) s_c[lane] = lane == C_K ? NTP + NMW : 0u;
      MEMFENCE(); LGKM0(); __builtin_amdgcn_s_barrier(); MEMFENCE();
      if (wave == 3) { NONCE(); }
      float sm = 0.f;
#pragma unroll
      for (int k = 0; k < 5; ++k)
#pragma unroll
        for (int d = 0; d < 4; ++d) sm += bf_lo(xv[k][d]) + bf_hi(xv[k][d]);
      const float mean = row_sum(sm) * (1.0f / DM);
      float sv = 0.f;
#pragma unroll
      for (int k = 0; k < 5; ++k)
#pragma unroll
        for (int d = 0; d < 4; ++d) { const float a = bf_lo(xv[k][d]) - mean, c2 = bf_hi(xv[k][d]) - mean; sv += a * a + c2 * c2; }
      const float rstd = 1.0f / sqrtf(row_sum(sv) * (1.0f / DM) + LN_EPS);
      float* s_xn = s_buf + wave * 320;
      if (t < 10) {
#pragma unroll
        for (int d = 0; d < 4; ++d) {
          s_xn[(t * 8 + 2 * d) * 4 + bb] = (bf_lo(xs[d]) - mean) * rstd;
          s_xn[(t * 8 + 2 * d + 1) * 4 + bb] = (bf_hi(xs[d]) - mean) * rstd;
        }
      }
      LGKM0();
      f32x2 qa[16];
#pragma unroll
      for (int i = 0; i < 16; ++i) qa[i] = f32x2{0.f, 0.f};
#pragma unroll
      for (int k = 0; k < 10; ++k) {
        const f32x4 xn = *reinterpret_cast<const f32x4*>(&s_xn[(k * 8 + r) * 4]);
#pragma unroll
        for (int d = 0; d < 4; ++d) {
          const f32x2 w2 = bf_pair(wv[k][d]);
#pragma unroll
          for (int e = 0; e < 4; ++e) qa[e * 4 + d] = fma2(xn[e], w2, qa[e * 4 + d]);
        }
      }
      float t16[16], t8[8];
#pragma unroll
      for (int i = 0; i < 16; ++i) t16[i] = tsum32(qa[i >> 1][i & 1], qa[8 + (i >> 1)][i & 1]);
#pragma unroll
      for (int i = 0; i < 8; ++i) t8[i] = tsum16(t16[i], t16[i + 8]);
#pragma unroll
      for (int i = 0; i < 8; ++i) t8[i] += dpp<DPP_ROR8>(t8[i]);
      const bool up = (lane & 8) != 0;
      f32x4* r4 = reinterpret_cast<f32x4*>(smem + OFF_RED);
      r4[wave * 64 + lane] = f32x4{up ? t8[4] : t8[0], up ? t8[5] : t8[1], up ? t8[6] : t8[2], up ? t8[7] : t8[3]};
      LGKM0();
      if (lds_add(s_c + C_P, lane) == NMW - 1) {
        asm volatile("" ::: "memory");
        const f32x4 o4 = (r4[lane] + r4[64 + lane]) + (r4[128 + lane] + r4[192 + lane]);
        st128(rs, WS_QP + ((hp * 2 + hf) * NB + bb) * DH + ck * 64 + j * 8 + (up ? 4 : 0), __builtin_bit_cast(u32x4, o4));
        VMCNT(0);
        if (lane == 0) s_c[F_OUT] = 1u;
      }
    } else {
      if (wave == 0) s_c[lane] = lane == C_K ? NTP + NMW : 0u;
      MEMFENCE(); LGKM0(); __builtin_amdgcn_s_barrier(); MEMFENCE();
      if (wave == 3) { NONCE(); }
    }
    MEMFENCE();
    if (wave == 3) {
      __builtin_amdgcn_s_setprio(3);
      const unsigned nonce = f;
      unsigned echoed = 0u;
      if (mm < NPROD) {
        echoed = ld32(rs, qf + (GS + c) * 32);
        asm volatile("" : "+v"(echoed) :: "memory");
        lds_wait<1>(s_c + F_OUT, 1u);
        if (lane < 32) st32(rs, qf + mm * 32 + lane, echoed);
#pragma unroll 1
        while ((int)(clk() - t0) < QWAIT - QBACK) {
          const unsigned nv = ld32(rs, qf + (GS + c) * 32);
          if (__any(nv != echoed)) {
            if (lane < 32) st32(rs, qf + mm * 32 + lane, nv);
            echoed = nv;
          }
          __builtin_amdgcn_s_sleep(4);
        }
      }
      until(t0, QWAIT);
#pragma unroll 1
      for (unsigned spin = 0; spin < SPIN_CAP; ++spin) {
        const unsigned nv = ld32(rs, qf + (GS + c) * 32);
        f = ld32(rs, qf + c * 32 + mm);
        const bool done = __all(c == mm || c >= NPROD || f == nonce);
        if (mm < NPROD && __any(nv != echoed)) {
          if (lane < 32) st32(rs, qf + mm * 32 + lane, nv);
          echoed = nv;
        }
        if (done) break;
        __builtin_amdgcn_s_sleep(8);
      }
      if (lane < GS) s_c[S_ECHO + lane] = echoed;
      if (lane < DH / 4) {
        u32x4 p[3][2];
#pragma unroll
        for (int it = 0; it < 3; ++it)
#pragma unroll
          for (int k = 0; k < 2; ++k) p[it][k] = ld128(rs, WS_QP + ((g * 2 + k) * NB + (it ? it + 1 : 0)) * DH + lane * 4);
#pragma unroll
        for (int it = 0; it < 3; ++it) {
          *reinterpret_cast<f32x4*>(&s_q[(it ? it + 3 : 0) * DH + lane * 4]) = (__builtin_bit_cast(f32x4, p[it][0]) + __builtin_bit_cast(f32x4, p[it][1])) * QSCALE;
          if (it != 1) { LGKM0(); if (lane == 0) s_c[F_Q] = it + 1; MEMFENCE(); }
        }
      }
    }
    __builtin_amdgcn_s_setprio(3);
    until(t0, due_k(NTP + wave));
    Table tt = {t0, 1 << 20, 0, 0};
    bool common = false;
    tile_load(ta, kb, poff(NTP + wave) + lo, 0);
    tile_load(tb, kb, poff(NTP + wave) + lo, 1);
    MEMFENCE();
    {
      f32x2 q[12];
      int cur = NTP + wave, nxt = lds_add(s_c + C_K, lane), passed = 0, ci = -1;
      for (;;) {
        const bool last = nxt >= NPAIR;
        const unsigned off = poff(last ? wave : pair_of(nxt)) + lo;
        const char* base = last ? vb : kb;
        const int it = seg_k(cur), cp = pair_of(cur);
        if (it != ci) {
          LGKM0();
          for (; passed < it; ++passed) lds_add(s_c + C_SC + passed, lane);
          lds_wait<1>(s_c + (it < 3 ? F_Q : F_Q2), min(it + 1, 3));
          q_load(q, s_q + seg_of_k(it) * DH, j);
          if (!common) { const unsigned d = lds_get(s_c + S_FIRST); if (d) { tt.t0 -= d - 1u; common = true; tt.was = 1 << 20; } }
          tt.agree(s_c + S_MOVED, s_c + S_MOVED + wave);
          ci = it;
        }
        unsigned tk = 0u;
        if (lane == 0) tk = __hip_atomic_fetch_add(s_c + C_K, 1u, __ATOMIC_RELAXED, __HIP_MEMORY_SCOPE_WORKGROUP);
        MEMFENCE();
        tt.gate(due_k(last ? NPAIR + wave : nxt), s_c + S_MOVED + wave, s_c + S_SLOW);
        MEMFENCE();
        const float sa = score_next(ta, q, base, off, 0);
        score_put(sa, s_sc + cp * 16 + r, j);
        MEMFENCE();
        const float sb = score_next(tb, q, base, off, 1);
        score_put(sb, s_sc + cp * 16 + 8 + r, j);
        MEMFENCE();
        if (last) break;
        cur = nxt; nxt = __builtin_amdgcn_readfirstlane(tk);
      }
      LGKM0();
      for (; passed < NS; ++passed) lds_add(s_c + C_SC + passed, lane);
    }
#pragma unroll 1
    for (int it = 0; it < NS; ++it) {
      lds_wait<1>(s_c + F_P, k_of_seg(it) + 1);
      if (!common) { const unsigned d = lds_get(s_c + S_FIRST); if (d) { tt.t0 -= d - 1u; common = true; tt.was = 1 << 20; } }
      tt.agree(s_c + S_MOVED, s_c + S_MOVED + wave);
      f32x2 acc[12];
#pragma unroll
      for (int i = 0; i < 12; ++i) acc[i] = f32x2{0.f, 0.f};
#pragma unroll 1
      for (int p = seg_beg(it) + wave; p < min(seg_beg(it + 1), NPAIR - NMW); p += NMW) {
        const unsigned off = poff(p + NMW) + lo;
        const float pa = s_sc[p * 16 + r], pb = s_sc[p * 16 + 8 + r];
        MEMFENCE();
        if (p + NMW < NPAIR - SPRINT * NMW) tt.gate(due_v(p + NMW), s_c + S_MOVED + wave, s_c + S_SLOW);
        MEMFENCE();
        pv_next(pa, ta, acc, vb, off, 0);
        pv_next(pb, tb, acc, vb, off, 1);
        if (((p + NMW) >> 2) == (NPAIR >> 2) - GLEAD) { if (lane == 0) __hip_atomic_fetch_add(s_c + C_LAST, 1u, __ATOMIC_RELAXED, __HIP_MEMORY_SCOPE_WORKGROUP); }
      }
      if (it == NS - 1) {
        const int p = NPAIR - NMW + wave;
        const float pa = s_sc[p * 16 + r], pb = s_sc[p * 16 + 8 + r];
        pv_last(pa, ta, acc);
        pv_last(pb, tb, acc);
      }
      float t12[12], t6[6];
#pragma unroll
      for (int i = 0; i < 12; ++i) t12[i] = tsum32(acc[i >> 1][i & 1], acc[6 + (i >> 1)][i & 1]);
#pragma unroll
      for (int i = 0; i < 6; ++i) t6[i] = tsum16(t12[i], t12[i + 6]);
#pragma unroll
      for (int i = 0; i < 6; ++i) t6[i] += dpp<DPP_ROR8>(t6[i]);
      if (it >= NRING) lds_wait<1>(s_c + F_FREE, it - NRING + 1);
      if ((lane & 8) == 0) {
        f32x2* dst = reinterpret_cast<f32x2*>(&s_buf[(it % NRING) * (NMW * DH) + wave * DH + j * 24 + (lane >> 4) * 6]);
        dst[0] = f32x2{t6[0], t6[1]}; dst[1] = f32x2{t6[2], t6[3]}; dst[2] = f32x2{t6[4], t6[5]};
      }
      CFENCE();
      if (it < NA) { if (lane == 0) __hip_atomic_fetch_add(s_c + C_PART + it, 1u, __ATOMIC_RELAXED, __HIP_MEMORY_SCOPE_WORKGROUP); }
    }
    publish(NS - 1);
    return;
  }

  {
    Tile pk[NRP * 2];
    __builtin_amdgcn_s_setprio(3);
#pragma unroll
    for (int e = 0; e < NLP * 2; ++e) {
      const char* src = kb + (poff((e >> 1) * 4 + tw) + lo + (e & 1) * 3072);
      if (e >= 2) until(t0, due_k(e * 2 + tw) - TLEAD);
#pragma unroll
      for (int i = 0; i < 3; ++i) glds16_nt(src + i * 128, lds_base + (e * 3 + i) * 1024);
      if (e == 1) __builtin_amdgcn_s_barrier();
      if (e * 3 + 3 > PFD) VMCNT(PFD);
    }
#pragma unroll
    for (int e = 0; e < NRP * 2; ++e) {
      until(t0, due_k((NLP * 2 + e) * 2 + tw) - TLEAD);
      tile_load(pk[e], kb, poff((NLP + (e >> 1)) * 4 + tw) + lo, e & 1);
      MEMFENCE();
      if (e + 1 < NRP * 2) VMCNT(PFD);
    }
    if (tw == 0) {
      r0 = ld32(rs, WS_HR + bid * 2);
      if (bid == 0) {
        u32x4 n;
#pragma unroll
        for (int k = 0; k < 4; ++k) n[k] = ld32(rs, WS_HA + (lane * 4 + k) * 32) + 1u;
        st128(rs, WS_HN + lane * 4, n);
      }
      const u32x4 z = {0u, 0u, 0u, 0u};
      if (lane < 48) {
#pragma unroll
        for (int it = NA; it < NS; ++it) st128(rs, WS_PO + xb * DH + lane * 4, z);
      }
      else st128(rs, WS_PML + (g * GS + mm) * 64 + (lane - 48) * 4, z);
      if (isG && g_cg == 0 && lane < 8) st128(rs, WS_ST + lane * 4, z);
      if (isW) st128(rs, WS_SLAB + (w_ks * NB + (lane >> 4)) * DM + w_ct * 64 + (lane & 15) * 4, z);
      else if (isG) { if (lane < 32) st128(rs, WS_ACT + g_cg * 128 + lane * 4, z); }
      else if (isH) {
#pragma unroll
        for (int k = 0; k < 3; ++k) st128(rs, WS_H + (xl - 2) * 768 + k * 256 + lane * 4, z);
      }
      else if (isC && lane < 48) st128(rs, WS_OC + (c_it * NG + g) * DH + lane * 4, z);
    }
    __builtin_amdgcn_s_setprio(0);
    lds_wait<1>(s_c + F_Q, HPG);
    f32x2 q[12];
    q_load(q, s_q, j);
    const u32x4* s_kv = reinterpret_cast<const u32x4*>(smem + OFF_REG + tw * REG) + lane;
    static_assert(NLP * 4 <= PPI && NTP <= HPG * PPI, "tail tiles: batch row 0 only");
#pragma unroll 1
    for (int e = 0; e < NLP * 2; ++e) {
      Tile tmp;
#pragma unroll
      for (int i = 0; i < 3; ++i) tmp.d[i] = s_kv[(e * 3 + i) * 64];
      score_put(tile_score(tmp, q), s_sc + ((e >> 1) * 4 + tw) * 16 + (e & 1) * 8 + r, j);
    }
#pragma unroll
    for (int e = 0; e < NRP * 2; ++e) {
      if ((NLP * 2 + e) * 2 == PPI) q_load(q, s_q + DH, j);
      score_put(tile_score(pk[e], q), s_sc + ((NLP + (e >> 1)) * 4 + tw) * 16 + (e & 1) * 8 + r, j);
      __builtin_amdgcn_sched_barrier(0);
    }
    asm volatile("s_waitcnt vmcnt(0) lgkmcnt(0)" ::: "memory");
#pragma unroll
    for (int it = 0; it * PPI < NTP; ++it) lds_add(s_c + C_SC + it, lane);
  }

  const int mr = lane & 15, qd = lane >> 4;
  unsigned char* reg = smem + OFF_REG + tw * REG;
  float* red = reinterpret_cast<float*>(smem + OFF_RED);
#define PERM_LO(hi_, lo_) __builtin_amdgcn_perm(hi_, lo_, 0x05040100u)
#define PERM_HI(hi_, lo_) __builtin_amdgcn_perm(hi_, lo_, 0x07060302u)
  int served = 0;
  auto done = [&](int n) {
    if (tw == 3 && served < min(n, NA)) {
      const int from = served;
      int ln = lane;
      asm volatile("" : "+v"(ln));
#pragma unroll 1
      for (; served < min(n, NA); ++served) {
        lds_wait<8>(s_c + C_PART + served, NMW);
        const f32x4 o = __builtin_bit_cast(f32x4, merged(served, ln < DH / 4 ? ln : 0));
        const float am = max_x16_32(row_max(fmaxf(fmaxf(fabsf(o[0]), fabsf(o[1])), fmaxf(fabsf(o[2]), fabsf(o[3])))));
        const float inv = am > 1e-30f ? 32767.0f / am : 0.f;
        const int q0 = __float2int_rn(o[0] * inv), q1 = __float2int_rn(o[1] * inv), q2 = __float2int_rn(o[2] * inv), q3 = __float2int_rn(o[3] * inv);
        if (ln < DH / 4) reinterpret_cast<u32x2*>(s_sc + served * (DH / 2))[ln] = u32x2{PERM_LO((unsigned)q1, (unsigned)q0), PERM_LO((unsigned)q3, (unsigned)q2)};
        const bool nan = __any((o[0] + o[1]) + (o[2] + o[3]) != (o[0] + o[1]) + (o[2] + o[3]));
        if (ln == 0) s_ml[served][2] = nan ? 0x7fc00000u : nz_bits(am * (1.0f / 32767.0f));
        LGKM0();
        if (ln == 0) s_c[F_FREE] = served + 1;
      }
#pragma unroll 1
      for (int c = from * 24 + ln; c < served * 24; c += 64) st128(rs, WS_PQ + xb * 5 * (DH / 2) + c * 4, reinterpret_cast<const u32x4*>(s_sc)[c]);
      VMCNT(0);
      if (ln < 8) st128(rs, WS_PML + xb * 64 + (from ? 32 : 0) + ln * 4, s_ml[ln]);
      if (ln == 0) __hip_atomic_fetch_add(s_c + F_DONE, (unsigned)(served - from), __ATOMIC_RELAXED, __HIP_MEMORY_SCOPE_WORKGROUP);
    }
    if (n > NA) lds_wait<1>(s_c + F_DONE, n); else lds_wait<8>(s_c + F_DONE, n);
  };
  auto done_all = [&]() {
#pragma unroll 1
    for (int n = 4; n <= NA; ++n) done(n);
  };
  const int lp = (lane < DH / 4 ? lane : 0) * 4;
  auto weight = [&](int it, int x0, int cnt) -> float {
    const int mlw = WS_PML + (x0 + (lane & (cnt - 1))) * 64 + (it < 4 ? 0 : 32) + it * 4;
    u32x4 ml;
#pragma unroll 1
    for (unsigned spin = 0; spin < SPIN_CAP; ++spin) {
      ml = ld128(rs, mlw);
      if (__all(min4(ml) != 0u)) break;
      __builtin_amdgcn_s_sleep(16);
    }
    const float mk = __uint_as_float(ml[0]);
    const float e = __builtin_amdgcn_exp2f(mk - max_x16_32(row_max(mk)));
    return e / (wave_sum(e * __uint_as_float(ml[1])) * ((float)cnt * (1.0f / 64.0f))) * __uint_as_float(ml[2]);
  };
  auto prep = [&]() {
    if (tw == 0) {
      const unsigned tok = __builtin_amdgcn_readfirstlane(r0) + 1u;
      const int c = lane & (GS - 1);
      unsigned echoed = 0u, qe = s_c[S_ECHO + c];
      bool first = true, released = false, sent = false;
#pragma unroll 1
      while ((int)(clk() - t0) < HST) {
        const unsigned qn = ld32(rs, qf + (GS + c) * 32);
        if (mm < NPROD && __any(qn != qe)) {
          if (lane < 32) st32(rs, qf + mm * 32 + lane, qn);
          qe = qn;
        }
        __builtin_amdgcn_s_sleep(64);
      }
      int slow = 0;
      auto lag = [&]() { return (lds_get(s_c + S_MOVED + 4) + lds_get(s_c + S_MOVED + 5) + lds_get(s_c + S_MOVED + 6) + lds_get(s_c + S_MOVED + 7)) >> 2; };
      const unsigned behind = lag();
#pragma unroll 1
      for (unsigned spin = 0; spin < SPIN_CAP && !released; ++spin) {
        const unsigned qn = ld32(rs, qf + (GS + c) * 32);
        const u32x2 rv = ld64(rs, WS_HR + bid * 2);
        const unsigned nv = __builtin_amdgcn_readfirstlane(ld32(rs, WS_HN + bid));
        if (__builtin_amdgcn_readfirstlane(rv[0]) - tok <= NRD) {
          released = true;
          slow = __builtin_amdgcn_readfirstlane(rv[1]) >> 16;
          if (lane == 0) { s_c[S_FIRST] = min((t0 - rv[1]) & 0xffffu, 300u) + 1u; s_c[S_SLOW] = slow; }
        } else {
          if (mm < NPROD && __any(qn != qe)) {
            if (lane < 32) st32(rs, qf + mm * 32 + lane, qn);
            qe = qn;
          }
          if (first || nv != echoed) {
            echoed = nv; first = false;
            if (lane < 8) st128(rs, WS_HA + bid * 32 + lane * 4, u32x4{echoed, tok, t0, behind});
          }
          if (bid == 0 && !sent) sent = leader_release(rs, lane, t0, 0u);
          __builtin_amdgcn_s_sleep(32);
        }
      }
      if (lane == 0) s_c[F_REL] = 1u;
      if (lane < DH / 4) {
        u32x4 p[3][2];
#pragma unroll
        for (int e = 0; e < 3; ++e)
#pragma unroll
          for (int k = 0; k < 2; ++k) p[e][k] = ld128(rs, WS_QP + (((e ? 8 + e * 8 + (xb >> 5) : xb >> 4) * 2 + k) * NB + 1) * DH + lane * 4);
#pragma unroll
        for (int e = 0; e < 3; ++e) *reinterpret_cast<f32x4*>(&s_q[(1 + e) * DH + lane * 4]) = (__builtin_bit_cast(f32x4, p[e][0]) + __builtin_bit_cast(f32x4, p[e][1])) * QSCALE;
      }
      LGKM0();
      if (lane == 0) s_c[F_Q2] = 3u;
      unsigned prev = slow;
#pragma unroll 1
      for (unsigned rd = 1; rd <= NRD; ++rd) {
        const int tr = RT0 + (int)(rd - 1) * RTS;
        until(t0, tr);
        const unsigned bh = lag();
        if (bh > RMIN && lane < 8) st128(rs, WS_HA + bid * 32 + lane * 4, u32x4{echoed + rd, tok, t0, bh});
        if (bid == 0) { until(t0, tr + 220); leader_release(rs, lane, t0, rd); }
        until(t0, tr + 500);
        const u32x2 rv = ld64(rs, WS_HR + bid * 2);
        if (__builtin_amdgcn_readfirstlane(rv[0]) != tok + rd) {
          prev = 0u;
          const int fl = -(int)min(min(lds_get(s_c + S_MOVED), lds_get(s_c + S_MOVED + 1)), min(lds_get(s_c + S_MOVED + 2), lds_get(s_c + S_MOVED + 3)));
          slow = slow > fl ? max(slow - GB, fl) : slow - (rd <= PRD ? PRB : 0);
          if (lane == 0) s_c[S_SLOW] = slow;
        } else {
          const unsigned ex = __builtin_amdgcn_readfirstlane(rv[1]) >> 16;
          slow += ex + min(ex, prev) * (rd == NRD ? FBL : FBP) / 100u;
          prev = ex;
          if (lane == 0) s_c[S_SLOW] = slow;
        }
      }
    }
    if (tw == 3) {
#pragma unroll 1
      for (int ik = 0; ik < NS; ++ik) {
        const int it = seg_of_k(ik), nv = (seg_beg(it + 1) - seg_beg(it)) >> 4;
        lds_wait<4>(s_c + C_SC + ik, ik == 0 ? NW : NMW);
        f32x4* sp = reinterpret_cast<f32x4*>(s_sc + seg_beg(it) * 16) + lane;
        f32x4 v[4];
#pragma unroll
        for (int k = 0; k < 4; ++k) v[k] = sp[(k & (nv - 1)) * 64];
        const f32x4 mx = __builtin_elementwise_max(__builtin_elementwise_max(v[0], v[1]), __builtin_elementwise_max(v[2], v[3]));
        const float m = max_x16_32(row_max(fmaxf(fmaxf(mx[0], mx[1]), fmaxf(mx[2], mx[3]))));
        float l = 0.f;
#pragma unroll
        for (int k = 0; k < 4; ++k) {
#pragma unroll
          for (int e = 0; e < 4; ++e) v[k][e] = __builtin_amdgcn_exp2f(v[k][e] - m);
          if (k < nv) {
            l += (v[k][0] + v[k][1]) + (v[k][2] + v[k][3]);
            sp[k * 64] = v[k];
          }
        }
        l = wave_sum(l);
        if (lane == 0) s_ml[it] = u32x4{nz_bits(m), nz_bits(l), 0x3f800000u, 0x3f800000u};
        LGKM0();
        if (lane == 0) s_c[F_P] = ik + 1;
      }
    }
  };
  auto pull = [&]() {
    typedef __attribute__((address_space(1))) const unsigned g_u32;
    const int id = (ro - 16) * 4 + tw;
    g_u32* p1[2];
#pragma unroll
    for (int i = 0; i < 2; ++i) {
      const int l = id * 60 + min(i * 32 + (lane >> 1), 59), m = l >= 1920 ? l - 1920 : l;
      p1[i] = (g_u32*)((l >= 1920 ? wu : wg) + (size_t)(m / 3) * FF + xl * 192 + (m % 3) * 64 + (lane & 1) * 32);
    }
    g_u32* p2 = (g_u32*)(wd + (size_t)(id * 24 + min(lane >> 1, 23)) * DM + xl * 64 + (lane & 1) * 32);
    asm volatile("" : "+v"(p1[0]), "+v"(p1[1]), "+v"(p2));
    if (GLEAD) lds_wait<1>(s_c + C_LAST, GCNT); else done(NS);
    if (PW) until(clk(), PW);
    const unsigned v0 = p1[0][0], v1 = p1[1][0];
    MEMFENCE();
    if (DP == 1) done(NS);
    if (DPW) until(clk(), DPW);
    const unsigned v2 = p2[0], v3 = p2[xl < 2 ? 256 : 0];
    MEMFENCE();
    return (v0 ^ v1) + (v2 ^ v3);
  };
  if (isW) {
    u32x4 bx[24];
    const char* p = reinterpret_cast<const char*>(wo + (size_t)((w_ks * 4 + tw) * DH + r) * DM + w_ct * 64 + j * 8);
#pragma unroll 1
    for (int i = 0; i < 24; ++i) { until(t0, tw == 0 ? W0START + i * (W0SPAN / 24) : W1START + i * (W1SPAN / 24)); glds16_nt(p, lds_base + i * 1024); p += 8 * DM * 2; VMCNT(WFD); }
    asm volatile("s_waitcnt vmcnt(0)" ::: "memory");
#pragma unroll
    for (int s = 0; s < 6; ++s) {
      u32x2 e[8];
#pragma unroll
      for (int k = 0; k < 8; ++k) e[k] = *reinterpret_cast<const u32x2*>(reg + (s * 32 + qd * 8 + k) * 128 + mr * 8);
#pragma unroll
      for (int k = 0; k < 4; ++k) {
        bx[s * 4 + 0][k] = PERM_LO(e[2 * k + 1][0], e[2 * k][0]); bx[s * 4 + 1][k] = PERM_HI(e[2 * k + 1][0], e[2 * k][0]);
        bx[s * 4 + 2][k] = PERM_LO(e[2 * k + 1][1], e[2 * k][1]); bx[s * 4 + 3][k] = PERM_HI(e[2 * k + 1][1], e[2 * k][1]);
      }
    }
#pragma unroll
    for (int s = 0; s < 24; s += 4) asm volatile("" : "+v"(bx[s]), "+v"(bx[s + 1]), "+v"(bx[s + 2]), "+v"(bx[s + 3]));
    LGKM0();
    prep();
    lds_wait<8>(s_c + F_REL, 1u);
    const int hh = w_ks * 4 + tw;
#pragma unroll 1
    for (int pass = 0; pass < 3; ++pass) {
      if (pass < 2) {
        done(pass ? NA : 4);
        pause(pass ? WDELAY2 : WDELAY2 + 300);
        const int b0 = pass * 2, b1 = pass + 1;
        u32x4 v0, v1;
#pragma unroll 1
        for (unsigned spin = 0; spin < SPIN_CAP; ++spin) {
          v0 = ld128(rs, WS_OC + (b0 * NH + hh) * DH + lp);
          v1 = ld128(rs, WS_OC + (b1 * NH + hh) * DH + lp);
          if (__all(min(min4(v0), min4(v1)) != 0u)) break;
          __builtin_amdgcn_s_sleep(16);
        }
        if (lane < DH / 4) {
          *reinterpret_cast<u32x2*>(reg + (b0 * 4) * AW_LD + lane * 8) = u32x2{v0[0], v0[1]};
          *reinterpret_cast<u32x2*>(reg + (b0 * 4 + 1) * AW_LD + lane * 8) = u32x2{v0[2], v0[3]};
          *reinterpret_cast<u32x2*>(reg + (b1 * 4) * AW_LD + lane * 8) = u32x2{v1[0], v1[1]};
          *reinterpret_cast<u32x2*>(reg + (b1 * 4 + 1) * AW_LD + lane * 8) = u32x2{v1[2], v1[3]};
        }
      } else {
        const float wk = weight(NS - 1, hh * GS, GS);
        float w[GS];
#pragma unroll
        for (int k = 0; k < GS; ++k) { w[k] = __uint_as_float(__builtin_amdgcn_readlane(__float_as_uint(wk), k)); asm volatile("" : "+s"(w[k])); }
        done(NS);
        __builtin_amdgcn_s_setprio(3);
        const int po = WS_PO + hh * GS * DH;
        u32x4 pv[GS];
#pragma unroll 1
        for (unsigned spin = 0; spin < SPIN_CAP; ++spin) {
#pragma unroll
          for (int k = 0; k < GS; ++k) pv[k] = __builtin_amdgcn_raw_buffer_load_b128(rs, lp * 4, (po + k * DH) * 4, AUX_SC1);
          unsigned mn = min4(pv[0]);
#pragma unroll
          for (int k = 1; k < GS; ++k) mn = min(mn, min4(pv[k]));
          if (__all(mn != 0u)) break;
          __builtin_amdgcn_s_sleep(1);
        }
        f32x4 o = w[0] * __builtin_bit_cast(f32x4, pv[0]), o2 = w[1] * __builtin_bit_cast(f32x4, pv[1]);
#pragma unroll
        for (int k = 2; k < GS; k += 2) { o += w[k] * __builtin_bit_cast(f32x4, pv[k]); o2 += w[k + 1] * __builtin_bit_cast(f32x4, pv[k + 1]); }
        u32x2 hi, lw;
        split4(o + o2, hi, lw);
        if (lane < DH / 4) {
          *reinterpret_cast<u32x2*>(reg + ((NB - 1) * 4) * AW_LD + lane * 8) = hi;
          *reinterpret_cast<u32x2*>(reg + ((NB - 1) * 4 + 1) * AW_LD + lane * 8) = lw;
        }
      }
      CFENCE();
      u32x4 a[6];
#pragma unroll
      for (int s = 0; s < 6; ++s) a[s] = *reinterpret_cast<const u32x4*>(reg + mr * AW_LD + s * 64 + qd * 16);
      __builtin_amdgcn_sched_barrier(0);
      f32x4 c[4];
#pragma unroll
      for (int t = 0; t < 4; ++t) c[t] = f32x4{0.f, 0.f, 0.f, 0.f};
#pragma unroll
      for (int s = 0; s < 6; ++s)
#pragma unroll
        for (int t = 0; t < 4; ++t) c[t] = mfma16(a[s], bx[s * 4 + t], c[t]);
      const bool mine = max(qd - 1, 0) == pass;
      f32x4* r4 = reinterpret_cast<f32x4*>(red);
      if (mine) r4[tw * 64 + lane] = f32x4{c[0][0] + c[0][1], c[1][0] + c[1][1], c[2][0] + c[2][1], c[3][0] + c[3][1]};
      const int k = arrive(s_c + C_W + pass);
      f32x4 sum = (r4[lane] + r4[64 + lane]) + (r4[128 + lane] + r4[192 + lane]);
      asm volatile("" : "+v"(sum));
      if (arrived(k) == 3) {
        if (mine) st128(rs, WS_SLAB + (w_ks * NB + qd) * DM + w_ct * 64 + mr * 4, u32x4{nz_bits(sum[0]), nz_bits(sum[1]), nz_bits(sum[2]), nz_bits(sum[3])});
      }
    }
  } else if (isG || isH) {
    f32x4* rst = reinterpret_cast<f32x4*>(red);
    const int hb = isH ? xl - 2 : NB - 1;
    uint2 wr4 = make_uint2(0u, 0u), xg = wr4;
    if (lane < 40) {
      wr4 = *reinterpret_cast<const uint2*>(wr + tw * 160 + lane * 4);
      xg = *reinterpret_cast<const uint2*>(x + row_of(hb) * DM + tw * 160 + lane * 4);
    }
    const float cq = bf_lo(*reinterpret_cast<const unsigned*>(x + row_of(qd) * DM));
    const float wsum = wave_sum((bf_lo(wr4.x) + bf_hi(wr4.x)) + (bf_lo(wr4.y) + bf_hi(wr4.y)));
    f32x4 xc = f32x4{bf_lo(xg.x), bf_hi(xg.x), bf_lo(xg.y), bf_hi(xg.y)} - __uint_as_float(__builtin_amdgcn_readlane(__float_as_uint(cq), hb * 16));
    asm volatile("" : "+v"(xc));
    prep();
    lds_wait<8>(s_c + F_REL, 1u);
    auto hrow = [&](bool fast) -> f32x4 {
      f32x4 hv = {0.f, 0.f, 0.f, 0.f};
      if (lane < 40) {
        const int off = (WS_SLAB + hb * DM + tw * 160 + lane * 4) * 4;
        u32x4 sl[KS];
#pragma unroll 1
        for (unsigned spin = 0; spin < SPIN_CAP; ++spin) {
#pragma unroll
          for (int ks = 0; ks < KS; ++ks) sl[ks] = __builtin_amdgcn_raw_buffer_load_b128(rs, off, ks * (NB * DM * 4), AUX_SC1);
          unsigned mn = min4(sl[0]);
#pragma unroll
          for (int ks = 1; ks < KS; ++ks) mn = min(mn, min4(sl[ks]));
          if (!__any(mn == 0u)) break;
          if (fast) __builtin_amdgcn_s_sleep(1); else __builtin_amdgcn_s_sleep(16);
        }
#define SL(k) __builtin_bit_cast(f32x4, sl[k])
        hv = xc + (((SL(0) + SL(1)) + (SL(2) + SL(3))) + ((SL(4) + SL(5)) + (SL(6) + SL(7))));
#undef SL
      }
      return hv;
    };
    auto sums = [&](f32x4 hv) -> f32x4 {
      const float s1 = wave_sum((hv[0] + hv[1]) + (hv[2] + hv[3]));
      const float s2 = wave_sum((hv[0] * hv[0] + hv[1] * hv[1]) + (hv[2] * hv[2] + hv[3] * hv[3]));
      const float dot = wave_sum((hv[0] * bf_lo(wr4.x) + hv[1] * bf_hi(wr4.x)) + (hv[2] * bf_lo(wr4.y) + hv[3] * bf_hi(wr4.y)));
      return f32x4{s1, s2, dot, wsum};
    };
    if (isH) {
      if (tw == 3 || hb == 2) done_all(); else done(4);
      if (tw != 3 || hb == 2) pause(hb < 2 ? HDELAY : HDELAY2);
      const f32x4 hv = hrow(false), sm = sums(hv);
      u32x2 hi, lw;
      split4(hv, hi, lw);
      const bool odd = (lane & 1) != 0;
      const unsigned n0 = __float_as_uint(dpp<DPP_XOR1>(__uint_as_float(odd ? hi[0] : lw[0]))), n1 = __float_as_uint(dpp<DPP_XOR1>(__uint_as_float(odd ? hi[1] : lw[1])));
      u32x4 v = odd ? u32x4{n0, n1, lw[0], lw[1]} : u32x4{hi[0], hi[1], n0, n1};
      if (lane >= 40) v = __builtin_bit_cast(u32x4, sm);
      if (lane < 48) st128(rs, WS_H + (hb * 4 + tw) * 192 + lane * 4, u32x4{max(v[0], 1u), max(v[1], 1u), max(v[2], 1u), max(v[3], 1u)});
      done_all();
      const unsigned pl = pull();
      asm volatile("" :: "v"(pl));
      return;
    }
    done_all();
    typedef __attribute__((address_space(1))) const u32x2 g_u32x2;
    g_u32x2* wp[15];
#pragma unroll
    for (int i = 0; i < 15; ++i) {
      wp[i] = (g_u32x2*)((mr < 8 ? wg : wu) + (size_t)(tw * 160 + qd * 8 + 1 + (i / 3) * 32 + (i % 3) * 3) * FF + g_cg * 32 + (mr & 7) * 4);
      asm volatile("" : "+v"(wp[i]));
    }
    done(NS);
    if (GW) until(clk(), GW);
    u32x2 e[40];
#pragma unroll
    for (int s = 0; s < 5; ++s)
#pragma unroll
      for (int k = 0; k < 8; ++k) e[s * 8 + k] = wp[s * 3 + k / 3][(k % 3 - 1) * (FF / 4)];
    u32x4 a[5], sm;
    const int hoff = (WS_H + (min(mr >> 2, NB - 2) * 4 + tw) * 192 + qd * 8 + (mr & 1) * 4) * 4;
#pragma unroll
    for (int s = 0; s < 5; ++s) a[s] = __builtin_amdgcn_raw_buffer_load_b128(rs, hoff + s * 128, 0, AUX_SC1);
    sm = ld128(rs, WS_H + (min(lane, NB - 2) * 4 + tw) * 192 + 160);
    MEMFENCE();
    u32x4 bx[20];
#pragma unroll
    for (int s = 0; s < 5; ++s) {
#pragma unroll
      for (int k = 0; k < 4; ++k) {
        bx[s * 4 + 0][k] = PERM_LO(e[s * 8 + 2 * k + 1][0], e[s * 8 + 2 * k][0]); bx[s * 4 + 1][k] = PERM_HI(e[s * 8 + 2 * k + 1][0], e[s * 8 + 2 * k][0]);
        bx[s * 4 + 2][k] = PERM_LO(e[s * 8 + 2 * k + 1][1], e[s * 8 + 2 * k][1]); bx[s * 4 + 3][k] = PERM_HI(e[s * 8 + 2 * k + 1][1], e[s * 8 + 2 * k][1]);
      }
      PIN4(bx, s * 4);
    }
    {
#pragma unroll 1
      for (unsigned spin = 0; spin < SPIN_CAP; ++spin) {
        unsigned mn = min4(sm);
#pragma unroll
        for (int s = 0; s < 5; ++s) mn = min(mn, min4(a[s]));
        if (__all(mn != 0u)) break;
        __builtin_amdgcn_s_sleep(2);
#pragma unroll
        for (int s = 0; s < 5; ++s) a[s] = __builtin_amdgcn_raw_buffer_load_b128(rs, hoff + s * 128, 0, AUX_SC1);
        sm = ld128(rs, WS_H + (min(lane, NB - 2) * 4 + tw) * 192 + 160);
      }
      if (lane < NB - 1) rst[lane * 4 + tw] = __builtin_bit_cast(f32x4, sm);
#pragma unroll
      for (int s = 0; s < 5; ++s) {
        if (mr & 2) a[s] = u32x4{0x3f803f80u, 0x3f803f80u, 0x3f803f80u, 0x3f803f80u};
        asm volatile("" : "+v"(a[s]));
      }
    }
    const f32x4 hv = hrow(true);
    if (lane < 40) {
      u32x2 hi, lw;
      split4(hv, hi, lw);
      *reinterpret_cast<u32x2*>(reg + lane * 8) = hi;
      *reinterpret_cast<u32x2*>(reg + AG_LD + lane * 8) = lw;
    }
    CFENCE();
    if ((mr & 14) == 12) {
#pragma unroll
      for (int s = 0; s < 5; ++s) a[s] = *reinterpret_cast<const u32x4*>(reg + (mr & 1) * AG_LD + s * 64 + qd * 16);
    }
    __builtin_amdgcn_sched_barrier(0);
    f32x4 c[4];
#pragma unroll
    for (int t = 0; t < 4; ++t) c[t] = f32x4{0.f, 0.f, 0.f, 0.f};
#pragma unroll
    for (int s = 0; s < 5; ++s)
#pragma unroll
      for (int t = 0; t < 4; ++t) c[t] = mfma16(a[s], bx[s * 4 + t], c[t]);
    { const f32x4 s3 = sums(hv); if (lane == 0) rst[(NB - 1) * 4 + tw] = s3; }
    *reinterpret_cast<f32x4*>(reg + 4096 + lane * 16) = f32x4{c[0][0] + c[0][1], c[1][0] + c[1][1], c[0][2], c[1][2]};
    *reinterpret_cast<f32x4*>(reg + 4096 + 1152 + lane * 16) = f32x4{c[2][0] + c[2][1], c[3][0] + c[3][1], c[2][2], c[3][2]};
    const int k = arrive(s_c + C_T);
    f32x4 ts[4], pg[4], pu[4];
#pragma unroll
    for (int w = 0; w < 4; ++w) {
      const unsigned char* src = smem + OFF_REG + w * REG + 4096 + (mr & 1) * 1152 + (qd * 16 + (mr >> 1)) * 16;
      ts[w] = rst[qd * 4 + w];
      pg[w] = *reinterpret_cast<const f32x4*>(src); pu[w] = *reinterpret_cast<const f32x4*>(src + 128);
    }
    PIN4(ts, 0); PIN4(pg, 0); PIN4(pu, 0);
    if (arrived(k) == 3) {
      const f32x4 t = (ts[0] + ts[1]) + (ts[2] + ts[3]), g4 = (pg[0] + pg[1]) + (pg[2] + pg[3]), u4 = (pu[0] + pu[1]) + (pu[2] + pu[3]);
      const float d = t[0] * (-1.0f / DM);
      const float rstd = __builtin_amdgcn_rsqf(fmaxf(t[1] + d * t[0], 0.f) * (1.0f / DM) + LN_EPS);
      const float g0 = rstd * (g4[0] + d * g4[2]), g1 = rstd * (g4[1] + d * g4[3]);
      const float a0 = g0 * __builtin_amdgcn_rcpf(1.0f + __expf(-g0)) * (rstd * (u4[0] + d * u4[2]));
      const float a1 = g1 * __builtin_amdgcn_rcpf(1.0f + __expf(-g1)) * (rstd * (u4[1] + d * u4[3]));
      const unsigned h0 = __float_as_uint(a0) & 0xffff0000u, h1 = __float_as_uint(a1) & 0xffff0000u;
      const unsigned hi = max(PERM_HI(h1, h0), 1u), lw = max(PERM_HI(__float_as_uint(a1 - __uint_as_float(h1)), __float_as_uint(a0 - __uint_as_float(h0))), 1u);
#define QB(v, c_) __float_as_uint(dpp<c_>(__uint_as_float(v)))
      const bool isl = (lane & 1) != 0;
      const u32x4 hq = {QB(hi, 0x00), QB(hi, 0x55), QB(hi, 0xAA), QB(hi, 0xFF)}, lq = {QB(lw, 0x00), QB(lw, 0x55), QB(lw, 0xAA), QB(lw, 0xFF)};
#undef QB
      const float rr = rstd * (t[2] + d * t[3]);
      const u32x4 sv = {nz_bits(cq - d), nz_bits(rstd), nz_bits(__builtin_amdgcn_rcpf(1.0f + __expf(-rr))), 0x3f800000u};
      const bool stat = g_cg == 0 && mr == 2;
      if ((lane & 2) == 0 || stat) st128(rs, stat ? WS_ST + qd * 4 : WS_ACT + g_cg * 128 + (qd * 2 + (lane & 1)) * 16 + (mr >> 2) * 4, stat ? sv : (isl ? lq : hq));
    }
  } else if (isD) {
    const int col = d_ct * 32 + (mr & 7) * 4 + (mr >> 3) * 2;
    const unsigned xd = *reinterpret_cast<const unsigned*>(x + row_of(qd) * DM + col);
    const __amdgpu_buffer_rsrc_t rd = __builtin_amdgcn_make_buffer_rsrc(const_cast<bf16*>(wd), 0, FF * DM * 2, 0x00020000);
    prep();
    lds_wait<8>(s_c + F_REL, 1u);
    done_all();
    const unsigned pl = pull();
    done(NS);
    asm volatile("" :: "v"(pl));
    const unsigned te = clk();
    if (DW) until(te, DW);
    u32x4 bd[24];
    {
      const int off = ((tw * 384 + (mr >> 3) * 32 + qd * 8) * DM + d_ct * 32 + (mr & 7) * 4) * 2;
      u32x2 e[48];
#pragma unroll
      for (int i = 0; i < 48; ++i) e[i] = __builtin_amdgcn_raw_buffer_load_b64(rd, off, ((i >> 3) * 64 + (i & 7)) * (DM * 2), 0);
      MEMFENCE();
#pragma unroll
      for (int s = 0; s < 6; ++s) {
#pragma unroll
        for (int k = 0; k < 4; ++k) {
          bd[s * 4 + 0][k] = PERM_LO(e[s * 8 + 2 * k + 1][0], e[s * 8 + 2 * k][0]); bd[s * 4 + 1][k] = PERM_HI(e[s * 8 + 2 * k + 1][0], e[s * 8 + 2 * k][0]);
          bd[s * 4 + 2][k] = PERM_LO(e[s * 8 + 2 * k + 1][1], e[s * 8 + 2 * k][1]); bd[s * 4 + 3][k] = PERM_HI(e[s * 8 + 2 * k + 1][1], e[s * 8 + 2 * k][1]);
        }
        PIN4(bd, s * 4);
      }
    }
    until(te, DLATE);
    f32x2 hv;
    int dl = 0;
    {
      const int off = (WS_SLAB + qd * DM + col) * 4;
      u32x2 sl[KS];
#pragma unroll 1
      for (unsigned spin = 0; spin < SPIN_CAP; ++spin) {
#pragma unroll
        for (int ks = 0; ks < KS; ++ks) sl[ks] = __builtin_amdgcn_raw_buffer_load_b64(rs, off, ks * (NB * DM * 4), AUX_SC1);
        unsigned mn = min(sl[0][0], sl[0][1]);
#pragma unroll
        for (int ks = 1; ks < KS; ++ks) mn = min(mn, min(sl[ks][0], sl[ks][1]));
        if (__all(mn != 0u)) break;
        dl = DLATE2;
        __builtin_amdgcn_s_sleep(4);
      }
#define SL(k) __builtin_bit_cast(f32x2, sl[k])
      hv = bf_pair(xd) + (((SL(0) + SL(1)) + (SL(2) + SL(3))) + ((SL(4) + SL(5)) + (SL(6) + SL(7))));
#undef SL
      asm volatile("" : "+v"(hv));
    }
    until(clk(), dl);
    u32x4 a[6], st;
    const int aoff = (WS_ACT + tw * 12 * 128 + (((mr >> 1) & 1) * 8 + (mr >> 2) * 2 + (mr & 1)) * 16 + qd * 4) * 4;
#pragma unroll 1
    for (unsigned spin = 0; spin < SPIN_CAP; ++spin) {
#pragma unroll
      for (int s = 0; s < 6; ++s) a[s] = __builtin_amdgcn_raw_buffer_load_b128(rs, aoff + s * 1024, 0, AUX_SC1);
      st = ld128(rs, WS_ST + qd * 4);
      unsigned mn = min4(st);
#pragma unroll
      for (int s = 0; s < 6; ++s) mn = min(mn, min4(a[s]));
      if (__all(mn != 0u)) break;
      __builtin_amdgcn_s_sleep(1);
    }
    f32x4 c[4];
#pragma unroll
    for (int t = 0; t < 4; ++t) c[t] = f32x4{0.f, 0.f, 0.f, 0.f};
#pragma unroll
    for (int s = 0; s < 6; ++s)
#pragma unroll
      for (int t = 0; t < 4; ++t) c[t] = mfma16(a[s], bd[s * 4 + t], c[t]);
    const bool up = mr >= 8;
    const float k0 = up ? c[2][2] + c[2][3] : c[0][0] + c[0][1], k1 = up ? c[3][2] + c[3][3] : c[1][0] + c[1][1];
    const float o0 = up ? c[0][2] + c[0][3] : c[2][0] + c[2][1], o1 = up ? c[1][2] + c[1][3] : c[3][0] + c[3][1];
    f32x2* r2 = reinterpret_cast<f32x2*>(red);
    r2[tw * 64 + lane] = f32x2{k0 + dpp<DPP_ROR8>(o0), k1 + dpp<DPP_ROR8>(o1)};
    const int k = arrive(s_c + C_T);
    f32x2 y = (r2[lane] + r2[64 + lane]) + (r2[128 + lane] + r2[192 + lane]);
    asm volatile("" : "+v"(y));
    if (arrived(k) == 3) {
      const f32x4 sf = __builtin_bit_cast(f32x4, st);
      const __hip_bfloat162 o2 = __halves2bfloat162(__float2bfloat16(hv[0] + ((hv[0] - sf[0]) * sf[1] + y[0] * sf[2])),
                                                    __float2bfloat16(hv[1] + ((hv[1] - sf[0]) * sf[1] + y[1] * sf[2])));
      __hip_atomic_store(reinterpret_cast<unsigned*>(out + row_of(qd) * DM + col), *reinterpret_cast<const unsigned*>(&o2), __ATOMIC_RELAXED, __HIP_MEMORY_SCOPE_AGENT);
    }
  } else {
    prep();
    if (isC && tw == 1) {
      lds_wait<8>(s_c + F_REL, 1u);
      const int cs = c_it != 1 ? c_it * 2 : max(g >> 3, 1), cnt = c_it != 1 ? 8 : (g < 16 ? 16 : 32), x0 = c_it != 1 ? g * 8 : (g < 16 ? g * 16 : (g & 7) * 32);
      done(c_it < 2 ? 4 : NA);
      pause(WDELAY);
      const float wk = weight(cs, x0, cnt);
      f32x4 o = {0.f, 0.f, 0.f, 0.f};
#pragma unroll 1
      for (int k0 = 0; k0 < cnt; k0 += 16) {
        u32x2 pv[16];
#pragma unroll
        for (int k = 0; k < 16; ++k) pv[k] = __builtin_amdgcn_raw_buffer_load_b64(rs, lp * 2, (WS_PQ + ((x0 + ((k0 + k) & (cnt - 1))) * 5 + cs) * (DH / 2)) * 4, AUX_EARLY);
#pragma unroll
        for (int k = 0; k < 16; ++k) {
          const float w = k0 + k < cnt ? __uint_as_float(__builtin_amdgcn_readlane(__float_as_uint(wk), k0 + k)) : 0.f;
          o += w * f32x4{(float)(short)pv[k][0], (float)((int)pv[k][0] >> 16), (float)(short)pv[k][1], (float)((int)pv[k][1] >> 16)};
        }
      }
      u32x2 hi, lw;
      split4(o, hi, lw);
      if (lane < DH / 4) st128(rs, WS_OC + (c_it * NG + g) * DH + lane * 4, u32x4{max(hi[0], 1u), max(hi[1], 1u), max(lw[0], 1u), max(lw[1], 1u)});
    }
    done_all();
    const unsigned pl = pull();
    asm volatile("" :: "v"(pl));
  }
  if (tw == 3) done_all();
}

extern "C" void kernel_launch(void* const* d_in, const int* in_sizes, int n_in, void* d_out, int out_size, void* d_ws,
                              size_t ws_size, hipStream_t stream) {
  const bf16* x = (const bf16*)d_in[0];
  const bf16* kc = (const bf16*)d_in[1];
  const bf16* vc = (const bf16*)d_in[2];
  const bf16* wq = (const bf16*)d_in[3];
  const bf16* wo = (const bf16*)d_in[4];
  const bf16* wr = (const bf16*)d_in[6];
  const bf16* wg = (const bf16*)d_in[7];
  const bf16* wu = (const bf16*)d_in[8];
  const bf16* wd = (const bf16*)d_in[9];
  fused_kernel<<<NWG, NW * 64, 0, stream>>>(x, kc, vc, wq, wo, wr, wg, wu, wd, (float*)d_ws, (bf16*)d_out);
}
```

```cpp
#include <hip/hip_runtime.h>
#include <hip/hip_bf16.h>
#include <stdint.h>

typedef __hip_bfloat16 bf16;
using u32x2 = __attribute__((ext_vector_type(2))) unsigned;
using u32x4 = __attribute__((ext_vector_type(4))) unsigned;
using f32x4 = __attribute__((ext_vector_type(4))) float;
using f32x2 = __attribute__((ext_vector_type(2))) float;
using bf16x8 = __attribute__((ext_vector_type(8))) __bf16;

constexpr int NB = 4, DM = 640, NH = 32, SK = 8192, DH = 192, HD = NH * DH, FF = 1536;
constexpr float LN_EPS = 1e-5f;
constexpr float QSCALE = 0.07216878364870322f * 1.4426950408889634f;

constexpr int NWG = 256;
constexpr int GS = 8, NG = NWG / GS, NI = GS / 2;
constexpr int HPG = NI / NB;
constexpr int PPI = 512 / GS;
constexpr int NPAIR = NI * PPI;

constexpr int TAUK = 1320, TAUV = 1332;
constexpr int TSTART = 170;
constexpr int WGAP = 165, WP0 = 64, WPN = 176;
constexpr int VGAP = 90;
constexpr int SPRINT = 4;
constexpr int JUMP = 145;
constexpr int HST = 2800, RT0 = 3900, RTS = 1000;
constexpr unsigned NRD = 5, RMIN = 50, FBP = 60, FBL = 150;
constexpr int W0START = 1700, W0SPAN = 1000, W1START = 2700, W1SPAN = 2600;
constexpr int ROT = 16, ROWS = 0xC9;
constexpr int FBM = 100, FBX = 15, FB0 = 170, FB1 = 55, FBS = 10;
constexpr int FBD = 20, HOLD = 400, GB = 24, GBS = 2;
constexpr unsigned PRD = 3; constexpr int PRB = 24;
constexpr int TLEAD = 40, PFD = 6, WFD = 3;
constexpr int QWAIT = 750, QBACK = 100;
constexpr int WDELAY = 100, WDELAY2 = 460, GLATE = 150, DLATE = 220, DLATE2 = 60;
constexpr int GLEAD = 1, GCNT = 4, DP = 0, DPW = 60, PW = 0, HDELAY = 1150, HDELAY2 = 780, GW = 30, DW = 50;

constexpr int KS = 8;
constexpr int WS_PML = 0;
constexpr int WS_PO = WS_PML + NWG * 64;
constexpr int WS_PQ = WS_PO + NWG * DH;
constexpr int WS_OC = WS_PQ + NWG * 5 * (DH / 2);
constexpr int WS_SLAB = WS_OC + (NB - 1) * NH * DH;
constexpr int WS_ACT = WS_SLAB + KS * NB * DM;
constexpr int WS_H = WS_ACT + NB * FF;
constexpr int WS_ST = WS_H + (NB - 1) * 4 * 192;
constexpr int WS_HN = WS_ST + 32;
constexpr int WS_HR = WS_HN + NWG;
constexpr int WS_HA = WS_HR + 2 * NWG;
constexpr int QFS = 2 * GS * 32;
constexpr int WS_QF = WS_HA + 32 * NWG;
constexpr int WS_QP = WS_QF + NG * QFS;
constexpr int WS_END = WS_QP + NH * 2 * NB * DH;
constexpr int NPROD = HPG * 6;
static_assert(WS_PO % 32 == 0 && WS_SLAB % 32 == 0 && WS_ACT % 32 == 0 && WS_ST % 32 == 0 && WS_HN % 32 == 0 && WS_QF % 32 == 0 && WS_QP % 32 == 0, "line alignment");


__device__ __forceinline__ int row_of(int b) { return (ROWS >> (2 * b)) & 3; }
__device__ __forceinline__ float bf_lo(unsigned u) { return __uint_as_float(u << 16); }
__device__ __forceinline__ float bf_hi(unsigned u) { return __uint_as_float(u & 0xffff0000u); }
__device__ __forceinline__ f32x2 bf_pair(unsigned u) { return f32x2{bf_lo(u), bf_hi(u)}; }
__device__ __forceinline__ f32x2 fma2(float a, f32x2 b, f32x2 c) { return __builtin_elementwise_fma(f32x2{a, a}, b, c); }
__device__ __forceinline__ f32x2 fma2(f32x2 a, f32x2 b, f32x2 c) { return __builtin_elementwise_fma(a, b, c); }

template <int CTRL> __device__ __forceinline__ float dpp(float x) {
  return __builtin_bit_cast(float, __builtin_amdgcn_mov_dpp(__builtin_bit_cast(int, x), CTRL, 0xf, 0xf, true));
}
constexpr int DPP_XOR1 = 0xB1, DPP_XOR2 = 0x4E, DPP_HMIRROR = 0x141, DPP_MIRROR = 0x140, DPP_ROR8 = 0x128;

__device__ __forceinline__ float sum_x16_32(float x) {
  auto s = __builtin_amdgcn_permlane16_swap(__float_as_uint(x), __float_as_uint(x), false, false);
  x = __uint_as_float(s[0]) + __uint_as_float(s[1]);
  auto t = __builtin_amdgcn_permlane32_swap(__float_as_uint(x), __float_as_uint(x), false, false);
  return __uint_as_float(t[0]) + __uint_as_float(t[1]);
}
__device__ __forceinline__ float max_x16_32(float x) {
  auto s = __builtin_amdgcn_permlane16_swap(__float_as_uint(x), __float_as_uint(x), false, false);
  x = fmaxf(__uint_as_float(s[0]), __uint_as_float(s[1]));
  auto t = __builtin_amdgcn_permlane32_swap(__float_as_uint(x), __float_as_uint(x), false, false);
  return fmaxf(__uint_as_float(t[0]), __uint_as_float(t[1]));
}
__device__ __forceinline__ float tsum32(float a, float b) {
  auto t = __builtin_amdgcn_permlane32_swap(__float_as_uint(a), __float_as_uint(b), false, false);
  return __uint_as_float(t[0]) + __uint_as_float(t[1]);
}
__device__ __forceinline__ float tsum16(float a, float b) {
  auto t = __builtin_amdgcn_permlane16_swap(__float_as_uint(a), __float_as_uint(b), false, false);
  return __uint_as_float(t[0]) + __uint_as_float(t[1]);
}
__device__ __forceinline__ float sum_r(float v) { v += dpp<DPP_ROR8>(v); return sum_x16_32(v); }
__device__ __forceinline__ float row_sum(float v) {
  v += dpp<DPP_XOR1>(v); v += dpp<DPP_XOR2>(v); v += dpp<DPP_HMIRROR>(v);
  return v + dpp<DPP_MIRROR>(v);
}
__device__ __forceinline__ float row_max(float v) {
  v = fmaxf(v, dpp<DPP_XOR1>(v)); v = fmaxf(v, dpp<DPP_XOR2>(v)); v = fmaxf(v, dpp<DPP_HMIRROR>(v));
  return fmaxf(v, dpp<DPP_MIRROR>(v));
}
__device__ __forceinline__ float wave_sum(float v) {
  v += dpp<DPP_XOR1>(v); v += dpp<DPP_XOR2>(v); v += dpp<DPP_HMIRROR>(v);
  return sum_r(v);
}

__device__ __forceinline__ u32x4 ld_nt(const char* p) { return __builtin_nontemporal_load(reinterpret_cast<const u32x4*>(p)); }

struct Tile { u32x4 d[3]; };

#define PIN4(a, o) asm volatile("" : "+v"((a)[(o) + 0]), "+v"((a)[(o) + 1]), "+v"((a)[(o) + 2]), "+v"((a)[(o) + 3]))
#define MEMFENCE() do { asm volatile("" ::: "memory"); __builtin_amdgcn_sched_barrier(0); } while (0)

__device__ __forceinline__ void glds16_nt(const void* gsrc, unsigned lds_dst) {
  unsigned keep;
  asm volatile("s_mov_b32 %0, m0\n\ts_mov_b32 m0, %2\n\ts_nop 0\n\tglobal_load_lds_dwordx4 %1, off nt\n\ts_mov_b32 m0, %0"
               : "=&s"(keep) : "v"(gsrc), "s"(lds_dst) : "memory");
}

__device__ __forceinline__ float tile_score(const Tile& k, const f32x2 (&q)[12]) {
  f32x2 s0 = {0.f, 0.f}, s1 = s0;
#pragma unroll
  for (int i = 0; i < 3; ++i) {
    s0 = fma2(bf_pair(k.d[i][0]), q[i * 4], s0);     s1 = fma2(bf_pair(k.d[i][1]), q[i * 4 + 1], s1);
    s0 = fma2(bf_pair(k.d[i][2]), q[i * 4 + 2], s0); s1 = fma2(bf_pair(k.d[i][3]), q[i * 4 + 3], s1);
  }
  s0 += s1;
  return s0[0] + s0[1];
}
__device__ __forceinline__ void score_put(float s, float* dst, int j) {
  s += dpp<DPP_XOR1>(s);
  s += dpp<DPP_XOR2>(s);
  s += dpp<DPP_HMIRROR>(s);
  if (j == 0) *dst = s;
}
__device__ __forceinline__ void tile_load(Tile& t, const char* base, unsigned off, int half) {
#pragma unroll
  for (int i = 0; i < 3; ++i) t.d[i] = ld_nt(base + off + half * 3072 + i * 128);
}
__device__ __forceinline__ float score_next(Tile& k, const f32x2 (&q)[12], const char* base, unsigned off, int half) {
  f32x2 s0 = {0.f, 0.f}, s1 = s0;
#pragma unroll
  for (int i = 0; i < 3; ++i) {
    s0 = fma2(bf_pair(k.d[i][0]), q[i * 4], s0);     s1 = fma2(bf_pair(k.d[i][1]), q[i * 4 + 1], s1);
    s0 = fma2(bf_pair(k.d[i][2]), q[i * 4 + 2], s0); s1 = fma2(bf_pair(k.d[i][3]), q[i * 4 + 3], s1);
    asm volatile("" : "+v"(s0), "+v"(s1));
    MEMFENCE();
    k.d[i] = ld_nt(base + off + half * 3072 + i * 128);
    MEMFENCE();
  }
  s0 += s1;
  return s0[0] + s0[1];
}
__device__ __forceinline__ void pv_next(float p, Tile& v, f32x2 (&acc)[12], const char* base, unsigned off, int half) {
#pragma unroll
  for (int i = 0; i < 3; ++i) {
    asm volatile("" : "+v"(v.d[i]), "+v"(p));
#pragma unroll
    for (int d = 0; d < 4; ++d) acc[i * 4 + d] = fma2(p, bf_pair(v.d[i][d]), acc[i * 4 + d]);
    PIN4(acc, i * 4);
    MEMFENCE();
    v.d[i] = ld_nt(base + off + half * 3072 + i * 128);
    MEMFENCE();
  }
}
__device__ __forceinline__ void pv_last(float p, Tile& v, f32x2 (&acc)[12]) {
#pragma unroll
  for (int i = 0; i < 3; ++i) {
    asm volatile("" : "+v"(v.d[i]), "+v"(p));
#pragma unroll
    for (int d = 0; d < 4; ++d) acc[i * 4 + d] = fma2(p, bf_pair(v.d[i][d]), acc[i * 4 + d]);
    PIN4(acc, i * 4);
  }
}
__device__ __forceinline__ void q_load(f32x2 (&q)[12], const float* src, int j) {
#pragma unroll
  for (int i = 0; i < 6; ++i) {
    const f32x4 a = *reinterpret_cast<const f32x4*>(src + (i >> 1) * 64 + j * 8 + (i & 1) * 4);
    q[i * 2] = f32x2{a[0], a[1]}; q[i * 2 + 1] = f32x2{a[2], a[3]};
  }
}
#define VMCNT(n) asm volatile("s_waitcnt vmcnt(%0)" :: "n"(n) : "memory")

constexpr int AUX_SC1 = 16, AUX_EARLY = 18;
constexpr unsigned SPIN_CAP = 1u << 17;
__device__ __forceinline__ unsigned nz_bits(float v) { return max(__float_as_uint(v), 1u); }
__device__ __forceinline__ bool all_nz(u32x4 v) { return v[0] != 0u && v[1] != 0u && v[2] != 0u && v[3] != 0u; }
__device__ __forceinline__ unsigned min4(u32x4 v) { return min(min(v[0], v[1]), min(v[2], v[3])); }
__device__ __forceinline__ unsigned ld32(__amdgpu_buffer_rsrc_t rs, int word) { return __builtin_amdgcn_raw_buffer_load_b32(rs, word * 4, 0, AUX_SC1); }
__device__ __forceinline__ u32x2 ld64(__amdgpu_buffer_rsrc_t rs, int word) { return __builtin_amdgcn_raw_buffer_load_b64(rs, word * 4, 0, AUX_SC1); }
__device__ __forceinline__ u32x4 ld128(__amdgpu_buffer_rsrc_t rs, int word) { return __builtin_amdgcn_raw_buffer_load_b128(rs, word * 4, 0, AUX_SC1); }
__device__ __forceinline__ void st32(__amdgpu_buffer_rsrc_t rs, int word, unsigned v) { __builtin_amdgcn_raw_buffer_store_b32(v, rs, word * 4, 0, AUX_SC1); }
__device__ __forceinline__ void st64(__amdgpu_buffer_rsrc_t rs, int word, u32x2 v) { __builtin_amdgcn_raw_buffer_store_b64(v, rs, word * 4, 0, AUX_SC1); }
__device__ __forceinline__ void st128(__amdgpu_buffer_rsrc_t rs, int word, u32x4 v) { __builtin_amdgcn_raw_buffer_store_b128(v, rs, word * 4, 0, AUX_SC1); }

__device__ __forceinline__ u32x4 ld128nt(__amdgpu_buffer_rsrc_t rs, int word) { return __builtin_amdgcn_raw_buffer_load_b128(rs, word * 4, 0, AUX_EARLY); }
__device__ __forceinline__ void st128nt(__amdgpu_buffer_rsrc_t rs, int word, u32x4 v) { __builtin_amdgcn_raw_buffer_store_b128(v, rs, word * 4, 0, AUX_EARLY); }

__device__ __forceinline__ bool leader_release(__amdgpu_buffer_rsrc_t rs, int lane, unsigned t0, unsigned rd) {
  u32x4 a[4];
#pragma unroll
  for (int k = 0; k < 4; ++k) a[k] = ld128(rs, WS_HA + (lane * 4 + k) * 32);
  const u32x4 n = ld128(rs, WS_HN + lane * 4);
  if (rd == 0u && !__all(a[0][0] == n[0] && a[1][0] == n[1] && a[2][0] == n[2] && a[3][0] == n[3])) return false;
  const float e = fmaxf(fmaxf((float)(int)(t0 - a[0][2]), (float)(int)(t0 - a[1][2])), fmaxf((float)(int)(t0 - a[2][2]), (float)(int)(t0 - a[3][2])));
  unsigned first = t0 - (unsigned)(int)max_x16_32(row_max(e));
  float ls = 0.f, lx = 0.f;
#pragma unroll
  for (int k = 0; k < 4; ++k) {
    const float l = a[k][0] == n[k] + rd ? (float)min(a[k][3] + (rd ? 0u : min(a[k][2] - first, 300u)), 2000u) : 0.f;
    ls += l; lx = fmaxf(lx, l);
  }
  ls = wave_sum(ls) * (1.0f / NWG); lx = max_x16_32(row_max(lx));
  const int extra = min(max((int)(ls * (FBM / 100.0f) + lx * (FBX / 100.0f)) - (rd ? FB1 + (int)(NRD - rd) * FBD : FB0), 0), 600);
  first = (first & 0xffffu) | ((unsigned)extra << 16);
  if (rd != 0u && ls * NWG < HOLD) return true;
  st128(rs, WS_HR + lane * 8, u32x4{a[0][1] + rd, first, a[1][1] + rd, first});
  st128(rs, WS_HR + lane * 8 + 4, u32x4{a[2][1] + rd, first, a[3][1] + rd, first});
  return true;
}

typedef __attribute__((address_space(3))) unsigned lds_u32;
__device__ __forceinline__ unsigned lds_add(lds_u32* p, int lane) {
  unsigned k = 0;
  if (lane == 0) k = __hip_atomic_fetch_add(p, 1u, __ATOMIC_RELAXED, __HIP_MEMORY_SCOPE_WORKGROUP);
  return __builtin_amdgcn_readfirstlane(k);
}
#define CFENCE() asm volatile("" ::: "memory")
__device__ __forceinline__ int arrive(lds_u32* p) {
  int v;
  unsigned long long keep;
  asm volatile("s_mov_b64 %1, exec\n\ts_mov_b64 exec, 1\n\tds_add_rtn_u32 %0, %2, %3\n\ts_mov_b64 exec, %1"
               : "=&v"(v), "=&s"(keep) : "v"((unsigned)(size_t)p), "v"(1u) : "memory");
  return v;
}
__device__ __forceinline__ int arrived(int v) {
  asm volatile("s_waitcnt lgkmcnt(0)" : "+v"(v) :: "memory");
  return __builtin_amdgcn_readfirstlane(v);
}
__device__ __forceinline__ unsigned lds_get(lds_u32* p) {
  return __builtin_amdgcn_readfirstlane(__hip_atomic_load(p, __ATOMIC_RELAXED, __HIP_MEMORY_SCOPE_WORKGROUP));
}
template <int SLEEP> __device__ __forceinline__ void lds_wait(lds_u32* p, unsigned n) {
#pragma unroll 1
  for (unsigned spin = 0; spin < (SPIN_CAP << 4); ++spin) {
    if (lds_get(p) >= n) break;
    __builtin_amdgcn_s_sleep(SLEEP);
  }
  asm volatile("" ::: "memory");
}
__device__ __forceinline__ void pause(unsigned ticks) {
  const unsigned long long t0 = __builtin_amdgcn_s_memrealtime();
#pragma unroll 1
  for (unsigned spin = 0; spin < 4096u && __builtin_amdgcn_s_memrealtime() - t0 < ticks; ++spin) __builtin_amdgcn_s_sleep(16);
}
__device__ __forceinline__ unsigned clk() { return (unsigned)__builtin_amdgcn_s_memrealtime(); }
struct Table {
  unsigned t0; int was, moved, slowed;
  __device__ __forceinline__ void gate(int due, lds_u32* mine, lds_u32* slow) {
    const int left = (int)lds_get(slow) - slowed, d = max(min(left, FBS), -GBS);
    t0 += d; was -= d; slowed += d;
    int late = (int)(clk() - t0) - due;
    if (late - was > JUMP) { t0 += late - was; moved += late - was; late = was; *mine = moved; }
    mine[4] = max(late - max(left - d, 0), 0);
    was = late;
#pragma unroll 1
    for (int spin = 0; spin < 65536 && late < 0; ++spin) { __builtin_amdgcn_s_sleep(1); late = (int)(clk() - t0) - due; }
  }
  __device__ __forceinline__ void agree(lds_u32* all, lds_u32* mine) {
    if (moved == 0) return;
    const int m = (int)min(min(lds_get(all), lds_get(all + 1)), min(lds_get(all + 2), lds_get(all + 3))) + 30;
    if (m < moved) { t0 -= moved - m; moved = m; was = 1 << 20; *mine = moved; }
  }
};
__device__ __forceinline__ void until(unsigned t0, int due) {
#pragma unroll 1
  for (int spin = 0; spin < 65536 && (int)(clk() - t0) < due; ++spin) __builtin_amdgcn_s_sleep(1);
}
#define LGKM0() asm volatile("s_waitcnt lgkmcnt(0)" ::: "memory")

__device__ __forceinline__ void split4(f32x4 v, u32x2& hi, u32x2& lo) {
  unsigned h[4], r[4];
#pragma unroll
  for (int i = 0; i < 4; ++i) { h[i] = __float_as_uint(v[i]) & 0xffff0000u; r[i] = __float_as_uint(v[i] - __uint_as_float(h[i])); }
  hi = u32x2{__builtin_amdgcn_perm(h[1], h[0], 0x07060302u), __builtin_amdgcn_perm(h[3], h[2], 0x07060302u)};
  lo = u32x2{__builtin_amdgcn_perm(r[1], r[0], 0x07060302u), __builtin_amdgcn_perm(r[3], r[2], 0x07060302u)};
}
__device__ __forceinline__ f32x4 mfma16(u32x4 a, u32x4 b, f32x4 c) {
  return __builtin_amdgcn_mfma_f32_16x16x32_bf16(__builtin_bit_cast(bf16x8, a), __builtin_bit_cast(bf16x8, b), c, 0, 0, 0);
}

constexpr int NW = 8, NMW = 4;
constexpr int NLP = 5, NRP = 7;
constexpr int NTP = 4 * (NLP + NRP);
constexpr int REG = NLP * 6 * 1024;
constexpr int NRING = 4;
constexpr int NS = 6, NA = NS - 1;
__device__ __forceinline__ int seg_beg(int s) { return s >= NS ? NPAIR : ((0xC87640 >> (4 * s)) & 15) * 16; }
__device__ __forceinline__ int seg_k(int t) { return (int)(0x5433222211110000ull >> (4 * (t >> 4))) & 15; }
__device__ __forceinline__ int seg_of_k(int k) { return (0x321540 >> (4 * k)) & 15; }
__device__ __forceinline__ int k_of_seg(int s) { return (0x215430 >> (4 * s)) & 15; }
__device__ __forceinline__ int pair_of(int t) { return t < 64 ? t : (t < 192 ? t + 64 : t - 128); }
constexpr int OFF_CNT = 0, OFF_REG = 256,
              OFF_SC = OFF_REG + 4 * REG, OFF_BUF = OFF_SC + NPAIR * 16 * 4, OFF_Q = OFF_BUF + NRING * NMW * DH * 4, OFF_ML = OFF_Q + NS * DH * 4,
              OFF_RED = OFF_ML + 128, OFF_RS = OFF_RED + 4 * 256 * 4, OFF_END = OFF_RS + 5 * 16;
enum { F_REL = 0, F_DONE, C_LAST, C_T = 3, F_OUT, C_P, C_K, F_P, F_Q = 8, F_FREE, S_FIRST, F_Q2, C_W = 12, C_SC = 16, C_PART = 24, S_ECHO = 32, S_MOVED = 48, S_SLOW = 56 };
__device__ __forceinline__ int due_k(int p) { return TSTART + ((p * TAUK + min(max(p - WP0, 0), WPN) * (WGAP * 64 / WPN)) >> 6); }
__device__ __forceinline__ int due_v(int p) { return TSTART + WGAP + ((NPAIR * TAUK + p * TAUV + max(p - NPAIR / 2, 0) * (VGAP * 64 / (NPAIR / 2))) >> 6); }
constexpr int AW_LD = DH * 2 + 16, AG_LD = 160 * 2 + 16;

__global__ void __launch_bounds__(NW * 64) fused_kernel(const bf16* __restrict__ x, const bf16* __restrict__ kc,
                                                        const bf16* __restrict__ vc, const bf16* __restrict__ wq,
                                                        const bf16* __restrict__ wo, const bf16* __restrict__ wr,
                                                        const bf16* __restrict__ wg, const bf16* __restrict__ wu,
                                                        const bf16* __restrict__ wd, float* ws, bf16* __restrict__ out) {
  __shared__ __attribute__((aligned(16))) unsigned char smem[OFF_END];
  float* s_sc = reinterpret_cast<float*>(smem + OFF_SC);
  float* s_buf = reinterpret_cast<float*>(smem + OFF_BUF);
  float* s_q = reinterpret_cast<float*>(smem + OFF_Q);
  u32x4* s_ml = reinterpret_cast<u32x4*>(smem + OFF_ML);
  lds_u32* s_c = (lds_u32*)(smem + OFF_CNT);

  const int tid = threadIdx.x, lane = tid & 63, r = lane >> 3, j = lane & 7;
  const int wave = __builtin_amdgcn_readfirstlane(tid >> 6);
  const int tw = wave - NMW;
  const int bid = blockIdx.x, xl = bid & 7, idx = bid >> 3;
  const int g = (xl * 32 + idx) / GS, mm = idx % GS;
  const __amdgpu_buffer_rsrc_t rs = __builtin_amdgcn_make_buffer_rsrc(ws, 0, WS_END * 4, 0x00020000);
  const int ro = ((idx & 3) << 3) | (idx >> 2);
  const bool isW = ro < 10, isG = ro >= 10 && ro < 16;
  const int w_ks = xl, w_ct = ro;
  const int g_cg = xl * 6 + (ro - 10);
  const int d_ct = (xl + ((ro - 16) >> 2) * 8) * 2 + (((ro - 16) >> 1) & 1);
  const bool isD = ro >= 16 && ro < 24 && (ro & 1) == 0 && d_ct < DM / 32;
  const bool isH = ro == 22 && xl >= 2 && xl < 5;
  const bool isC = (mm & 7) == 3 || (mm & 7) >= 6;
  const int c_it = (mm >> 3) * 3 + max((mm & 7) - 5, 0);
  const int qf = WS_QF + g * QFS;
  const char* kb = reinterpret_cast<const char*>(kc);
  const char* vb = reinterpret_cast<const char*>(vc);
  const unsigned lo = (r * 24 + j) * 16;
  const int xb = g * GS + mm, c0 = mm * 8 + g * ROT;
  const int b2 = (2 * NH + (xb >> 4)) * 1024 + (xb & 15) * 8, b3 = (2 * NH + 16 + (xb >> 5)) * 1024 + (xb & 31) * 8;
  auto poff = [&](int p) -> unsigned {
    const int turn = (p & 63) >> 2;
    int t = (row_of(p >> 6) * NH + g) * 1024 + ((turn * 64 + (p & 3) * 2 + c0) & 1023);
    if ((p >> 6) == 1) t = (turn < 8 ? b2 + turn * 128 : b3 + (turn & 4) * 2048 + (turn & 3) * 256) + (p & 3) * 2;
    return (unsigned)t * 3072u;
  };
  const unsigned lds_base = (unsigned)(size_t)(__attribute__((address_space(3))) unsigned char*)smem + OFF_REG + tw * REG;
  auto merged = [&](int it, int ln) -> u32x4 {
    const float* src = s_buf + (it % NRING) * (NMW * DH) + ((ln >> 1) & 7) * 24 + (ln >> 4) * 8 + (ln & 1) * 4;
    const f32x4 o = (*reinterpret_cast<const f32x4*>(src) + *reinterpret_cast<const f32x4*>(src + DH)) +
                    (*reinterpret_cast<const f32x4*>(src + 2 * DH) + *reinterpret_cast<const f32x4*>(src + 3 * DH));
    return u32x4{nz_bits(o[0]), nz_bits(o[1]), nz_bits(o[2]), nz_bits(o[3])};
  };
  auto publish = [&](int it) -> bool {
    const int k = arrive(s_c + C_PART + it);
    u32x4 o = merged(it, lane < DH / 4 ? lane : 0);
    asm volatile("" : "+v"(o));
    if (arrived(k) != NMW - 1) return false;
    if (lane < DH / 4) st128(rs, WS_PO + xb * DH + lane * 4, o);
    if (lane == 0) __hip_atomic_fetch_add(s_c + F_DONE, 1u, __ATOMIC_RELAXED, __HIP_MEMORY_SCOPE_WORKGROUP);
    return true;
  };
#define NONCE() do { unsigned n_ = __builtin_amdgcn_readfirstlane(f) + 1u; while (__any(f == n_)) ++n_; \
    if (lane < 32) st32(rs, qf + (GS + mm) * 32 + lane, n_); f = n_; } while (0)
  unsigned r0 = 0u;
  unsigned t0 = clk();
  if (wave < NMW) {
    Tile ta, tb;
    const int c = lane & (GS - 1);
    unsigned f = 0u;
    if (wave == 3) f = ld32(rs, qf + c * 32 + mm);
    if (mm < NPROD) {
      const int hp = g + (mm / 6) * NG, ck = (mm % 6) >> 1, hf = mm & 1, jp = hf * 4 + wave;
      const int bb = lane >> 4, t = lane & 15;
      const u32x4* xp = reinterpret_cast<const u32x4*>(x + row_of(bb) * DM);
      u32x4 xv[5], xs = {0u, 0u, 0u, 0u};
#pragma unroll
      for (int k = 0; k < 5; ++k) xv[k] = xp[t + 16 * k];
      if (t < 10) xs = xp[jp * 10 + t];
      const u32x4* wp = reinterpret_cast<const u32x4*>(wq + (size_t)(jp * 80 + r) * HD + hp * DH + ck * 64) + j;
      u32x4 wv[10];
#pragma unroll
      for (int k = 0; k < 10; ++k) wv[k] = __builtin_nontemporal_load(wp + (size_t)k * 8 * (HD / 8));
      if (wave == 0) s_c[lane] = lane == C_K ? NTP + NMW : 0u;
      MEMFENCE(); LGKM0(); __builtin_amdgcn_s_barrier(); MEMFENCE();
      if (wave == 3) { NONCE(); }
      float sm = 0.f;
#pragma unroll
      for (int k = 0; k < 5; ++k)
#pragma unroll
        for (int d = 0; d < 4; ++d) sm += bf_lo(xv[k][d]) + bf_hi(xv[k][d]);
      const float mean = row_sum(sm) * (1.0f / DM);
      float sv = 0.f;
#pragma unroll
      for (int k = 0; k < 5; ++k)
#pragma unroll
        for (int d = 0; d < 4; ++d) { const float a = bf_lo(xv[k][d]) - mean, c2 = bf_hi(xv[k][d]) - mean; sv += a * a + c2 * c2; }
      const float rstd = 1.0f / sqrtf(row_sum(sv) * (1.0f / DM) + LN_EPS);
      float* s_xn = s_buf + wave * 320;
      if (t < 10) {
#pragma unroll
        for (int d = 0; d < 4; ++d) {
          s_xn[(t * 8 + 2 * d) * 4 + bb] = (bf_lo(xs[d]) - mean) * rstd;
          s_xn[(t * 8 + 2 * d + 1) * 4 + bb] = (bf_hi(xs[d]) - mean) * rstd;
        }
      }
      LGKM0();
      f32x2 qa[16];
#pragma unroll
      for (int i = 0; i < 16; ++i) qa[i] = f32x2{0.f, 0.f};
#pragma unroll
      for (int k = 0; k < 10; ++k) {
        const f32x4 xn = *reinterpret_cast<const f32x4*>(&s_xn[(k * 8 + r) * 4]);
#pragma unroll
        for (int d = 0; d < 4; ++d) {
          const f32x2 w2 = bf_pair(wv[k][d]);
#pragma unroll
          for (int e = 0; e < 4; ++e) qa[e * 4 + d] = fma2(xn[e], w2, qa[e * 4 + d]);
        }
      }
      float t16[16], t8[8];
#pragma unroll
      for (int i = 0; i < 16; ++i) t16[i] = tsum32(qa[i >> 1][i & 1], qa[8 + (i >> 1)][i & 1]);
#pragma unroll
      for (int i = 0; i < 8; ++i) t8[i] = tsum16(t16[i], t16[i + 8]);
#pragma unroll
      for (int i = 0; i < 8; ++i) t8[i] += dpp<DPP_ROR8>(t8[i]);
      const bool up = (lane & 8) != 0;
      f32x4* r4 = reinterpret_cast<f32x4*>(smem + OFF_RED);
      r4[wave * 64 + lane] = f32x4{up ? t8[4] : t8[0], up ? t8[5] : t8[1], up ? t8[6] : t8[2], up ? t8[7] : t8[3]};
      LGKM0();
      if (lds_add(s_c + C_P, lane) == NMW - 1) {
        asm volatile("" ::: "memory");
        const f32x4 o4 = (r4[lane] + r4[64 + lane]) + (r4[128 + lane] + r4[192 + lane]);
        st128nt(rs, WS_QP + ((hp * 2 + hf) * NB + bb) * DH + ck * 64 + j * 8 + (up ? 4 : 0), __builtin_bit_cast(u32x4, o4));
        VMCNT(0);
        if (lane == 0) s_c[F_OUT] = 1u;
      }
    } else {
      if (wave == 0) s_c[lane] = lane == C_K ? NTP + NMW : 0u;
      MEMFENCE(); LGKM0(); __builtin_amdgcn_s_barrier(); MEMFENCE();
      if (wave == 3) { NONCE(); }
    }
    MEMFENCE();
    if (wave == 3) {
      __builtin_amdgcn_s_setprio(3);
      const unsigned nonce = f;
      unsigned echoed = 0u;
      if (mm < NPROD) {
        echoed = ld32(rs, qf + (GS + c) * 32);
        asm volatile("" : "+v"(echoed) :: "memory");
        lds_wait<1>(s_c + F_OUT, 1u);
        if (lane < 32) st32(rs, qf + mm * 32 + lane, echoed);
#pragma unroll 1
        while ((int)(clk() - t0) < QWAIT - QBACK) {
          const unsigned nv = ld32(rs, qf + (GS + c) * 32);
          if (__any(nv != echoed)) {
            if (lane < 32) st32(rs, qf + mm * 32 + lane, nv);
            echoed = nv;
          }
          __builtin_amdgcn_s_sleep(4);
        }
      }
      until(t0, QWAIT);
#pragma unroll 1
      for (unsigned spin = 0; spin < SPIN_CAP; ++spin) {
        const unsigned nv = ld32(rs, qf + (GS + c) * 32);
        f = ld32(rs, qf + c * 32 + mm);
        const bool done = __all(c == mm || c >= NPROD || f == nonce);
        if (mm < NPROD && __any(nv != echoed)) {
          if (lane < 32) st32(rs, qf + mm * 32 + lane, nv);
          echoed = nv;
        }
        if (done) break;
        __builtin_amdgcn_s_sleep(8);
      }
      if (lane < GS) s_c[S_ECHO + lane] = echoed;
      if (lane < DH / 4) {
        u32x4 p[3][2];
#pragma unroll
        for (int it = 0; it < 3; ++it)
#pragma unroll
          for (int k = 0; k < 2; ++k) p[it][k] = ld128nt(rs, WS_QP + ((g * 2 + k) * NB + (it ? it + 1 : 0)) * DH + lane * 4);
#pragma unroll
        for (int it = 0; it < 3; ++it) {
          *reinterpret_cast<f32x4*>(&s_q[(it ? it + 3 : 0) * DH + lane * 4]) = (__builtin_bit_cast(f32x4, p[it][0]) + __builtin_bit_cast(f32x4, p[it][1])) * QSCALE;
          if (it != 1) { LGKM0(); if (lane == 0) s_c[F_Q] = it + 1; MEMFENCE(); }
        }
      }
    }
    __builtin_amdgcn_s_setprio(3);
    until(t0, due_k(NTP + wave));
    Table tt = {t0, 1 << 20, 0, 0};
    bool common = false;
    tile_load(ta, kb, poff(NTP + wave) + lo, 0);
    tile_load(tb, kb, poff(NTP + wave) + lo, 1);
    MEMFENCE();
    {
      f32x2 q[12];
      int cur = NTP + wave, nxt = lds_add(s_c + C_K, lane), passed = 0, ci = -1;
      for (;;) {
        const bool last = nxt >= NPAIR;
        const unsigned off = poff(last ? wave : pair_of(nxt)) + lo;
        const char* base = last ? vb : kb;
        const int it = seg_k(cur), cp = pair_of(cur);
        if (it != ci) {
          LGKM0();
          for (; passed < it; ++passed) lds_add(s_c + C_SC + passed, lane);
          lds_wait<1>(s_c + (it < 3 ? F_Q : F_Q2), min(it + 1, 3));
          q_load(q, s_q + seg_of_k(it) * DH, j);
          if (!common) { const unsigned d = lds_get(s_c + S_FIRST); if (d) { tt.t0 -= d - 1u; common = true; tt.was = 1 << 20; } }
          tt.agree(s_c + S_MOVED, s_c + S_MOVED + wave);
          ci = it;
        }
        unsigned tk = 0u;
        if (lane == 0) tk = __hip_atomic_fetch_add(s_c + C_K, 1u, __ATOMIC_RELAXED, __HIP_MEMORY_SCOPE_WORKGROUP);
        MEMFENCE();
        tt.gate(due_k(last ? NPAIR + wave : nxt), s_c + S_MOVED + wave, s_c + S_SLOW);
        MEMFENCE();
        const float sa = score_next(ta, q, base, off, 0);
        score_put(sa, s_sc + cp * 16 + r, j);
        MEMFENCE();
        const float sb = score_next(tb, q, base, off, 1);
        score_put(sb, s_sc + cp * 16 + 8 + r, j);
        MEMFENCE();
        if (last) break;
        cur = nxt; nxt = __builtin_amdgcn_readfirstlane(tk);
      }
      LGKM0();
      for (; passed < NS; ++passed) lds_add(s_c + C_SC + passed, lane);
    }
#pragma unroll 1
    for (int it = 0; it < NS; ++it) {
      lds_wait<1>(s_c + F_P, k_of_seg(it) + 1);
      if (!common) { const unsigned d = lds_get(s_c + S_FIRST); if (d) { tt.t0 -= d - 1u; common = true; tt.was = 1 << 20; } }
      tt.agree(s_c + S_MOVED, s_c + S_MOVED + wave);
      f32x2 acc[12];
#pragma unroll
      for (int i = 0; i < 12; ++i) acc[i] = f32x2{0.f, 0.f};
#pragma unroll 1
      for (int p = seg_beg(it) + wave; p < min(seg_beg(it + 1), NPAIR - NMW); p += NMW) {
        const unsigned off = poff(p + NMW) + lo;
        const float pa = s_sc[p * 16 + r], pb = s_sc[p * 16 + 8 + r];
        MEMFENCE();
        if (p + NMW < NPAIR - SPRINT * NMW) tt.gate(due_v(p + NMW), s_c + S_MOVED + wave, s_c + S_SLOW);
        MEMFENCE();
        pv_next(pa, ta, acc, vb, off, 0);
        pv_next(pb, tb, acc, vb, off, 1);
        if (((p + NMW) >> 2) == (NPAIR >> 2) - GLEAD) { if (lane == 0) __hip_atomic_fetch_add(s_c + C_LAST, 1u, __ATOMIC_RELAXED, __HIP_MEMORY_SCOPE_WORKGROUP); }
      }
      if (it == NS - 1) {
        const int p = NPAIR - NMW + wave;
        const float pa = s_sc[p * 16 + r], pb = s_sc[p * 16 + 8 + r];
        pv_last(pa, ta, acc);
        pv_last(pb, tb, acc);
      }
      float t12[12], t6[6];
#pragma unroll
      for (int i = 0; i < 12; ++i) t12[i] = tsum32(acc[i >> 1][i & 1], acc[6 + (i >> 1)][i & 1]);
#pragma unroll
      for (int i = 0; i < 6; ++i) t6[i] = tsum16(t12[i], t12[i + 6]);
#pragma unroll
      for (int i = 0; i < 6; ++i) t6[i] += dpp<DPP_ROR8>(t6[i]);
      if (it >= NRING) lds_wait<1>(s_c + F_FREE, it - NRING + 1);
      if ((lane & 8) == 0) {
        f32x2* dst = reinterpret_cast<f32x2*>(&s_buf[(it % NRING) * (NMW * DH) + wave * DH + j * 24 + (lane >> 4) * 6]);
        dst[0] = f32x2{t6[0], t6[1]}; dst[1] = f32x2{t6[2], t6[3]}; dst[2] = f32x2{t6[4], t6[5]};
      }
      CFENCE();
      if (it < NA) { if (lane == 0) __hip_atomic_fetch_add(s_c + C_PART + it, 1u, __ATOMIC_RELAXED, __HIP_MEMORY_SCOPE_WORKGROUP); }
    }
    publish(NS - 1);
    return;
  }

  {
    Tile pk[NRP * 2];
    __builtin_amdgcn_s_setprio(3);
#pragma unroll
    for (int e = 0; e < NLP * 2; ++e) {
      const char* src = kb + (poff((e >> 1) * 4 + tw) + lo + (e & 1) * 3072);
      if (e >= 2) until(t0, due_k(e * 2 + tw) - TLEAD);
#pragma unroll
      for (int i = 0; i < 3; ++i) glds16_nt(src + i * 128, lds_base + (e * 3 + i) * 1024);
      if (e == 1) __builtin_amdgcn_s_barrier();
      if (e * 3 + 3 > PFD) VMCNT(PFD);
    }
#pragma unroll
    for (int e = 0; e < NRP * 2; ++e) {
      until(t0, due_k((NLP * 2 + e) * 2 + tw) - TLEAD);
      tile_load(pk[e], kb, poff((NLP + (e >> 1)) * 4 + tw) + lo, e & 1);
      MEMFENCE();
      if (e + 1 < NRP * 2) VMCNT(PFD);
    }
    if (tw == 0) {
      r0 = ld32(rs, WS_HR + bid * 2);
      if (bid == 0) {
        u32x4 n;
#pragma unroll
        for (int k = 0; k < 4; ++k) n[k] = ld32(rs, WS_HA + (lane * 4 + k) * 32) + 1u;
        st128(rs, WS_HN + lane * 4, n);
      }
      const u32x4 z = {0u, 0u, 0u, 0u};
      if (lane < 48) {
#pragma unroll
        for (int it = NA; it < NS; ++it) st128(rs, WS_PO + xb * DH + lane * 4, z);
      }
      else st128(rs, WS_PML + (g * GS + mm) * 64 + (lane - 48) * 4, z);
      if (isG && g_cg == 0 && lane < 8) st128(rs, WS_ST + lane * 4, z);
      if (isW) st128(rs, WS_SLAB + (w_ks * NB + (lane >> 4)) * DM + w_ct * 64 + (lane & 15) * 4, z);
      else if (isG) { if (lane < 32) st128(rs, WS_ACT + g_cg * 128 + lane * 4, z); }
      else if (isH) {
#pragma unroll
        for (int k = 0; k < 3; ++k) st128(rs, WS_H + (xl - 2) * 768 + k * 256 + lane * 4, z);
      }
      else if (isC && lane < 48) st128(rs, WS_OC + (c_it * NG + g) * DH + lane * 4, z);
    }
    __builtin_amdgcn_s_setprio(0);
    lds_wait<1>(s_c + F_Q, HPG);
    f32x2 q[12];
    q_load(q, s_q, j);
    const u32x4* s_kv = reinterpret_cast<const u32x4*>(smem + OFF_REG + tw * REG) + lane;
    static_assert(NLP * 4 <= PPI && NTP <= HPG * PPI, "tail tiles: batch row 0 only");
#pragma unroll 1
    for (int e = 0; e < NLP * 2; ++e) {
      Tile tmp;
#pragma unroll
      for (int i = 0; i < 3; ++i) tmp.d[i] = s_kv[(e * 3 + i) * 64];
      score_put(tile_score(tmp, q), s_sc + ((e >> 1) * 4 + tw) * 16 + (e & 1) * 8 + r, j);
    }
#pragma unroll
    for (int e = 0; e < NRP * 2; ++e) {
      if ((NLP * 2 + e) * 2 == PPI) q_load(q, s_q + DH, j);
      score_put(tile_score(pk[e], q), s_sc + ((NLP + (e >> 1)) * 4 + tw) * 16 + (e & 1) * 8 + r, j);
      __builtin_amdgcn_sched_barrier(0);
    }
    asm volatile("s_waitcnt vmcnt(0) lgkmcnt(0)" ::: "memory");
#pragma unroll
    for (int it = 0; it * PPI < NTP; ++it) lds_add(s_c + C_SC + it, lane);
  }

  const int mr = lane & 15, qd = lane >> 4;
  unsigned char* reg = smem + OFF_REG + tw * REG;
  float* red = reinterpret_cast<float*>(smem + OFF_RED);
#define PERM_LO(hi_, lo_) __builtin_amdgcn_perm(hi_, lo_, 0x05040100u)
#define PERM_HI(hi_, lo_) __builtin_amdgcn_perm(hi_, lo_, 0x07060302u)
  int served = 0;
  auto done = [&](int n) {
    if (tw == 3 && served < min(n, NA)) {
      const int from = served;
      int ln = lane;
      asm volatile("" : "+v"(ln));
#pragma unroll 1
      for (; served < min(n, NA); ++served) {
        lds_wait<8>(s_c + C_PART + served, NMW);
        const f32x4 o = __builtin_bit_cast(f32x4, merged(served, ln < DH / 4 ? ln : 0));
        const float am = max_x16_32(row_max(fmaxf(fmaxf(fabsf(o[0]), fabsf(o[1])), fmaxf(fabsf(o[2]), fabsf(o[3])))));
        const float inv = am > 1e-30f ? 32767.0f / am : 0.f;
        const int q0 = __float2int_rn(o[0] * inv), q1 = __float2int_rn(o[1] * inv), q2 = __float2int_rn(o[2] * inv), q3 = __float2int_rn(o[3] * inv);
        if (ln < DH / 4) reinterpret_cast<u32x2*>(s_sc + served * (DH / 2))[ln] = u32x2{PERM_LO((unsigned)q1, (unsigned)q0), PERM_LO((unsigned)q3, (unsigned)q2)};
        const bool nan = __any((o[0] + o[1]) + (o[2] + o[3]) != (o[0] + o[1]) + (o[2] + o[3]));
        if (ln == 0) s_ml[served][2] = nan ? 0x7fc00000u : nz_bits(am * (1.0f / 32767.0f));
        LGKM0();
        if (ln == 0) s_c[F_FREE] = served + 1;
      }
#pragma unroll 1
      for (int c = from * 24 + ln; c < served * 24; c += 64) st128nt(rs, WS_PQ + xb * 5 * (DH / 2) + c * 4, reinterpret_cast<const u32x4*>(s_sc)[c]);
      VMCNT(0);
      if (ln < 8) st128(rs, WS_PML + xb * 64 + (from ? 32 : 0) + ln * 4, s_ml[ln]);
      if (ln == 0) __hip_atomic_fetch_add(s_c + F_DONE, (unsigned)(served - from), __ATOMIC_RELAXED, __HIP_MEMORY_SCOPE_WORKGROUP);
    }
    if (n > NA) lds_wait<1>(s_c + F_DONE, n); else lds_wait<8>(s_c + F_DONE, n);
  };
  auto done_all = [&]() {
#pragma unroll 1
    for (int n = 4; n <= NA; ++n) done(n);
  };
  const int lp = (lane < DH / 4 ? lane : 0) * 4;
  auto weight = [&](int it, int x0, int cnt) -> float {
    const int mlw = WS_PML + (x0 + (lane & (cnt - 1))) * 64 + (it < 4 ? 0 : 32) + it * 4;
    u32x4 ml;
#pragma unroll 1
    for (unsigned spin = 0; spin < SPIN_CAP; ++spin) {
      ml = ld128(rs, mlw);
      if (__all(min4(ml) != 0u)) break;
      __builtin_amdgcn_s_sleep(16);
    }
    const float mk = __uint_as_float(ml[0]);
    const float e = __builtin_amdgcn_exp2f(mk - max_x16_32(row_max(mk)));
    return e / (wave_sum(e * __uint_as_float(ml[1])) * ((float)cnt * (1.0f / 64.0f))) * __uint_as_float(ml[2]);
  };
  auto prep = [&]() {
    if (tw == 0) {
      const unsigned tok = __builtin_amdgcn_readfirstlane(r0) + 1u;
      const int c = lane & (GS - 1);
      unsigned echoed = 0u, qe = s_c[S_ECHO + c];
      bool first = true, released = false, sent = false;
#pragma unroll 1
      while ((int)(clk() - t0) < HST) {
        const unsigned qn = ld32(rs, qf + (GS + c) * 32);
        if (mm < NPROD && __any(qn != qe)) {
          if (lane < 32) st32(rs, qf + mm * 32 + lane, qn);
          qe = qn;
        }
        __builtin_amdgcn_s_sleep(64);
      }
      int slow = 0;
      auto lag = [&]() { return (lds_get(s_c + S_MOVED + 4) + lds_get(s_c + S_MOVED + 5) + lds_get(s_c + S_MOVED + 6) + lds_get(s_c + S_MOVED + 7)) >> 2; };
      const unsigned behind = lag();
#pragma unroll 1
      for (unsigned spin = 0; spin < SPIN_CAP && !released; ++spin) {
        const unsigned qn = ld32(rs, qf + (GS + c) * 32);
        const u32x2 rv = ld64(rs, WS_HR + bid * 2);
        const unsigned nv = __builtin_amdgcn_readfirstlane(ld32(rs, WS_HN + bid));
        if (__builtin_amdgcn_readfirstlane(rv[0]) - tok <= NRD) {
          released = true;
          slow = __builtin_amdgcn_readfirstlane(rv[1]) >> 16;
          if (lane == 0) { s_c[S_FIRST] = min((t0 - rv[1]) & 0xffffu, 300u) + 1u; s_c[S_SLOW] = slow; }
        } else {
          if (mm < NPROD && __any(qn != qe)) {
            if (lane < 32) st32(rs, qf + mm * 32 + lane, qn);
            qe = qn;
          }
          if (first || nv != echoed) {
            echoed = nv; first = false;
            if (lane < 8) st128(rs, WS_HA + bid * 32 + lane * 4, u32x4{echoed, tok, t0, behind});
          }
          if (bid == 0 && !sent) sent = leader_release(rs, lane, t0, 0u);
          __builtin_amdgcn_s_sleep(32);
        }
      }
      if (lane == 0) s_c[F_REL] = 1u;
      if (lane < DH / 4) {
        u32x4 p[3][2];
#pragma unroll
        for (int e = 0; e < 3; ++e)
#pragma unroll
          for (int k = 0; k < 2; ++k) p[e][k] = ld128nt(rs, WS_QP + (((e ? 8 + e * 8 + (xb >> 5) : xb >> 4) * 2 + k) * NB + 1) * DH + lane * 4);
#pragma unroll
        for (int e = 0; e < 3; ++e) *reinterpret_cast<f32x4*>(&s_q[(1 + e) * DH + lane * 4]) = (__builtin_bit_cast(f32x4, p[e][0]) + __builtin_bit_cast(f32x4, p[e][1])) * QSCALE;
      }
      LGKM0();
      if (lane == 0) s_c[F_Q2] = 3u;
      unsigned prev = slow;
#pragma unroll 1
      for (unsigned rd = 1; rd <= NRD; ++rd) {
        const int tr = RT0 + (int)(rd - 1) * RTS;
        until(t0, tr);
        const unsigned bh = lag();
        if (bh > RMIN && lane < 8) st128(rs, WS_HA + bid * 32 + lane * 4, u32x4{echoed + rd, tok, t0, bh});
        if (bid == 0) { until(t0, tr + 220); leader_release(rs, lane, t0, rd); }
        until(t0, tr + 500);
        const u32x2 rv = ld64(rs, WS_HR + bid * 2);
        if (__builtin_amdgcn_readfirstlane(rv[0]) != tok + rd) {
          prev = 0u;
          const int fl = -(int)min(min(lds_get(s_c + S_MOVED), lds_get(s_c + S_MOVED + 1)), min(lds_get(s_c + S_MOVED + 2), lds_get(s_c + S_MOVED + 3)));
          slow = slow > fl ? max(slow - GB, fl) : slow - (rd <= PRD ? PRB : 0);
          if (lane == 0) s_c[S_SLOW] = slow;
        } else {
          const unsigned ex = __builtin_amdgcn_readfirstlane(rv[1]) >> 16;
          slow += ex + min(ex, prev) * (rd == NRD ? FBL : FBP) / 100u;
          prev = ex;
          if (lane == 0) s_c[S_SLOW] = slow;
        }
      }
    }
    if (tw == 3) {
#pragma unroll 1
      for (int ik = 0; ik < NS; ++ik) {
        const int it = seg_of_k(ik), nv = (seg_beg(it + 1) - seg_beg(it)) >> 4;
        lds_wait<4>(s_c + C_SC + ik, ik == 0 ? NW : NMW);
        f32x4* sp = reinterpret_cast<f32x4*>(s_sc + seg_beg(it) * 16) + lane;
        f32x4 v[4];
#pragma unroll
        for (int k = 0; k < 4; ++k) v[k] = sp[(k & (nv - 1)) * 64];
        const f32x4 mx = __builtin_elementwise_max(__builtin_elementwise_max(v[0], v[1]), __builtin_elementwise_max(v[2], v[3]));
        const float m = max_x16_32(row_max(fmaxf(fmaxf(mx[0], mx[1]), fmaxf(mx[2], mx[3]))));
        float l = 0.f;
#pragma unroll
        for (int k = 0; k < 4; ++k) {
#pragma unroll
          for (int e = 0; e < 4; ++e) v[k][e] = __builtin_amdgcn_exp2f(v[k][e] - m);
          if (k < nv) {
            l += (v[k][0] + v[k][1]) + (v[k][2] + v[k][3]);
            sp[k * 64] = v[k];
          }
        }
        l = wave_sum(l);
        if (lane == 0) s_ml[it] = u32x4{nz_bits(m), nz_bits(l), 0x3f800000u, 0x3f800000u};
        LGKM0();
        if (lane == 0) s_c[F_P] = ik + 1;
      }
    }
  };
  auto pull = [&]() {
    typedef __attribute__((address_space(1))) const unsigned g_u32;
    const int id = (ro - 16) * 4 + tw;
    g_u32* p1[2];
#pragma unroll
    for (int i = 0; i < 2; ++i) {
      const int l = id * 60 + min(i * 32 + (lane >> 1), 59), m = l >= 1920 ? l - 1920 : l;
      p1[i] = (g_u32*)((l >= 1920 ? wu : wg) + (size_t)(m / 3) * FF + xl * 192 + (m % 3) * 64 + (lane & 1) * 32);
    }
    g_u32* p2 = (g_u32*)(wd + (size_t)(id * 24 + min(lane >> 1, 23)) * DM + xl * 64 + (lane & 1) * 32);
    asm volatile("" : "+v"(p1[0]), "+v"(p1[1]), "+v"(p2));
    if (GLEAD) lds_wait<1>(s_c + C_LAST, GCNT); else done(NS);
    if (PW) until(clk(), PW);
    const unsigned v0 = p1[0][0], v1 = p1[1][0];
    MEMFENCE();
    if (DP == 1) done(NS);
    if (DPW) until(clk(), DPW);
    const unsigned v2 = p2[0], v3 = p2[xl < 2 ? 256 : 0];
    MEMFENCE();
    return (v0 ^ v1) + (v2 ^ v3);
  };
  if (isW) {
    u32x4 bx[24];
    const char* p = reinterpret_cast<const char*>(wo + (size_t)((w_ks * 4 + tw) * DH + r) * DM + w_ct * 64 + j * 8);
#pragma unroll 1
    for (int i = 0; i < 24; ++i) { until(t0, tw == 0 ? W0START + i * (W0SPAN / 24) : W1START + i * (W1SPAN / 24)); glds16_nt(p, lds_base + i * 1024); p += 8 * DM * 2; VMCNT(WFD); }
    asm volatile("s_waitcnt vmcnt(0)" ::: "memory");
#pragma unroll
    for (int s = 0; s < 6; ++s) {
      u32x2 e[8];
#pragma unroll
      for (int k = 0; k < 8; ++k) e[k] = *reinterpret_cast<const u32x2*>(reg + (s * 32 + qd * 8 + k) * 128 + mr * 8);
#pragma unroll
      for (int k = 0; k < 4; ++k) {
        bx[s * 4 + 0][k] = PERM_LO(e[2 * k + 1][0], e[2 * k][0]); bx[s * 4 + 1][k] = PERM_HI(e[2 * k + 1][0], e[2 * k][0]);
        bx[s * 4 + 2][k] = PERM_LO(e[2 * k + 1][1], e[2 * k][1]); bx[s * 4 + 3][k] = PERM_HI(e[2 * k + 1][1], e[2 * k][1]);
      }
    }
#pragma unroll
    for (int s = 0; s < 24; s += 4) asm volatile("" : "+v"(bx[s]), "+v"(bx[s + 1]), "+v"(bx[s + 2]), "+v"(bx[s + 3]));
    LGKM0();
    prep();
    lds_wait<8>(s_c + F_REL, 1u);
    const int hh = w_ks * 4 + tw;
#pragma unroll 1
    for (int pass = 0; pass < 3; ++pass) {
      if (pass < 2) {
        done(pass ? NA : 4);
        pause(pass ? WDELAY2 : WDELAY2 + 300);
        const int b0 = pass * 2, b1 = pass + 1;
        u32x4 v0, v1;
#pragma unroll 1
        for (unsigned spin = 0; spin < SPIN_CAP; ++spin) {
          v0 = ld128(rs, WS_OC + (b0 * NH + hh) * DH + lp);
          v1 = ld128(rs, WS_OC + (b1 * NH + hh) * DH + lp);
          if (__all(min(min4(v0), min4(v1)) != 0u)) break;
          __builtin_amdgcn_s_sleep(16);
        }
        if (lane < DH / 4) {
          *reinterpret_cast<u32x2*>(reg + (b0 * 4) * AW_LD + lane * 8) = u32x2{v0[0], v0[1]};
          *reinterpret_cast<u32x2*>(reg + (b0 * 4 + 1) * AW_LD + lane * 8) = u32x2{v0[2], v0[3]};
          *reinterpret_cast<u32x2*>(reg + (b1 * 4) * AW_LD + lane * 8) = u32x2{v1[0], v1[1]};
          *reinterpret_cast<u32x2*>(reg + (b1 * 4 + 1) * AW_LD + lane * 8) = u32x2{v1[2], v1[3]};
        }
      } else {
        const float wk = weight(NS - 1, hh * GS, GS);
        float w[GS];
#pragma unroll
        for (int k = 0; k < GS; ++k) { w[k] = __uint_as_float(__builtin_amdgcn_readlane(__float_as_uint(wk), k)); asm volatile("" : "+s"(w[k])); }
        done(NS);
        __builtin_amdgcn_s_setprio(3);
        const int po = WS_PO + hh * GS * DH;
        u32x4 pv[GS];
#pragma unroll 1
        for (unsigned spin = 0; spin < SPIN_CAP; ++spin) {
#pragma unroll
          for (int k = 0; k < GS; ++k) pv[k] = __builtin_amdgcn_raw_buffer_load_b128(rs, lp * 4, (po + k * DH) * 4, AUX_SC1);
          unsigned mn = min4(pv[0]);
#pragma unroll
          for (int k = 1; k < GS; ++k) mn = min(mn, min4(pv[k]));
          if (__all(mn != 0u)) break;
          __builtin_amdgcn_s_sleep(1);
        }
        f32x4 o = w[0] * __builtin_bit_cast(f32x4, pv[0]), o2 = w[1] * __builtin_bit_cast(f32x4, pv[1]);
#pragma unroll
        for (int k = 2; k < GS; k += 2) { o += w[k] * __builtin_bit_cast(f32x4, pv[k]); o2 += w[k + 1] * __builtin_bit_cast(f32x4, pv[k + 1]); }
        u32x2 hi, lw;
        split4(o + o2, hi, lw);
        if (lane < DH / 4) {
          *reinterpret_cast<u32x2*>(reg + ((NB - 1) * 4) * AW_LD + lane * 8) = hi;
          *reinterpret_cast<u32x2*>(reg + ((NB - 1) * 4 + 1) * AW_LD + lane * 8) = lw;
        }
      }
      CFENCE();
      u32x4 a[6];
#pragma unroll
      for (int s = 0; s < 6; ++s) a[s] = *reinterpret_cast<const u32x4*>(reg + mr * AW_LD + s * 64 + qd * 16);
      __builtin_amdgcn_sched_barrier(0);
      f32x4 c[4];
#pragma unroll
      for (int t = 0; t < 4; ++t) c[t] = f32x4{0.f, 0.f, 0.f, 0.f};
#pragma unroll
      for (int s = 0; s < 6; ++s)
#pragma unroll
        for (int t = 0; t < 4; ++t) c[t] = mfma16(a[s], bx[s * 4 + t], c[t]);
      const bool mine = max(qd - 1, 0) == pass;
      f32x4* r4 = reinterpret_cast<f32x4*>(red);
      if (mine) r4[tw * 64 + lane] = f32x4{c[0][0] + c[0][1], c[1][0] + c[1][1], c[2][0] + c[2][1], c[3][0] + c[3][1]};
      const int k = arrive(s_c + C_W + pass);
      f32x4 sum = (r4[lane] + r4[64 + lane]) + (r4[128 + lane] + r4[192 + lane]);
      asm volatile("" : "+v"(sum));
      if (arrived(k) == 3) {
        if (mine) st128(rs, WS_SLAB + (w_ks * NB + qd) * DM + w_ct * 64 + mr * 4, u32x4{nz_bits(sum[0]), nz_bits(sum[1]), nz_bits(sum[2]), nz_bits(sum[3])});
      }
    }
  } else if (isG || isH) {
    f32x4* rst = reinterpret_cast<f32x4*>(red);
    const int hb = isH ? xl - 2 : NB - 1;
    uint2 wr4 = make_uint2(0u, 0u), xg = wr4;
    if (lane < 40) {
      wr4 = *reinterpret_cast<const uint2*>(wr + tw * 160 + lane * 4);
      xg = *reinterpret_cast<const uint2*>(x + row_of(hb) * DM + tw * 160 + lane * 4);
    }
    const float cq = bf_lo(*reinterpret_cast<const unsigned*>(x + row_of(qd) * DM));
    const float wsum = wave_sum((bf_lo(wr4.x) + bf_hi(wr4.x)) + (bf_lo(wr4.y) + bf_hi(wr4.y)));
    f32x4 xc = f32x4{bf_lo(xg.x), bf_hi(xg.x), bf_lo(xg.y), bf_hi(xg.y)} - __uint_as_float(__builtin_amdgcn_readlane(__float_as_uint(cq), hb * 16));
    asm volatile("" : "+v"(xc));
    prep();
    lds_wait<8>(s_c + F_REL, 1u);
    auto hrow = [&](bool fast) -> f32x4 {
      f32x4 hv = {0.f, 0.f, 0.f, 0.f};
      if (lane < 40) {
        const int off = (WS_SLAB + hb * DM + tw * 160 + lane * 4) * 4;
        u32x4 sl[KS];
#pragma unroll 1
        for (unsigned spin = 0; spin < SPIN_CAP; ++spin) {
#pragma unroll
          for (int ks = 0; ks < KS; ++ks) sl[ks] = __builtin_amdgcn_raw_buffer_load_b128(rs, off, ks * (NB * DM * 4), AUX_SC1);
          unsigned mn = min4(sl[0]);
#pragma unroll
          for (int ks = 1; ks < KS; ++ks) mn = min(mn, min4(sl[ks]));
          if (!__any(mn == 0u)) break;
          if (fast) __builtin_amdgcn_s_sleep(1); else __builtin_amdgcn_s_sleep(16);
        }
#define SL(k) __builtin_bit_cast(f32x4, sl[k])
        hv = xc + (((SL(0) + SL(1)) + (SL(2) + SL(3))) + ((SL(4) + SL(5)) + (SL(6) + SL(7))));
#undef SL
      }
      return hv;
    };
    auto sums = [&](f32x4 hv) -> f32x4 {
      const float s1 = wave_sum((hv[0] + hv[1]) + (hv[2] + hv[3]));
      const float s2 = wave_sum((hv[0] * hv[0] + hv[1] * hv[1]) + (hv[2] * hv[2] + hv[3] * hv[3]));
      const float dot = wave_sum((hv[0] * bf_lo(wr4.x) + hv[1] * bf_hi(wr4.x)) + (hv[2] * bf_lo(wr4.y) + hv[3] * bf_hi(wr4.y)));
      return f32x4{s1, s2, dot, wsum};
    };
    if (isH) {
      if (tw == 3 || hb == 2) done_all(); else done(4);
      if (tw != 3 || hb == 2) pause(hb < 2 ? HDELAY : HDELAY2);
      const f32x4 hv = hrow(false), sm = sums(hv);
      u32x2 hi, lw;
      split4(hv, hi, lw);
      const bool odd = (lane & 1) != 0;
      const unsigned n0 = __float_as_uint(dpp<DPP_XOR1>(__uint_as_float(odd ? hi[0] : lw[0]))), n1 = __float_as_uint(dpp<DPP_XOR1>(__uint_as_float(odd ? hi[1] : lw[1])));
      u32x4 v = odd ? u32x4{n0, n1, lw[0], lw[1]} : u32x4{hi[0], hi[1], n0, n1};
      if (lane >= 40) v = __builtin_bit_cast(u32x4, sm);
      if (lane < 48) st128(rs, WS_H + (hb * 4 + tw) * 192 + lane * 4, u32x4{max(v[0], 1u), max(v[1], 1u), max(v[2], 1u), max(v[3], 1u)});
      done_all();
      const unsigned pl = pull();
      asm volatile("" :: "v"(pl));
      return;
    }
    done_all();
    typedef __attribute__((address_space(1))) const u32x2 g_u32x2;
    g_u32x2* wp[15];
#pragma unroll
    for (int i = 0; i < 15; ++i) {
      wp[i] = (g_u32x2*)((mr < 8 ? wg : wu) + (size_t)(tw * 160 + qd * 8 + 1 + (i / 3) * 32 + (i % 3) * 3) * FF + g_cg * 32 + (mr & 7) * 4);
      asm volatile("" : "+v"(wp[i]));
    }
    done(NS);
    if (GW) until(clk(), GW);
    u32x2 e[40];
#pragma unroll
    for (int s = 0; s < 5; ++s)
#pragma unroll
      for (int k = 0; k < 8; ++k) e[s * 8 + k] = wp[s * 3 + k / 3][(k % 3 - 1) * (FF / 4)];
    u32x4 a[5], sm;
    const int hoff = (WS_H + (min(mr >> 2, NB - 2) * 4 + tw) * 192 + qd * 8 + (mr & 1) * 4) * 4;
#pragma unroll
    for (int s = 0; s < 5; ++s) a[s] = __builtin_amdgcn_raw_buffer_load_b128(rs, hoff + s * 128, 0, AUX_SC1);
    sm = ld128(rs, WS_H + (min(lane, NB - 2) * 4 + tw) * 192 + 160);
    MEMFENCE();
    u32x4 bx[20];
#pragma unroll
    for (int s = 0; s < 5; ++s) {
#pragma unroll
      for (int k = 0; k < 4; ++k) {
        bx[s * 4 + 0][k] = PERM_LO(e[s * 8 + 2 * k + 1][0], e[s * 8 + 2 * k][0]); bx[s * 4 + 1][k] = PERM_HI(e[s * 8 + 2 * k + 1][0], e[s * 8 + 2 * k][0]);
        bx[s * 4 + 2][k] = PERM_LO(e[s * 8 + 2 * k + 1][1], e[s * 8 + 2 * k][1]); bx[s * 4 + 3][k] = PERM_HI(e[s * 8 + 2 * k + 1][1], e[s * 8 + 2 * k][1]);
      }
      PIN4(bx, s * 4);
    }
    {
#pragma unroll 1
      for (unsigned spin = 0; spin < SPIN_CAP; ++spin) {
        unsigned mn = min4(sm);
#pragma unroll
        for (int s = 0; s < 5; ++s) mn = min(mn, min4(a[s]));
        if (__all(mn != 0u)) break;
        __builtin_amdgcn_s_sleep(2);
#pragma unroll
        for (int s = 0; s < 5; ++s) a[s] = __builtin_amdgcn_raw_buffer_load_b128(rs, hoff + s * 128, 0, AUX_SC1);
        sm = ld128(rs, WS_H + (min(lane, NB - 2) * 4 + tw) * 192 + 160);
      }
      if (lane < NB - 1) rst[lane * 4 + tw] = __builtin_bit_cast(f32x4, sm);
#pragma unroll
      for (int s = 0; s < 5; ++s) {
        if (mr & 2) a[s] = u32x4{0x3f803f80u, 0x3f803f80u, 0x3f803f80u, 0x3f803f80u};
        asm volatile("" : "+v"(a[s]));
      }
    }
    const f32x4 hv = hrow(true);
    if (lane < 40) {
      u32x2 hi, lw;
      split4(hv, hi, lw);
      *reinterpret_cast<u32x2*>(reg + lane * 8) = hi;
      *reinterpret_cast<u32x2*>(reg + AG_LD + lane * 8) = lw;
    }
    CFENCE();
    if ((mr & 14) == 12) {
#pragma unroll
      for (int s = 0; s < 5; ++s) a[s] = *reinterpret_cast<const u32x4*>(reg + (mr & 1) * AG_LD + s * 64 + qd * 16);
    }
    __builtin_amdgcn_sched_barrier(0);
    f32x4 c[4];
#pragma unroll
    for (int t = 0; t < 4; ++t) c[t] = f32x4{0.f, 0.f, 0.f, 0.f};
#pragma unroll
    for (int s = 0; s < 5; ++s)
#pragma unroll
      for (int t = 0; t < 4; ++t) c[t] = mfma16(a[s], bx[s * 4 + t], c[t]);
    { const f32x4 s3 = sums(hv); if (lane == 0) rst[(NB - 1) * 4 + tw] = s3; }
    *reinterpret_cast<f32x4*>(reg + 4096 + lane * 16) = f32x4{c[0][0] + c[0][1], c[1][0] + c[1][1], c[0][2], c[1][2]};
    *reinterpret_cast<f32x4*>(reg + 4096 + 1152 + lane * 16) = f32x4{c[2][0] + c[2][1], c[3][0] + c[3][1], c[2][2], c[3][2]};
    const int k = arrive(s_c + C_T);
    f32x4 ts[4], pg[4], pu[4];
#pragma unroll
    for (int w = 0; w < 4; ++w) {
      const unsigned char* src = smem + OFF_REG + w * REG + 4096 + (mr & 1) * 1152 + (qd * 16 + (mr >> 1)) * 16;
      ts[w] = rst[qd * 4 + w];
      pg[w] = *reinterpret_cast<const f32x4*>(src); pu[w] = *reinterpret_cast<const f32x4*>(src + 128);
    }
    PIN4(ts, 0); PIN4(pg, 0); PIN4(pu, 0);
    if (arrived(k) == 3) {
      const f32x4 t = (ts[0] + ts[1]) + (ts[2] + ts[3]), g4 = (pg[0] + pg[1]) + (pg[2] + pg[3]), u4 = (pu[0] + pu[1]) + (pu[2] + pu[3]);
      const float d = t[0] * (-1.0f / DM);
      const float rstd = __builtin_amdgcn_rsqf(fmaxf(t[1] + d * t[0], 0.f) * (1.0f / DM) + LN_EPS);
      const float g0 = rstd * (g4[0] + d * g4[2]), g1 = rstd * (g4[1] + d * g4[3]);
      const float a0 = g0 * __builtin_amdgcn_rcpf(1.0f + __expf(-g0)) * (rstd * (u4[0] + d * u4[2]));
      const float a1 = g1 * __builtin_amdgcn_rcpf(1.0f + __expf(-g1)) * (rstd * (u4[1] + d * u4[3]));
      const unsigned h0 = __float_as_uint(a0) & 0xffff0000u, h1 = __float_as_uint(a1) & 0xffff0000u;
      const unsigned hi = max(PERM_HI(h1, h0), 1u), lw = max(PERM_HI(__float_as_uint(a1 - __uint_as_float(h1)), __float_as_uint(a0 - __uint_as_float(h0))), 1u);
#define QB(v, c_) __float_as_uint(dpp<c_>(__uint_as_float(v)))
      const bool isl = (lane & 1) != 0;
      const u32x4 hq = {QB(hi, 0x00), QB(hi, 0x55), QB(hi, 0xAA), QB(hi, 0xFF)}, lq = {QB(lw, 0x00), QB(lw, 0x55), QB(lw, 0xAA), QB(lw, 0xFF)};
#undef QB
      const float rr = rstd * (t[2] + d * t[3]);
      const u32x4 sv = {nz_bits(cq - d), nz_bits(rstd), nz_bits(__builtin_amdgcn_rcpf(1.0f + __expf(-rr))), 0x3f800000u};
      const bool stat = g_cg == 0 && mr == 2;
      if ((lane & 2) == 0 || stat) st128(rs, stat ? WS_ST + qd * 4 : WS_ACT + g_cg * 128 + (qd * 2 + (lane & 1)) * 16 + (mr >> 2) * 4, stat ? sv : (isl ? lq : hq));
    }
  } else if (isD) {
    const int col = d_ct * 32 + (mr & 7) * 4 + (mr >> 3) * 2;
    const unsigned xd = *reinterpret_cast<const unsigned*>(x + row_of(qd) * DM + col);
    const __amdgpu_buffer_rsrc_t rd = __builtin_amdgcn_make_buffer_rsrc(const_cast<bf16*>(wd), 0, FF * DM * 2, 0x00020000);
    prep();
    lds_wait<8>(s_c + F_REL, 1u);
    done_all();
    const unsigned pl = pull();
    done(NS);
    asm volatile("" :: "v"(pl));
    const unsigned te = clk();
    if (DW) until(te, DW);
    u32x4 bd[24];
    {
      const int off = ((tw * 384 + (mr >> 3) * 32 + qd * 8) * DM + d_ct * 32 + (mr & 7) * 4) * 2;
      u32x2 e[48];
#pragma unroll
      for (int i = 0; i < 48; ++i) e[i] = __builtin_amdgcn_raw_buffer_load_b64(rd, off, ((i >> 3) * 64 + (i & 7)) * (DM * 2), 0);
      MEMFENCE();
#pragma unroll
      for (int s = 0; s < 6; ++s) {
#pragma unroll
        for (int k = 0; k < 4; ++k) {
          bd[s * 4 + 0][k] = PERM_LO(e[s * 8 + 2 * k + 1][0], e[s * 8 + 2 * k][0]); bd[s * 4 + 1][k] = PERM_HI(e[s * 8 + 2 * k + 1][0], e[s * 8 + 2 * k][0]);
          bd[s * 4 + 2][k] = PERM_LO(e[s * 8 + 2 * k + 1][1], e[s * 8 + 2 * k][1]); bd[s * 4 + 3][k] = PERM_HI(e[s * 8 + 2 * k + 1][1], e[s * 8 + 2 * k][1]);
        }
        PIN4(bd, s * 4);
      }
    }
    until(te, DLATE);
    f32x2 hv;
    int dl = 0;
    {
      const int off = (WS_SLAB + qd * DM + col) * 4;
      u32x2 sl[KS];
#pragma unroll 1
      for (unsigned spin = 0; spin < SPIN_CAP; ++spin) {
#pragma unroll
        for (int ks = 0; ks < KS; ++ks) sl[ks] = __builtin_amdgcn_raw_buffer_load_b64(rs, off, ks * (NB * DM * 4), AUX_SC1);
        unsigned mn = min(sl[0][0], sl[0][1]);
#pragma unroll
        for (int ks = 1; ks < KS; ++ks) mn = min(mn, min(sl[ks][0], sl[ks][1]));
        if (__all(mn != 0u)) break;
        dl = DLATE2;
        __builtin_amdgcn_s_sleep(4);
      }
#define SL(k) __builtin_bit_cast(f32x2, sl[k])
      hv = bf_pair(xd) + (((SL(0) + SL(1)) + (SL(2) + SL(3))) + ((SL(4) + SL(5)) + (SL(6) + SL(7))));
#undef SL
      asm volatile("" : "+v"(hv));
    }
    until(clk(), dl);
    u32x4 a[6], st;
    const int aoff = (WS_ACT + tw * 12 * 128 + (((mr >> 1) & 1) * 8 + (mr >> 2) * 2 + (mr & 1)) * 16 + qd * 4) * 4;
#pragma unroll 1
    for (unsigned spin = 0; spin < SPIN_CAP; ++spin) {
#pragma unroll
      for (int s = 0; s < 6; ++s) a[s] = __builtin_amdgcn_raw_buffer_load_b128(rs, aoff + s * 1024, 0, AUX_SC1);
      st = ld128(rs, WS_ST + qd * 4);
      unsigned mn = min4(st);
#pragma unroll
      for (int s = 0; s < 6; ++s) mn = min(mn, min4(a[s]));
      if (__all(mn != 0u)) break;
      __builtin_amdgcn_s_sleep(1);
    }
    f32x4 c[4];
#pragma unroll
    for (int t = 0; t < 4; ++t) c[t] = f32x4{0.f, 0.f, 0.f, 0.f};
#pragma unroll
    for (int s = 0; s < 6; ++s)
#pragma unroll
      for (int t = 0; t < 4; ++t) c[t] = mfma16(a[s], bd[s * 4 + t], c[t]);
    const bool up = mr >= 8;
    const float k0 = up ? c[2][2] + c[2][3] : c[0][0] + c[0][1], k1 = up ? c[3][2] + c[3][3] : c[1][0] + c[1][1];
    const float o0 = up ? c[0][2] + c[0][3] : c[2][0] + c[2][1], o1 = up ? c[1][2] + c[1][3] : c[3][0] + c[3][1];
    f32x2* r2 = reinterpret_cast<f32x2*>(red);
    r2[tw * 64 + lane] = f32x2{k0 + dpp<DPP_ROR8>(o0), k1 + dpp<DPP_ROR8>(o1)};
    const int k = arrive(s_c + C_T);
    f32x2 y = (r2[lane] + r2[64 + lane]) + (r2[128 + lane] + r2[192 + lane]);
    asm volatile("" : "+v"(y));
    if (arrived(k) == 3) {
      const f32x4 sf = __builtin_bit_cast(f32x4, st);
      const __hip_bfloat162 o2 = __halves2bfloat162(__float2bfloat16(hv[0] + ((hv[0] - sf[0]) * sf[1] + y[0] * sf[2])),
                                                    __float2bfloat16(hv[1] + ((hv[1] - sf[0]) * sf[1] + y[1] * sf[2])));
      __hip_atomic_store(reinterpret_cast<unsigned*>(out + row_of(qd) * DM + col), *reinterpret_cast<const unsigned*>(&o2), __ATOMIC_RELAXED, __HIP_MEMORY_SCOPE_AGENT);
    }
  } else {
    prep();
    if (isC && tw == 1) {
      lds_wait<8>(s_c + F_REL, 1u);
      const int cs = c_it != 1 ? c_it * 2 : max(g >> 3, 1), cnt = c_it != 1 ? 8 : (g < 16 ? 16 : 32), x0 = c_it != 1 ? g * 8 : (g < 16 ? g * 16 : (g & 7) * 32);
      done(c_it < 2 ? 4 : NA);
      pause(WDELAY);
      const float wk = weight(cs, x0, cnt);
      f32x4 o = {0.f, 0.f, 0.f, 0.f};
#pragma unroll 1
      for (int k0 = 0; k0 < cnt; k0 += 16) {
        u32x2 pv[16];
#pragma unroll
        for (int k = 0; k < 16; ++k) pv[k] = __builtin_amdgcn_raw_buffer_load_b64(rs, lp * 2, (WS_PQ + ((x0 + ((k0 + k) & (cnt - 1))) * 5 + cs) * (DH / 2)) * 4, AUX_EARLY);
#pragma unroll
        for (int k = 0; k < 16; ++k) {
          const float w = k0 + k < cnt ? __uint_as_float(__builtin_amdgcn_readlane(__float_as_uint(wk), k0 + k)) : 0.f;
          o += w * f32x4{(float)(short)pv[k][0], (float)((int)pv[k][0] >> 16), (float)(short)pv[k][1], (float)((int)pv[k][1] >> 16)};
        }
      }
      u32x2 hi, lw;
      split4(o, hi, lw);
      if (lane < DH / 4) st128(rs, WS_OC + (c_it * NG + g) * DH + lane * 4, u32x4{max(hi[0], 1u), max(hi[1], 1u), max(lw[0], 1u), max(lw[1], 1u)});
    }
    done_all();
    const unsigned pl = pull();
    asm volatile("" :: "v"(pl));
  }
  if (tw == 3) done_all();
}

extern "C" void kernel_launch(void* const* d_in, const int* in_sizes, int n_in, void* d_out, int out_size, void* d_ws,
                              size_t ws_size, hipStream_t stream) {
  const bf16* x = (const bf16*)d_in[0];
  const bf16* kc = (const bf16*)d_in[1];
  const bf16* vc = (const bf16*)d_in[2];
  const bf16* wq = (const bf16*)d_in[3];
  const bf16* wo = (const bf16*)d_in[4];
  const bf16* wr = (const bf16*)d_in[6];
  const bf16* wg = (const bf16*)d_in[7];
  const bf16* wu = (const bf16*)d_in[8];
  const bf16* wd = (const bf16*)d_in[9];
  fused_kernel<<<NWG, NW * 64, 0, stream>>>(x, kc, vc, wq, wo, wr, wg, wu, wd, (float*)d_ws, (bf16*)d_out);
}
```

```cpp
#include <hip/hip_runtime.h>
#include <hip/hip_bf16.h>
#include <stdint.h>

typedef __hip_bfloat16 bf16;
using u32x2 = __attribute__((ext_vector_type(2))) unsigned;
using u32x4 = __attribute__((ext_vector_type(4))) unsigned;
using f32x4 = __attribute__((ext_vector_type(4))) float;
using f32x2 = __attribute__((ext_vector_type(2))) float;
using bf16x8 = __attribute__((ext_vector_type(8))) __bf16;

constexpr int NB = 4, DM = 640, NH = 32, SK = 8192, DH = 192, HD = NH * DH, FF = 1536;
constexpr float LN_EPS = 1e-5f;
constexpr float QSCALE = 0.07216878364870322f * 1.4426950408889634f;

constexpr int NWG = 256;
constexpr int GS = 8, NG = NWG / GS, NI = GS / 2;
constexpr int HPG = NI / NB;
constexpr int PPI = 512 / GS;
constexpr int NPAIR = NI * PPI;

constexpr int TAUK = 1320, TAUV = 1338;
constexpr int TSTART = 200;
constexpr int WGAP = 165, WP0 = 64, WPN = 176;
constexpr int VGAP = 90;
constexpr int SPRINT = 4;
constexpr int JUMP = 145;
constexpr int HST = 2800, RT0 = 3900, RTS = 1000;
constexpr unsigned NRD = 5, RMIN = 50, FBP = 60, FBL = 150;
constexpr int W0START = 1700, W0SPAN = 1000, W1START = 2700, W1SPAN = 2600;
constexpr int ROT = 16, ROWS = 0xC9;
constexpr int FBM = 100, FBX = 15, FB0 = 170, FB1 = 55, FBS = 10;
constexpr int FBD = 20, HOLD = 400, GB = 24, GBS = 2;
constexpr unsigned PRD = 3; constexpr int PRB = 24;
constexpr int TLEAD = 40, PFD = 6, WFD = 3;
constexpr int QWAIT = 750, QBACK = 100;
constexpr int WDELAY = 100, WDELAY2 = 460, GLATE = 150, DLATE = 220, DLATE2 = 60;
constexpr int GLEAD = 1, GCNT = 4, DP = 0, DPW = 60, PW = 0, HDELAY = 1150, HDELAY2 = 780, GW = 30, DW = 50;

constexpr int KS = 8;
constexpr int WS_PML = 0;
constexpr int WS_PO = WS_PML + NWG * 64;
constexpr int WS_PQ = WS_PO + NWG * DH;
constexpr int WS_OC = WS_PQ + NWG * 5 * (DH / 2);
constexpr int WS_SLAB = WS_OC + (NB - 1) * NH * DH;
constexpr int WS_ACT = WS_SLAB + KS * NB * DM;
constexpr int WS_H = WS_ACT + NB * FF;
constexpr int WS_ST = WS_H + (NB - 1) * 4 * 192;
constexpr int WS_HN = WS_ST + 32;
constexpr int WS_HR = WS_HN + NWG;
constexpr int WS_HA = WS_HR + 2 * NWG;
constexpr int QFS = 2 * GS * 32;
constexpr int WS_QF = WS_HA + 32 * NWG;
constexpr int WS_QP = WS_QF + NG * QFS;
constexpr int WS_END = WS_QP + NH * 2 * NB * DH;
constexpr int NPROD = HPG * 6;
static_assert(WS_PO % 32 == 0 && WS_SLAB % 32 == 0 && WS_ACT % 32 == 0 && WS_ST % 32 == 0 && WS_HN % 32 == 0 && WS_QF % 32 == 0 && WS_QP % 32 == 0, "line alignment");


__device__ __forceinline__ int row_of(int b) { return (ROWS >> (2 * b)) & 3; }
__device__ __forceinline__ float bf_lo(unsigned u) { return __uint_as_float(u << 16); }
__device__ __forceinline__ float bf_hi(unsigned u) { return __uint_as_float(u & 0xffff0000u); }
__device__ __forceinline__ f32x2 bf_pair(unsigned u) { return f32x2{bf_lo(u), bf_hi(u)}; }
__device__ __forceinline__ f32x2 fma2(float a, f32x2 b, f32x2 c) { return __builtin_elementwise_fma(f32x2{a, a}, b, c); }
__device__ __forceinline__ f32x2 fma2(f32x2 a, f32x2 b, f32x2 c) { return __builtin_elementwise_fma(a, b, c); }

template <int CTRL> __device__ __forceinline__ float dpp(float x) {
  return __builtin_bit_cast(float, __builtin_amdgcn_mov_dpp(__builtin_bit_cast(int, x), CTRL, 0xf, 0xf, true));
}
constexpr int DPP_XOR1 = 0xB1, DPP_XOR2 = 0x4E, DPP_HMIRROR = 0x141, DPP_MIRROR = 0x140, DPP_ROR8 = 0x128;

__device__ __forceinline__ float sum_x16_32(float x) {
  auto s = __builtin_amdgcn_permlane16_swap(__float_as_uint(x), __float_as_uint(x), false, false);
  x = __uint_as_float(s[0]) + __uint_as_float(s[1]);
  auto t = __builtin_amdgcn_permlane32_swap(__float_as_uint(x), __float_as_uint(x), false, false);
  return __uint_as_float(t[0]) + __uint_as_float(t[1]);
}
__device__ __forceinline__ float max_x16_32(float x) {
  auto s = __builtin_amdgcn_permlane16_swap(__float_as_uint(x), __float_as_uint(x), false, false);
  x = fmaxf(__uint_as_float(s[0]), __uint_as_float(s[1]));
  auto t = __builtin_amdgcn_permlane32_swap(__float_as_uint(x), __float_as_uint(x), false, false);
  return fmaxf(__uint_as_float(t[0]), __uint_as_float(t[1]));
}
__device__ __forceinline__ float tsum32(float a, float b) {
  auto t = __builtin_amdgcn_permlane32_swap(__float_as_uint(a), __float_as_uint(b), false, false);
  return __uint_as_float(t[0]) + __uint_as_float(t[1]);
}
__device__ __forceinline__ float tsum16(float a, float b) {
  auto t = __builtin_amdgcn_permlane16_swap(__float_as_uint(a), __float_as_uint(b), false, false);
  return __uint_as_float(t[0]) + __uint_as_float(t[1]);
}
__device__ __forceinline__ float sum_r(float v) { v += dpp<DPP_ROR8>(v); return sum_x16_32(v); }
__device__ __forceinline__ float row_sum(float v) {
  v += dpp<DPP_XOR1>(v); v += dpp<DPP_XOR2>(v); v += dpp<DPP_HMIRROR>(v);
  return v + dpp<DPP_MIRROR>(v);
}
__device__ __forceinline__ float row_max(float v) {
  v = fmaxf(v, dpp<DPP_XOR1>(v)); v = fmaxf(v, dpp<DPP_XOR2>(v)); v = fmaxf(v, dpp<DPP_HMIRROR>(v));
  return fmaxf(v, dpp<DPP_MIRROR>(v));
}
__device__ __forceinline__ float wave_sum(float v) {
  v += dpp<DPP_XOR1>(v); v += dpp<DPP_XOR2>(v); v += dpp<DPP_HMIRROR>(v);
  return sum_r(v);
}

__device__ __forceinline__ u32x4 ld_nt(const char* p) { return __builtin_nontemporal_load(reinterpret_cast<const u32x4*>(p)); }

struct Tile { u32x4 d[3]; };

#define PIN4(a, o) asm volatile("" : "+v"((a)[(o) + 0]), "+v"((a)[(o) + 1]), "+v"((a)[(o) + 2]), "+v"((a)[(o) + 3]))
#define MEMFENCE() do { asm volatile("" ::: "memory"); __builtin_amdgcn_sched_barrier(0); } while (0)

__device__ __forceinline__ void glds16_nt(const void* gsrc, unsigned lds_dst) {
  unsigned keep;
  asm volatile("s_mov_b32 %0, m0\n\ts_mov_b32 m0, %2\n\ts_nop 0\n\tglobal_load_lds_dwordx4 %1, off nt\n\ts_mov_b32 m0, %0"
               : "=&s"(keep) : "v"(gsrc), "s"(lds_dst) : "memory");
}

__device__ __forceinline__ float tile_score(const Tile& k, const f32x2 (&q)[12]) {
  f32x2 s0 = {0.f, 0.f}, s1 = s0;
#pragma unroll
  for (int i = 0; i < 3; ++i) {
    s0 = fma2(bf_pair(k.d[i][0]), q[i * 4], s0);     s1 = fma2(bf_pair(k.d[i][1]), q[i * 4 + 1], s1);
    s0 = fma2(bf_pair(k.d[i][2]), q[i * 4 + 2], s0); s1 = fma2(bf_pair(k.d[i][3]), q[i * 4 + 3], s1);
  }
  s0 += s1;
  return s0[0] + s0[1];
}
__device__ __forceinline__ void score_put(float s, float* dst, int j) {
  s += dpp<DPP_XOR1>(s);
  s += dpp<DPP_XOR2>(s);
  s += dpp<DPP_HMIRROR>(s);
  if (j == 0) *dst = s;
}
__device__ __forceinline__ void tile_load(Tile& t, const char* base, unsigned off, int half) {
#pragma unroll
  for (int i = 0; i < 3; ++i) t.d[i] = ld_nt(base + off + half * 3072 + i * 128);
}
__device__ __forceinline__ float score_next(Tile& k, const f32x2 (&q)[12], const char* base, unsigned off, int half) {
  f32x2 s0 = {0.f, 0.f}, s1 = s0;
#pragma unroll
  for (int i = 0; i < 3; ++i) {
    s0 = fma2(bf_pair(k.d[i][0]), q[i * 4], s0);     s1 = fma2(bf_pair(k.d[i][1]), q[i * 4 + 1], s1);
    s0 = fma2(bf_pair(k.d[i][2]), q[i * 4 + 2], s0); s1 = fma2(bf_pair(k.d[i][3]), q[i * 4 + 3], s1);
    asm volatile("" : "+v"(s0), "+v"(s1));
    MEMFENCE();
    k.d[i] = ld_nt(base + off + half * 3072 + i * 128);
    MEMFENCE();
  }
  s0 += s1;
  return s0[0] + s0[1];
}
__device__ __forceinline__ void pv_next(float p, Tile& v, f32x2 (&acc)[12], const char* base, unsigned off, int half) {
#pragma unroll
  for (int i = 0; i < 3; ++i) {
    asm volatile("" : "+v"(v.d[i]), "+v"(p));
#pragma unroll
    for (int d = 0; d < 4; ++d) acc[i * 4 + d] = fma2(p, bf_pair(v.d[i][d]), acc[i * 4 + d]);
    PIN4(acc, i * 4);
    MEMFENCE();
    v.d[i] = ld_nt(base + off + half * 3072 + i * 128);
    MEMFENCE();
  }
}
__device__ __forceinline__ void pv_last(float p, Tile& v, f32x2 (&acc)[12]) {
#pragma unroll
  for (int i = 0; i < 3; ++i) {
    asm volatile("" : "+v"(v.d[i]), "+v"(p));
#pragma unroll
    for (int d = 0; d < 4; ++d) acc[i * 4 + d] = fma2(p, bf_pair(v.d[i][d]), acc[i * 4 + d]);
    PIN4(acc, i * 4);
  }
}
__device__ __forceinline__ void q_load(f32x2 (&q)[12], const float* src, int j) {
#pragma unroll
  for (int i = 0; i < 6; ++i) {
    const f32x4 a = *reinterpret_cast<const f32x4*>(src + (i >> 1) * 64 + j * 8 + (i & 1) * 4);
    q[i * 2] = f32x2{a[0], a[1]}; q[i * 2 + 1] = f32x2{a[2], a[3]};
  }
}
#define VMCNT(n) asm volatile("s_waitcnt vmcnt(%0)" :: "n"(n) : "memory")

constexpr int AUX_SC1 = 16, AUX_EARLY = 18;
constexpr unsigned SPIN_CAP = 1u << 17;
__device__ __forceinline__ unsigned nz_bits(float v) { return max(__float_as_uint(v), 1u); }
__device__ __forceinline__ bool all_nz(u32x4 v) { return v[0] != 0u && v[1] != 0u && v[2] != 0u && v[3] != 0u; }
__device__ __forceinline__ unsigned min4(u32x4 v) { return min(min(v[0], v[1]), min(v[2], v[3])); }
__device__ __forceinline__ unsigned ld32(__amdgpu_buffer_rsrc_t rs, int word) { return __builtin_amdgcn_raw_buffer_load_b32(rs, word * 4, 0, AUX_SC1); }
__device__ __forceinline__ u32x2 ld64(__amdgpu_buffer_rsrc_t rs, int word) { return __builtin_amdgcn_raw_buffer_load_b64(rs, word * 4, 0, AUX_SC1); }
__device__ __forceinline__ u32x4 ld128(__amdgpu_buffer_rsrc_t rs, int word) { return __builtin_amdgcn_raw_buffer_load_b128(rs, word * 4, 0, AUX_SC1); }
__device__ __forceinline__ void st32(__amdgpu_buffer_rsrc_t rs, int word, unsigned v) { __builtin_amdgcn_raw_buffer_store_b32(v, rs, word * 4, 0, AUX_SC1); }
__device__ __forceinline__ void st64(__amdgpu_buffer_rsrc_t rs, int word, u32x2 v) { __builtin_amdgcn_raw_buffer_store_b64(v, rs, word * 4, 0, AUX_SC1); }
__device__ __forceinline__ void st128(__amdgpu_buffer_rsrc_t rs, int word, u32x4 v) { __builtin_amdgcn_raw_buffer_store_b128(v, rs, word * 4, 0, AUX_SC1); }

__device__ __forceinline__ u32x4 ld128nt(__amdgpu_buffer_rsrc_t rs, int word) { return __builtin_amdgcn_raw_buffer_load_b128(rs, word * 4, 0, AUX_EARLY); }
__device__ __forceinline__ void st128nt(__amdgpu_buffer_rsrc_t rs, int word, u32x4 v) { __builtin_amdgcn_raw_buffer_store_b128(v, rs, word * 4, 0, AUX_EARLY); }

__device__ __forceinline__ bool leader_release(__amdgpu_buffer_rsrc_t rs, int lane, unsigned t0, unsigned rd) {
  u32x4 a[4];
#pragma unroll
  for (int k = 0; k < 4; ++k) a[k] = ld128(rs, WS_HA + (lane * 4 + k) * 32);
  const u32x4 n = ld128(rs, WS_HN + lane * 4);
  if (rd == 0u && !__all(a[0][0] == n[0] && a[1][0] == n[1] && a[2][0] == n[2] && a[3][0] == n[3])) return false;
  const float e = fmaxf(fmaxf((float)(int)(t0 - a[0][2]), (float)(int)(t0 - a[1][2])), fmaxf((float)(int)(t0 - a[2][2]), (float)(int)(t0 - a[3][2])));
  unsigned first = t0 - (unsigned)(int)max_x16_32(row_max(e));
  float ls = 0.f, lx = 0.f;
#pragma unroll
  for (int k = 0; k < 4; ++k) {
    const float l = a[k][0] == n[k] + rd ? (float)min(a[k][3] + (rd ? 0u : min(a[k][2] - first, 300u)), 2000u) : 0.f;
    ls += l; lx = fmaxf(lx, l);
  }
  ls = wave_sum(ls) * (1.0f / NWG); lx = max_x16_32(row_max(lx));
  const int extra = min(max((int)(ls * (FBM / 100.0f) + lx * (FBX / 100.0f)) - (rd ? FB1 + (int)(NRD - rd) * FBD : FB0), 0), 600);
  first = (first & 0xffffu) | ((unsigned)extra << 16);
  if (rd != 0u && ls * NWG < HOLD) return true;
  st128(rs, WS_HR + lane * 8, u32x4{a[0][1] + rd, first, a[1][1] + rd, first});
  st128(rs, WS_HR + lane * 8 + 4, u32x4{a[2][1] + rd, first, a[3][1] + rd, first});
  return true;
}

typedef __attribute__((address_space(3))) unsigned lds_u32;
__device__ __forceinline__ unsigned lds_add(lds_u32* p, int lane) {
  unsigned k = 0;
  if (lane == 0) k = __hip_atomic_fetch_add(p, 1u, __ATOMIC_RELAXED, __HIP_MEMORY_SCOPE_WORKGROUP);
  return __builtin_amdgcn_readfirstlane(k);
}
#define CFENCE() asm volatile("" ::: "memory")
__device__ __forceinline__ int arrive(lds_u32* p) {
  int v;
  unsigned long long keep;
  asm volatile("s_mov_b64 %1, exec\n\ts_mov_b64 exec, 1\n\tds_add_rtn_u32 %0, %2, %3\n\ts_mov_b64 exec, %1"
               : "=&v"(v), "=&s"(keep) : "v"((unsigned)(size_t)p), "v"(1u) : "memory");
  return v;
}
__device__ __forceinline__ int arrived(int v) {
  asm volatile("s_waitcnt lgkmcnt(0)" : "+v"(v) :: "memory");
  return __builtin_amdgcn_readfirstlane(v);
}
__device__ __forceinline__ unsigned lds_get(lds_u32* p) {
  return __builtin_amdgcn_readfirstlane(__hip_atomic_load(p, __ATOMIC_RELAXED, __HIP_MEMORY_SCOPE_WORKGROUP));
}
template <int SLEEP> __device__ __forceinline__ void lds_wait(lds_u32* p, unsigned n) {
#pragma unroll 1
  for (unsigned spin = 0; spin < (SPIN_CAP << 4); ++spin) {
    if (lds_get(p) >= n) break;
    __builtin_amdgcn_s_sleep(SLEEP);
  }
  asm volatile("" ::: "memory");
}
__device__ __forceinline__ void pause(unsigned ticks) {
  const unsigned long long t0 = __builtin_amdgcn_s_memrealtime();
#pragma unroll 1
  for (unsigned spin = 0; spin < 4096u && __builtin_amdgcn_s_memrealtime() - t0 < ticks; ++spin) __builtin_amdgcn_s_sleep(16);
}
__device__ __forceinline__ unsigned clk() { return (unsigned)__builtin_amdgcn_s_memrealtime(); }
struct Table {
  unsigned t0; int was, moved, slowed;
  __device__ __forceinline__ void gate(int due, lds_u32* mine, lds_u32* slow) {
    const int left = (int)lds_get(slow) - slowed, d = max(min(left, FBS), -GBS);
    t0 += d; was -= d; slowed += d;
    int late = (int)(clk() - t0) - due;
    if (late - was > JUMP) { t0 += late - was; moved += late - was; late = was; *mine = moved; }
    mine[4] = max(late - max(left - d, 0), 0);
    was = late;
#pragma unroll 1
    for (int spin = 0; spin < 65536 && late < 0; ++spin) { __builtin_amdgcn_s_sleep(1); late = (int)(clk() - t0) - due; }
  }
  __device__ __forceinline__ void agree(lds_u32* all, lds_u32* mine) {
    if (moved == 0) return;
    const int m = (int)min(min(lds_get(all), lds_get(all + 1)), min(lds_get(all + 2), lds_get(all + 3))) + 30;
    if (m < moved) { t0 -= moved - m; moved = m; was = 1 << 20; *mine = moved; }
  }
};
__device__ __forceinline__ void until(unsigned t0, int due) {
#pragma unroll 1
  for (int spin = 0; spin < 65536 && (int)(clk() - t0) < due; ++spin) __builtin_amdgcn_s_sleep(1);
}
#define LGKM0() asm volatile("s_waitcnt lgkmcnt(0)" ::: "memory")

__device__ __forceinline__ void split4(f32x4 v, u32x2& hi, u32x2& lo) {
  unsigned h[4], r[4];
#pragma unroll
  for (int i = 0; i < 4; ++i) { h[i] = __float_as_uint(v[i]) & 0xffff0000u; r[i] = __float_as_uint(v[i] - __uint_as_float(h[i])); }
  hi = u32x2{__builtin_amdgcn_perm(h[1], h[0], 0x07060302u), __builtin_amdgcn_perm(h[3], h[2], 0x07060302u)};
  lo = u32x2{__builtin_amdgcn_perm(r[1], r[0], 0x07060302u), __builtin_amdgcn_perm(r[3], r[2], 0x07060302u)};
}
__device__ __forceinline__ f32x4 mfma16(u32x4 a, u32x4 b, f32x4 c) {
  return __builtin_amdgcn_mfma_f32_16x16x32_bf16(__builtin_bit_cast(bf16x8, a), __builtin_bit_cast(bf16x8, b), c, 0, 0, 0);
}

constexpr int NW = 8, NMW = 4;
constexpr int NLP = 5, NRP = 7;
constexpr int NTP = 4 * (NLP + NRP);
constexpr int REG = NLP * 6 * 1024;
constexpr int NRING = 4;
constexpr int NS = 6, NA = NS - 1;
__device__ __forceinline__ int seg_beg(int s) { return s >= NS ? NPAIR : ((0xC87640 >> (4 * s)) & 15) * 16; }
__device__ __forceinline__ int seg_k(int t) { return (int)(0x5433222211110000ull >> (4 * (t >> 4))) & 15; }
__device__ __forceinline__ int seg_of_k(int k) { return (0x321540 >> (4 * k)) & 15; }
__device__ __forceinline__ int k_of_seg(int s) { return (0x215430 >> (4 * s)) & 15; }
__device__ __forceinline__ int pair_of(int t) { return t < 64 ? t : (t < 192 ? t + 64 : t - 128); }
constexpr int OFF_CNT = 0, OFF_REG = 256,
              OFF_SC = OFF_REG + 4 * REG, OFF_BUF = OFF_SC + NPAIR * 16 * 4, OFF_Q = OFF_BUF + NRING * NMW * DH * 4, OFF_ML = OFF_Q + NS * DH * 4,
              OFF_RED = OFF_ML + 128, OFF_RS = OFF_RED + 4 * 256 * 4, OFF_END = OFF_RS + 5 * 16;
enum { F_REL = 0, F_DONE, C_LAST, C_T = 3, F_OUT, C_P, C_K, F_P, F_Q = 8, F_FREE, S_FIRST, F_Q2, C_W = 12, C_SC = 16, C_PART = 24, S_ECHO = 32, S_MOVED = 48, S_SLOW = 56 };
__device__ __forceinline__ int due_k(int p) { return TSTART + ((p * TAUK + min(max(p - WP0, 0), WPN) * (WGAP * 64 / WPN)) >> 6); }
__device__ __forceinline__ int due_v(int p) { return TSTART + WGAP + ((NPAIR * TAUK + p * TAUV + max(p - NPAIR / 2, 0) * (VGAP * 64 / (NPAIR / 2))) >> 6); }
constexpr int AW_LD = DH * 2 + 16, AG_LD = 160 * 2 + 16;

__global__ void __launch_bounds__(NW * 64) fused_kernel(const bf16* __restrict__ x, const bf16* __restrict__ kc,
                                                        const bf16* __restrict__ vc, const bf16* __restrict__ wq,
                                                        const bf16* __restrict__ wo, const bf16* __restrict__ wr,
                                                        const bf16* __restrict__ wg, const bf16* __restrict__ wu,
                                                        const bf16* __restrict__ wd, float* ws, bf16* __restrict__ out) {
  __shared__ __attribute__((aligned(16))) unsigned char smem[OFF_END];
  float* s_sc = reinterpret_cast<float*>(smem + OFF_SC);
  float* s_buf = reinterpret_cast<float*>(smem + OFF_BUF);
  float* s_q = reinterpret_cast<float*>(smem + OFF_Q);
  u32x4* s_ml = reinterpret_cast<u32x4*>(smem + OFF_ML);
  lds_u32* s_c = (lds_u32*)(smem + OFF_CNT);

  const int tid = threadIdx.x, lane = tid & 63, r = lane >> 3, j = lane & 7;
  const int wave = __builtin_amdgcn_readfirstlane(tid >> 6);
  const int tw = wave - NMW;
  const int bid = blockIdx.x, xl = bid & 7, idx = bid >> 3;
  const int g = (xl * 32 + idx) / GS, mm = idx % GS;
  const __amdgpu_buffer_rsrc_t rs = __builtin_amdgcn_make_buffer_rsrc(ws, 0, WS_END * 4, 0x00020000);
  const int ro = ((idx & 3) << 3) | (idx >> 2);
  const bool isW = ro < 10, isG = ro >= 10 && ro < 16;
  const int w_ks = xl, w_ct = ro;
  const int g_cg = xl * 6 + (ro - 10);
  const int d_ct = (xl + ((ro - 16) >> 2) * 8) * 2 + (((ro - 16) >> 1) & 1);
  const bool isD = ro >= 16 && ro < 24 && (ro & 1) == 0 && d_ct < DM / 32;
  const bool isH = ro == 22 && xl >= 2 && xl < 5;
  const bool isC = (mm & 7) == 3 || (mm & 7) >= 6;
  const int c_it = (mm >> 3) * 3 + max((mm & 7) - 5, 0);
  const int qf = WS_QF + g * QFS;
  const char* kb = reinterpret_cast<const char*>(kc);
  const char* vb = reinterpret_cast<const char*>(vc);
  const unsigned lo = (r * 24 + j) * 16;
  const int xb = g * GS + mm, c0 = mm * 8 + g * ROT;
  const int b2 = (2 * NH + (xb >> 4)) * 1024 + (xb & 15) * 8, b3 = (2 * NH + 16 + (xb >> 5)) * 1024 + (xb & 31) * 8;
  auto poff = [&](int p) -> unsigned {
    const int turn = (p & 63) >> 2;
    int t = (row_of(p >> 6) * NH + g) * 1024 + ((turn * 64 + (p & 3) * 2 + c0) & 1023);
    if ((p >> 6) == 1) t = (turn < 8 ? b2 + turn * 128 : b3 + (turn & 4) * 2048 + (turn & 3) * 256) + (p & 3) * 2;
    return (unsigned)t * 3072u;
  };
  const unsigned lds_base = (unsigned)(size_t)(__attribute__((address_space(3))) unsigned char*)smem + OFF_REG + tw * REG;
  auto merged = [&](int it, int ln) -> u32x4 {
    const float* src = s_buf + (it % NRING) * (NMW * DH) + ((ln >> 1) & 7) * 24 + (ln >> 4) * 8 + (ln & 1) * 4;
    const f32x4 o = (*reinterpret_cast<const f32x4*>(src) + *reinterpret_cast<const f32x4*>(src + DH)) +
                    (*reinterpret_cast<const f32x4*>(src + 2 * DH) + *reinterpret_cast<const f32x4*>(src + 3 * DH));
    return u32x4{nz_bits(o[0]), nz_bits(o[1]), nz_bits(o[2]), nz_bits(o[3])};
  };
  auto publish = [&](int it) -> bool {
    const int k = arrive(s_c + C_PART + it);
    u32x4 o = merged(it, lane < DH / 4 ? lane : 0);
    asm volatile("" : "+v"(o));
    if (arrived(k) != NMW - 1) return false;
    if (lane < DH / 4) st128(rs, WS_PO + xb * DH + lane * 4, o);
    if (lane == 0) __hip_atomic_fetch_add(s_c + F_DONE, 1u, __ATOMIC_RELAXED, __HIP_MEMORY_SCOPE_WORKGROUP);
    return true;
  };
#define NONCE() do { unsigned n_ = __builtin_amdgcn_readfirstlane(f) + 1u; while (__any(f == n_)) ++n_; \
    if (lane < 32) st32(rs, qf + (GS + mm) * 32 + lane, n_); f = n_; } while (0)
  unsigned r0 = 0u;
  unsigned t0 = clk();
  if (wave < NMW) {
    Tile ta, tb;
    const int c = lane & (GS - 1);
    unsigned f = 0u;
    if (wave == 3) f = ld32(rs, qf + c * 32 + mm);
    if (mm < NPROD) {
      const int hp = g + (mm / 6) * NG, ck = (mm % 6) >> 1, hf = mm & 1, jp = hf * 4 + wave;
      const int bb = lane >> 4, t = lane & 15;
      const u32x4* xp = reinterpret_cast<const u32x4*>(x + row_of(bb) * DM);
      u32x4 xv[5], xs = {0u, 0u, 0u, 0u};
#pragma unroll
      for (int k = 0; k < 5; ++k) xv[k] = xp[t + 16 * k];
      if (t < 10) xs = xp[jp * 10 + t];
      const u32x4* wp = reinterpret_cast<const u32x4*>(wq + (size_t)(jp * 80 + r) * HD + hp * DH + ck * 64) + j;
      u32x4 wv[10];
#pragma unroll
      for (int k = 0; k < 10; ++k) wv[k] = __builtin_nontemporal_load(wp + (size_t)k * 8 * (HD / 8));
      if (wave == 0) s_c[lane] = lane == C_K ? NTP + NMW : 0u;
      MEMFENCE(); LGKM0(); __builtin_amdgcn_s_barrier(); MEMFENCE();
      if (wave == 3) { NONCE(); }
      float sm = 0.f;
#pragma unroll
      for (int k = 0; k < 5; ++k)
#pragma unroll
        for (int d = 0; d < 4; ++d) sm += bf_lo(xv[k][d]) + bf_hi(xv[k][d]);
      const float mean = row_sum(sm) * (1.0f / DM);
      float sv = 0.f;
#pragma unroll
      for (int k = 0; k < 5; ++k)
#pragma unroll
        for (int d = 0; d < 4; ++d) { const float a = bf_lo(xv[k][d]) - mean, c2 = bf_hi(xv[k][d]) - mean; sv += a * a + c2 * c2; }
      const float rstd = 1.0f / sqrtf(row_sum(sv) * (1.0f / DM) + LN_EPS);
      float* s_xn = s_buf + wave * 320;
      if (t < 10) {
#pragma unroll
        for (int d = 0; d < 4; ++d) {
          s_xn[(t * 8 + 2 * d) * 4 + bb] = (bf_lo(xs[d]) - mean) * rstd;
          s_xn[(t * 8 + 2 * d + 1) * 4 + bb] = (bf_hi(xs[d]) - mean) * rstd;
        }
      }
      LGKM0();
      f32x2 qa[16];
#pragma unroll
      for (int i = 0; i < 16; ++i) qa[i] = f32x2{0.f, 0.f};
#pragma unroll
      for (int k = 0; k < 10; ++k) {
        const f32x4 xn = *reinterpret_cast<const f32x4*>(&s_xn[(k * 8 + r) * 4]);
#pragma unroll
        for (int d = 0; d < 4; ++d) {
          const f32x2 w2 = bf_pair(wv[k][d]);
#pragma unroll
          for (int e = 0; e < 4; ++e) qa[e * 4 + d] = fma2(xn[e], w2, qa[e * 4 + d]);
        }
      }
      float t16[16], t8[8];
#pragma unroll
      for (int i = 0; i < 16; ++i) t16[i] = tsum32(qa[i >> 1][i & 1], qa[8 + (i >> 1)][i & 1]);
#pragma unroll
      for (int i = 0; i < 8; ++i) t8[i] = tsum16(t16[i], t16[i + 8]);
#pragma unroll
      for (int i = 0; i < 8; ++i) t8[i] += dpp<DPP_ROR8>(t8[i]);
      const bool up = (lane & 8) != 0;
      f32x4* r4 = reinterpret_cast<f32x4*>(smem + OFF_RED);
      r4[wave * 64 + lane] = f32x4{up ? t8[4] : t8[0], up ? t8[5] : t8[1], up ? t8[6] : t8[2], up ? t8[7] : t8[3]};
      LGKM0();
      if (lds_add(s_c + C_P, lane) == NMW - 1) {
        asm volatile("" ::: "memory");
        const f32x4 o4 = (r4[lane] + r4[64 + lane]) + (r4[128 + lane] + r4[192 + lane]);
        st128nt(rs, WS_QP + ((hp * 2 + hf) * NB + bb) * DH + ck * 64 + j * 8 + (up ? 4 : 0), __builtin_bit_cast(u32x4, o4));
        VMCNT(0);
        if (lane == 0) s_c[F_OUT] = 1u;
      }
    } else {
      if (wave == 0) s_c[lane] = lane == C_K ? NTP + NMW : 0u;
      MEMFENCE(); LGKM0(); __builtin_amdgcn_s_barrier(); MEMFENCE();
      if (wave == 3) { NONCE(); }
    }
    MEMFENCE();
    if (wave == 3) {
      __builtin_amdgcn_s_setprio(3);
      const unsigned nonce = f;
      unsigned echoed = 0u;
      if (mm < NPROD) {
        echoed = ld32(rs, qf + (GS + c) * 32);
        asm volatile("" : "+v"(echoed) :: "memory");
        lds_wait<1>(s_c + F_OUT, 1u);
        if (lane < 32) st32(rs, qf + mm * 32 + lane, echoed);
#pragma unroll 1
        while ((int)(clk() - t0) < QWAIT - QBACK) {
          const unsigned nv = ld32(rs, qf + (GS + c) * 32);
          if (__any(nv != echoed)) {
            if (lane < 32) st32(rs, qf + mm * 32 + lane, nv);
            echoed = nv;
          }
          __builtin_amdgcn_s_sleep(4);
        }
      }
      until(t0, QWAIT);
#pragma unroll 1
      for (unsigned spin = 0; spin < SPIN_CAP; ++spin) {
        const unsigned nv = ld32(rs, qf + (GS + c) * 32);
        f = ld32(rs, qf + c * 32 + mm);
        const bool done = __all(c == mm || c >= NPROD || f == nonce);
        if (mm < NPROD && __any(nv != echoed)) {
          if (lane < 32) st32(rs, qf + mm * 32 + lane, nv);
          echoed = nv;
        }
        if (done) break;
        __builtin_amdgcn_s_sleep(8);
      }
      if (lane < GS) s_c[S_ECHO + lane] = echoed;
      if (lane < DH / 4) {
        u32x4 p[3][2];
#pragma unroll
        for (int it = 0; it < 3; ++it)
#pragma unroll
          for (int k = 0; k < 2; ++k) p[it][k] = ld128nt(rs, WS_QP + ((g * 2 + k) * NB + (it ? it + 1 : 0)) * DH + lane * 4);
#pragma unroll
        for (int it = 0; it < 3; ++it) {
          *reinterpret_cast<f32x4*>(&s_q[(it ? it + 3 : 0) * DH + lane * 4]) = (__builtin_bit_cast(f32x4, p[it][0]) + __builtin_bit_cast(f32x4, p[it][1])) * QSCALE;
          if (it != 1) { LGKM0(); if (lane == 0) s_c[F_Q] = it + 1; MEMFENCE(); }
        }
      }
    }
    __builtin_amdgcn_s_setprio(3);
    until(t0, due_k(NTP + wave));
    Table tt = {t0, 1 << 20, 0, 0};
    bool common = false;
    tile_load(ta, kb, poff(NTP + wave) + lo, 0);
    tile_load(tb, kb, poff(NTP + wave) + lo, 1);
    MEMFENCE();
    {
      f32x2 q[12];
      int cur = NTP + wave, nxt = lds_add(s_c + C_K, lane), passed = 0, ci = -1;
      for (;;) {
        const bool last = nxt >= NPAIR;
        const unsigned off = poff(last ? wave : pair_of(nxt)) + lo;
        const char* base = last ? vb : kb;
        const int it = seg_k(cur), cp = pair_of(cur);
        if (it != ci) {
          LGKM0();
          for (; passed < it; ++passed) lds_add(s_c + C_SC + passed, lane);
          lds_wait<1>(s_c + (it < 3 ? F_Q : F_Q2), min(it + 1, 3));
          q_load(q, s_q + seg_of_k(it) * DH, j);
          if (!common) { const unsigned d = lds_get(s_c + S_FIRST); if (d) { tt.t0 -= d - 1u; common = true; tt.was = 1 << 20; } }
          tt.agree(s_c + S_MOVED, s_c + S_MOVED + wave);
          ci = it;
        }
        unsigned tk = 0u;
        if (lane == 0) tk = __hip_atomic_fetch_add(s_c + C_K, 1u, __ATOMIC_RELAXED, __HIP_MEMORY_SCOPE_WORKGROUP);
        MEMFENCE();
        tt.gate(due_k(last ? NPAIR + wave : nxt), s_c + S_MOVED + wave, s_c + S_SLOW);
        MEMFENCE();
        const float sa = score_next(ta, q, base, off, 0);
        score_put(sa, s_sc + cp * 16 + r, j);
        MEMFENCE();
        const float sb = score_next(tb, q, base, off, 1);
        score_put(sb, s_sc + cp * 16 + 8 + r, j);
        MEMFENCE();
        if (last) break;
        cur = nxt; nxt = __builtin_amdgcn_readfirstlane(tk);
      }
      LGKM0();
      for (; passed < NS; ++passed) lds_add(s_c + C_SC + passed, lane);
    }
#pragma unroll 1
    for (int it = 0; it < NS; ++it) {
      lds_wait<1>(s_c + F_P, k_of_seg(it) + 1);
      if (!common) { const unsigned d = lds_get(s_c + S_FIRST); if (d) { tt.t0 -= d - 1u; common = true; tt.was = 1 << 20; } }
      tt.agree(s_c + S_MOVED, s_c + S_MOVED + wave);
      f32x2 acc[12];
#pragma unroll
      for (int i = 0; i < 12; ++i) acc[i] = f32x2{0.f, 0.f};
#pragma unroll 1
      for (int p = seg_beg(it) + wave; p < min(seg_beg(it + 1), NPAIR - NMW); p += NMW) {
        const unsigned off = poff(p + NMW) + lo;
        const float pa = s_sc[p * 16 + r], pb = s_sc[p * 16 + 8 + r];
        MEMFENCE();
        if (p + NMW < NPAIR - SPRINT * NMW) tt.gate(due_v(p + NMW), s_c + S_MOVED + wave, s_c + S_SLOW);
        MEMFENCE();
        pv_next(pa, ta, acc, vb, off, 0);
        pv_next(pb, tb, acc, vb, off, 1);
        if (((p + NMW) >> 2) == (NPAIR >> 2) - GLEAD) { if (lane == 0) __hip_atomic_fetch_add(s_c + C_LAST, 1u, __ATOMIC_RELAXED, __HIP_MEMORY_SCOPE_WORKGROUP); }
      }
      if (it == NS - 1) {
        const int p = NPAIR - NMW + wave;
        const float pa = s_sc[p * 16 + r], pb = s_sc[p * 16 + 8 + r];
        pv_last(pa, ta, acc);
        pv_last(pb, tb, acc);
      }
      float t12[12], t6[6];
#pragma unroll
      for (int i = 0; i < 12; ++i) t12[i] = tsum32(acc[i >> 1][i & 1], acc[6 + (i >> 1)][i & 1]);
#pragma unroll
      for (int i = 0; i < 6; ++i) t6[i] = tsum16(t12[i], t12[i + 6]);
#pragma unroll
      for (int i = 0; i < 6; ++i) t6[i] += dpp<DPP_ROR8>(t6[i]);
      if (it >= NRING) lds_wait<1>(s_c + F_FREE, it - NRING + 1);
      if ((lane & 8) == 0) {
        f32x2* dst = reinterpret_cast<f32x2*>(&s_buf[(it % NRING) * (NMW * DH) + wave * DH + j * 24 + (lane >> 4) * 6]);
        dst[0] = f32x2{t6[0], t6[1]}; dst[1] = f32x2{t6[2], t6[3]}; dst[2] = f32x2{t6[4], t6[5]};
      }
      CFENCE();
      if (it < NA) { if (lane == 0) __hip_atomic_fetch_add(s_c + C_PART + it, 1u, __ATOMIC_RELAXED, __HIP_MEMORY_SCOPE_WORKGROUP); }
    }
    publish(NS - 1);
    return;
  }

  {
    Tile pk[NRP * 2];
    __builtin_amdgcn_s_setprio(3);
#pragma unroll
    for (int e = 0; e < NLP * 2; ++e) {
      const char* src = kb + (poff((e >> 1) * 4 + tw) + lo + (e & 1) * 3072);
      if (e >= 2) until(t0, due_k(e * 2 + tw) - TLEAD);
#pragma unroll
      for (int i = 0; i < 3; ++i) glds16_nt(src + i * 128, lds_base + (e * 3 + i) * 1024);
      if (e == 1) __builtin_amdgcn_s_barrier();
      if (e * 3 + 3 > PFD) VMCNT(PFD);
    }
#pragma unroll
    for (int e = 0; e < NRP * 2; ++e) {
      until(t0, due_k((NLP * 2 + e) * 2 + tw) - TLEAD);
      tile_load(pk[e], kb, poff((NLP + (e >> 1)) * 4 + tw) + lo, e & 1);
      MEMFENCE();
      if (e + 1 < NRP * 2) VMCNT(PFD);
    }
    if (tw == 0) {
      r0 = ld32(rs, WS_HR + bid * 2);
      if (bid == 0) {
        u32x4 n;
#pragma unroll
        for (int k = 0; k < 4; ++k) n[k] = ld32(rs, WS_HA + (lane * 4 + k) * 32) + 1u;
        st128(rs, WS_HN + lane * 4, n);
      }
      const u32x4 z = {0u, 0u, 0u, 0u};
      if (lane < 48) {
#pragma unroll
        for (int it = NA; it < NS; ++it) st128(rs, WS_PO + xb * DH + lane * 4, z);
      }
      else st128(rs, WS_PML + (g * GS + mm) * 64 + (lane - 48) * 4, z);
      if (isG && g_cg == 0 && lane < 8) st128(rs, WS_ST + lane * 4, z);
      if (isW) st128(rs, WS_SLAB + (w_ks * NB + (lane >> 4)) * DM + w_ct * 64 + (lane & 15) * 4, z);
      else if (isG) { if (lane < 32) st128(rs, WS_ACT + g_cg * 128 + lane * 4, z); }
      else if (isH) {
#pragma unroll
        for (int k = 0; k < 3; ++k) st128(rs, WS_H + (xl - 2) * 768 + k * 256 + lane * 4, z);
      }
      else if (isC && lane < 48) st128(rs, WS_OC + (c_it * NG + g) * DH + lane * 4, z);
    }
    __builtin_amdgcn_s_setprio(0);
    lds_wait<1>(s_c + F_Q, HPG);
    f32x2 q[12];
    q_load(q, s_q, j);
    const u32x4* s_kv = reinterpret_cast<const u32x4*>(smem + OFF_REG + tw * REG) + lane;
    static_assert(NLP * 4 <= PPI && NTP <= HPG * PPI, "tail tiles: batch row 0 only");
#pragma unroll 1
    for (int e = 0; e < NLP * 2; ++e) {
      Tile tmp;
#pragma unroll
      for (int i = 0; i < 3; ++i) tmp.d[i] = s_kv[(e * 3 + i) * 64];
      score_put(tile_score(tmp, q), s_sc + ((e >> 1) * 4 + tw) * 16 + (e & 1) * 8 + r, j);
    }
#pragma unroll
    for (int e = 0; e < NRP * 2; ++e) {
      if ((NLP * 2 + e) * 2 == PPI) q_load(q, s_q + DH, j);
      score_put(tile_score(pk[e], q), s_sc + ((NLP + (e >> 1)) * 4 + tw) * 16 + (e & 1) * 8 + r, j);
      __builtin_amdgcn_sched_barrier(0);
    }
    asm volatile("s_waitcnt vmcnt(0) lgkmcnt(0)" ::: "memory");
#pragma unroll
    for (int it = 0; it * PPI < NTP; ++it) lds_add(s_c + C_SC + it, lane);
  }

  const int mr = lane & 15, qd = lane >> 4;
  unsigned char* reg = smem + OFF_REG + tw * REG;
  float* red = reinterpret_cast<float*>(smem + OFF_RED);
#define PERM_LO(hi_, lo_) __builtin_amdgcn_perm(hi_, lo_, 0x05040100u)
#define PERM_HI(hi_, lo_) __builtin_amdgcn_perm(hi_, lo_, 0x07060302u)
  int served = 0;
  auto done = [&](int n) {
    if (tw == 3 && served < min(n, NA)) {
      const int from = served;
      int ln = lane;
      asm volatile("" : "+v"(ln));
#pragma unroll 1
      for (; served < min(n, NA); ++served) {
        lds_wait<8>(s_c + C_PART + served, NMW);
        const f32x4 o = __builtin_bit_cast(f32x4, merged(served, ln < DH / 4 ? ln : 0));
        const float am = max_x16_32(row_max(fmaxf(fmaxf(fabsf(o[0]), fabsf(o[1])), fmaxf(fabsf(o[2]), fabsf(o[3])))));
        const float inv = am > 1e-30f ? 32767.0f / am : 0.f;
        const int q0 = __float2int_rn(o[0] * inv), q1 = __float2int_rn(o[1] * inv), q2 = __float2int_rn(o[2] * inv), q3 = __float2int_rn(o[3] * inv);
        if (ln < DH / 4) reinterpret_cast<u32x2*>(s_sc + served * (DH / 2))[ln] = u32x2{PERM_LO((unsigned)q1, (unsigned)q0), PERM_LO((unsigned)q3, (unsigned)q2)};
        const bool nan = __any((o[0] + o[1]) + (o[2] + o[3]) != (o[0] + o[1]) + (o[2] + o[3]));
        if (ln == 0) s_ml[served][2] = nan ? 0x7fc00000u : nz_bits(am * (1.0f / 32767.0f));
        LGKM0();
        if (ln == 0) s_c[F_FREE] = served + 1;
      }
#pragma unroll 1
      for (int c = from * 24 + ln; c < served * 24; c += 64) st128nt(rs, WS_PQ + xb * 5 * (DH / 2) + c * 4, reinterpret_cast<const u32x4*>(s_sc)[c]);
      VMCNT(0);
      if (ln < 8) st128(rs, WS_PML + xb * 64 + (from ? 32 : 0) + ln * 4, s_ml[ln]);
      if (ln == 0) __hip_atomic_fetch_add(s_c + F_DONE, (unsigned)(served - from), __ATOMIC_RELAXED, __HIP_MEMORY_SCOPE_WORKGROUP);
    }
    if (n > NA) lds_wait<1>(s_c + F_DONE, n); else lds_wait<8>(s_c + F_DONE, n);
  };
  auto done_all = [&]() {
#pragma unroll 1
    for (int n = 4; n <= NA; ++n) done(n);
  };
  const int lp = (lane < DH / 4 ? lane : 0) * 4;
  auto weight = [&](int it, int x0, int cnt) -> float {
    const int mlw = WS_PML + (x0 + (lane & (cnt - 1))) * 64 + (it < 4 ? 0 : 32) + it * 4;
    u32x4 ml;
#pragma unroll 1
    for (unsigned spin = 0; spin < SPIN_CAP; ++spin) {
      ml = ld128(rs, mlw);
      if (__all(min4(ml) != 0u)) break;
      __builtin_amdgcn_s_sleep(16);
    }
    const float mk = __uint_as_float(ml[0]);
    const float e = __builtin_amdgcn_exp2f(mk - max_x16_32(row_max(mk)));
    return e / (wave_sum(e * __uint_as_float(ml[1])) * ((float)cnt * (1.0f / 64.0f))) * __uint_as_float(ml[2]);
  };
  auto prep = [&]() {
    if (tw == 0) {
      const unsigned tok = __builtin_amdgcn_readfirstlane(r0) + 1u;
      const int c = lane & (GS - 1);
      unsigned echoed = 0u, qe = s_c[S_ECHO + c];
      bool first = true, released = false, sent = false;
#pragma unroll 1
      while ((int)(clk() - t0) < HST) {
        const unsigned qn = ld32(rs, qf + (GS + c) * 32);
        if (mm < NPROD && __any(qn != qe)) {
          if (lane < 32) st32(rs, qf + mm * 32 + lane, qn);
          qe = qn;
        }
        __builtin_amdgcn_s_sleep(64);
      }
      int slow = 0;
      auto lag = [&]() { return (lds_get(s_c + S_MOVED + 4) + lds_get(s_c + S_MOVED + 5) + lds_get(s_c + S_MOVED + 6) + lds_get(s_c + S_MOVED + 7)) >> 2; };
      const unsigned behind = lag();
#pragma unroll 1
      for (unsigned spin = 0; spin < SPIN_CAP && !released; ++spin) {
        const unsigned qn = ld32(rs, qf + (GS + c) * 32);
        const u32x2 rv = ld64(rs, WS_HR + bid * 2);
        const unsigned nv = __builtin_amdgcn_readfirstlane(ld32(rs, WS_HN + bid));
        if (__builtin_amdgcn_readfirstlane(rv[0]) - tok <= NRD) {
          released = true;
          slow = __builtin_amdgcn_readfirstlane(rv[1]) >> 16;
          if (lane == 0) { s_c[S_FIRST] = min((t0 - rv[1]) & 0xffffu, 300u) + 1u; s_c[S_SLOW] = slow; }
        } else {
          if (mm < NPROD && __any(qn != qe)) {
            if (lane < 32) st32(rs, qf + mm * 32 + lane, qn);
            qe = qn;
          }
          if (first || nv != echoed) {
            echoed = nv; first = false;
            if (lane < 8) st128(rs, WS_HA + bid * 32 + lane * 4, u32x4{echoed, tok, t0, behind});
          }
          if (bid == 0 && !sent) sent = leader_release(rs, lane, t0, 0u);
          __builtin_amdgcn_s_sleep(32);
        }
      }
      if (lane == 0) s_c[F_REL] = 1u;
      if (lane < DH / 4) {
        u32x4 p[3][2];
#pragma unroll
        for (int e = 0; e < 3; ++e)
#pragma unroll
          for (int k = 0; k < 2; ++k) p[e][k] = ld128nt(rs, WS_QP + (((e ? 8 + e * 8 + (xb >> 5) : xb >> 4) * 2 + k) * NB + 1) * DH + lane * 4);
#pragma unroll
        for (int e = 0; e < 3; ++e) *reinterpret_cast<f32x4*>(&s_q[(1 + e) * DH + lane * 4]) = (__builtin_bit_cast(f32x4, p[e][0]) + __builtin_bit_cast(f32x4, p[e][1])) * QSCALE;
      }
      LGKM0();
      if (lane == 0) s_c[F_Q2] = 3u;
      unsigned prev = slow;
#pragma unroll 1
      for (unsigned rd = 1; rd <= NRD; ++rd) {
        const int tr = RT0 + (int)(rd - 1) * RTS;
        until(t0, tr);
        const unsigned bh = lag();
        if (bh > RMIN && lane < 8) st128(rs, WS_HA + bid * 32 + lane * 4, u32x4{echoed + rd, tok, t0, bh});
        if (bid == 0) { until(t0, tr + 220); leader_release(rs, lane, t0, rd); }
        until(t0, tr + 500);
        const u32x2 rv = ld64(rs, WS_HR + bid * 2);
        if (__builtin_amdgcn_readfirstlane(rv[0]) != tok + rd) {
          prev = 0u;
          const int fl = -(int)min(min(lds_get(s_c + S_MOVED), lds_get(s_c + S_MOVED + 1)), min(lds_get(s_c + S_MOVED + 2), lds_get(s_c + S_MOVED + 3)));
          slow = slow > fl ? max(slow - GB, fl) : slow - (rd <= PRD ? PRB : 0);
          if (lane == 0) s_c[S_SLOW] = slow;
        } else {
          const unsigned ex = __builtin_amdgcn_readfirstlane(rv[1]) >> 16;
          slow += ex + min(ex, prev) * (rd == NRD ? FBL : FBP) / 100u;
          prev = ex;
          if (lane == 0) s_c[S_SLOW] = slow;
        }
      }
    }
    if (tw == 3) {
#pragma unroll 1
      for (int ik = 0; ik < NS; ++ik) {
        const int it = seg_of_k(ik), nv = (seg_beg(it + 1) - seg_beg(it)) >> 4;
        lds_wait<4>(s_c + C_SC + ik, ik == 0 ? NW : NMW);
        f32x4* sp = reinterpret_cast<f32x4*>(s_sc + seg_beg(it) * 16) + lane;
        f32x4 v[4];
#pragma unroll
        for (int k = 0; k < 4; ++k) v[k] = sp[(k & (nv - 1)) * 64];
        const f32x4 mx = __builtin_elementwise_max(__builtin_elementwise_max(v[0], v[1]), __builtin_elementwise_max(v[2], v[3]));
        const float m = max_x16_32(row_max(fmaxf(fmaxf(mx[0], mx[1]), fmaxf(mx[2], mx[3]))));
        float l = 0.f;
#pragma unroll
        for (int k = 0; k < 4; ++k) {
#pragma unroll
          for (int e = 0; e < 4; ++e) v[k][e] = __builtin_amdgcn_exp2f(v[k][e] - m);
          if (k < nv) {
            l += (v[k][0] + v[k][1]) + (v[k][2] + v[k][3]);
            sp[k * 64] = v[k];
          }
        }
        l = wave_sum(l);
        if (lane == 0) s_ml[it] = u32x4{nz_bits(m), nz_bits(l), 0x3f800000u, 0x3f800000u};
        LGKM0();
        if (lane == 0) s_c[F_P] = ik + 1;
      }
    }
  };
  auto pull = [&]() {
    typedef __attribute__((address_space(1))) const unsigned g_u32;
    const int id = (ro - 16) * 4 + tw;
    g_u32* p1[2];
#pragma unroll
    for (int i = 0; i < 2; ++i) {
      const int l = id * 60 + min(i * 32 + (lane >> 1), 59), m = l >= 1920 ? l - 1920 : l;
      p1[i] = (g_u32*)((l >= 1920 ? wu : wg) + (size_t)(m / 3) * FF + xl * 192 + (m % 3) * 64 + (lane & 1) * 32);
    }
    g_u32* p2 = (g_u32*)(wd + (size_t)(id * 24 + min(lane >> 1, 23)) * DM + xl * 64 + (lane & 1) * 32);
    asm volatile("" : "+v"(p1[0]), "+v"(p1[1]), "+v"(p2));
    if (GLEAD) lds_wait<1>(s_c + C_LAST, GCNT); else done(NS);
    if (PW) until(clk(), PW);
    const unsigned v0 = p1[0][0], v1 = p1[1][0];
    MEMFENCE();
    if (DP == 1) done(NS);
    if (DPW) until(clk(), DPW);
    const unsigned v2 = p2[0], v3 = p2[xl < 2 ? 256 : 0];
    MEMFENCE();
    return (v0 ^ v1) + (v2 ^ v3);
  };
  if (isW) {
    u32x4 bx[24];
    const char* p = reinterpret_cast<const char*>(wo + (size_t)((w_ks * 4 + tw) * DH + r) * DM + w_ct * 64 + j * 8);
#pragma unroll 1
    for (int i = 0; i < 24; ++i) { until(t0, tw == 0 ? W0START + i * (W0SPAN / 24) : W1START + i * (W1SPAN / 24)); glds16_nt(p, lds_base + i * 1024); p += 8 * DM * 2; VMCNT(WFD); }
    asm volatile("s_waitcnt vmcnt(0)" ::: "memory");
#pragma unroll
    for (int s = 0; s < 6; ++s) {
      u32x2 e[8];
#pragma unroll
      for (int k = 0; k < 8; ++k) e[k] = *reinterpret_cast<const u32x2*>(reg + (s * 32 + qd * 8 + k) * 128 + mr * 8);
#pragma unroll
      for (int k = 0; k < 4; ++k) {
        bx[s * 4 + 0][k] = PERM_LO(e[2 * k + 1][0], e[2 * k][0]); bx[s * 4 + 1][k] = PERM_HI(e[2 * k + 1][0], e[2 * k][0]);
        bx[s * 4 + 2][k] = PERM_LO(e[2 * k + 1][1], e[2 * k][1]); bx[s * 4 + 3][k] = PERM_HI(e[2 * k + 1][1], e[2 * k][1]);
      }
    }
#pragma unroll
    for (int s = 0; s < 24; s += 4) asm volatile("" : "+v"(bx[s]), "+v"(bx[s + 1]), "+v"(bx[s + 2]), "+v"(bx[s + 3]));
    LGKM0();
    prep();
    lds_wait<8>(s_c + F_REL, 1u);
    const int hh = w_ks * 4 + tw;
#pragma unroll 1
    for (int pass = 0; pass < 3; ++pass) {
      if (pass < 2) {
        done(pass ? NA : 4);
        pause(pass ? WDELAY2 : WDELAY2 + 300);
        const int b0 = pass * 2, b1 = pass + 1;
        u32x4 v0, v1;
#pragma unroll 1
        for (unsigned spin = 0; spin < SPIN_CAP; ++spin) {
          v0 = ld128(rs, WS_OC + (b0 * NH + hh) * DH + lp);
          v1 = ld128(rs, WS_OC + (b1 * NH + hh) * DH + lp);
          if (__all(min(min4(v0), min4(v1)) != 0u)) break;
          __builtin_amdgcn_s_sleep(16);
        }
        if (lane < DH / 4) {
          *reinterpret_cast<u32x2*>(reg + (b0 * 4) * AW_LD + lane * 8) = u32x2{v0[0], v0[1]};
          *reinterpret_cast<u32x2*>(reg + (b0 * 4 + 1) * AW_LD + lane * 8) = u32x2{v0[2], v0[3]};
          *reinterpret_cast<u32x2*>(reg + (b1 * 4) * AW_LD + lane * 8) = u32x2{v1[0], v1[1]};
          *reinterpret_cast<u32x2*>(reg + (b1 * 4 + 1) * AW_LD + lane * 8) = u32x2{v1[2], v1[3]};
        }
      } else {
        const float wk = weight(NS - 1, hh * GS, GS);
        float w[GS];
#pragma unroll
        for (int k = 0; k < GS; ++k) { w[k] = __uint_as_float(__builtin_amdgcn_readlane(__float_as_uint(wk), k)); asm volatile("" : "+s"(w[k])); }
        done(NS);
        __builtin_amdgcn_s_setprio(3);
        const int po = WS_PO + hh * GS * DH;
        u32x4 pv[GS];
#pragma unroll 1
        for (unsigned spin = 0; spin < SPIN_CAP; ++spin) {
#pragma unroll
          for (int k = 0; k < GS; ++k) pv[k] = __builtin_amdgcn_raw_buffer_load_b128(rs, lp * 4, (po + k * DH) * 4, AUX_SC1);
          unsigned mn = min4(pv[0]);
#pragma unroll
          for (int k = 1; k < GS; ++k) mn = min(mn, min4(pv[k]));
          if (__all(mn != 0u)) break;
          __builtin_amdgcn_s_sleep(1);
        }
        f32x4 o = w[0] * __builtin_bit_cast(f32x4, pv[0]), o2 = w[1] * __builtin_bit_cast(f32x4, pv[1]);
#pragma unroll
        for (int k = 2; k < GS; k += 2) { o += w[k] * __builtin_bit_cast(f32x4, pv[k]); o2 += w[k + 1] * __builtin_bit_cast(f32x4, pv[k + 1]); }
        u32x2 hi, lw;
        split4(o + o2, hi, lw);
        if (lane < DH / 4) {
          *reinterpret_cast<u32x2*>(reg + ((NB - 1) * 4) * AW_LD + lane * 8) = hi;
          *reinterpret_cast<u32x2*>(reg + ((NB - 1) * 4 + 1) * AW_LD + lane * 8) = lw;
        }
      }
      CFENCE();
      u32x4 a[6];
#pragma unroll
      for (int s = 0; s < 6; ++s) a[s] = *reinterpret_cast<const u32x4*>(reg + mr * AW_LD + s * 64 + qd * 16);
      __builtin_amdgcn_sched_barrier(0);
      f32x4 c[4];
#pragma unroll
      for (int t = 0; t < 4; ++t) c[t] = f32x4{0.f, 0.f, 0.f, 0.f};
#pragma unroll
      for (int s = 0; s < 6; ++s)
#pragma unroll
        for (int t = 0; t < 4; ++t) c[t] = mfma16(a[s], bx[s * 4 + t], c[t]);
      const bool mine = max(qd - 1, 0) == pass;
      f32x4* r4 = reinterpret_cast<f32x4*>(red);
      if (mine) r4[tw * 64 + lane] = f32x4{c[0][0] + c[0][1], c[1][0] + c[1][1], c[2][0] + c[2][1], c[3][0] + c[3][1]};
      const int k = arrive(s_c + C_W + pass);
      f32x4 sum = (r4[lane] + r4[64 + lane]) + (r4[128 + lane] + r4[192 + lane]);
      asm volatile("" : "+v"(sum));
      if (arrived(k) == 3) {
        if (mine) st128(rs, WS_SLAB + (w_ks * NB + qd) * DM + w_ct * 64 + mr * 4, u32x4{nz_bits(sum[0]), nz_bits(sum[1]), nz_bits(sum[2]), nz_bits(sum[3])});
      }
    }
  } else if (isG || isH) {
    f32x4* rst = reinterpret_cast<f32x4*>(red);
    const int hb = isH ? xl - 2 : NB - 1;
    uint2 wr4 = make_uint2(0u, 0u), xg = wr4;
    if (lane < 40) {
      wr4 = *reinterpret_cast<const uint2*>(wr + tw * 160 + lane * 4);
      xg = *reinterpret_cast<const uint2*>(x + row_of(hb) * DM + tw * 160 + lane * 4);
    }
    const float cq = bf_lo(*reinterpret_cast<const unsigned*>(x + row_of(qd) * DM));
    const float wsum = wave_sum((bf_lo(wr4.x) + bf_hi(wr4.x)) + (bf_lo(wr4.y) + bf_hi(wr4.y)));
    f32x4 xc = f32x4{bf_lo(xg.x), bf_hi(xg.x), bf_lo(xg.y), bf_hi(xg.y)} - __uint_as_float(__builtin_amdgcn_readlane(__float_as_uint(cq), hb * 16));
    asm volatile("" : "+v"(xc));
    prep();
    lds_wait<8>(s_c + F_REL, 1u);
    auto hrow = [&](bool fast) -> f32x4 {
      f32x4 hv = {0.f, 0.f, 0.f, 0.f};
      if (lane < 40) {
        const int off = (WS_SLAB + hb * DM + tw * 160 + lane * 4) * 4;
        u32x4 sl[KS];
#pragma unroll 1
        for (unsigned spin = 0; spin < SPIN_CAP; ++spin) {
#pragma unroll
          for (int ks = 0; ks < KS; ++ks) sl[ks] = __builtin_amdgcn_raw_buffer_load_b128(rs, off, ks * (NB * DM * 4), AUX_SC1);
          unsigned mn = min4(sl[0]);
#pragma unroll
          for (int ks = 1; ks < KS; ++ks) mn = min(mn, min4(sl[ks]));
          if (!__any(mn == 0u)) break;
          if (fast) __builtin_amdgcn_s_sleep(1); else __builtin_amdgcn_s_sleep(16);
        }
#define SL(k) __builtin_bit_cast(f32x4, sl[k])
        hv = xc + (((SL(0) + SL(1)) + (SL(2) + SL(3))) + ((SL(4) + SL(5)) + (SL(6) + SL(7))));
#undef SL
      }
      return hv;
    };
    auto sums = [&](f32x4 hv) -> f32x4 {
      const float s1 = wave_sum((hv[0] + hv[1]) + (hv[2] + hv[3]));
      const float s2 = wave_sum((hv[0] * hv[0] + hv[1] * hv[1]) + (hv[2] * hv[2] + hv[3] * hv[3]));
      const float dot = wave_sum((hv[0] * bf_lo(wr4.x) + hv[1] * bf_hi(wr4.x)) + (hv[2] * bf_lo(wr4.y) + hv[3] * bf_hi(wr4.y)));
      return f32x4{s1, s2, dot, wsum};
    };
    if (isH) {
      if (tw == 3 || hb == 2) done_all(); else done(4);
      if (tw != 3 || hb == 2) pause(hb < 2 ? HDELAY : HDELAY2);
      const f32x4 hv = hrow(false), sm = sums(hv);
      u32x2 hi, lw;
      split4(hv, hi, lw);
      const bool odd = (lane & 1) != 0;
      const unsigned n0 = __float_as_uint(dpp<DPP_XOR1>(__uint_as_float(odd ? hi[0] : lw[0]))), n1 = __float_as_uint(dpp<DPP_XOR1>(__uint_as_float(odd ? hi[1] : lw[1])));
      u32x4 v = odd ? u32x4{n0, n1, lw[0], lw[1]} : u32x4{hi[0], hi[1], n0, n1};
      if (lane >= 40) v = __builtin_bit_cast(u32x4, sm);
      if (lane < 48) st128(rs, WS_H + (hb * 4 + tw) * 192 + lane * 4, u32x4{max(v[0], 1u), max(v[1], 1u), max(v[2], 1u), max(v[3], 1u)});
      done_all();
      const unsigned pl = pull();
      asm volatile("" :: "v"(pl));
      return;
    }
    done_all();
    typedef __attribute__((address_space(1))) const u32x2 g_u32x2;
    g_u32x2* wp[15];
#pragma unroll
    for (int i = 0; i < 15; ++i) {
      wp[i] = (g_u32x2*)((mr < 8 ? wg : wu) + (size_t)(tw * 160 + qd * 8 + 1 + (i / 3) * 32 + (i % 3) * 3) * FF + g_cg * 32 + (mr & 7) * 4);
      asm volatile("" : "+v"(wp[i]));
    }
    done(NS);
    if (GW) until(clk(), GW);
    u32x2 e[40];
#pragma unroll
    for (int s = 0; s < 5; ++s)
#pragma unroll
      for (int k = 0; k < 8; ++k) e[s * 8 + k] = wp[s * 3 + k / 3][(k % 3 - 1) * (FF / 4)];
    u32x4 a[5], sm;
    const int hoff = (WS_H + (min(mr >> 2, NB - 2) * 4 + tw) * 192 + qd * 8 + (mr & 1) * 4) * 4;
#pragma unroll
    for (int s = 0; s < 5; ++s) a[s] = __builtin_amdgcn_raw_buffer_load_b128(rs, hoff + s * 128, 0, AUX_SC1);
    sm = ld128(rs, WS_H + (min(lane, NB - 2) * 4 + tw) * 192 + 160);
    MEMFENCE();
    u32x4 bx[20];
#pragma unroll
    for (int s = 0; s < 5; ++s) {
#pragma unroll
      for (int k = 0; k < 4; ++k) {
        bx[s * 4 + 0][k] = PERM_LO(e[s * 8 + 2 * k + 1][0], e[s * 8 + 2 * k][0]); bx[s * 4 + 1][k] = PERM_HI(e[s * 8 + 2 * k + 1][0], e[s * 8 + 2 * k][0]);
        bx[s * 4 + 2][k] = PERM_LO(e[s * 8 + 2 * k + 1][1], e[s * 8 + 2 * k][1]); bx[s * 4 + 3][k] = PERM_HI(e[s * 8 + 2 * k + 1][1], e[s * 8 + 2 * k][1]);
      }
      PIN4(bx, s * 4);
    }
    {
#pragma unroll 1
      for (unsigned spin = 0; spin < SPIN_CAP; ++spin) {
        unsigned mn = min4(sm);
#pragma unroll
        for (int s = 0; s < 5; ++s) mn = min(mn, min4(a[s]));
        if (__all(mn != 0u)) break;
        __builtin_amdgcn_s_sleep(2);
#pragma unroll
        for (int s = 0; s < 5; ++s) a[s] = __builtin_amdgcn_raw_buffer_load_b128(rs, hoff + s * 128, 0, AUX_SC1);
        sm = ld128(rs, WS_H + (min(lane, NB - 2) * 4 + tw) * 192 + 160);
      }
      if (lane < NB - 1) rst[lane * 4 + tw] = __builtin_bit_cast(f32x4, sm);
#pragma unroll
      for (int s = 0; s < 5; ++s) {
        if (mr & 2) a[s] = u32x4{0x3f803f80u, 0x3f803f80u, 0x3f803f80u, 0x3f803f80u};
        asm volatile("" : "+v"(a[s]));
      }
    }
    const f32x4 hv = hrow(true);
    if (lane < 40) {
      u32x2 hi, lw;
      split4(hv, hi, lw);
      *reinterpret_cast<u32x2*>(reg + lane * 8) = hi;
      *reinterpret_cast<u32x2*>(reg + AG_LD + lane * 8) = lw;
    }
    CFENCE();
    if ((mr & 14) == 12) {
#pragma unroll
      for (int s = 0; s < 5; ++s) a[s] = *reinterpret_cast<const u32x4*>(reg + (mr & 1) * AG_LD + s * 64 + qd * 16);
    }
    __builtin_amdgcn_sched_barrier(0);
    f32x4 c[4];
#pragma unroll
    for (int t = 0; t < 4; ++t) c[t] = f32x4{0.f, 0.f, 0.f, 0.f};
#pragma unroll
    for (int s = 0; s < 5; ++s)
#pragma unroll
      for (int t = 0; t < 4; ++t) c[t] = mfma16(a[s], bx[s * 4 + t], c[t]);
    { const f32x4 s3 = sums(hv); if (lane == 0) rst[(NB - 1) * 4 + tw] = s3; }
    *reinterpret_cast<f32x4*>(reg + 4096 + lane * 16) = f32x4{c[0][0] + c[0][1], c[1][0] + c[1][1], c[0][2], c[1][2]};
    *reinterpret_cast<f32x4*>(reg + 4096 + 1152 + lane * 16) = f32x4{c[2][0] + c[2][1], c[3][0] + c[3][1], c[2][2], c[3][2]};
    const int k = arrive(s_c + C_T);
    f32x4 ts[4], pg[4], pu[4];
#pragma unroll
    for (int w = 0; w < 4; ++w) {
      const unsigned char* src = smem + OFF_REG + w * REG + 4096 + (mr & 1) * 1152 + (qd * 16 + (mr >> 1)) * 16;
      ts[w] = rst[qd * 4 + w];
      pg[w] = *reinterpret_cast<const f32x4*>(src); pu[w] = *reinterpret_cast<const f32x4*>(src + 128);
    }
    PIN4(ts, 0); PIN4(pg, 0); PIN4(pu, 0);
    if (arrived(k) == 3) {
      const f32x4 t = (ts[0] + ts[1]) + (ts[2] + ts[3]), g4 = (pg[0] + pg[1]) + (pg[2] + pg[3]), u4 = (pu[0] + pu[1]) + (pu[2] + pu[3]);
      const float d = t[0] * (-1.0f / DM);
      const float rstd = __builtin_amdgcn_rsqf(fmaxf(t[1] + d * t[0], 0.f) * (1.0f / DM) + LN_EPS);
      const float g0 = rstd * (g4[0] + d * g4[2]), g1 = rstd * (g4[1] + d * g4[3]);
      const float a0 = g0 * __builtin_amdgcn_rcpf(1.0f + __expf(-g0)) * (rstd * (u4[0] + d * u4[2]));
      const float a1 = g1 * __builtin_amdgcn_rcpf(1.0f + __expf(-g1)) * (rstd * (u4[1] + d * u4[3]));
      const unsigned h0 = __float_as_uint(a0) & 0xffff0000u, h1 = __float_as_uint(a1) & 0xffff0000u;
      const unsigned hi = max(PERM_HI(h1, h0), 1u), lw = max(PERM_HI(__float_as_uint(a1 - __uint_as_float(h1)), __float_as_uint(a0 - __uint_as_float(h0))), 1u);
#define QB(v, c_) __float_as_uint(dpp<c_>(__uint_as_float(v)))
      const bool isl = (lane & 1) != 0;
      const u32x4 hq = {QB(hi, 0x00), QB(hi, 0x55), QB(hi, 0xAA), QB(hi, 0xFF)}, lq = {QB(lw, 0x00), QB(lw, 0x55), QB(lw, 0xAA), QB(lw, 0xFF)};
#undef QB
      const float rr = rstd * (t[2] + d * t[3]);
      const u32x4 sv = {nz_bits(cq - d), nz_bits(rstd), nz_bits(__builtin_amdgcn_rcpf(1.0f + __expf(-rr))), 0x3f800000u};
      const bool stat = g_cg == 0 && mr == 2;
      if ((lane & 2) == 0 || stat) st128(rs, stat ? WS_ST + qd * 4 : WS_ACT + g_cg * 128 + (qd * 2 + (lane & 1)) * 16 + (mr >> 2) * 4, stat ? sv : (isl ? lq : hq));
    }
  } else if (isD) {
    const int col = d_ct * 32 + (mr & 7) * 4 + (mr >> 3) * 2;
    const unsigned xd = *reinterpret_cast<const unsigned*>(x + row_of(qd) * DM + col);
    const __amdgpu_buffer_rsrc_t rd = __builtin_amdgcn_make_buffer_rsrc(const_cast<bf16*>(wd), 0, FF * DM * 2, 0x00020000);
    prep();
    lds_wait<8>(s_c + F_REL, 1u);
    done_all();
    const unsigned pl = pull();
    done(NS);
    asm volatile("" :: "v"(pl));
    const unsigned te = clk();
    if (DW) until(te, DW);
    u32x4 bd[24];
    {
      const int off = ((tw * 384 + (mr >> 3) * 32 + qd * 8) * DM + d_ct * 32 + (mr & 7) * 4) * 2;
      u32x2 e[48];
#pragma unroll
      for (int i = 0; i < 48; ++i) e[i] = __builtin_amdgcn_raw_buffer_load_b64(rd, off, ((i >> 3) * 64 + (i & 7)) * (DM * 2), 0);
      MEMFENCE();
#pragma unroll
      for (int s = 0; s < 6; ++s) {
#pragma unroll
        for (int k = 0; k < 4; ++k) {
          bd[s * 4 + 0][k] = PERM_LO(e[s * 8 + 2 * k + 1][0], e[s * 8 + 2 * k][0]); bd[s * 4 + 1][k] = PERM_HI(e[s * 8 + 2 * k + 1][0], e[s * 8 + 2 * k][0]);
          bd[s * 4 + 2][k] = PERM_LO(e[s * 8 + 2 * k + 1][1], e[s * 8 + 2 * k][1]); bd[s * 4 + 3][k] = PERM_HI(e[s * 8 + 2 * k + 1][1], e[s * 8 + 2 * k][1]);
        }
        PIN4(bd, s * 4);
      }
    }
    until(te, DLATE);
    f32x2 hv;
    int dl = 0;
    {
      const int off = (WS_SLAB + qd * DM + col) * 4;
      u32x2 sl[KS];
#pragma unroll 1
      for (unsigned spin = 0; spin < SPIN_CAP; ++spin) {
#pragma unroll
        for (int ks = 0; ks < KS; ++ks) sl[ks] = __builtin_amdgcn_raw_buffer_load_b64(rs, off, ks * (NB * DM * 4), AUX_SC1);
        unsigned mn = min(sl[0][0], sl[0][1]);
#pragma unroll
        for (int ks = 1; ks < KS; ++ks) mn = min(mn, min(sl[ks][0], sl[ks][1]));
        if (__all(mn != 0u)) break;
        dl = DLATE2;
        __builtin_amdgcn_s_sleep(4);
      }
#define SL(k) __builtin_bit_cast(f32x2, sl[k])
      hv = bf_pair(xd) + (((SL(0) + SL(1)) + (SL(2) + SL(3))) + ((SL(4) + SL(5)) + (SL(6) + SL(7))));
#undef SL
      asm volatile("" : "+v"(hv));
    }
    until(clk(), dl);
    u32x4 a[6], st;
    const int aoff = (WS_ACT + tw * 12 * 128 + (((mr >> 1) & 1) * 8 + (mr >> 2) * 2 + (mr & 1)) * 16 + qd * 4) * 4;
#pragma unroll 1
    for (unsigned spin = 0; spin < SPIN_CAP; ++spin) {
#pragma unroll
      for (int s = 0; s < 6; ++s) a[s] = __builtin_amdgcn_raw_buffer_load_b128(rs, aoff + s * 1024, 0, AUX_SC1);
      st = ld128(rs, WS_ST + qd * 4);
      unsigned mn = min4(st);
#pragma unroll
      for (int s = 0; s < 6; ++s) mn = min(mn, min4(a[s]));
      if (__all(mn != 0u)) break;
      __builtin_amdgcn_s_sleep(1);
    }
    f32x4 c[4];
#pragma unroll
    for (int t = 0; t < 4; ++t) c[t] = f32x4{0.f, 0.f, 0.f, 0.f};
#pragma unroll
    for (int s = 0; s < 6; ++s)
#pragma unroll
      for (int t = 0; t < 4; ++t) c[t] = mfma16(a[s], bd[s * 4 + t], c[t]);
    const bool up = mr >= 8;
    const float k0 = up ? c[2][2] + c[2][3] : c[0][0] + c[0][1], k1 = up ? c[3][2] + c[3][3] : c[1][0] + c[1][1];
    const float o0 = up ? c[0][2] + c[0][3] : c[2][0] + c[2][1], o1 = up ? c[1][2] + c[1][3] : c[3][0] + c[3][1];
    f32x2* r2 = reinterpret_cast<f32x2*>(red);
    r2[tw * 64 + lane] = f32x2{k0 + dpp<DPP_ROR8>(o0), k1 + dpp<DPP_ROR8>(o1)};
    const int k = arrive(s_c + C_T);
    f32x2 y = (r2[lane] + r2[64 + lane]) + (r2[128 + lane] + r2[192 + lane]);
    asm volatile("" : "+v"(y));
    if (arrived(k) == 3) {
      {
        until(t0, 13000);
        const char* pb = vb + (size_t)(370u << 20) + (size_t)d_ct * 65536;
        unsigned o = __builtin_nontemporal_load(reinterpret_cast<const unsigned*>(pb + 60000));
        asm volatile("s_waitcnt vmcnt(0)" : "+v"(o) :: "memory");
        o = 0u;
        const unsigned ta = clk();
#pragma unroll 1
        for (int i = 0; i < 20; ++i) {
          unsigned v = __builtin_nontemporal_load(reinterpret_cast<const unsigned*>(pb + o));
          asm volatile("s_waitcnt vmcnt(0)\n\tv_and_b32 %0, 0, %0" : "+v"(v) :: "memory");
          o += 2176u + v;
        }
        const unsigned tb = clk();
        until(t0, 15000 + (int)(tb - ta) * 2);
      }
      const f32x4 sf = __builtin_bit_cast(f32x4, st);
      const __hip_bfloat162 o2 = __halves2bfloat162(__float2bfloat16(hv[0] + ((hv[0] - sf[0]) * sf[1] + y[0] * sf[2])),
                                                    __float2bfloat16(hv[1] + ((hv[1] - sf[0]) * sf[1] + y[1] * sf[2])));
      __hip_atomic_store(reinterpret_cast<unsigned*>(out + row_of(qd) * DM + col), *reinterpret_cast<const unsigned*>(&o2), __ATOMIC_RELAXED, __HIP_MEMORY_SCOPE_AGENT);
    }
  } else {
    prep();
    if (isC && tw == 1) {
      lds_wait<8>(s_c + F_REL, 1u);
      const int cs = c_it != 1 ? c_it * 2 : max(g >> 3, 1), cnt = c_it != 1 ? 8 : (g < 16 ? 16 : 32), x0 = c_it != 1 ? g * 8 : (g < 16 ? g * 16 : (g & 7) * 32);
      done(c_it < 2 ? 4 : NA);
      pause(WDELAY);
      const float wk = weight(cs, x0, cnt);
      f32x4 o = {0.f, 0.f, 0.f, 0.f};
#pragma unroll 1
      for (int k0 = 0; k0 < cnt; k0 += 16) {
        u32x2 pv[16];
#pragma unroll
        for (int k = 0; k < 16; ++k) pv[k] = __builtin_amdgcn_raw_buffer_load_b64(rs, lp * 2, (WS_PQ + ((x0 + ((k0 + k) & (cnt - 1))) * 5 + cs) * (DH / 2)) * 4, AUX_EARLY);
#pragma unroll
        for (int k = 0; k < 16; ++k) {
          const float w = k0 + k < cnt ? __uint_as_float(__builtin_amdgcn_readlane(__float_as_uint(wk), k0 + k)) : 0.f;
          o += w * f32x4{(float)(short)pv[k][0], (float)((int)pv[k][0] >> 16), (float)(short)pv[k][1], (float)((int)pv[k][1] >> 16)};
        }
      }
      u32x2 hi, lw;
      split4(o, hi, lw);
      if (lane < DH / 4) st128(rs, WS_OC + (c_it * NG + g) * DH + lane * 4, u32x4{max(hi[0], 1u), max(hi[1], 1u), max(lw[0], 1u), max(lw[1], 1u)});
    }
    done_all();
    const unsigned pl = pull();
    asm volatile("" :: "v"(pl));
  }
  if (tw == 3) done_all();
}

extern "C" void kernel_launch(void* const* d_in, const int* in_sizes, int n_in, void* d_out, int out_size, void* d_ws,
                              size_t ws_size, hipStream_t stream) {
  const bf16* x = (const bf16*)d_in[0];
  const bf16* kc = (const bf16*)d_in[1];
  const bf16* vc = (const bf16*)d_in[2];
  const bf16* wq = (const bf16*)d_in[3];
  const bf16* wo = (const bf16*)d_in[4];
  const bf16* wr = (const bf16*)d_in[6];
  const bf16* wg = (const bf16*)d_in[7];
  const bf16* wu = (const bf16*)d_in[8];
  const bf16* wd = (const bf16*)d_in[9];
  fused_kernel<<<NWG, NW * 64, 0, stream>>>(x, kc, vc, wq, wo, wr, wg, wu, wd, (float*)d_ws, (bf16*)d_out);
}
```

```cpp
#include <hip/hip_runtime.h>
#include <hip/hip_bf16.h>
#include <stdint.h>

typedef __hip_bfloat16 bf16;
using u32x2 = __attribute__((ext_vector_type(2))) unsigned;
using u32x4 = __attribute__((ext_vector_type(4))) unsigned;
using f32x4 = __attribute__((ext_vector_type(4))) float;
using f32x2 = __attribute__((ext_vector_type(2))) float;
using bf16x8 = __attribute__((ext_vector_type(8))) __bf16;

constexpr int NB = 4, DM = 640, NH = 32, SK = 8192, DH = 192, HD = NH * DH, FF = 1536;
constexpr float LN_EPS = 1e-5f;
constexpr float QSCALE = 0.07216878364870322f * 1.4426950408889634f;

constexpr int NWG = 256;
constexpr int GS = 8, NG = NWG / GS, NI = GS / 2;
constexpr int HPG = NI / NB;
constexpr int PPI = 512 / GS;
constexpr int NPAIR = NI * PPI;

constexpr int TAUK = 1320, TAUV = 1332;
constexpr int TSTART = 170;
constexpr int WGAP = 165, WP0 = 64, WPN = 176;
constexpr int VGAP = 90;
constexpr int DEEP1 = 100, DEEP2 = 200, SDEEP = 0, SPACE = 0, LAGOFF = 20, ENDT = 12, ENDS = 2, PHASE = 0;
constexpr int SPRINT = 0;
constexpr int JUMP = 145;
constexpr int HST = 2800, RT0 = 3900, RTS = 1000;
constexpr unsigned NRD = 5, RMIN = 100, FBP = 60, FBL = 150;
constexpr int W0START = 1700, W0SPAN = 1000, W1START = 2700, W1SPAN = 2600;
constexpr int ROT = 16, ROWS = 0xC9;
constexpr int FBM = 100, FBX = 15, FB0 = 170, FB1 = 55, FBS = 10;
constexpr int FBD = 20, HOLD = 800, GB = 24, GBS = 2;
constexpr unsigned PRD = 3; constexpr int PRB = 24;
constexpr int TLEAD = 40, PFD = 6, WFD = 3;
constexpr int QWAIT = 750, QBACK = 100;
constexpr int WDELAY = 100, WDELAY2 = 460, DLATE = 100, DLATE2 = 115;
constexpr int CPF = 1, CPT = 0, CODE = 24 * 64 * 55;
constexpr int GLEAD = 1, GCNT = 4, DP = 0, DPW = 60, GE = 0, PW = 30, PS = 0, HDELAY = 1150, HDELAY2 = 780, GW = 0, DW = 0;

constexpr int KS = 8;
constexpr int WS_PML = 0;
constexpr int WS_PO = WS_PML + NWG * 64;
constexpr int WS_PQ = WS_PO + NWG * DH;
constexpr int WS_OC = WS_PQ + NWG * 5 * (DH / 2);
constexpr int WS_SLAB = WS_OC + (NB - 1) * NH * DH;
constexpr int WS_ACT = WS_SLAB + KS * NB * DM;
constexpr int WS_H = WS_ACT + NB * FF / 2;
constexpr int WS_ST = WS_H + (NB - 1) * 4 * 192;
constexpr int WS_HN = WS_ST + 32;
constexpr int WS_HR = WS_HN + NWG;
constexpr int WS_HA = WS_HR + 2 * NWG;
constexpr int QFS = 2 * GS * 32;
constexpr int WS_QF = WS_HA + 32 * NWG;
constexpr int WS_QP = WS_QF + NG * QFS;
constexpr int WS_END = WS_QP + NH * 2 * NB * DH;
constexpr int NPROD = HPG * 6;
static_assert(WS_PO % 32 == 0 && WS_SLAB % 32 == 0 && WS_ACT % 32 == 0 && WS_ST % 32 == 0 && WS_HN % 32 == 0 && WS_QF % 32 == 0 && WS_QP % 32 == 0, "line alignment");


__device__ __forceinline__ int row_of(int b) { return (ROWS >> (2 * b)) & 3; }
__device__ __forceinline__ float bf_lo(unsigned u) { return __uint_as_float(u << 16); }
__device__ __forceinline__ float bf_hi(unsigned u) { return __uint_as_float(u & 0xffff0000u); }
__device__ __forceinline__ f32x2 bf_pair(unsigned u) { return f32x2{bf_lo(u), bf_hi(u)}; }
__device__ __forceinline__ f32x2 fma2(float a, f32x2 b, f32x2 c) { return __builtin_elementwise_fma(f32x2{a, a}, b, c); }
__device__ __forceinline__ f32x2 fma2(f32x2 a, f32x2 b, f32x2 c) { return __builtin_elementwise_fma(a, b, c); }

template <int CTRL> __device__ __forceinline__ float dpp(float x) {
  return __builtin_bit_cast(float, __builtin_amdgcn_mov_dpp(__builtin_bit_cast(int, x), CTRL, 0xf, 0xf, true));
}
constexpr int DPP_XOR1 = 0xB1, DPP_XOR2 = 0x4E, DPP_HMIRROR = 0x141, DPP_MIRROR = 0x140, DPP_ROR8 = 0x128;

__device__ __forceinline__ float sum_x16_32(float x) {
  auto s = __builtin_amdgcn_permlane16_swap(__float_as_uint(x), __float_as_uint(x), false, false);
  x = __uint_as_float(s[0]) + __uint_as_float(s[1]);
  auto t = __builtin_amdgcn_permlane32_swap(__float_as_uint(x), __float_as_uint(x), false, false);
  return __uint_as_float(t[0]) + __uint_as_float(t[1]);
}
__device__ __forceinline__ float max_x16_32(float x) {
  auto s = __builtin_amdgcn_permlane16_swap(__float_as_uint(x), __float_as_uint(x), false, false);
  x = fmaxf(__uint_as_float(s[0]), __uint_as_float(s[1]));
  auto t = __builtin_amdgcn_permlane32_swap(__float_as_uint(x), __float_as_uint(x), false, false);
  return fmaxf(__uint_as_float(t[0]), __uint_as_float(t[1]));
}
__device__ __forceinline__ float tsum32(float a, float b) {
  auto t = __builtin_amdgcn_permlane32_swap(__float_as_uint(a), __float_as_uint(b), false, false);
  return __uint_as_float(t[0]) + __uint_as_float(t[1]);
}
__device__ __forceinline__ float tsum16(float a, float b) {
  auto t = __builtin_amdgcn_permlane16_swap(__float_as_uint(a), __float_as_uint(b), false, false);
  return __uint_as_float(t[0]) + __uint_as_float(t[1]);
}
__device__ __forceinline__ float sum_r(float v) { v += dpp<DPP_ROR8>(v); return sum_x16_32(v); }
__device__ __forceinline__ float row_sum(float v) {
  v += dpp<DPP_XOR1>(v); v += dpp<DPP_XOR2>(v); v += dpp<DPP_HMIRROR>(v);
  return v + dpp<DPP_MIRROR>(v);
}
__device__ __forceinline__ float row_max(float v) {
  v = fmaxf(v, dpp<DPP_XOR1>(v)); v = fmaxf(v, dpp<DPP_XOR2>(v)); v = fmaxf(v, dpp<DPP_HMIRROR>(v));
  return fmaxf(v, dpp<DPP_MIRROR>(v));
}
__device__ __forceinline__ float wave_sum(float v) {
  v += dpp<DPP_XOR1>(v); v += dpp<DPP_XOR2>(v); v += dpp<DPP_HMIRROR>(v);
  return sum_r(v);
}

__device__ __forceinline__ u32x4 ld_nt(const char* p) { return __builtin_nontemporal_load(reinterpret_cast<const u32x4*>(p)); }

struct Tile { u32x4 d[3]; };

#define PIN4(a, o) asm volatile("" : "+v"((a)[(o) + 0]), "+v"((a)[(o) + 1]), "+v"((a)[(o) + 2]), "+v"((a)[(o) + 3]))
#define MEMFENCE() do { asm volatile("" ::: "memory"); __builtin_amdgcn_sched_barrier(0); } while (0)

__device__ __forceinline__ void glds16_nt(const void* gsrc, unsigned lds_dst) {
  unsigned keep;
  asm volatile("s_mov_b32 %0, m0\n\ts_mov_b32 m0, %2\n\ts_nop 0\n\tglobal_load_lds_dwordx4 %1, off nt\n\ts_mov_b32 m0, %0"
               : "=&s"(keep) : "v"(gsrc), "s"(lds_dst) : "memory");
}

__device__ __forceinline__ float tile_score(const Tile& k, const f32x2 (&q)[12]) {
  f32x2 s0 = {0.f, 0.f}, s1 = s0;
#pragma unroll
  for (int i = 0; i < 3; ++i) {
    s0 = fma2(bf_pair(k.d[i][0]), q[i * 4], s0);     s1 = fma2(bf_pair(k.d[i][1]), q[i * 4 + 1], s1);
    s0 = fma2(bf_pair(k.d[i][2]), q[i * 4 + 2], s0); s1 = fma2(bf_pair(k.d[i][3]), q[i * 4 + 3], s1);
  }
  s0 += s1;
  return s0[0] + s0[1];
}
__device__ __forceinline__ void score_put(float s, float* dst, int j) {
  s += dpp<DPP_XOR1>(s);
  s += dpp<DPP_XOR2>(s);
  s += dpp<DPP_HMIRROR>(s);
  if (j == 0) *dst = s;
}
__device__ __forceinline__ void tile_load(Tile& t, const char* base, unsigned off, int half) {
#pragma unroll
  for (int i = 0; i < 3; ++i) t.d[i] = ld_nt(base + off + half * 3072 + i * 128);
}
__device__ __forceinline__ void pair_issue(Tile& ta, Tile& tb, const char* base, unsigned off, int deep) {
  deep = __builtin_amdgcn_readfirstlane(deep);
#pragma unroll
  for (int i = 0; i < 6; ++i) {
    asm volatile("s_cmp_gt_u32 %0, 1\n\ts_cbranch_scc1 .Lpi%=\n\ts_waitcnt vmcnt(8)\n\ts_cmp_eq_u32 %0, 1\n\ts_cbranch_scc1 .Lpi%=\n\ts_sleep %1\n\ts_waitcnt vmcnt(5)\n.Lpi%=:"
                 :: "s"(deep), "n"(i ? SPACE : 0) : "scc", "memory");
    (i < 3 ? ta : tb).d[i % 3] = ld_nt(base + off + (i / 3) * 3072 + (i % 3) * 128);
    MEMFENCE();
  }
}
__device__ __forceinline__ void pv_last(float p, Tile& v, f32x2 (&acc)[12]) {
#pragma unroll
  for (int i = 0; i < 3; ++i) {
    asm volatile("" : "+v"(v.d[i]), "+v"(p));
#pragma unroll
    for (int d = 0; d < 4; ++d) acc[i * 4 + d] = fma2(p, bf_pair(v.d[i][d]), acc[i * 4 + d]);
    PIN4(acc, i * 4);
  }
}
__device__ __forceinline__ void q_load(f32x2 (&q)[12], const float* src, int j) {
#pragma unroll
  for (int i = 0; i < 6; ++i) {
    const f32x4 a = *reinterpret_cast<const f32x4*>(src + (i >> 1) * 64 + j * 8 + (i & 1) * 4);
    q[i * 2] = f32x2{a[0], a[1]}; q[i * 2 + 1] = f32x2{a[2], a[3]};
  }
}
#define VMCNT(n) asm volatile("s_waitcnt vmcnt(%0)" :: "n"(n) : "memory")

constexpr int AUX_SC1 = 16, AUX_EARLY = 18;
constexpr unsigned SPIN_CAP = 1u << 17;
__device__ __forceinline__ unsigned nz_bits(float v) { return max(__float_as_uint(v), 1u); }
__device__ __forceinline__ bool all_nz(u32x4 v) { return v[0] != 0u && v[1] != 0u && v[2] != 0u && v[3] != 0u; }
__device__ __forceinline__ unsigned min4(u32x4 v) { return min(min(v[0], v[1]), min(v[2], v[3])); }
__device__ __forceinline__ unsigned ld32(__amdgpu_buffer_rsrc_t rs, int word) { return __builtin_amdgcn_raw_buffer_load_b32(rs, word * 4, 0, AUX_SC1); }
__device__ __forceinline__ u32x2 ld64(__amdgpu_buffer_rsrc_t rs, int word) { return __builtin_amdgcn_raw_buffer_load_b64(rs, word * 4, 0, AUX_SC1); }
__device__ __forceinline__ u32x4 ld128(__amdgpu_buffer_rsrc_t rs, int word) { return __builtin_amdgcn_raw_buffer_load_b128(rs, word * 4, 0, AUX_SC1); }
__device__ __forceinline__ void st32(__amdgpu_buffer_rsrc_t rs, int word, unsigned v) { __builtin_amdgcn_raw_buffer_store_b32(v, rs, word * 4, 0, AUX_SC1); }
__device__ __forceinline__ void st64(__amdgpu_buffer_rsrc_t rs, int word, u32x2 v) { __builtin_amdgcn_raw_buffer_store_b64(v, rs, word * 4, 0, AUX_SC1); }
__device__ __forceinline__ void st128(__amdgpu_buffer_rsrc_t rs, int word, u32x4 v) { __builtin_amdgcn_raw_buffer_store_b128(v, rs, word * 4, 0, AUX_SC1); }

__device__ __forceinline__ u32x4 ld128nt(__amdgpu_buffer_rsrc_t rs, int word) { return __builtin_amdgcn_raw_buffer_load_b128(rs, word * 4, 0, AUX_EARLY); }
__device__ __forceinline__ void st128nt(__amdgpu_buffer_rsrc_t rs, int word, u32x4 v) { __builtin_amdgcn_raw_buffer_store_b128(v, rs, word * 4, 0, AUX_EARLY); }

__device__ __forceinline__ bool leader_release(__amdgpu_buffer_rsrc_t rs, int lane, unsigned t0, unsigned rd) {
  u32x4 a[4];
#pragma unroll
  for (int k = 0; k < 4; ++k) a[k] = ld128(rs, WS_HA + (lane * 4 + k) * 32);
  const u32x4 n = ld128(rs, WS_HN + lane * 4);
  if (rd == 0u && !__all(a[0][0] == n[0] && a[1][0] == n[1] && a[2][0] == n[2] && a[3][0] == n[3])) return false;
  const float e = fmaxf(fmaxf((float)(int)(t0 - a[0][2]), (float)(int)(t0 - a[1][2])), fmaxf((float)(int)(t0 - a[2][2]), (float)(int)(t0 - a[3][2])));
  unsigned first = t0 - (unsigned)(int)max_x16_32(row_max(e));
  float ls = 0.f, lx = 0.f;
#pragma unroll
  for (int k = 0; k < 4; ++k) {
    const float l = a[k][0] == n[k] + rd ? (float)min(a[k][3] + (rd ? 0u : min(a[k][2] - first, 300u)), 2000u) : 0.f;
    ls += l; lx = fmaxf(lx, l);
  }
  ls = wave_sum(ls) * (1.0f / NWG); lx = max_x16_32(row_max(lx));
  const int extra = min(max((int)(ls * (FBM / 100.0f) + lx * (FBX / 100.0f)) - (rd ? FB1 + (int)(NRD - rd) * FBD : FB0), 0), 600);
  first = (first & 0xffffu) | ((unsigned)extra << 16);
  if (rd != 0u && ls * NWG < HOLD) return true;
  st128(rs, WS_HR + lane * 8, u32x4{a[0][1] + rd, first, a[1][1] + rd, first});
  st128(rs, WS_HR + lane * 8 + 4, u32x4{a[2][1] + rd, first, a[3][1] + rd, first});
  return true;
}

typedef __attribute__((address_space(3))) unsigned lds_u32;
__device__ __forceinline__ unsigned lds_add(lds_u32* p, int lane) {
  unsigned k = 0;
  if (lane == 0) k = __hip_atomic_fetch_add(p, 1u, __ATOMIC_RELAXED, __HIP_MEMORY_SCOPE_WORKGROUP);
  return __builtin_amdgcn_readfirstlane(k);
}
#define CFENCE() asm volatile("" ::: "memory")
__device__ __forceinline__ int arrive(lds_u32* p) {
  int v;
  unsigned long long keep;
  asm volatile("s_mov_b64 %1, exec\n\ts_mov_b64 exec, 1\n\tds_add_rtn_u32 %0, %2, %3\n\ts_mov_b64 exec, %1"
               : "=&v"(v), "=&s"(keep) : "v"((unsigned)(size_t)p), "v"(1u) : "memory");
  return v;
}
__device__ __forceinline__ int arrived(int v) {
  asm volatile("s_waitcnt lgkmcnt(0)" : "+v"(v) :: "memory");
  return __builtin_amdgcn_readfirstlane(v);
}
__device__ __forceinline__ unsigned lds_get(lds_u32* p) {
  return __builtin_amdgcn_readfirstlane(__hip_atomic_load(p, __ATOMIC_RELAXED, __HIP_MEMORY_SCOPE_WORKGROUP));
}
template <int SLEEP> __device__ __forceinline__ void lds_wait(lds_u32* p, unsigned n) {
#pragma unroll 1
  for (unsigned spin = 0; spin < (SPIN_CAP << 4); ++spin) {
    if (lds_get(p) >= n) break;
    __builtin_amdgcn_s_sleep(SLEEP);
  }
  asm volatile("" ::: "memory");
}
__device__ __forceinline__ void pause(unsigned ticks) {
  const unsigned long long t0 = __builtin_amdgcn_s_memrealtime();
#pragma unroll 1
  for (unsigned spin = 0; spin < 4096u && __builtin_amdgcn_s_memrealtime() - t0 < ticks; ++spin) __builtin_amdgcn_s_sleep(16);
}
__device__ __forceinline__ unsigned clk() { return (unsigned)__builtin_amdgcn_s_memrealtime(); }
struct Table {
  unsigned t0; int was, moved, slowed;
  __device__ __forceinline__ int gate(int due, lds_u32* mine, lds_u32* slow, bool hold = true, int sh = 0) {
    const int left = (int)lds_get(slow) - slowed, d = max(min(left, FBS), -GBS);
    t0 += d; was -= d; slowed += d;
    int late = (int)(clk() - t0) - due;
    if (late - was > JUMP) { t0 += late - was; moved += late - was; late = was; *mine = moved; }
    const int lag = max(late - max(left - d, 0) - LAGOFF, 0);
    mine[4] = lag;
    was = late;
#pragma unroll 1
    for (int spin = 0; spin < 65536 && hold && late < 0; ++spin) { __builtin_amdgcn_s_sleep(1); late = (int)(clk() - t0) - due; }
    return max(lag < (DEEP1 >> sh) ? 0 : (lag < (DEEP2 >> sh) ? 1 : 2), hold ? 0 : SDEEP);
  }
  __device__ __forceinline__ void agree(lds_u32* all, lds_u32* mine) {
    if (moved == 0) return;
    const int m = (int)min(min(lds_get(all), lds_get(all + 1)), min(lds_get(all + 2), lds_get(all + 3))) + 30;
    if (m < moved) { t0 -= moved - m; moved = m; was = 1 << 20; *mine = moved; }
  }
};
__device__ __forceinline__ void until(unsigned t0, int due) {
#pragma unroll 1
  for (int spin = 0; spin < 65536 && (int)(clk() - t0) < due; ++spin) __builtin_amdgcn_s_sleep(1);
}
#define LGKM0() asm volatile("s_waitcnt lgkmcnt(0)" ::: "memory")

__device__ __forceinline__ void split4(f32x4 v, u32x2& hi, u32x2& lo) {
  unsigned h[4], r[4];
#pragma unroll
  for (int i = 0; i < 4; ++i) { h[i] = __float_as_uint(v[i]) & 0xffff0000u; r[i] = __float_as_uint(v[i] - __uint_as_float(h[i])); }
  hi = u32x2{__builtin_amdgcn_perm(h[1], h[0], 0x07060302u), __builtin_amdgcn_perm(h[3], h[2], 0x07060302u)};
  lo = u32x2{__builtin_amdgcn_perm(r[1], r[0], 0x07060302u), __builtin_amdgcn_perm(r[3], r[2], 0x07060302u)};
}
__device__ __forceinline__ u32x4 split8(f32x4 v, bool odd) {
  u32x2 hi, lo;
  split4(v, hi, lo);
  const unsigned n0 = __float_as_uint(dpp<DPP_XOR1>(__uint_as_float(odd ? hi[0] : lo[0]))), n1 = __float_as_uint(dpp<DPP_XOR1>(__uint_as_float(odd ? hi[1] : lo[1])));
  return u32x4{max(odd ? n0 : hi[0], 1u), max(odd ? n1 : hi[1], 1u), max(odd ? lo[0] : n0, 1u), max(odd ? lo[1] : n1, 1u)};
}
__device__ __forceinline__ f32x4 mfma16(u32x4 a, u32x4 b, f32x4 c) {
  return __builtin_amdgcn_mfma_f32_16x16x32_bf16(__builtin_bit_cast(bf16x8, a), __builtin_bit_cast(bf16x8, b), c, 0, 0, 0);
}

constexpr int NW = 8, NMW = 4;
constexpr int NLP = 5, NRP = 7;
constexpr int NTP = 4 * (NLP + NRP);
constexpr int REG = NLP * 6 * 1024;
constexpr int NRING = 4;
constexpr int NS = 6, NA = NS - 1;
__device__ __forceinline__ int seg_beg(int s) { return s >= NS ? NPAIR : ((0xC87640 >> (4 * s)) & 15) * 16; }
__device__ __forceinline__ int seg_k(int t) { return (int)(0x5433222211110000ull >> (4 * (t >> 4))) & 15; }
__device__ __forceinline__ int seg_of_k(int k) { return (0x321540 >> (4 * k)) & 15; }
__device__ __forceinline__ int k_of_seg(int s) { return (0x215430 >> (4 * s)) & 15; }
__device__ __forceinline__ int pair_of(int t) { return t < 64 ? t : (t < 192 ? t + 64 : t - 128); }
constexpr int OFF_CNT = 0, OFF_REG = 256,
              OFF_SC = OFF_REG + 4 * REG, OFF_BUF = OFF_SC + NPAIR * 16 * 4, OFF_Q = OFF_BUF + NRING * NMW * DH * 4, OFF_ML = OFF_Q + NS * DH * 4,
              OFF_RED = OFF_ML + 128, OFF_RS = OFF_RED + 4 * 256 * 4, OFF_END = OFF_RS + 5 * 16;
enum { F_REL = 0, F_DONE, C_LAST, C_T = 3, F_OUT, C_P, C_K, F_P, F_Q = 8, F_FREE, S_FIRST, F_Q2, C_W = 12, C_SC = 16, C_PART = 24, S_ECHO = 32, S_MOVED = 48, S_SLOW = 56 };
__device__ __forceinline__ int due_k(int p) { return TSTART + ((p * TAUK + min(max(p - WP0, 0), WPN) * (WGAP * 64 / WPN)) >> 6); }
__device__ __forceinline__ int due_v(int p) { return TSTART + WGAP + ((NPAIR * TAUK + p * TAUV + max(p - NPAIR / 2, 0) * (VGAP * 64 / (NPAIR / 2))) >> 6); }
constexpr int AW_LD = DH * 2 + 16, AG_LD = 160 * 2 + 16;

__global__ void __launch_bounds__(NW * 64) fused_kernel(const bf16* __restrict__ x, const bf16* __restrict__ kc,
                                                        const bf16* __restrict__ vc, const bf16* __restrict__ wq,
                                                        const bf16* __restrict__ wo, const bf16* __restrict__ wr,
                                                        const bf16* __restrict__ wg, const bf16* __restrict__ wu,
                                                        const bf16* __restrict__ wd, float* ws, bf16* __restrict__ out) {
  __shared__ __attribute__((aligned(16))) unsigned char smem[OFF_END];
  float* s_sc = reinterpret_cast<float*>(smem + OFF_SC);
  float* s_buf = reinterpret_cast<float*>(smem + OFF_BUF);
  float* s_q = reinterpret_cast<float*>(smem + OFF_Q);
  u32x4* s_ml = reinterpret_cast<u32x4*>(smem + OFF_ML);
  lds_u32* s_c = (lds_u32*)(smem + OFF_CNT);

  const int tid = threadIdx.x, lane = tid & 63, r = lane >> 3, j = lane & 7;
  const int wave = __builtin_amdgcn_readfirstlane(tid >> 6);
  const int tw = wave - NMW;
  const int bid = blockIdx.x, xl = bid & 7, idx = bid >> 3;
  const int g = (xl * 32 + idx) / GS, mm = idx % GS;
  const __amdgpu_buffer_rsrc_t rs = __builtin_amdgcn_make_buffer_rsrc(ws, 0, WS_END * 4, 0x00020000);
  const int ro = ((idx & 3) << 3) | (idx >> 2);
  const bool isW = ro < 10, isG = ro >= 10 && ro < 16;
  const int w_ks = xl, w_ct = ro;
  const int g_cg = xl * 6 + (ro - 10);
  const int d_ct = (xl + ((ro - 16) >> 2) * 8) * 2 + (((ro - 16) >> 1) & 1);
  const bool isD = ro >= 16 && ro < 24 && (ro & 1) == 0 && d_ct < DM / 32;
  const bool isH = ro == 22 && xl >= 2 && xl < 5;
  const bool isC = (mm & 7) == 3 || (mm & 7) >= 6;
  const int c_it = (mm >> 3) * 3 + max((mm & 7) - 5, 0);
  const int qf = WS_QF + g * QFS;
  const char* kb = reinterpret_cast<const char*>(kc);
  const char* vb = reinterpret_cast<const char*>(vc);
  const unsigned lo = (r * 24 + j) * 16;
  const int xb = g * GS + mm, c0 = mm * 8 + g * ROT;
  const int b2 = (2 * NH + (xb >> 4)) * 1024 + (xb & 15) * 8, b3 = (2 * NH + 16 + (xb >> 5)) * 1024 + (xb & 31) * 8;
  auto poff = [&](int p) -> unsigned {
    const int turn = (p & 63) >> 2;
    int t = (row_of(p >> 6) * NH + g) * 1024 + ((turn * 64 + (p & 3) * 2 + c0) & 1023);
    if ((p >> 6) == 1) t = (turn < 8 ? b2 + turn * 128 : b3 + (turn & 4) * 2048 + (turn & 3) * 256) + (p & 3) * 2;
    return (unsigned)t * 3072u;
  };
  const unsigned lds_base = (unsigned)(size_t)(__attribute__((address_space(3))) unsigned char*)smem + OFF_REG + tw * REG;
  auto merged = [&](int it, int ln) -> f32x4 {
    const float* src = s_buf + (it % NRING) * (NMW * DH) + ((ln >> 1) & 7) * 24 + (ln >> 4) * 8 + (ln & 1) * 4;
    return (*reinterpret_cast<const f32x4*>(src) + *reinterpret_cast<const f32x4*>(src + DH)) +
           (*reinterpret_cast<const f32x4*>(src + 2 * DH) + *reinterpret_cast<const f32x4*>(src + 3 * DH));
  };
  auto publish = [&](int it) -> bool {
    const int k = arrive(s_c + C_PART + it);
    const f32x4 m = merged(it, lane < DH / 4 ? lane : 0);
    u32x4 o = {nz_bits(m[0]), nz_bits(m[1]), nz_bits(m[2]), nz_bits(m[3])};
    asm volatile("" : "+v"(o));
    if (arrived(k) != NMW - 1) return false;
    if (lane < DH / 4) st128(rs, WS_PO + xb * DH + lane * 4, o);
    if (lane == 0) __hip_atomic_fetch_add(s_c + F_DONE, 1u, __ATOMIC_RELAXED, __HIP_MEMORY_SCOPE_WORKGROUP);
    return true;
  };
#define NONCE() do { unsigned n_ = __builtin_amdgcn_readfirstlane(f) + 1u; while (__any(f == n_)) ++n_; \
    if (lane < 32) st32(rs, qf + (GS + mm) * 32 + lane, n_); f = n_; } while (0)
  unsigned long long pc;
  asm volatile("s_getpc_b64 %0" : "=s"(pc));
  unsigned r0 = 0u;
  unsigned t0 = clk();
  if (wave < NMW) {
    Tile ta, tb, tc, td;
    const int c = lane & (GS - 1);
    unsigned f = 0u;
    if (wave == 3) f = ld32(rs, qf + c * 32 + mm);
    if (mm < NPROD) {
      const int hp = g + (mm / 6) * NG, ck = (mm % 6) >> 1, hf = mm & 1, jp = hf * 4 + wave;
      const int bb = lane >> 4, t = lane & 15;
      const u32x4* xp = reinterpret_cast<const u32x4*>(x + row_of(bb) * DM);
      u32x4 xv[5], xs = {0u, 0u, 0u, 0u};
#pragma unroll
      for (int k = 0; k < 5; ++k) xv[k] = xp[t + 16 * k];
      if (t < 10) xs = xp[jp * 10 + t];
      const u32x4* wp = reinterpret_cast<const u32x4*>(wq + (size_t)(jp * 80 + r) * HD + hp * DH + ck * 64) + j;
      u32x4 wv[10];
#pragma unroll
      for (int k = 0; k < 10; ++k) wv[k] = __builtin_nontemporal_load(wp + (size_t)k * 8 * (HD / 8));
      if (wave == 0) s_c[lane] = lane == C_K ? NTP + NMW : 0u;
      MEMFENCE(); LGKM0(); __builtin_amdgcn_s_barrier(); MEMFENCE();
      if (wave == 3) { NONCE(); }
      float sm = 0.f;
#pragma unroll
      for (int k = 0; k < 5; ++k)
#pragma unroll
        for (int d = 0; d < 4; ++d) sm += bf_lo(xv[k][d]) + bf_hi(xv[k][d]);
      const float mean = row_sum(sm) * (1.0f / DM);
      float sv = 0.f;
#pragma unroll
      for (int k = 0; k < 5; ++k)
#pragma unroll
        for (int d = 0; d < 4; ++d) { const float a = bf_lo(xv[k][d]) - mean, c2 = bf_hi(xv[k][d]) - mean; sv += a * a + c2 * c2; }
      const float rstd = 1.0f / sqrtf(row_sum(sv) * (1.0f / DM) + LN_EPS);
      float* s_xn = s_buf + wave * 320;
      if (t < 10) {
#pragma unroll
        for (int d = 0; d < 4; ++d) {
          s_xn[(t * 8 + 2 * d) * 4 + bb] = (bf_lo(xs[d]) - mean) * rstd;
          s_xn[(t * 8 + 2 * d + 1) * 4 + bb] = (bf_hi(xs[d]) - mean) * rstd;
        }
      }
      LGKM0();
      f32x2 qa[16];
#pragma unroll
      for (int i = 0; i < 16; ++i) qa[i] = f32x2{0.f, 0.f};
#pragma unroll
      for (int k = 0; k < 10; ++k) {
        const f32x4 xn = *reinterpret_cast<const f32x4*>(&s_xn[(k * 8 + r) * 4]);
#pragma unroll
        for (int d = 0; d < 4; ++d) {
          const f32x2 w2 = bf_pair(wv[k][d]);
#pragma unroll
          for (int e = 0; e < 4; ++e) qa[e * 4 + d] = fma2(xn[e], w2, qa[e * 4 + d]);
        }
      }
      float t16[16], t8[8];
#pragma unroll
      for (int i = 0; i < 16; ++i) t16[i] = tsum32(qa[i >> 1][i & 1], qa[8 + (i >> 1)][i & 1]);
#pragma unroll
      for (int i = 0; i < 8; ++i) t8[i] = tsum16(t16[i], t16[i + 8]);
#pragma unroll
      for (int i = 0; i < 8; ++i) t8[i] += dpp<DPP_ROR8>(t8[i]);
      const bool up = (lane & 8) != 0;
      f32x4* r4 = reinterpret_cast<f32x4*>(smem + OFF_RED);
      r4[wave * 64 + lane] = f32x4{up ? t8[4] : t8[0], up ? t8[5] : t8[1], up ? t8[6] : t8[2], up ? t8[7] : t8[3]};
      LGKM0();
      if (lds_add(s_c + C_P, lane) == NMW - 1) {
        asm volatile("" ::: "memory");
        const f32x4 o4 = (r4[lane] + r4[64 + lane]) + (r4[128 + lane] + r4[192 + lane]);
        st128nt(rs, WS_QP + ((hp * 2 + hf) * NB + bb) * DH + ck * 64 + j * 8 + (up ? 4 : 0), __builtin_bit_cast(u32x4, o4));
        VMCNT(0);
        if (lane == 0) s_c[F_OUT] = 1u;
      }
    } else {
      if (wave == 0) s_c[lane] = lane == C_K ? NTP + NMW : 0u;
      MEMFENCE(); LGKM0(); __builtin_amdgcn_s_barrier(); MEMFENCE();
      if (wave == 3) { NONCE(); }
      else if (CPF && lane < CODE / (24 * 64)) {
        typedef __attribute__((address_space(1))) const unsigned g_u32;
        until(t0, CPT);
        const unsigned v = *(g_u32*)((pc & ~255ull) + (((idx >> 3) * 2 + (mm & 1)) * 3 + wave) * (CODE / 24) + lane * 64);
        asm volatile("" :: "v"(v));
      }
    }
    MEMFENCE();
    if (wave == 3) {
      __builtin_amdgcn_s_setprio(3);
      const unsigned nonce = f;
      unsigned echoed = 0u;
      if (mm < NPROD) {
        echoed = ld32(rs, qf + (GS + c) * 32);
        asm volatile("" : "+v"(echoed) :: "memory");
        lds_wait<1>(s_c + F_OUT, 1u);
        if (lane < 32) st32(rs, qf + mm * 32 + lane, echoed);
#pragma unroll 1
        while ((int)(clk() - t0) < QWAIT - QBACK) {
          const unsigned nv = ld32(rs, qf + (GS + c) * 32);
          if (__any(nv != echoed)) {
            if (lane < 32) st32(rs, qf + mm * 32 + lane, nv);
            echoed = nv;
          }
          __builtin_amdgcn_s_sleep(4);
        }
      }
      until(t0, QWAIT);
#pragma unroll 1
      for (unsigned spin = 0; spin < SPIN_CAP; ++spin) {
        const unsigned nv = ld32(rs, qf + (GS + c) * 32);
        f = ld32(rs, qf + c * 32 + mm);
        const bool done = __all(c == mm || c >= NPROD || f == nonce);
        if (mm < NPROD && __any(nv != echoed)) {
          if (lane < 32) st32(rs, qf + mm * 32 + lane, nv);
          echoed = nv;
        }
        if (done) break;
        __builtin_amdgcn_s_sleep(8);
      }
      if (lane < GS) s_c[S_ECHO + lane] = echoed;
      if (lane < DH / 4) {
        u32x4 p[3][2];
#pragma unroll
        for (int it = 0; it < 3; ++it)
#pragma unroll
          for (int k = 0; k < 2; ++k) p[it][k] = ld128nt(rs, WS_QP + ((g * 2 + k) * NB + (it ? it + 1 : 0)) * DH + lane * 4);
#pragma unroll
        for (int it = 0; it < 3; ++it) {
          *reinterpret_cast<f32x4*>(&s_q[(it ? it + 3 : 0) * DH + lane * 4]) = (__builtin_bit_cast(f32x4, p[it][0]) + __builtin_bit_cast(f32x4, p[it][1])) * QSCALE;
          if (it != 1) { LGKM0(); if (lane == 0) s_c[F_Q] = it + 1; MEMFENCE(); }
        }
      }
    }
    __builtin_amdgcn_s_setprio(3);
    until(t0, due_k(NTP + wave));
    Table tt = {t0 + ((idx * PHASE) >> 5), 1 << 20, 0, 0};
    bool common = false;
    tile_load(ta, kb, poff(NTP + wave) + lo, 0);
    tile_load(tb, kb, poff(NTP + wave) + lo, 1);
    MEMFENCE();
    {
      f32x2 q[12];
      int cur = NTP + wave, nxt = lds_add(s_c + C_K, lane), passed = 0, ci = -1;
      auto kstep = [&](Tile& xa, Tile& xb_, Tile& ya, Tile& yb) __attribute__((always_inline)) -> bool {
        const bool last = nxt >= NPAIR;
        const unsigned off = poff(last ? wave : pair_of(nxt)) + lo;
        const char* base = last ? vb : kb;
        const int it = seg_k(cur), cp = pair_of(cur);
        if (it != ci) {
          LGKM0();
          for (; passed < it; ++passed) lds_add(s_c + C_SC + passed, lane);
          lds_wait<1>(s_c + (it < 3 ? F_Q : F_Q2), min(it + 1, 3));
          q_load(q, s_q + seg_of_k(it) * DH, j);
          if (!common) { const unsigned d = lds_get(s_c + S_FIRST); if (d) { tt.t0 -= d - 1u; common = true; tt.was = 1 << 20; } }
          tt.agree(s_c + S_MOVED, s_c + S_MOVED + wave);
          ci = it;
        }
        unsigned tk = 0u;
        if (lane == 0) tk = __hip_atomic_fetch_add(s_c + C_K, 1u, __ATOMIC_RELAXED, __HIP_MEMORY_SCOPE_WORKGROUP);
        MEMFENCE();
        const int deep = tt.gate(due_k(last ? NPAIR + wave : nxt), s_c + S_MOVED + wave, s_c + S_SLOW);
        MEMFENCE();
        pair_issue(ya, yb, base, off, deep);
        score_put(tile_score(xa, q), s_sc + cp * 16 + r, j);
        score_put(tile_score(xb_, q), s_sc + cp * 16 + 8 + r, j);
        MEMFENCE();
        cur = nxt; nxt = __builtin_amdgcn_readfirstlane(tk);
        return last;
      };
      bool swap;
      for (;;) {
        if (kstep(ta, tb, tc, td)) { swap = true; break; }
        if (kstep(tc, td, ta, tb)) { swap = false; break; }
      }
      LGKM0();
      for (; passed < NS; ++passed) lds_add(s_c + C_SC + passed, lane);
      __builtin_amdgcn_s_waitcnt(0x0F70);
      if (swap) { ta = tc; tb = td; }
    }
#pragma unroll 1
    for (int it = 0; it < NS; ++it) {
      lds_wait<1>(s_c + F_P, k_of_seg(it) + 1);
      if (!common) { const unsigned d = lds_get(s_c + S_FIRST); if (d) { tt.t0 -= d - 1u; common = true; tt.was = 1 << 20; } }
      tt.agree(s_c + S_MOVED, s_c + S_MOVED + wave);
      f32x2 acc[12];
#pragma unroll
      for (int i = 0; i < 12; ++i) acc[i] = f32x2{0.f, 0.f};
      auto vstep = [&](Tile& xa, Tile& xb_, Tile& ya, Tile& yb, int p) __attribute__((always_inline)) {
        const unsigned off = poff(p + NMW) + lo;
        const float pa = s_sc[p * 16 + r], pb = s_sc[p * 16 + 8 + r];
        MEMFENCE();
        const int deep = tt.gate(due_v(p + NMW), s_c + S_MOVED + wave, s_c + S_SLOW, p + NMW < NPAIR - SPRINT * NMW, p >= NPAIR - ENDT * NMW ? ENDS : 0);
        MEMFENCE();
        pair_issue(ya, yb, vb, off, deep);
        if (p + 2 * NMW >= NPAIR) { if (lane == 0) __hip_atomic_fetch_add(s_c + C_LAST, 1u, __ATOMIC_RELAXED, __HIP_MEMORY_SCOPE_WORKGROUP); }
        pv_last(pa, xa, acc);
        pv_last(pb, xb_, acc);
        MEMFENCE();
      };
#pragma unroll 1
      for (int p = seg_beg(it) + wave; p < min(seg_beg(it + 1), NPAIR - 2 * NMW); p += 2 * NMW) {
        vstep(ta, tb, tc, td, p);
        vstep(tc, td, ta, tb, p + NMW);
      }
      if (it == NS - 1) {
        vstep(ta, tb, tc, td, NPAIR - 2 * NMW + wave);
        const int p = NPAIR - NMW + wave;
        const float pa = s_sc[p * 16 + r], pb = s_sc[p * 16 + 8 + r];
        pv_last(pa, tc, acc);
        pv_last(pb, td, acc);
      }
      float t12[12], t6[6];
#pragma unroll
      for (int i = 0; i < 12; ++i) t12[i] = tsum32(acc[i >> 1][i & 1], acc[6 + (i >> 1)][i & 1]);
#pragma unroll
      for (int i = 0; i < 6; ++i) t6[i] = tsum16(t12[i], t12[i + 6]);
#pragma unroll
      for (int i = 0; i < 6; ++i) t6[i] += dpp<DPP_ROR8>(t6[i]);
      if (it >= NRING) lds_wait<1>(s_c + F_FREE, it - NRING + 1);
      if ((lane & 8) == 0) {
        f32x2* dst = reinterpret_cast<f32x2*>(&s_buf[(it % NRING) * (NMW * DH) + wave * DH + j * 24 + (lane >> 4) * 6]);
        dst[0] = f32x2{t6[0], t6[1]}; dst[1] = f32x2{t6[2], t6[3]}; dst[2] = f32x2{t6[4], t6[5]};
      }
      CFENCE();
      if (it < NA) { if (lane == 0) __hip_atomic_fetch_add(s_c + C_PART + it, 1u, __ATOMIC_RELAXED, __HIP_MEMORY_SCOPE_WORKGROUP); }
    }
    publish(NS - 1);
    return;
  }

  {
    Tile pk[NRP * 2];
    __builtin_amdgcn_s_setprio(3);
#pragma unroll
    for (int e = 0; e < NLP * 2; ++e) {
      const char* src = kb + (poff((e >> 1) * 4 + tw) + lo + (e & 1) * 3072);
      if (e >= 2) until(t0, due_k(e * 2 + tw) - TLEAD);
#pragma unroll
      for (int i = 0; i < 3; ++i) glds16_nt(src + i * 128, lds_base + (e * 3 + i) * 1024);
      if (e == 1) __builtin_amdgcn_s_barrier();
      if (e * 3 + 3 > PFD) VMCNT(PFD);
    }
#pragma unroll
    for (int e = 0; e < NRP * 2; ++e) {
      until(t0, due_k((NLP * 2 + e) * 2 + tw) - TLEAD);
      tile_load(pk[e], kb, poff((NLP + (e >> 1)) * 4 + tw) + lo, e & 1);
      MEMFENCE();
      if (e + 1 < NRP * 2) VMCNT(PFD);
    }
    if (tw == 0) {
      r0 = ld32(rs, WS_HR + bid * 2);
      if (bid == 0) {
        u32x4 n;
#pragma unroll
        for (int k = 0; k < 4; ++k) n[k] = ld32(rs, WS_HA + (lane * 4 + k) * 32) + 1u;
        st128(rs, WS_HN + lane * 4, n);
      }
      const u32x4 z = {0u, 0u, 0u, 0u};
      if (lane < 48) {
#pragma unroll
        for (int it = NA; it < NS; ++it) st128(rs, WS_PO + xb * DH + lane * 4, z);
      }
      else st128(rs, WS_PML + (g * GS + mm) * 64 + (lane - 48) * 4, z);
      if (isG && g_cg == 0 && lane < 8) st128(rs, WS_ST + lane * 4, z);
      if (isW) st128(rs, WS_SLAB + (w_ks * NB + (lane >> 4)) * DM + w_ct * 64 + (lane & 15) * 4, z);
      else if (isG) { if (lane < 16) st128(rs, WS_ACT + g_cg * 64 + lane * 4, z); }
      else if (isH) {
#pragma unroll
        for (int k = 0; k < 3; ++k) st128(rs, WS_H + (xl - 2) * 768 + k * 256 + lane * 4, z);
      }
      else if (isC && lane < 48) st128(rs, WS_OC + (c_it * NG + g) * DH + lane * 4, z);
    }
    __builtin_amdgcn_s_setprio(0);
    lds_wait<1>(s_c + F_Q, HPG);
    f32x2 q[12];
    q_load(q, s_q, j);
    const u32x4* s_kv = reinterpret_cast<const u32x4*>(smem + OFF_REG + tw * REG) + lane;
    static_assert(NLP * 4 <= PPI && NTP <= HPG * PPI, "tail tiles: batch row 0 only");
#pragma unroll 1
    for (int e = 0; e < NLP * 2; ++e) {
      Tile tmp;
#pragma unroll
      for (int i = 0; i < 3; ++i) tmp.d[i] = s_kv[(e * 3 + i) * 64];
      score_put(tile_score(tmp, q), s_sc + ((e >> 1) * 4 + tw) * 16 + (e & 1) * 8 + r, j);
    }
#pragma unroll
    for (int e = 0; e < NRP * 2; ++e) {
      if ((NLP * 2 + e) * 2 == PPI) q_load(q, s_q + DH, j);
      score_put(tile_score(pk[e], q), s_sc + ((NLP + (e >> 1)) * 4 + tw) * 16 + (e & 1) * 8 + r, j);
      __builtin_amdgcn_sched_barrier(0);
    }
    asm volatile("s_waitcnt vmcnt(0) lgkmcnt(0)" ::: "memory");
#pragma unroll
    for (int it = 0; it * PPI < NTP; ++it) lds_add(s_c + C_SC + it, lane);
  }

  const int mr = lane & 15, qd = lane >> 4;
  unsigned char* reg = smem + OFF_REG + tw * REG;
  float* red = reinterpret_cast<float*>(smem + OFF_RED);
#define PERM_LO(hi_, lo_) __builtin_amdgcn_perm(hi_, lo_, 0x05040100u)
#define PERM_HI(hi_, lo_) __builtin_amdgcn_perm(hi_, lo_, 0x07060302u)
  int served = 0;
  auto done = [&](int n) {
    if (tw == 3 && served < min(n, NA)) {
      const int from = served;
      int ln = lane;
      asm volatile("" : "+v"(ln));
#pragma unroll 1
      for (; served < min(n, NA); ++served) {
        lds_wait<8>(s_c + C_PART + served, NMW);
        const f32x4 o = merged(served, ln < DH / 4 ? ln : 0);
        const float am = max_x16_32(row_max(fmaxf(fmaxf(fabsf(o[0]), fabsf(o[1])), fmaxf(fabsf(o[2]), fabsf(o[3])))));
        const float inv = am > 1e-30f ? 32767.0f / am : 0.f;
        const int q0 = __float2int_rn(o[0] * inv), q1 = __float2int_rn(o[1] * inv), q2 = __float2int_rn(o[2] * inv), q3 = __float2int_rn(o[3] * inv);
        if (ln < DH / 4) reinterpret_cast<u32x2*>(s_sc + served * (DH / 2))[ln] = u32x2{PERM_LO((unsigned)q1, (unsigned)q0), PERM_LO((unsigned)q3, (unsigned)q2)};
        const bool nan = __any((o[0] + o[1]) + (o[2] + o[3]) != (o[0] + o[1]) + (o[2] + o[3]));
        if (ln == 0) s_ml[served][2] = nan ? 0x7fc00000u : nz_bits(am * (1.0f / 32767.0f));
        LGKM0();
        if (ln == 0) s_c[F_FREE] = served + 1;
      }
#pragma unroll 1
      for (int c = from * 24 + ln; c < served * 24; c += 64) st128nt(rs, WS_PQ + xb * 5 * (DH / 2) + c * 4, reinterpret_cast<const u32x4*>(s_sc)[c]);
      VMCNT(0);
      if (ln < 8) st128(rs, WS_PML + xb * 64 + (from ? 32 : 0) + ln * 4, s_ml[ln]);
      if (ln == 0) __hip_atomic_fetch_add(s_c + F_DONE, (unsigned)(served - from), __ATOMIC_RELAXED, __HIP_MEMORY_SCOPE_WORKGROUP);
    }
    if (n > NA) lds_wait<1>(s_c + F_DONE, n); else lds_wait<8>(s_c + F_DONE, n);
  };
  auto done_all = [&]() {
#pragma unroll 1
    for (int n = 4; n <= NA; ++n) done(n);
  };
  const int lp = (lane < DH / 4 ? lane : 0) * 4;
  auto weight = [&](int it, int x0, int cnt) -> float {
    const int mlw = WS_PML + (x0 + (lane & (cnt - 1))) * 64 + (it < 4 ? 0 : 32) + it * 4;
    u32x4 ml;
#pragma unroll 1
    for (unsigned spin = 0; spin < SPIN_CAP; ++spin) {
      ml = ld128(rs, mlw);
      if (__all(min4(ml) != 0u)) break;
      __builtin_amdgcn_s_sleep(16);
    }
    const float mk = __uint_as_float(ml[0]);
    const float e = __builtin_amdgcn_exp2f(mk - max_x16_32(row_max(mk)));
    return e / (wave_sum(e * __uint_as_float(ml[1])) * ((float)cnt * (1.0f / 64.0f))) * __uint_as_float(ml[2]);
  };
  auto prep = [&]() {
    if (tw == 0) {
      const unsigned tok = __builtin_amdgcn_readfirstlane(r0) + 1u;
      const int c = lane & (GS - 1);
      unsigned echoed = 0u, qe = s_c[S_ECHO + c];
      bool first = true, released = false, sent = false;
#pragma unroll 1
      while ((int)(clk() - t0) < HST) {
        const unsigned qn = ld32(rs, qf + (GS + c) * 32);
        if (mm < NPROD && __any(qn != qe)) {
          if (lane < 32) st32(rs, qf + mm * 32 + lane, qn);
          qe = qn;
        }
        __builtin_amdgcn_s_sleep(64);
      }
      int slow = 0;
      auto lag = [&]() { return (lds_get(s_c + S_MOVED + 4) + lds_get(s_c + S_MOVED + 5) + lds_get(s_c + S_MOVED + 6) + lds_get(s_c + S_MOVED + 7)) >> 2; };
      const unsigned behind = lag();
#pragma unroll 1
      for (unsigned spin = 0; spin < SPIN_CAP && !released; ++spin) {
        const unsigned qn = ld32(rs, qf + (GS + c) * 32);
        const u32x2 rv = ld64(rs, WS_HR + bid * 2);
        const unsigned nv = __builtin_amdgcn_readfirstlane(ld32(rs, WS_HN + bid));
        if (__builtin_amdgcn_readfirstlane(rv[0]) - tok <= NRD) {
          released = true;
          slow = __builtin_amdgcn_readfirstlane(rv[1]) >> 16;
          if (lane == 0) { s_c[S_FIRST] = min((t0 - rv[1]) & 0xffffu, 300u) + 1u; s_c[S_SLOW] = slow; }
        } else {
          if (mm < NPROD && __any(qn != qe)) {
            if (lane < 32) st32(rs, qf + mm * 32 + lane, qn);
            qe = qn;
          }
          if (first || nv != echoed) {
            echoed = nv; first = false;
            if (lane < 8) st128(rs, WS_HA + bid * 32 + lane * 4, u32x4{echoed, tok, t0, behind});
          }
          if (bid == 0 && !sent) sent = leader_release(rs, lane, t0, 0u);
          __builtin_amdgcn_s_sleep(32);
        }
      }
      if (lane == 0) s_c[F_REL] = 1u;
      if (lane < DH / 4) {
        u32x4 p[3][2];
#pragma unroll
        for (int e = 0; e < 3; ++e)
#pragma unroll
          for (int k = 0; k < 2; ++k) p[e][k] = ld128nt(rs, WS_QP + (((e ? 8 + e * 8 + (xb >> 5) : xb >> 4) * 2 + k) * NB + 1) * DH + lane * 4);
#pragma unroll
        for (int e = 0; e < 3; ++e) *reinterpret_cast<f32x4*>(&s_q[(1 + e) * DH + lane * 4]) = (__builtin_bit_cast(f32x4, p[e][0]) + __builtin_bit_cast(f32x4, p[e][1])) * QSCALE;
      }
      LGKM0();
      if (lane == 0) s_c[F_Q2] = 3u;
      unsigned prev = slow;
#pragma unroll 1
      for (unsigned rd = 1; rd <= NRD; ++rd) {
        const int tr = RT0 + (int)(rd - 1) * RTS;
        until(t0, tr);
        const unsigned bh = lag();
        if (bh > RMIN && lane < 8) st128(rs, WS_HA + bid * 32 + lane * 4, u32x4{echoed + rd, tok, t0, bh});
        if (bid == 0) { until(t0, tr + 220); leader_release(rs, lane, t0, rd); }
        until(t0, tr + 500);
        const u32x2 rv = ld64(rs, WS_HR + bid * 2);
        if (__builtin_amdgcn_readfirstlane(rv[0]) != tok + rd) {
          prev = 0u;
          const int fl = -(int)min(min(lds_get(s_c + S_MOVED), lds_get(s_c + S_MOVED + 1)), min(lds_get(s_c + S_MOVED + 2), lds_get(s_c + S_MOVED + 3)));
          slow = slow > fl ? max(slow - GB, fl) : slow - (rd <= PRD ? PRB : 0);
          if (lane == 0) s_c[S_SLOW] = slow;
        } else {
          const unsigned ex = __builtin_amdgcn_readfirstlane(rv[1]) >> 16;
          slow += ex + min(ex, prev) * (rd == NRD ? FBL : FBP) / 100u;
          prev = ex;
          if (lane == 0) s_c[S_SLOW] = slow;
        }
      }
    }
    if (tw == 3) {
#pragma unroll 1
      for (int ik = 0; ik < NS; ++ik) {
        const int it = seg_of_k(ik), nv = (seg_beg(it + 1) - seg_beg(it)) >> 4;
        lds_wait<4>(s_c + C_SC + ik, ik == 0 ? NW : NMW);
        f32x4* sp = reinterpret_cast<f32x4*>(s_sc + seg_beg(it) * 16) + lane;
        f32x4 v[4];
#pragma unroll
        for (int k = 0; k < 4; ++k) v[k] = sp[(k & (nv - 1)) * 64];
        const f32x4 mx = __builtin_elementwise_max(__builtin_elementwise_max(v[0], v[1]), __builtin_elementwise_max(v[2], v[3]));
        const float m = max_x16_32(row_max(fmaxf(fmaxf(mx[0], mx[1]), fmaxf(mx[2], mx[3]))));
        float l = 0.f;
#pragma unroll
        for (int k = 0; k < 4; ++k) {
#pragma unroll
          for (int e = 0; e < 4; ++e) v[k][e] = __builtin_amdgcn_exp2f(v[k][e] - m);
          if (k < nv) {
            l += (v[k][0] + v[k][1]) + (v[k][2] + v[k][3]);
            sp[k * 64] = v[k];
          }
        }
        l = wave_sum(l);
        if (lane == 0) s_ml[it] = u32x4{nz_bits(m), nz_bits(l), 0x3f800000u, 0x3f800000u};
        LGKM0();
        if (lane == 0) s_c[F_P] = ik + 1;
      }
    }
  };
  auto pull = [&]() {
    typedef __attribute__((address_space(1))) const unsigned g_u32;
    const int id = (ro - 16) * 4 + tw;
    constexpr int RW = 160 - 32 * GE, LW = RW * 4 * 3 * 2 / 64;
    g_u32* p1[2];
#pragma unroll
    for (int i = 0; i < 2; ++i) {
      const int l = id * LW + min(i * 32 + (lane >> 1), max(LW - 1, 0)), m = l >= LW * 32 ? l - LW * 32 : l, rr = m / 3;
      p1[i] = (g_u32*)((l >= LW * 32 ? wu : wg) + (size_t)(rr / max(RW, 1) * 160 + 32 * GE + rr % max(RW, 1)) * FF + xl * 192 + (m % 3) * 64 + (lane & 1) * 32);
    }
    g_u32* p2 = (g_u32*)(wd + (size_t)(id * 24 + min(lane >> 1, 23)) * DM + xl * 64 + (lane & 1) * 32);
    asm volatile("" : "+v"(p1[0]), "+v"(p1[1]), "+v"(p2));
    if (GLEAD) lds_wait<1>(s_c + C_LAST, GCNT); else done(NS);
    const unsigned tp = clk() + PW + tw * PS;
    until(tp, 0);
    const unsigned v0 = LW > 0 ? p1[0][0] : 0u;
    MEMFENCE();
    until(tp, 4 * PS);
    const unsigned v1 = LW > 32 ? p1[1][0] : 0u;
    MEMFENCE();
    until(tp, DPW);
    const unsigned v2 = p2[0], v3 = p2[xl < 2 ? 256 : 0];
    MEMFENCE();
    return (v0 ^ v1) + (v2 ^ v3);
  };
  if (isW) {
    u32x4 bx[24];
    const char* p = reinterpret_cast<const char*>(wo + (size_t)((w_ks * 4 + tw) * DH + r) * DM + w_ct * 64 + j * 8);
#pragma unroll 1
    for (int i = 0; i < 24; ++i) { until(t0, tw == 0 ? W0START + i * (W0SPAN / 24) : W1START + i * (W1SPAN / 24)); glds16_nt(p, lds_base + i * 1024); p += 8 * DM * 2; VMCNT(WFD); }
    asm volatile("s_waitcnt vmcnt(0)" ::: "memory");
#pragma unroll
    for (int s = 0; s < 6; ++s) {
      u32x2 e[8];
#pragma unroll
      for (int k = 0; k < 8; ++k) e[k] = *reinterpret_cast<const u32x2*>(reg + (s * 32 + qd * 8 + k) * 128 + mr * 8);
#pragma unroll
      for (int k = 0; k < 4; ++k) {
        bx[s * 4 + 0][k] = PERM_LO(e[2 * k + 1][0], e[2 * k][0]); bx[s * 4 + 1][k] = PERM_HI(e[2 * k + 1][0], e[2 * k][0]);
        bx[s * 4 + 2][k] = PERM_LO(e[2 * k + 1][1], e[2 * k][1]); bx[s * 4 + 3][k] = PERM_HI(e[2 * k + 1][1], e[2 * k][1]);
      }
    }
#pragma unroll
    for (int s = 0; s < 24; s += 4) asm volatile("" : "+v"(bx[s]), "+v"(bx[s + 1]), "+v"(bx[s + 2]), "+v"(bx[s + 3]));
    LGKM0();
    prep();
    lds_wait<8>(s_c + F_REL, 1u);
    const int hh = w_ks * 4 + tw;
#pragma unroll 1
    for (int pass = 0; pass < 3; ++pass) {
      if (pass < 2) {
        done(pass ? NA : 4);
        pause(pass ? WDELAY2 : WDELAY2 + 300);
        const int b0 = pass * 2, b1 = pass + 1;
        u32x4 v0, v1;
#pragma unroll 1
        for (unsigned spin = 0; spin < SPIN_CAP; ++spin) {
          v0 = ld128(rs, WS_OC + (b0 * NH + hh) * DH + lp);
          v1 = ld128(rs, WS_OC + (b1 * NH + hh) * DH + lp);
          if (__all(min(min4(v0), min4(v1)) != 0u)) break;
          __builtin_amdgcn_s_sleep(16);
        }
        if (lane < DH / 4) {
          *reinterpret_cast<u32x2*>(reg + (b0 * 4) * AW_LD + lane * 8) = u32x2{v0[0], v0[1]};
          *reinterpret_cast<u32x2*>(reg + (b0 * 4 + 1) * AW_LD + lane * 8) = u32x2{v0[2], v0[3]};
          *reinterpret_cast<u32x2*>(reg + (b1 * 4) * AW_LD + lane * 8) = u32x2{v1[0], v1[1]};
          *reinterpret_cast<u32x2*>(reg + (b1 * 4 + 1) * AW_LD + lane * 8) = u32x2{v1[2], v1[3]};
        }
      } else {
        const float wk = weight(NS - 1, hh * GS, GS);
        float w[GS];
#pragma unroll
        for (int k = 0; k < GS; ++k) { w[k] = __uint_as_float(__builtin_amdgcn_readlane(__float_as_uint(wk), k)); asm volatile("" : "+s"(w[k])); }
        done(NS);
        __builtin_amdgcn_s_setprio(3);
        const int po = WS_PO + hh * GS * DH;
        u32x4 pv[GS];
#pragma unroll 1
        for (unsigned spin = 0; spin < SPIN_CAP; ++spin) {
#pragma unroll
          for (int k = 0; k < GS; ++k) pv[k] = __builtin_amdgcn_raw_buffer_load_b128(rs, lp * 4, (po + k * DH) * 4, AUX_SC1);
          unsigned mn = min4(pv[0]);
#pragma unroll
          for (int k = 1; k < GS; ++k) mn = min(mn, min4(pv[k]));
          if (__all(mn != 0u)) break;
          __builtin_amdgcn_s_sleep(1);
        }
        f32x4 o = w[0] * __builtin_bit_cast(f32x4, pv[0]), o2 = w[1] * __builtin_bit_cast(f32x4, pv[1]);
#pragma unroll
        for (int k = 2; k < GS; k += 2) { o += w[k] * __builtin_bit_cast(f32x4, pv[k]); o2 += w[k + 1] * __builtin_bit_cast(f32x4, pv[k + 1]); }
        u32x2 hi, lw;
        split4(o + o2, hi, lw);
        if (lane < DH / 4) {
          *reinterpret_cast<u32x2*>(reg + ((NB - 1) * 4) * AW_LD + lane * 8) = hi;
          *reinterpret_cast<u32x2*>(reg + ((NB - 1) * 4 + 1) * AW_LD + lane * 8) = lw;
        }
      }
      CFENCE();
      u32x4 a[6];
#pragma unroll
      for (int s = 0; s < 6; ++s) a[s] = *reinterpret_cast<const u32x4*>(reg + mr * AW_LD + s * 64 + qd * 16);
      __builtin_amdgcn_sched_barrier(0);
      f32x4 c[4];
#pragma unroll
      for (int t = 0; t < 4; ++t) c[t] = f32x4{0.f, 0.f, 0.f, 0.f};
#pragma unroll
      for (int s = 0; s < 6; ++s)
#pragma unroll
        for (int t = 0; t < 4; ++t) c[t] = mfma16(a[s], bx[s * 4 + t], c[t]);
      const bool mine = max(qd - 1, 0) == pass;
      f32x4* r4 = reinterpret_cast<f32x4*>(red);
      if (mine) r4[tw * 64 + lane] = f32x4{c[0][0] + c[0][1], c[1][0] + c[1][1], c[2][0] + c[2][1], c[3][0] + c[3][1]};
      const int k = arrive(s_c + C_W + pass);
      f32x4 sum = (r4[lane] + r4[64 + lane]) + (r4[128 + lane] + r4[192 + lane]);
      asm volatile("" : "+v"(sum));
      if (arrived(k) == 3) {
        if (mine) st128(rs, WS_SLAB + (w_ks * NB + qd) * DM + w_ct * 64 + mr * 4, u32x4{nz_bits(sum[0]), nz_bits(sum[1]), nz_bits(sum[2]), nz_bits(sum[3])});
      }
    }
  } else if (isG || isH) {
    f32x4* rst = reinterpret_cast<f32x4*>(red);
    const int hb = isH ? xl - 2 : NB - 1;
    uint2 wr4 = make_uint2(0u, 0u), xg = wr4;
    if (lane < 40) {
      wr4 = *reinterpret_cast<const uint2*>(wr + tw * 160 + lane * 4);
      xg = *reinterpret_cast<const uint2*>(x + row_of(hb) * DM + tw * 160 + lane * 4);
    }
    const int eb = mr >> 2;
    const float cq = bf_lo(*reinterpret_cast<const unsigned*>(x + row_of(eb) * DM));
    const float wsum = wave_sum((bf_lo(wr4.x) + bf_hi(wr4.x)) + (bf_lo(wr4.y) + bf_hi(wr4.y)));
    f32x4 xc = f32x4{bf_lo(xg.x), bf_hi(xg.x), bf_lo(xg.y), bf_hi(xg.y)} - __uint_as_float(__builtin_amdgcn_readlane(__float_as_uint(cq), hb * 4));
    asm volatile("" : "+v"(xc));
    typedef __attribute__((address_space(1))) const u32x4 g_u32x4;
    g_u32x4* wp[10];
    wp[0] = (g_u32x4*)(((mr & 4) ? wu : wg) + (size_t)(tw * 160 + qd * 8 + (mr >> 3) * 4) * FF + g_cg * 32 + (mr & 3) * 8);
    u32x4 e[20], bx[20];
    auto frag = [&](int s) __attribute__((always_inline)) {
      unsigned w[8][2];
#pragma unroll
      for (int i = 0; i < 4; ++i)
#pragma unroll
        for (int d = 0; d < 2; ++d) {
          w[i][d] = __builtin_amdgcn_update_dpp(e[s * 4 + i][d], e[s * 4 + i][2 + d], DPP_ROR8, 0xf, 0xc, false);
          w[4 + i][d] = __builtin_amdgcn_update_dpp(e[s * 4 + i][2 + d], e[s * 4 + i][d], DPP_ROR8, 0xf, 0x3, false);
        }
#pragma unroll
      for (int k = 0; k < 4; ++k) {
        bx[s * 4 + 0][k] = PERM_LO(w[2 * k + 1][0], w[2 * k][0]); bx[s * 4 + 1][k] = PERM_HI(w[2 * k + 1][0], w[2 * k][0]);
        bx[s * 4 + 2][k] = PERM_LO(w[2 * k + 1][1], w[2 * k][1]); bx[s * 4 + 3][k] = PERM_HI(w[2 * k + 1][1], w[2 * k][1]);
      }
      PIN4(bx, s * 4);
    };
    if (isG) {
#pragma unroll
      for (int i = 0; i < 4 * GE; ++i) {
        until(t0, tw == 0 ? W0START + i * (W0SPAN / max(4 * GE, 1)) : W1START + i * (W1SPAN / max(4 * GE, 1)));
        e[i] = __builtin_nontemporal_load(wp[0] + ((i >> 2) * 32 + (i & 3)) * (FF / 8));
        MEMFENCE();
      }
#pragma unroll
      for (int s = 0; s < GE; ++s) frag(s);
    }
    prep();
    lds_wait<8>(s_c + F_REL, 1u);
    auto hrow = [&](bool fast) -> f32x4 {
      f32x4 hv = {0.f, 0.f, 0.f, 0.f};
      if (lane < 40) {
        const int off = (WS_SLAB + hb * DM + tw * 160 + lane * 4) * 4;
        u32x4 sl[KS];
#pragma unroll 1
        for (unsigned spin = 0; spin < SPIN_CAP; ++spin) {
#pragma unroll
          for (int ks = 0; ks < KS; ++ks) sl[ks] = __builtin_amdgcn_raw_buffer_load_b128(rs, off, ks * (NB * DM * 4), AUX_SC1);
          unsigned mn = min4(sl[0]);
#pragma unroll
          for (int ks = 1; ks < KS; ++ks) mn = min(mn, min4(sl[ks]));
          if (!__any(mn == 0u)) break;
          if (fast) __builtin_amdgcn_s_sleep(1); else __builtin_amdgcn_s_sleep(16);
        }
#define SL(k) __builtin_bit_cast(f32x4, sl[k])
        hv = xc + (((SL(0) + SL(1)) + (SL(2) + SL(3))) + ((SL(4) + SL(5)) + (SL(6) + SL(7))));
#undef SL
      }
      return hv;
    };
    auto sums = [&](f32x4 hv) -> f32x4 {
      const float s1 = wave_sum((hv[0] + hv[1]) + (hv[2] + hv[3]));
      const float s2 = wave_sum((hv[0] * hv[0] + hv[1] * hv[1]) + (hv[2] * hv[2] + hv[3] * hv[3]));
      const float dot = wave_sum((hv[0] * bf_lo(wr4.x) + hv[1] * bf_hi(wr4.x)) + (hv[2] * bf_lo(wr4.y) + hv[3] * bf_hi(wr4.y)));
      return f32x4{s1, s2, dot, wsum};
    };
    if (isH) {
      if (tw == 3 || hb == 2) done_all(); else done(4);
      if (tw != 3 || hb == 2) pause(hb < 2 ? HDELAY : HDELAY2);
      const f32x4 hv = hrow(false), sm = sums(hv);
      u32x4 v = split8(hv, (lane & 1) != 0);
      if (lane >= 40) v = u32x4{nz_bits(sm[0]), nz_bits(sm[1]), nz_bits(sm[2]), nz_bits(sm[3])};
      if (lane < 48) st128(rs, WS_H + (hb * 4 + tw) * 192 + lane * 4, v);
      done_all();
      const unsigned pl = pull();
      asm volatile("" :: "v"(pl));
      return;
    }
    done_all();
#pragma unroll
    for (int i = 2 * GE; i < 10; ++i) { wp[i] = wp[0] + ((i >> 1) * 32 + (i & 1) * 2) * (FF / 8); asm volatile("" : "+v"(wp[i])); }
    done(NS);
    if (GW) until(clk(), GW);
#pragma unroll
    for (int i = 4 * GE; i < 20; ++i) e[i] = wp[i >> 1][(i & 1) * (FF / 8)];
    u32x4 a[5], sm;
    const int hoff = (WS_H + (min(mr >> 2, NB - 2) * 4 + tw) * 192 + qd * 8 + (mr & 1) * 4) * 4;
#pragma unroll
    for (int s = 0; s < 5; ++s) a[s] = __builtin_amdgcn_raw_buffer_load_b128(rs, hoff + s * 128, 0, AUX_SC1);
    sm = ld128(rs, WS_H + (min(lane, NB - 2) * 4 + tw) * 192 + 160);
    MEMFENCE();
#pragma unroll
    for (int s = GE; s < 5; ++s) frag(s);
    {
#pragma unroll 1
      for (unsigned spin = 0; spin < SPIN_CAP; ++spin) {
        unsigned mn = min4(sm);
#pragma unroll
        for (int s = 0; s < 5; ++s) mn = min(mn, min4(a[s]));
        if (__all(mn != 0u)) break;
        __builtin_amdgcn_s_sleep(2);
#pragma unroll
        for (int s = 0; s < 5; ++s) a[s] = __builtin_amdgcn_raw_buffer_load_b128(rs, hoff + s * 128, 0, AUX_SC1);
        sm = ld128(rs, WS_H + (min(lane, NB - 2) * 4 + tw) * 192 + 160);
      }
      if (lane < NB - 1) rst[lane * 4 + tw] = __builtin_bit_cast(f32x4, sm);
#pragma unroll
      for (int s = 0; s < 5; ++s) {
        if (mr & 2) a[s] = u32x4{0x3f803f80u, 0x3f803f80u, 0x3f803f80u, 0x3f803f80u};
        asm volatile("" : "+v"(a[s]));
      }
    }
    const f32x4 hv = hrow(true);
    if (lane < 40) {
      u32x2 hi, lw;
      split4(hv, hi, lw);
      *reinterpret_cast<u32x2*>(reg + lane * 8) = hi;
      *reinterpret_cast<u32x2*>(reg + AG_LD + lane * 8) = lw;
    }
    CFENCE();
    if ((mr & 14) == 12) {
#pragma unroll
      for (int s = 0; s < 5; ++s) a[s] = *reinterpret_cast<const u32x4*>(reg + (mr & 1) * AG_LD + s * 64 + qd * 16);
    }
    __builtin_amdgcn_sched_barrier(0);
    f32x4 c[4];
#pragma unroll
    for (int t = 0; t < 4; ++t) c[t] = f32x4{0.f, 0.f, 0.f, 0.f};
#pragma unroll
    for (int s = 0; s < 5; ++s)
#pragma unroll
      for (int t = 0; t < 4; ++t) c[t] = mfma16(a[s], bx[s * 4 + t], c[t]);
    { const f32x4 s3 = sums(hv); if (lane == 0) rst[(NB - 1) * 4 + tw] = s3; }
    const int slot = qd * 16 + (mr & 7) * 2 + (mr >> 3);
    *reinterpret_cast<f32x4*>(reg + 4096 + slot * 16) = f32x4{c[0][0] + c[0][1], c[1][0] + c[1][1], c[0][2], c[1][2]};
    *reinterpret_cast<f32x4*>(reg + 4096 + 1152 + slot * 16) = f32x4{c[2][0] + c[2][1], c[3][0] + c[3][1], c[2][2], c[3][2]};
    const int k = arrive(s_c + C_T);
    f32x4 ts[4], pg[4], pu[4];
#pragma unroll
    for (int w = 0; w < 4; ++w) {
      const unsigned char* src = smem + OFF_REG + w * REG + 4096 + (lane & 1) * 1152 + (eb * 16 + qd * 2 + ((lane >> 1) & 1)) * 16;
      ts[w] = rst[eb * 4 + w];
      pg[w] = *reinterpret_cast<const f32x4*>(src); pu[w] = *reinterpret_cast<const f32x4*>(src + 128);
    }
    PIN4(ts, 0); PIN4(pg, 0); PIN4(pu, 0);
    if (arrived(k) == 3) {
      const f32x4 t = (ts[0] + ts[1]) + (ts[2] + ts[3]), g4 = (pg[0] + pg[1]) + (pg[2] + pg[3]), u4 = (pu[0] + pu[1]) + (pu[2] + pu[3]);
      const float d = t[0] * (-1.0f / DM);
      const float rstd = __builtin_amdgcn_rsqf(fmaxf(t[1] + d * t[0], 0.f) * (1.0f / DM) + LN_EPS);
      const float g0 = rstd * (g4[0] + d * g4[2]), g1 = rstd * (g4[1] + d * g4[3]);
      const float a0 = g0 * __builtin_amdgcn_rcpf(1.0f + __expf(-g0)) * (rstd * (u4[0] + d * u4[2]));
      const float a1 = g1 * __builtin_amdgcn_rcpf(1.0f + __expf(-g1)) * (rstd * (u4[1] + d * u4[3]));
      const __hip_bfloat162 a2 = __halves2bfloat162(__float2bfloat16(a0), __float2bfloat16(a1));
      const float aw = __uint_as_float(max(*reinterpret_cast<const unsigned*>(&a2), 1u));
      const u32x4 av = {__float_as_uint(aw), __float_as_uint(dpp<0x55>(aw)), __float_as_uint(dpp<0xAA>(aw)), __float_as_uint(dpp<0xFF>(aw))};
      const float rr = rstd * (t[2] + d * t[3]);
      const u32x4 sv = {nz_bits(cq - d), nz_bits(rstd), nz_bits(__builtin_amdgcn_rcpf(1.0f + __expf(-rr))), 0x3f800000u};
      const bool stat = g_cg == 0 && (lane & 51) == 1;
      if ((lane & 3) == 0 || stat) st128(rs, stat ? WS_ST + eb * 4 : WS_ACT + g_cg * 64 + lane, stat ? sv : av);
    }
  } else if (isD) {
    const uint2 xd = *reinterpret_cast<const uint2*>(x + row_of(tw) * DM + d_ct * 32 + j * 4);
    const __amdgpu_buffer_rsrc_t rd = __builtin_amdgcn_make_buffer_rsrc(const_cast<bf16*>(wd), 0, FF * DM * 2, 0x00020000);
    prep();
    lds_wait<8>(s_c + F_REL, 1u);
    done_all();
    const unsigned pl = pull();
    done(NS);
    asm volatile("" :: "v"(pl));
    const unsigned te = clk();
    if (DW) until(te, DW);
    u32x4 bd[24];
    {
      const int off = ((tw * 384 + (mr >> 3) * 32 + qd * 8 + (mr & 4)) * DM + d_ct * 32 + (mr & 3) * 8) * 2;
      u32x4 e[24];
#pragma unroll
      for (int i = 0; i < 24; ++i) e[i] = __builtin_amdgcn_raw_buffer_load_b128(rd, off + (i & 3) * (DM * 2), (i >> 2) * 64 * (DM * 2), 0);
      MEMFENCE();
#pragma unroll
      for (int s = 0; s < 6; ++s) {
        unsigned w[8][2];
#pragma unroll
        for (int i = 0; i < 4; ++i)
#pragma unroll
          for (int d = 0; d < 2; ++d) {
            w[i][d] = __builtin_amdgcn_update_dpp(e[s * 4 + i][d], e[s * 4 + i][2 + d], 0x114, 0xf, 0xa, false);
            w[4 + i][d] = __builtin_amdgcn_update_dpp(e[s * 4 + i][2 + d], e[s * 4 + i][d], 0x104, 0xf, 0x5, false);
          }
#pragma unroll
        for (int k = 0; k < 4; ++k) {
          bd[s * 4 + 0][k] = PERM_LO(w[2 * k + 1][0], w[2 * k][0]); bd[s * 4 + 1][k] = PERM_HI(w[2 * k + 1][0], w[2 * k][0]);
          bd[s * 4 + 2][k] = PERM_LO(w[2 * k + 1][1], w[2 * k][1]); bd[s * 4 + 3][k] = PERM_HI(w[2 * k + 1][1], w[2 * k][1]);
        }
        PIN4(bd, s * 4);
      }
    }
    until(te, DLATE);
    {
      u32x4 sl;
      unsigned spin = 0;
#pragma unroll 1
      for (; spin < SPIN_CAP; ++spin) {
        sl = ld128(rs, WS_SLAB + (r * NB + tw) * DM + d_ct * 32 + j * 4);
        if (__all(min4(sl) != 0u)) break;
        __builtin_amdgcn_s_sleep(2);
      }
      if (tw == NB - 1 && lane == 0) s_c[C_W + 1] = clk() + (spin ? DLATE2 : 0);
      f32x4 hv = {bf_lo(xd.x), bf_hi(xd.x), bf_lo(xd.y), bf_hi(xd.y)};
#pragma unroll
      for (int i = 0; i < 4; ++i) hv[i] += sum_r(__uint_as_float(sl[i]));
      if (lane < 8) reinterpret_cast<f32x4*>(red + 512 + tw * 32)[lane] = hv;
      LGKM0();
      lds_add(s_c + C_W, lane);
      lds_wait<1>(s_c + C_W, 4u);
    }
    const int col = (mr & 3) * 8 + (mr & 4) + (mr >> 3) * 2;
    f32x2 hv = *reinterpret_cast<const f32x2*>(red + 512 + qd * 32 + col);
    asm volatile("" : "+v"(hv));
    u32x4 a[6], st;
#pragma unroll
    for (int s = 0; s < 6; ++s) a[s] = u32x4{1u, 1u, 1u, 1u};
    const int aoff = (WS_ACT + tw * 12 * 64 + ((mr >> 1) & 1) * 64 + qd * 16 + (mr >> 2) * 4) * 4;
    until(lds_get(s_c + C_W + 1), 0);
#pragma unroll 1
    for (unsigned spin = 0; spin < SPIN_CAP; ++spin) {
      st = ld128(rs, WS_ST + qd * 4);
      if ((mr & 1) == 0) {
#pragma unroll
        for (int s = 0; s < 6; ++s) a[s] = __builtin_amdgcn_raw_buffer_load_b128(rs, aoff + s * 512, 0, AUX_SC1);
      }
      unsigned mn = min4(st);
#pragma unroll
      for (int s = 0; s < 6; ++s) mn = min(mn, min4(a[s]));
      if (__all(mn != 0u)) break;
      __builtin_amdgcn_s_sleep(1);
    }
    f32x4 c[4];
#pragma unroll
    for (int t = 0; t < 4; ++t) c[t] = f32x4{0.f, 0.f, 0.f, 0.f};
#pragma unroll
    for (int s = 0; s < 6; ++s)
#pragma unroll
      for (int t = 0; t < 4; ++t) c[t] = mfma16(a[s], bd[s * 4 + t], c[t]);
    const f32x4 sf = __builtin_bit_cast(f32x4, st);
    hv += (hv - sf[0]) * sf[1];
    const bool up = mr >= 8;
    const float k0 = up ? c[2][2] : c[0][0], k1 = up ? c[3][2] : c[1][0], o0 = up ? c[0][2] : c[2][0], o1 = up ? c[1][2] : c[3][0];
    f32x2* r2 = reinterpret_cast<f32x2*>(red);
    r2[tw * 64 + lane] = f32x2{k0 + dpp<DPP_ROR8>(o0), k1 + dpp<DPP_ROR8>(o1)};
    const int k = arrive(s_c + C_T);
    f32x2 y = hv + ((r2[lane] + r2[64 + lane]) + (r2[128 + lane] + r2[192 + lane])) * sf[2];
    asm volatile("" : "+v"(y));
    if (arrived(k) == 3) {
      const __hip_bfloat162 o2 = __halves2bfloat162(__float2bfloat16(y[0]), __float2bfloat16(y[1]));
      __hip_atomic_store(reinterpret_cast<unsigned*>(out + row_of(qd) * DM + d_ct * 32 + col), *reinterpret_cast<const unsigned*>(&o2), __ATOMIC_RELAXED, __HIP_MEMORY_SCOPE_AGENT);
    }
  } else {
    prep();
    if (isC && tw == 1) {
      lds_wait<8>(s_c + F_REL, 1u);
      const int cs = c_it != 1 ? c_it * 2 : max(g >> 3, 1), cnt = c_it != 1 ? 8 : (g < 16 ? 16 : 32), x0 = c_it != 1 ? g * 8 : (g < 16 ? g * 16 : (g & 7) * 32);
      done(c_it < 2 ? 4 : NA);
      pause(WDELAY);
      const float wk = weight(cs, x0, cnt);
      f32x4 o = {0.f, 0.f, 0.f, 0.f};
#pragma unroll 1
      for (int k0 = 0; k0 < cnt; k0 += 16) {
        u32x2 pv[16];
#pragma unroll
        for (int k = 0; k < 16; ++k) pv[k] = __builtin_amdgcn_raw_buffer_load_b64(rs, lp * 2, (WS_PQ + ((x0 + ((k0 + k) & (cnt - 1))) * 5 + cs) * (DH / 2)) * 4, AUX_EARLY);
#pragma unroll
        for (int k = 0; k < 16; ++k) {
          const float w = k0 + k < cnt ? __uint_as_float(__builtin_amdgcn_readlane(__float_as_uint(wk), k0 + k)) : 0.f;
          o += w * f32x4{(float)(short)pv[k][0], (float)((int)pv[k][0] >> 16), (float)(short)pv[k][1], (float)((int)pv[k][1] >> 16)};
        }
      }
      u32x2 hi, lw;
      split4(o, hi, lw);
      if (lane < DH / 4) st128(rs, WS_OC + (c_it * NG + g) * DH + lane * 4, u32x4{max(hi[0], 1u), max(hi[1], 1u), max(lw[0], 1u), max(lw[1], 1u)});
    }
    done_all();
    const unsigned pl = pull();
    asm volatile("" :: "v"(pl));
  }
  if (tw == 3) done_all();
}

extern "C" void kernel_launch(void* const* d_in, const int* in_sizes, int n_in, void* d_out, int out_size, void* d_ws,
                              size_t ws_size, hipStream_t stream) {
  const bf16* x = (const bf16*)d_in[0];
  const bf16* kc = (const bf16*)d_in[1];
  const bf16* vc = (const bf16*)d_in[2];
  const bf16* wq = (const bf16*)d_in[3];
  const bf16* wo = (const bf16*)d_in[4];
  const bf16* wr = (const bf16*)d_in[6];
  const bf16* wg = (const bf16*)d_in[7];
  const bf16* wu = (const bf16*)d_in[8];
  const bf16* wd = (const bf16*)d_in[9];
  fused_kernel<<<NWG, NW * 64, 0, stream>>>(x, kc, vc, wq, wo, wr, wg, wu, wd, (float*)d_ws, (bf16*)d_out);
}
```

```cpp
#include <hip/hip_runtime.h>
#include <hip/hip_bf16.h>
#include <stdint.h>

typedef __hip_bfloat16 bf16;
using u32x2 = __attribute__((ext_vector_type(2))) unsigned;
using u32x4 = __attribute__((ext_vector_type(4))) unsigned;
using f32x4 = __attribute__((ext_vector_type(4))) float;
using f32x2 = __attribute__((ext_vector_type(2))) float;
using bf16x8 = __attribute__((ext_vector_type(8))) __bf16;

constexpr int NB = 4, DM = 640, NH = 32, SK = 8192, DH = 192, HD = NH * DH, FF = 1536;
constexpr float LN_EPS = 1e-5f;
constexpr float QSCALE = 0.07216878364870322f * 1.4426950408889634f;

constexpr int NWG = 256;
constexpr int GS = 8, NG = NWG / GS, NI = GS / 2;
constexpr int HPG = NI / NB;
constexpr int PPI = 512 / GS;
constexpr int NPAIR = NI * PPI;

constexpr int TAUK = 1320, TAUV = 1332;
constexpr int TSTART = 140;
constexpr int WGAP = 230, WP0 = 64, WPN = 176;
constexpr int VGAP = 50;
constexpr int DEEP1 = 100, DEEP2 = 200, SDEEP = 0, COARSE = 1, LAGOFF = 20, ENDT = 12, ENDS = 2, PHASE = 0;
constexpr int SPRINT = 0;
constexpr int JUMP = 145;
constexpr int HST = 2800, RT0 = 3900, RTS = 1000;
constexpr unsigned NRD = 5, RMIN = 100, FBP = 60, FBL = 150;
constexpr int W0START = 1700, W0SPAN = 1000, W1START = 2700, W1SPAN = 2600;
constexpr int ROT = 16, ROWS = 0xC9;
constexpr int FBM = 100, FBX = 15, FB0 = 170, FB1 = 55, FBS = 10;
constexpr int FBD = 20, HOLD = 800, GB = 24, GBS = 2, GBX = 60, GBF = 12, FOLD = 1;
constexpr unsigned PRD = 3; constexpr int PRB = 24;
constexpr int TLEAD = 40, PFD = 6, WFD = 3;
constexpr int QWAIT = 750, QBACK = 100;
constexpr int WDELAY = 100, WDELAY2 = 460, DLATE = 100, DLATE2 = 115;
constexpr int CPF = 1, CPT = 0, CODE = 24 * 64 * 48;
constexpr int GLEAD = 1, GCNT = 4, DP = 0, DPW = 0, GE = 5, PW = 30, PS = 0, HDELAY = 1150, HDELAY2 = 780, GW = 0, DW = 0;

constexpr int KS = 8;
constexpr int WS_PML = 0;
constexpr int WS_PO = WS_PML + NWG * 64;
constexpr int WS_PQ = WS_PO + NWG * DH;
constexpr int WS_OC = WS_PQ + NWG * 5 * (DH / 2);
constexpr int WS_SLAB = WS_OC + (NB - 1) * NH * DH;
constexpr int WS_ACT = WS_SLAB + KS * NB * DM;
constexpr int WS_H = WS_ACT + NB * FF / 2;
constexpr int WS_ST = WS_H + (NB - 1) * 4 * 192;
constexpr int WS_HN = WS_ST + 32;
constexpr int WS_HR = WS_HN + NWG;
constexpr int WS_HA = WS_HR + 2 * NWG;
constexpr int QFS = 2 * GS * 32;
constexpr int WS_QF = WS_HA + 32 * NWG;
constexpr int WS_QP = WS_QF + NG * QFS;
constexpr int WS_END = WS_QP + NH * 2 * NB * DH;
constexpr int NPROD = HPG * 6;
static_assert(WS_PO % 32 == 0 && WS_SLAB % 32 == 0 && WS_ACT % 32 == 0 && WS_ST % 32 == 0 && WS_HN % 32 == 0 && WS_QF % 32 == 0 && WS_QP % 32 == 0, "line alignment");


__device__ __forceinline__ int row_of(int b) { return (ROWS >> (2 * b)) & 3; }
__device__ __forceinline__ float bf_lo(unsigned u) { return __uint_as_float(u << 16); }
__device__ __forceinline__ float bf_hi(unsigned u) { return __uint_as_float(u & 0xffff0000u); }
__device__ __forceinline__ f32x2 bf_pair(unsigned u) { return f32x2{bf_lo(u), bf_hi(u)}; }
__device__ __forceinline__ f32x2 fma2(float a, f32x2 b, f32x2 c) { return __builtin_elementwise_fma(f32x2{a, a}, b, c); }
__device__ __forceinline__ f32x2 fma2(f32x2 a, f32x2 b, f32x2 c) { return __builtin_elementwise_fma(a, b, c); }

template <int CTRL> __device__ __forceinline__ float dpp(float x) {
  return __builtin_bit_cast(float, __builtin_amdgcn_mov_dpp(__builtin_bit_cast(int, x), CTRL, 0xf, 0xf, true));
}
constexpr int DPP_XOR1 = 0xB1, DPP_XOR2 = 0x4E, DPP_HMIRROR = 0x141, DPP_MIRROR = 0x140, DPP_ROR8 = 0x128;

__device__ __forceinline__ float sum_x16_32(float x) {
  auto s = __builtin_amdgcn_permlane16_swap(__float_as_uint(x), __float_as_uint(x), false, false);
  x = __uint_as_float(s[0]) + __uint_as_float(s[1]);
  auto t = __builtin_amdgcn_permlane32_swap(__float_as_uint(x), __float_as_uint(x), false, false);
  return __uint_as_float(t[0]) + __uint_as_float(t[1]);
}
__device__ __forceinline__ float max_x16_32(float x) {
  auto s = __builtin_amdgcn_permlane16_swap(__float_as_uint(x), __float_as_uint(x), false, false);
  x = fmaxf(__uint_as_float(s[0]), __uint_as_float(s[1]));
  auto t = __builtin_amdgcn_permlane32_swap(__float_as_uint(x), __float_as_uint(x), false, false);
  return fmaxf(__uint_as_float(t[0]), __uint_as_float(t[1]));
}
__device__ __forceinline__ float tsum32(float a, float b) {
  auto t = __builtin_amdgcn_permlane32_swap(__float_as_uint(a), __float_as_uint(b), false, false);
  return __uint_as_float(t[0]) + __uint_as_float(t[1]);
}
__device__ __forceinline__ float tsum16(float a, float b) {
  auto t = __builtin_amdgcn_permlane16_swap(__float_as_uint(a), __float_as_uint(b), false, false);
  return __uint_as_float(t[0]) + __uint_as_float(t[1]);
}
__device__ __forceinline__ float sum_r(float v) { v += dpp<DPP_ROR8>(v); return sum_x16_32(v); }
__device__ __forceinline__ float row_sum(float v) {
  v += dpp<DPP_XOR1>(v); v += dpp<DPP_XOR2>(v); v += dpp<DPP_HMIRROR>(v);
  return v + dpp<DPP_MIRROR>(v);
}
__device__ __forceinline__ float row_max(float v) {
  v = fmaxf(v, dpp<DPP_XOR1>(v)); v = fmaxf(v, dpp<DPP_XOR2>(v)); v = fmaxf(v, dpp<DPP_HMIRROR>(v));
  return fmaxf(v, dpp<DPP_MIRROR>(v));
}
__device__ __forceinline__ float wave_sum(float v) {
  v += dpp<DPP_XOR1>(v); v += dpp<DPP_XOR2>(v); v += dpp<DPP_HMIRROR>(v);
  return sum_r(v);
}

__device__ __forceinline__ u32x4 ld_nt(const char* p) { return __builtin_nontemporal_load(reinterpret_cast<const u32x4*>(p)); }

struct Tile { u32x4 d[3]; };

#define PIN4(a, o) asm volatile("" : "+v"((a)[(o) + 0]), "+v"((a)[(o) + 1]), "+v"((a)[(o) + 2]), "+v"((a)[(o) + 3]))
#define MEMFENCE() do { asm volatile("" ::: "memory"); __builtin_amdgcn_sched_barrier(0); } while (0)

__device__ __forceinline__ void glds16_nt(const void* gsrc, unsigned lds_dst) {
  unsigned keep;
  asm volatile("s_mov_b32 %0, m0\n\ts_mov_b32 m0, %2\n\ts_nop 0\n\tglobal_load_lds_dwordx4 %1, off nt\n\ts_mov_b32 m0, %0"
               : "=&s"(keep) : "v"(gsrc), "s"(lds_dst) : "memory");
}

using bf16x2 = __attribute__((ext_vector_type(2))) __bf16;
__device__ __forceinline__ float dot2(unsigned a, unsigned b, float c) { return __builtin_amdgcn_fdot2_f32_bf16(__builtin_bit_cast(bf16x2, a), __builtin_bit_cast(bf16x2, b), c, false); }
__device__ __forceinline__ float tile_score(const Tile& k, const u32x2 (&q)[12]) {
  float s0 = 0.f, s1 = 0.f;
#pragma unroll
  for (int i = 0; i < 3; ++i) {
    u32x4 v = k.d[i];
    asm("" : "+v"(v));
#pragma unroll
    for (int d = 0; d < 4; ++d) { s0 = dot2(v[d], q[i * 4 + d][0], s0); s1 = dot2(v[d], q[i * 4 + d][1], s1); }
  }
  return s0 + s1;
}
__device__ __forceinline__ void pair_put(float a, float b, float* dst, int j) {
  float s = (j < 4 ? a : b) + dpp<DPP_HMIRROR>(j < 4 ? b : a);
  s += dpp<DPP_XOR1>(s);
  s += dpp<DPP_XOR2>(s);
  if ((j & 3) == 0) dst[j * 2] = s;
}
__device__ __forceinline__ void score_put(float s, float* dst, int j) {
  s += dpp<DPP_XOR1>(s);
  s += dpp<DPP_XOR2>(s);
  s += dpp<DPP_HMIRROR>(s);
  if (j == 0) *dst = s;
}
__device__ __forceinline__ void tile_load(Tile& t, const char* base, unsigned off, int half) {
#pragma unroll
  for (int i = 0; i < 3; ++i) t.d[i] = ld_nt(base + off + half * 3072 + i * 128);
}
__device__ __forceinline__ void pair_issue(Tile& ta, Tile& tb, const char* base, unsigned off, int deep) {
  deep = __builtin_amdgcn_readfirstlane(deep);
#define WAIT_IF(level, n) asm volatile("s_cmp_lg_u32 %0, " #level "\n\ts_cbranch_scc1 .Lpi%=\n\ts_waitcnt vmcnt(" #n ")\n.Lpi%=:" :: "s"(deep) : "scc", "memory")
  const char* p = base + off;
  if constexpr (COARSE) {
    WAIT_IF(0, 3);
    ta.d[0] = ld_nt(p); ta.d[1] = ld_nt(p + 128); ta.d[2] = ld_nt(p + 256); MEMFENCE();
    WAIT_IF(0, 3); WAIT_IF(1, 6);
    tb.d[0] = ld_nt(p + 3072); tb.d[1] = ld_nt(p + 3200); tb.d[2] = ld_nt(p + 3328); MEMFENCE();
  } else {
    WAIT_IF(0, 5); ta.d[0] = ld_nt(p); MEMFENCE();
    WAIT_IF(0, 5); ta.d[1] = ld_nt(p + 128); MEMFENCE();
    WAIT_IF(0, 5); ta.d[2] = ld_nt(p + 256); MEMFENCE();
    WAIT_IF(1, 6);
    WAIT_IF(0, 5); tb.d[0] = ld_nt(p + 3072); MEMFENCE();
    WAIT_IF(0, 5); tb.d[1] = ld_nt(p + 3200); MEMFENCE();
    WAIT_IF(0, 5); tb.d[2] = ld_nt(p + 3328); MEMFENCE();
  }
#undef WAIT_IF
}
__device__ __forceinline__ void pv_last(float p, Tile& v, f32x2 (&acc)[12]) {
#pragma unroll
  for (int i = 0; i < 3; ++i) {
    asm volatile("" : "+v"(v.d[i]), "+v"(p));
#pragma unroll
    for (int d = 0; d < 4; ++d) acc[i * 4 + d] = fma2(p, bf_pair(v.d[i][d]), acc[i * 4 + d]);
    PIN4(acc, i * 4);
  }
}
__device__ __forceinline__ void q_load(u32x2 (&q)[12], const float* src, int j) {
#pragma unroll
  for (int i = 0; i < 6; ++i) {
    const u32x4 a = *reinterpret_cast<const u32x4*>(src + (i >> 1) * 64 + j * 8 + (i & 1) * 4);
    q[i * 2] = u32x2{a[0], a[1]}; q[i * 2 + 1] = u32x2{a[2], a[3]};
  }
}
#define VMCNT(n) asm volatile("s_waitcnt vmcnt(%0)" :: "n"(n) : "memory")

constexpr int AUX_SC1 = 16, AUX_EARLY = 18;
constexpr unsigned SPIN_CAP = 1u << 17;
__device__ __forceinline__ unsigned nz_bits(float v) { return max(__float_as_uint(v), 1u); }
__device__ __forceinline__ bool all_nz(u32x4 v) { return v[0] != 0u && v[1] != 0u && v[2] != 0u && v[3] != 0u; }
__device__ __forceinline__ unsigned min4(u32x4 v) { return min(min(v[0], v[1]), min(v[2], v[3])); }
__device__ __forceinline__ unsigned ld32(__amdgpu_buffer_rsrc_t rs, int word) { return __builtin_amdgcn_raw_buffer_load_b32(rs, word * 4, 0, AUX_SC1); }
__device__ __forceinline__ u32x2 ld64(__amdgpu_buffer_rsrc_t rs, int word) { return __builtin_amdgcn_raw_buffer_load_b64(rs, word * 4, 0, AUX_SC1); }
__device__ __forceinline__ u32x4 ld128(__amdgpu_buffer_rsrc_t rs, int word) { return __builtin_amdgcn_raw_buffer_load_b128(rs, word * 4, 0, AUX_SC1); }
__device__ __forceinline__ void st32(__amdgpu_buffer_rsrc_t rs, int word, unsigned v) { __builtin_amdgcn_raw_buffer_store_b32(v, rs, word * 4, 0, AUX_SC1); }
__device__ __forceinline__ void st64(__amdgpu_buffer_rsrc_t rs, int word, u32x2 v) { __builtin_amdgcn_raw_buffer_store_b64(v, rs, word * 4, 0, AUX_SC1); }
__device__ __forceinline__ void st128(__amdgpu_buffer_rsrc_t rs, int word, u32x4 v) { __builtin_amdgcn_raw_buffer_store_b128(v, rs, word * 4, 0, AUX_SC1); }

__device__ __forceinline__ u32x4 ld128nt(__amdgpu_buffer_rsrc_t rs, int word) { return __builtin_amdgcn_raw_buffer_load_b128(rs, word * 4, 0, AUX_EARLY); }
__device__ __forceinline__ void st128nt(__amdgpu_buffer_rsrc_t rs, int word, u32x4 v) { __builtin_amdgcn_raw_buffer_store_b128(v, rs, word * 4, 0, AUX_EARLY); }

__device__ __forceinline__ bool leader_release(__amdgpu_buffer_rsrc_t rs, int lane, unsigned t0, unsigned rd) {
  u32x4 a[4];
#pragma unroll
  for (int k = 0; k < 4; ++k) a[k] = ld128(rs, WS_HA + (lane * 4 + k) * 32);
  const u32x4 n = ld128(rs, WS_HN + lane * 4);
  if (rd == 0u && !__all(a[0][0] == n[0] && a[1][0] == n[1] && a[2][0] == n[2] && a[3][0] == n[3])) return false;
  const float e = fmaxf(fmaxf((float)(int)(t0 - a[0][2]), (float)(int)(t0 - a[1][2])), fmaxf((float)(int)(t0 - a[2][2]), (float)(int)(t0 - a[3][2])));
  unsigned first = t0 - (unsigned)(int)max_x16_32(row_max(e));
  float ls = 0.f, lx = 0.f;
#pragma unroll
  for (int k = 0; k < 4; ++k) {
    const float l = a[k][0] == n[k] + rd ? (float)min(a[k][3] + (rd ? 0u : min(a[k][2] - first, 300u)), 2000u) : 0.f;
    ls += l; lx = fmaxf(lx, l);
  }
  ls = wave_sum(ls) * (1.0f / NWG); lx = max_x16_32(row_max(lx));
  const int extra = min(max((int)(ls * (FBM / 100.0f) + lx * (FBX / 100.0f)) - (rd ? FB1 + (int)(NRD - rd) * FBD : FB0), 0), 600);
  first = (first & 0xffffu) | ((unsigned)extra << 16);
  if (rd != 0u && ls * NWG < HOLD) return true;
  st128(rs, WS_HR + lane * 8, u32x4{a[0][1] + rd, first, a[1][1] + rd, first});
  st128(rs, WS_HR + lane * 8 + 4, u32x4{a[2][1] + rd, first, a[3][1] + rd, first});
  return true;
}

typedef __attribute__((address_space(3))) unsigned lds_u32;
__device__ __forceinline__ unsigned lds_add(lds_u32* p, int lane) {
  unsigned k = 0;
  if (lane == 0) k = __hip_atomic_fetch_add(p, 1u, __ATOMIC_RELAXED, __HIP_MEMORY_SCOPE_WORKGROUP);
  return __builtin_amdgcn_readfirstlane(k);
}
#define CFENCE() asm volatile("" ::: "memory")
__device__ __forceinline__ int arrive(lds_u32* p) {
  int v;
  unsigned long long keep;
  asm volatile("s_mov_b64 %1, exec\n\ts_mov_b64 exec, 1\n\tds_add_rtn_u32 %0, %2, %3\n\ts_mov_b64 exec, %1"
               : "=&v"(v), "=&s"(keep) : "v"((unsigned)(size_t)p), "v"(1u) : "memory");
  return v;
}
__device__ __forceinline__ int arrived(int v) {
  asm volatile("s_waitcnt lgkmcnt(0)" : "+v"(v) :: "memory");
  return __builtin_amdgcn_readfirstlane(v);
}
__device__ __forceinline__ unsigned lds_get(lds_u32* p) {
  return __builtin_amdgcn_readfirstlane(__hip_atomic_load(p, __ATOMIC_RELAXED, __HIP_MEMORY_SCOPE_WORKGROUP));
}
template <int SLEEP> __device__ __forceinline__ void lds_wait(lds_u32* p, unsigned n) {
#pragma unroll 1
  for (unsigned spin = 0; spin < (SPIN_CAP << 4); ++spin) {
    if (lds_get(p) >= n) break;
    __builtin_amdgcn_s_sleep(SLEEP);
  }
  asm volatile("" ::: "memory");
}
__device__ __forceinline__ void pause(unsigned ticks) {
  const unsigned long long t0 = __builtin_amdgcn_s_memrealtime();
#pragma unroll 1
  for (unsigned spin = 0; spin < 4096u && __builtin_amdgcn_s_memrealtime() - t0 < ticks; ++spin) __builtin_amdgcn_s_sleep(16);
}
__device__ __forceinline__ unsigned clk() { return (unsigned)__builtin_amdgcn_s_memrealtime(); }
struct Table {
  unsigned t0; int was, moved, slowed, folded;
  u32x2 told;
  __device__ __forceinline__ int gate(int due, lds_u32* mine, bool hold = true, int sh = 0) {
    const int fold = __builtin_amdgcn_readfirstlane(told[1]);
    if (fold != folded) { const int f = min(fold - folded, moved); moved -= f; slowed += f; folded = fold; *mine = moved; }
    const int left = (int)__builtin_amdgcn_readfirstlane(told[0]) - slowed, d = max(min(left, FBS), left < -GBX ? -GBF : -GBS);
    t0 += d; was -= d; slowed += d;
    int late = (int)(clk() - t0) - due;
    if (late - was > JUMP) { t0 += late - was; moved += late - was; late = was; *mine = moved; }
    const int lag = max(late - max(left - d, 0) - LAGOFF, 0);
    mine[4] = lag;
    was = late;
#pragma unroll 1
    for (int spin = 0; spin < 65536 && hold && late < 0; ++spin) { __builtin_amdgcn_s_sleep(1); late = (int)(clk() - t0) - due; }
    return max(lag < (DEEP1 >> sh) ? 0 : (lag < (DEEP2 >> sh) ? 1 : 2), hold ? 0 : SDEEP);
  }
  __device__ __forceinline__ void listen(lds_u32* slow) {
    told = __builtin_bit_cast(u32x2, __hip_atomic_load((__attribute__((address_space(3))) unsigned long long*)slow, __ATOMIC_RELAXED, __HIP_MEMORY_SCOPE_WORKGROUP));
  }
  __device__ __forceinline__ void agree(lds_u32* all, lds_u32* mine) {
    if (moved == 0) return;
    const int m = (int)min(min(lds_get(all), lds_get(all + 1)), min(lds_get(all + 2), lds_get(all + 3))) + 30;
    if (m < moved) { t0 -= moved - m; moved = m; was = 1 << 20; *mine = moved; }
  }
};
__device__ __forceinline__ void until(unsigned t0, int due) {
#pragma unroll 1
  for (int spin = 0; spin < 65536 && (int)(clk() - t0) < due; ++spin) __builtin_amdgcn_s_sleep(1);
}
#define LGKM0() asm volatile("s_waitcnt lgkmcnt(0)" ::: "memory")

__device__ __forceinline__ void split4(f32x4 v, u32x2& hi, u32x2& lo) {
  unsigned h[4], r[4];
#pragma unroll
  for (int i = 0; i < 4; ++i) { h[i] = __float_as_uint(v[i]) & 0xffff0000u; r[i] = __float_as_uint(v[i] - __uint_as_float(h[i])); }
  hi = u32x2{__builtin_amdgcn_perm(h[1], h[0], 0x07060302u), __builtin_amdgcn_perm(h[3], h[2], 0x07060302u)};
  lo = u32x2{__builtin_amdgcn_perm(r[1], r[0], 0x07060302u), __builtin_amdgcn_perm(r[3], r[2], 0x07060302u)};
}
__device__ __forceinline__ u32x4 split8(f32x4 v, bool odd) {
  u32x2 hi, lo;
  split4(v, hi, lo);
  const unsigned n0 = __float_as_uint(dpp<DPP_XOR1>(__uint_as_float(odd ? hi[0] : lo[0]))), n1 = __float_as_uint(dpp<DPP_XOR1>(__uint_as_float(odd ? hi[1] : lo[1])));
  return u32x4{max(odd ? n0 : hi[0], 1u), max(odd ? n1 : hi[1], 1u), max(odd ? lo[0] : n0, 1u), max(odd ? lo[1] : n1, 1u)};
}
__device__ __forceinline__ u32x4 q_pack(u32x4 a, u32x4 b) {
  u32x2 hi, lo;
  split4((__builtin_bit_cast(f32x4, a) + __builtin_bit_cast(f32x4, b)) * QSCALE, hi, lo);
  return u32x4{hi[0], lo[0], hi[1], lo[1]};
}
__device__ __forceinline__ f32x4 mfma16(u32x4 a, u32x4 b, f32x4 c) {
  return __builtin_amdgcn_mfma_f32_16x16x32_bf16(__builtin_bit_cast(bf16x8, a), __builtin_bit_cast(bf16x8, b), c, 0, 0, 0);
}

constexpr int NW = 8, NMW = 4;
constexpr int NLP = 5, NRP = 7;
constexpr int NTP = 4 * (NLP + NRP);
constexpr int REG = NLP * 6 * 1024;
constexpr int NRING = 4;
constexpr int NS = 6, NA = NS - 1;
__device__ __forceinline__ int seg_beg(int s) { return s >= NS ? NPAIR : ((0xC87640 >> (4 * s)) & 15) * 16; }
__device__ __forceinline__ int seg_k(int t) { return (int)(0x5433222211110000ull >> (4 * (t >> 4))) & 15; }
__device__ __forceinline__ int seg_of_k(int k) { return (0x321540 >> (4 * k)) & 15; }
__device__ __forceinline__ int k_of_seg(int s) { return (0x215430 >> (4 * s)) & 15; }
__device__ __forceinline__ int pair_of(int t) { return t < 64 ? t : (t < 192 ? t + 64 : t - 128); }
constexpr int OFF_CNT = 0, OFF_REG = 256,
              OFF_SC = OFF_REG + 4 * REG, OFF_BUF = OFF_SC + NPAIR * 16 * 4, OFF_Q = OFF_BUF + NRING * NMW * DH * 4, OFF_ML = OFF_Q + NS * DH * 4,
              OFF_RED = OFF_ML + 128, OFF_RS = OFF_RED + 4 * 256 * 4, OFF_END = OFF_RS + 5 * 16;
enum { F_REL = 0, F_DONE, C_LAST, C_T = 3, F_OUT, C_P, C_K, F_P, F_Q = 8, F_FREE, S_FIRST, F_Q2, C_W = 12, C_SC = 16, C_PART = 24, S_ECHO = 32, S_MOVED = 48, S_SLOW = 56 };
__device__ __forceinline__ int due_k(int p) { return TSTART + ((p * TAUK + min(max(p - WP0, 0), WPN) * (WGAP * 64 / WPN)) >> 6); }
__device__ __forceinline__ int due_v(int p) { return TSTART + WGAP + ((NPAIR * TAUK + p * TAUV + max(p - NPAIR / 2, 0) * (VGAP * 64 / (NPAIR / 2))) >> 6); }
constexpr int AW_LD = DH * 2 + 16, AG_LD = 160 * 2 + 16;

__global__ void __launch_bounds__(NW * 64) fused_kernel(const bf16* __restrict__ x, const bf16* __restrict__ kc,
                                                        const bf16* __restrict__ vc, const bf16* __restrict__ wq,
                                                        const bf16* __restrict__ wo, const bf16* __restrict__ wr,
                                                        const bf16* __restrict__ wg, const bf16* __restrict__ wu,
                                                        const bf16* __restrict__ wd, float* ws, bf16* __restrict__ out) {
  __shared__ __attribute__((aligned(16))) unsigned char smem[OFF_END];
  float* s_sc = reinterpret_cast<float*>(smem + OFF_SC);
  float* s_buf = reinterpret_cast<float*>(smem + OFF_BUF);
  float* s_q = reinterpret_cast<float*>(smem + OFF_Q);
  u32x4* s_ml = reinterpret_cast<u32x4*>(smem + OFF_ML);
  lds_u32* s_c = (lds_u32*)(smem + OFF_CNT);

  const int tid = threadIdx.x, lane = tid & 63, r = lane >> 3, j = lane & 7;
  const int wave = __builtin_amdgcn_readfirstlane(tid >> 6);
  const int tw = wave - NMW;
  const int bid = blockIdx.x, xl = bid & 7, idx = bid >> 3;
  const int g = (xl * 32 + idx) / GS, mm = idx % GS;
  const __amdgpu_buffer_rsrc_t rs = __builtin_amdgcn_make_buffer_rsrc(ws, 0, WS_END * 4, 0x00020000);
  const int ro = ((idx & 3) << 3) | (idx >> 2);
  const bool isW = ro < 10, isG = ro >= 10 && ro < 16;
  const int w_ks = xl, w_ct = ro;
  const int g_cg = xl * 6 + (ro - 10);
  const int d_ct = (xl + ((ro - 16) >> 2) * 8) * 2 + (((ro - 16) >> 1) & 1);
  const bool isD = ro >= 16 && ro < 24 && (ro & 1) == 0 && d_ct < DM / 32;
  const bool isH = ro == 22 && xl >= 2 && xl < 5;
  const bool isC = (mm & 7) == 3 || (mm & 7) >= 6;
  const int c_it = (mm >> 3) * 3 + max((mm & 7) - 5, 0);
  const int qf = WS_QF + g * QFS;
  const char* kb = reinterpret_cast<const char*>(kc);
  const char* vb = reinterpret_cast<const char*>(vc);
  const unsigned lo = (r * 24 + j) * 16;
  const int xb = g * GS + mm, c0 = mm * 8 + g * ROT;
  const int b2 = (2 * NH + (xb >> 4)) * 1024 + (xb & 15) * 8, b3 = (2 * NH + 16 + (xb >> 5)) * 1024 + (xb & 31) * 8;
  auto poff = [&](int p) -> unsigned {
    const int turn = (p & 63) >> 2;
    int t = (row_of(p >> 6) * NH + g) * 1024 + ((turn * 64 + (p & 3) * 2 + c0) & 1023);
    if ((p >> 6) == 1) t = (turn < 8 ? b2 + turn * 128 : b3 + (turn & 4) * 2048 + (turn & 3) * 256) + (p & 3) * 2;
    return (unsigned)t * 3072u;
  };
  const unsigned lds_base = (unsigned)(size_t)(__attribute__((address_space(3))) unsigned char*)smem + OFF_REG + tw * REG;
  auto merged = [&](int it, int ln) -> f32x4 {
    const float* src = s_buf + (it % NRING) * (NMW * DH) + ((ln >> 1) & 7) * 24 + (ln >> 4) * 8 + (ln & 1) * 4;
    return (*reinterpret_cast<const f32x4*>(src) + *reinterpret_cast<const f32x4*>(src + DH)) +
           (*reinterpret_cast<const f32x4*>(src + 2 * DH) + *reinterpret_cast<const f32x4*>(src + 3 * DH));
  };
  auto publish = [&](int it) -> bool {
    const int k = arrive(s_c + C_PART + it);
    const f32x4 m = merged(it, lane < DH / 4 ? lane : 0);
    u32x4 o = {nz_bits(m[0]), nz_bits(m[1]), nz_bits(m[2]), nz_bits(m[3])};
    asm volatile("" : "+v"(o));
    if (arrived(k) != NMW - 1) return false;
    if (lane < DH / 4) st128(rs, WS_PO + xb * DH + lane * 4, o);
    if (lane == 0) __hip_atomic_fetch_add(s_c + F_DONE, 1u, __ATOMIC_RELAXED, __HIP_MEMORY_SCOPE_WORKGROUP);
    return true;
  };
#define NONCE() do { unsigned n_ = __builtin_amdgcn_readfirstlane(f) + 1u; while (__any(f == n_)) ++n_; \
    if (lane < 32) st32(rs, qf + (GS + mm) * 32 + lane, n_); f = n_; } while (0)
  unsigned long long pc;
  asm volatile("s_getpc_b64 %0" : "=s"(pc));
  unsigned r0 = 0u;
  unsigned t0 = clk();
  if (wave < NMW) {
    Tile ta, tb, tc, td;
    const int c = lane & (GS - 1);
    unsigned f = 0u;
    if (wave == 3) f = ld32(rs, qf + c * 32 + mm);
    if (mm < NPROD) {
      const int hp = g + (mm / 6) * NG, ck = (mm % 6) >> 1, hf = mm & 1, jp = hf * 4 + wave;
      const int bb = lane >> 4, t = lane & 15;
      const u32x4* xp = reinterpret_cast<const u32x4*>(x + row_of(bb) * DM);
      u32x4 xv[5], xs = {0u, 0u, 0u, 0u};
#pragma unroll
      for (int k = 0; k < 5; ++k) xv[k] = xp[t + 16 * k];
      if (t < 10) xs = xp[jp * 10 + t];
      const u32x4* wp = reinterpret_cast<const u32x4*>(wq + (size_t)(jp * 80 + r) * HD + hp * DH + ck * 64) + j;
      u32x4 wv[10];
#pragma unroll
      for (int k = 0; k < 10; ++k) wv[k] = __builtin_nontemporal_load(wp + (size_t)k * 8 * (HD / 8));
      if (wave == 0) s_c[lane] = lane == C_K ? NTP + NMW : 0u;
      MEMFENCE(); LGKM0(); __builtin_amdgcn_s_barrier(); MEMFENCE();
      if (wave == 3) { NONCE(); }
      float sm = 0.f;
#pragma unroll
      for (int k = 0; k < 5; ++k)
#pragma unroll
        for (int d = 0; d < 4; ++d) sm += bf_lo(xv[k][d]) + bf_hi(xv[k][d]);
      const float mean = row_sum(sm) * (1.0f / DM);
      float sv = 0.f;
#pragma unroll
      for (int k = 0; k < 5; ++k)
#pragma unroll
        for (int d = 0; d < 4; ++d) { const float a = bf_lo(xv[k][d]) - mean, c2 = bf_hi(xv[k][d]) - mean; sv += a * a + c2 * c2; }
      const float rstd = 1.0f / sqrtf(row_sum(sv) * (1.0f / DM) + LN_EPS);
      float* s_xn = s_buf + wave * 320;
      if (t < 10) {
#pragma unroll
        for (int d = 0; d < 4; ++d) {
          s_xn[(t * 8 + 2 * d) * 4 + bb] = (bf_lo(xs[d]) - mean) * rstd;
          s_xn[(t * 8 + 2 * d + 1) * 4 + bb] = (bf_hi(xs[d]) - mean) * rstd;
        }
      }
      LGKM0();
      f32x2 qa[16];
#pragma unroll
      for (int i = 0; i < 16; ++i) qa[i] = f32x2{0.f, 0.f};
#pragma unroll
      for (int k = 0; k < 10; ++k) {
        const f32x4 xn = *reinterpret_cast<const f32x4*>(&s_xn[(k * 8 + r) * 4]);
#pragma unroll
        for (int d = 0; d < 4; ++d) {
          const f32x2 w2 = bf_pair(wv[k][d]);
#pragma unroll
          for (int e = 0; e < 4; ++e) qa[e * 4 + d] = fma2(xn[e], w2, qa[e * 4 + d]);
        }
      }
      float t16[16], t8[8];
#pragma unroll
      for (int i = 0; i < 16; ++i) t16[i] = tsum32(qa[i >> 1][i & 1], qa[8 + (i >> 1)][i & 1]);
#pragma unroll
      for (int i = 0; i < 8; ++i) t8[i] = tsum16(t16[i], t16[i + 8]);
#pragma unroll
      for (int i = 0; i < 8; ++i) t8[i] += dpp<DPP_ROR8>(t8[i]);
      const bool up = (lane & 8) != 0;
      f32x4* r4 = reinterpret_cast<f32x4*>(smem + OFF_RED);
      r4[wave * 64 + lane] = f32x4{up ? t8[4] : t8[0], up ? t8[5] : t8[1], up ? t8[6] : t8[2], up ? t8[7] : t8[3]};
      LGKM0();
      if (lds_add(s_c + C_P, lane) == NMW - 1) {
        asm volatile("" ::: "memory");
        const f32x4 o4 = (r4[lane] + r4[64 + lane]) + (r4[128 + lane] + r4[192 + lane]);
        st128nt(rs, WS_QP + ((hp * 2 + hf) * NB + bb) * DH + ck * 64 + j * 8 + (up ? 4 : 0), __builtin_bit_cast(u32x4, o4));
        VMCNT(0);
        if (lane == 0) s_c[F_OUT] = 1u;
      }
    } else {
      if (wave == 0) s_c[lane] = lane == C_K ? NTP + NMW : 0u;
      MEMFENCE(); LGKM0(); __builtin_amdgcn_s_barrier(); MEMFENCE();
      if (wave == 3) { NONCE(); }
      else if (CPF && lane < CODE / (24 * 64)) {
        typedef __attribute__((address_space(1))) const unsigned g_u32;
        until(t0, CPT);
        const unsigned v = *(g_u32*)((pc & ~255ull) + (((idx >> 3) * 2 + (mm & 1)) * 3 + wave) * (CODE / 24) + lane * 64);
        asm volatile("" :: "v"(v));
      }
    }
    MEMFENCE();
    if (wave == 3) {
      __builtin_amdgcn_s_setprio(3);
      const unsigned nonce = f;
      unsigned echoed = 0u;
      if (mm < NPROD) {
        echoed = ld32(rs, qf + (GS + c) * 32);
        asm volatile("" : "+v"(echoed) :: "memory");
        lds_wait<1>(s_c + F_OUT, 1u);
        if (lane < 32) st32(rs, qf + mm * 32 + lane, echoed);
#pragma unroll 1
        while ((int)(clk() - t0) < QWAIT - QBACK) {
          const unsigned nv = ld32(rs, qf + (GS + c) * 32);
          if (__any(nv != echoed)) {
            if (lane < 32) st32(rs, qf + mm * 32 + lane, nv);
            echoed = nv;
          }
          __builtin_amdgcn_s_sleep(4);
        }
      }
      until(t0, QWAIT);
#pragma unroll 1
      for (unsigned spin = 0; spin < SPIN_CAP; ++spin) {
        const unsigned nv = ld32(rs, qf + (GS + c) * 32);
        f = ld32(rs, qf + c * 32 + mm);
        const bool done = __all(c == mm || c >= NPROD || f == nonce);
        if (mm < NPROD && __any(nv != echoed)) {
          if (lane < 32) st32(rs, qf + mm * 32 + lane, nv);
          echoed = nv;
        }
        if (done) break;
        __builtin_amdgcn_s_sleep(8);
      }
      if (lane < GS) s_c[S_ECHO + lane] = echoed;
      if (lane < DH / 4) {
        u32x4 p[3][2];
#pragma unroll
        for (int it = 0; it < 3; ++it)
#pragma unroll
          for (int k = 0; k < 2; ++k) p[it][k] = ld128nt(rs, WS_QP + ((g * 2 + k) * NB + (it ? it + 1 : 0)) * DH + lane * 4);
#pragma unroll
        for (int it = 0; it < 3; ++it) {
          *reinterpret_cast<u32x4*>(&s_q[(it ? it + 3 : 0) * DH + lane * 4]) = q_pack(p[it][0], p[it][1]);
          if (it != 1) { LGKM0(); if (lane == 0) s_c[F_Q] = it + 1; MEMFENCE(); }
        }
      }
    }
    __builtin_amdgcn_s_setprio(3);
    until(t0, due_k(NTP + wave));
    Table tt = {t0 + ((idx * PHASE) >> 5), 1 << 20, 0, 0, 0, {0u, 0u}};
    bool common = false;
    tile_load(ta, kb, poff(NTP + wave) + lo, 0);
    tile_load(tb, kb, poff(NTP + wave) + lo, 1);
    MEMFENCE();
    {
      u32x2 q[12];
      int cur = NTP + wave, nxt = lds_add(s_c + C_K, lane), passed = 0, ci = -1;
      auto kstep = [&](Tile& xa, Tile& xb_, Tile& ya, Tile& yb) __attribute__((always_inline)) -> bool {
        const bool last = nxt >= NPAIR;
        const unsigned off = poff(last ? wave : pair_of(nxt)) + lo;
        const char* base = last ? vb : kb;
        const int it = seg_k(cur), cp = pair_of(cur);
        if (it != ci) {
          LGKM0();
          for (; passed < it; ++passed) lds_add(s_c + C_SC + passed, lane);
          lds_wait<1>(s_c + (it < 3 ? F_Q : F_Q2), min(it + 1, 3));
          q_load(q, s_q + seg_of_k(it) * DH, j);
          if (!common) { const unsigned d = lds_get(s_c + S_FIRST); if (d) { tt.t0 -= d - 1u; common = true; tt.was = 1 << 20; } }
          tt.agree(s_c + S_MOVED, s_c + S_MOVED + wave);
          ci = it;
        }
        MEMFENCE();
        const int deep = tt.gate(due_k(last ? NPAIR + wave : nxt), s_c + S_MOVED + wave);
        MEMFENCE();
        pair_issue(ya, yb, base, off, deep);
        const int tk = arrive(s_c + C_K);
        tt.listen(s_c + S_SLOW + 2 * wave);
        pair_put(tile_score(xa, q), tile_score(xb_, q), s_sc + cp * 16 + r, j);
        MEMFENCE();
        cur = nxt; nxt = arrived(tk);
        return last;
      };
      bool swap;
      for (;;) {
        if (kstep(ta, tb, tc, td)) { swap = true; break; }
        if (kstep(tc, td, ta, tb)) { swap = false; break; }
      }
      LGKM0();
      for (; passed < NS; ++passed) lds_add(s_c + C_SC + passed, lane);
      __builtin_amdgcn_s_waitcnt(0x0F70);
      if (swap) { ta = tc; tb = td; }
    }
#pragma unroll 1
    for (int it = 0; it < NS; ++it) {
      lds_wait<1>(s_c + F_P, k_of_seg(it) + 1);
      if (!common) { const unsigned d = lds_get(s_c + S_FIRST); if (d) { tt.t0 -= d - 1u; common = true; tt.was = 1 << 20; } }
      tt.agree(s_c + S_MOVED, s_c + S_MOVED + wave);
      f32x2 acc[12];
#pragma unroll
      for (int i = 0; i < 12; ++i) acc[i] = f32x2{0.f, 0.f};
      auto vstep = [&](Tile& xa, Tile& xb_, Tile& ya, Tile& yb, int p) __attribute__((always_inline)) {
        const unsigned off = poff(p + NMW) + lo;
        MEMFENCE();
        const int deep = tt.gate(due_v(p + NMW), s_c + S_MOVED + wave, p + NMW < NPAIR - SPRINT * NMW, p >= NPAIR - ENDT * NMW ? ENDS : 0);
        MEMFENCE();
        pair_issue(ya, yb, vb, off, deep);
        const float pa = s_sc[p * 16 + r], pb = s_sc[p * 16 + 8 + r];
        tt.listen(s_c + S_SLOW + 2 * wave);
        if (p + 2 * NMW >= NPAIR) { if (lane == 0) __hip_atomic_fetch_add(s_c + C_LAST, 1u, __ATOMIC_RELAXED, __HIP_MEMORY_SCOPE_WORKGROUP); }
        pv_last(pa, xa, acc);
        pv_last(pb, xb_, acc);
        MEMFENCE();
      };
#pragma unroll 1
      for (int p = seg_beg(it) + wave; p < min(seg_beg(it + 1), NPAIR - 2 * NMW); p += 2 * NMW) {
        vstep(ta, tb, tc, td, p);
        vstep(tc, td, ta, tb, p + NMW);
      }
      if (it == NS - 1) {
        vstep(ta, tb, tc, td, NPAIR - 2 * NMW + wave);
        const int p = NPAIR - NMW + wave;
        const float pa = s_sc[p * 16 + r], pb = s_sc[p * 16 + 8 + r];
        pv_last(pa, tc, acc);
        pv_last(pb, td, acc);
      }
      float t12[12], t6[6];
#pragma unroll
      for (int i = 0; i < 12; ++i) t12[i] = tsum32(acc[i >> 1][i & 1], acc[6 + (i >> 1)][i & 1]);
#pragma unroll
      for (int i = 0; i < 6; ++i) t6[i] = tsum16(t12[i], t12[i + 6]);
#pragma unroll
      for (int i = 0; i < 6; ++i) t6[i] += dpp<DPP_ROR8>(t6[i]);
      if (it >= NRING) lds_wait<1>(s_c + F_FREE, it - NRING + 1);
      if ((lane & 8) == 0) {
        f32x2* dst = reinterpret_cast<f32x2*>(&s_buf[(it % NRING) * (NMW * DH) + wave * DH + j * 24 + (lane >> 4) * 6]);
        dst[0] = f32x2{t6[0], t6[1]}; dst[1] = f32x2{t6[2], t6[3]}; dst[2] = f32x2{t6[4], t6[5]};
      }
      CFENCE();
      if (it < NA) { if (lane == 0) __hip_atomic_fetch_add(s_c + C_PART + it, 1u, __ATOMIC_RELAXED, __HIP_MEMORY_SCOPE_WORKGROUP); }
    }
    publish(NS - 1);
    return;
  }

  {
    Tile pk[NRP * 2];
    __builtin_amdgcn_s_setprio(3);
#pragma unroll
    for (int e = 0; e < NLP * 2; ++e) {
      const char* src = kb + (poff((e >> 1) * 4 + tw) + lo + (e & 1) * 3072);
      if (e >= 2) until(t0, due_k(e * 2 + tw) - TLEAD);
#pragma unroll
      for (int i = 0; i < 3; ++i) glds16_nt(src + i * 128, lds_base + (e * 3 + i) * 1024);
      if (e == 1) __builtin_amdgcn_s_barrier();
      if (e * 3 + 3 > PFD) VMCNT(PFD);
    }
#pragma unroll
    for (int e = 0; e < NRP * 2; ++e) {
      until(t0, due_k((NLP * 2 + e) * 2 + tw) - TLEAD);
      tile_load(pk[e], kb, poff((NLP + (e >> 1)) * 4 + tw) + lo, e & 1);
      MEMFENCE();
      if (e + 1 < NRP * 2) VMCNT(PFD);
    }
    if (tw == 0) {
      r0 = ld32(rs, WS_HR + bid * 2);
      if (bid == 0) {
        u32x4 n;
#pragma unroll
        for (int k = 0; k < 4; ++k) n[k] = ld32(rs, WS_HA + (lane * 4 + k) * 32) + 1u;
        st128(rs, WS_HN + lane * 4, n);
      }
      const u32x4 z = {0u, 0u, 0u, 0u};
      if (lane < 48) {
#pragma unroll
        for (int it = NA; it < NS; ++it) st128(rs, WS_PO + xb * DH + lane * 4, z);
      }
      else st128(rs, WS_PML + (g * GS + mm) * 64 + (lane - 48) * 4, z);
      if (isG && g_cg == 0 && lane < 8) st128(rs, WS_ST + lane * 4, z);
      if (isW) st128(rs, WS_SLAB + (w_ks * NB + (lane >> 4)) * DM + w_ct * 64 + (lane & 15) * 4, z);
      else if (isG) { if (lane < 16) st128(rs, WS_ACT + g_cg * 64 + lane * 4, z); }
      else if (isH) {
#pragma unroll
        for (int k = 0; k < 3; ++k) st128(rs, WS_H + (xl - 2) * 768 + k * 256 + lane * 4, z);
      }
      else if (isC && lane < 48) st128(rs, WS_OC + (c_it * NG + g) * DH + lane * 4, z);
    }
    __builtin_amdgcn_s_setprio(0);
    lds_wait<1>(s_c + F_Q, HPG);
    u32x2 q[12];
    q_load(q, s_q, j);
    const u32x4* s_kv = reinterpret_cast<const u32x4*>(smem + OFF_REG + tw * REG) + lane;
    static_assert(NLP * 4 <= PPI && NTP <= HPG * PPI, "tail tiles: batch row 0 only");
#pragma unroll 1
    for (int e = 0; e < NLP * 2; ++e) {
      Tile tmp;
#pragma unroll
      for (int i = 0; i < 3; ++i) tmp.d[i] = s_kv[(e * 3 + i) * 64];
      score_put(tile_score(tmp, q), s_sc + ((e >> 1) * 4 + tw) * 16 + (e & 1) * 8 + r, j);
    }
#pragma unroll
    for (int e = 0; e < NRP * 2; ++e) {
      if ((NLP * 2 + e) * 2 == PPI) q_load(q, s_q + DH, j);
      score_put(tile_score(pk[e], q), s_sc + ((NLP + (e >> 1)) * 4 + tw) * 16 + (e & 1) * 8 + r, j);
      __builtin_amdgcn_sched_barrier(0);
    }
    asm volatile("s_waitcnt vmcnt(0) lgkmcnt(0)" ::: "memory");
#pragma unroll
    for (int it = 0; it * PPI < NTP; ++it) lds_add(s_c + C_SC + it, lane);
  }

  const int mr = lane & 15, qd = lane >> 4;
  unsigned char* reg = smem + OFF_REG + tw * REG;
  float* red = reinterpret_cast<float*>(smem + OFF_RED);
#define PERM_LO(hi_, lo_) __builtin_amdgcn_perm(hi_, lo_, 0x05040100u)
#define PERM_HI(hi_, lo_) __builtin_amdgcn_perm(hi_, lo_, 0x07060302u)
  int served = 0;
  auto done = [&](int n) {
    if (tw == 3 && served < min(n, NA)) {
      const int from = served;
      int ln = lane;
      asm volatile("" : "+v"(ln));
#pragma unroll 1
      for (; served < min(n, NA); ++served) {
        lds_wait<8>(s_c + C_PART + served, NMW);
        const f32x4 o = merged(served, ln < DH / 4 ? ln : 0);
        const float am = max_x16_32(row_max(fmaxf(fmaxf(fabsf(o[0]), fabsf(o[1])), fmaxf(fabsf(o[2]), fabsf(o[3])))));
        const float inv = am > 1e-30f ? 32767.0f / am : 0.f;
        const int q0 = __float2int_rn(o[0] * inv), q1 = __float2int_rn(o[1] * inv), q2 = __float2int_rn(o[2] * inv), q3 = __float2int_rn(o[3] * inv);
        if (ln < DH / 4) reinterpret_cast<u32x2*>(s_sc + served * (DH / 2))[ln] = u32x2{PERM_LO((unsigned)q1, (unsigned)q0), PERM_LO((unsigned)q3, (unsigned)q2)};
        const bool nan = __any((o[0] + o[1]) + (o[2] + o[3]) != (o[0] + o[1]) + (o[2] + o[3]));
        if (ln == 0) s_ml[served][2] = nan ? 0x7fc00000u : nz_bits(am * (1.0f / 32767.0f));
        LGKM0();
        if (ln == 0) s_c[F_FREE] = served + 1;
      }
#pragma unroll 1
      for (int c = from * 24 + ln; c < served * 24; c += 64) st128nt(rs, WS_PQ + xb * 5 * (DH / 2) + c * 4, reinterpret_cast<const u32x4*>(s_sc)[c]);
      VMCNT(0);
      if (ln < 8) st128(rs, WS_PML + xb * 64 + (from ? 32 : 0) + ln * 4, s_ml[ln]);
      if (ln == 0) __hip_atomic_fetch_add(s_c + F_DONE, (unsigned)(served - from), __ATOMIC_RELAXED, __HIP_MEMORY_SCOPE_WORKGROUP);
    }
    if (n > NA) lds_wait<1>(s_c + F_DONE, n); else lds_wait<8>(s_c + F_DONE, n);
  };
  auto done_all = [&]() {
#pragma unroll 1
    for (int n = 4; n <= NA; ++n) done(n);
  };
  const int lp = (lane < DH / 4 ? lane : 0) * 4;
  auto weight = [&](int it, int x0, int cnt) -> float {
    const int mlw = WS_PML + (x0 + (lane & (cnt - 1))) * 64 + (it < 4 ? 0 : 32) + it * 4;
    u32x4 ml;
#pragma unroll 1
    for (unsigned spin = 0; spin < SPIN_CAP; ++spin) {
      ml = ld128(rs, mlw);
      if (__all(min4(ml) != 0u)) break;
      __builtin_amdgcn_s_sleep(16);
    }
    const float mk = __uint_as_float(ml[0]);
    const float e = __builtin_amdgcn_exp2f(mk - max_x16_32(row_max(mk)));
    return e / (wave_sum(e * __uint_as_float(ml[1])) * ((float)cnt * (1.0f / 64.0f))) * __uint_as_float(ml[2]);
  };
  auto prep = [&]() {
    if (tw == 0) {
      const unsigned tok = __builtin_amdgcn_readfirstlane(r0) + 1u;
      const int c = lane & (GS - 1);
      unsigned echoed = 0u, qe = s_c[S_ECHO + c];
      bool first = true, released = false, sent = false;
#pragma unroll 1
      while ((int)(clk() - t0) < HST) {
        const unsigned qn = ld32(rs, qf + (GS + c) * 32);
        if (mm < NPROD && __any(qn != qe)) {
          if (lane < 32) st32(rs, qf + mm * 32 + lane, qn);
          qe = qn;
        }
        __builtin_amdgcn_s_sleep(64);
      }
      int slow = 0;
      unsigned fold = 0u;
      auto lag = [&]() {
        const unsigned v = __hip_atomic_load(s_c + S_MOVED + (lane & 7), __ATOMIC_RELAXED, __HIP_MEMORY_SCOPE_WORKGROUP);
        if (FOLD) fold += v;
        unsigned a = 0u;
#pragma unroll
        for (int k = FOLD ? 0 : 4; k < 8; ++k) a += __builtin_amdgcn_readlane(v, k);
        return a >> 2;
      };
      auto tell = [&]() { if (lane < 4) { s_c[S_SLOW + 2 * lane] = slow; s_c[S_SLOW + 2 * lane + 1] = fold; } };
      const unsigned behind = lag();
#pragma unroll 1
      for (unsigned spin = 0; spin < SPIN_CAP && !released; ++spin) {
        const unsigned qn = ld32(rs, qf + (GS + c) * 32);
        const u32x2 rv = ld64(rs, WS_HR + bid * 2);
        const unsigned nv = __builtin_amdgcn_readfirstlane(ld32(rs, WS_HN + bid));
        if (__builtin_amdgcn_readfirstlane(rv[0]) - tok <= NRD) {
          released = true;
          slow = __builtin_amdgcn_readfirstlane(rv[1]) >> 16;
          if (lane == 0) { s_c[S_FIRST] = min((t0 - rv[1]) & 0xffffu, 300u) + 1u; }
        } else {
          if (mm < NPROD && __any(qn != qe)) {
            if (lane < 32) st32(rs, qf + mm * 32 + lane, qn);
            qe = qn;
          }
          if (first || nv != echoed) {
            echoed = nv; first = false;
            if (lane < 8) st128(rs, WS_HA + bid * 32 + lane * 4, u32x4{echoed, tok, t0, behind});
          }
          if (bid == 0 && !sent) sent = leader_release(rs, lane, t0, 0u);
          __builtin_amdgcn_s_sleep(32);
        }
      }
      tell();
      if (lane == 0) s_c[F_REL] = 1u;
      if (lane < DH / 4) {
        u32x4 p[3][2];
#pragma unroll
        for (int e = 0; e < 3; ++e)
#pragma unroll
          for (int k = 0; k < 2; ++k) p[e][k] = ld128nt(rs, WS_QP + (((e ? 8 + e * 8 + (xb >> 5) : xb >> 4) * 2 + k) * NB + 1) * DH + lane * 4);
#pragma unroll
        for (int e = 0; e < 3; ++e) *reinterpret_cast<u32x4*>(&s_q[(1 + e) * DH + lane * 4]) = q_pack(p[e][0], p[e][1]);
      }
      LGKM0();
      if (lane == 0) s_c[F_Q2] = 3u;
      unsigned prev = slow;
#pragma unroll 1
      for (unsigned rd = 1; rd <= NRD; ++rd) {
        const int tr = RT0 + (int)(rd - 1) * RTS;
        until(t0, tr);
        const unsigned bh = lag();
        if (bh > RMIN && lane < 8) st128(rs, WS_HA + bid * 32 + lane * 4, u32x4{echoed + rd, tok, t0, bh});
        if (bid == 0) { until(t0, tr + 220); leader_release(rs, lane, t0, rd); }
        until(t0, tr + 500);
        const u32x2 rv = ld64(rs, WS_HR + bid * 2);
        if (__builtin_amdgcn_readfirstlane(rv[0]) != tok + rd) {
          prev = 0u;
          slow = slow > 0 ? max(slow - GB, 0) : slow - (rd <= PRD ? PRB : 0);
        } else {
          const unsigned ex = __builtin_amdgcn_readfirstlane(rv[1]) >> 16;
          slow += ex + min(ex, prev) * (rd == NRD ? FBL : FBP) / 100u;
          prev = ex;
        }
        tell();
      }
    }
    if (tw == 3) {
#pragma unroll 1
      for (int ik = 0; ik < NS; ++ik) {
        const int it = seg_of_k(ik), nv = (seg_beg(it + 1) - seg_beg(it)) >> 4;
        lds_wait<4>(s_c + C_SC + ik, ik == 0 ? NW : NMW);
        f32x4* sp = reinterpret_cast<f32x4*>(s_sc + seg_beg(it) * 16) + lane;
        f32x4 v[4];
#pragma unroll
        for (int k = 0; k < 4; ++k) v[k] = sp[(k & (nv - 1)) * 64];
        const f32x4 mx = __builtin_elementwise_max(__builtin_elementwise_max(v[0], v[1]), __builtin_elementwise_max(v[2], v[3]));
        const float m = max_x16_32(row_max(fmaxf(fmaxf(mx[0], mx[1]), fmaxf(mx[2], mx[3]))));
        float l = 0.f;
#pragma unroll
        for (int k = 0; k < 4; ++k) {
#pragma unroll
          for (int e = 0; e < 4; ++e) v[k][e] = __builtin_amdgcn_exp2f(v[k][e] - m);
          if (k < nv) {
            l += (v[k][0] + v[k][1]) + (v[k][2] + v[k][3]);
            sp[k * 64] = v[k];
          }
        }
        l = wave_sum(l);
        if (lane == 0) s_ml[it] = u32x4{nz_bits(m), nz_bits(l), 0x3f800000u, 0x3f800000u};
        LGKM0();
        if (lane == 0) s_c[F_P] = ik + 1;
      }
    }
  };
  auto pull = [&]() {
    typedef __attribute__((address_space(1))) const unsigned g_u32;
    const int id = (ro - 16) * 4 + tw;
    constexpr int RW = 160 - 32 * GE, LW = RW * 4 * 3 * 2 / 64;
    g_u32* p1[2];
#pragma unroll
    for (int i = 0; i < 2; ++i) {
      const int l = id * LW + min(i * 32 + (lane >> 1), max(LW - 1, 0)), m = l >= LW * 32 ? l - LW * 32 : l, rr = m / 3;
      p1[i] = (g_u32*)((l >= LW * 32 ? wu : wg) + (size_t)(rr / max(RW, 1) * 160 + 32 * GE + rr % max(RW, 1)) * FF + xl * 192 + (m % 3) * 64 + (lane & 1) * 32);
    }
    g_u32* p2 = (g_u32*)(wd + (size_t)(id * 24 + min(lane >> 1, 23)) * DM + xl * 64 + (lane & 1) * 32);
    asm volatile("" : "+v"(p1[0]), "+v"(p1[1]), "+v"(p2));
    if (GLEAD) lds_wait<1>(s_c + C_LAST, GCNT); else done(NS);
    const unsigned tp = clk() + PW + tw * PS;
    until(tp, 0);
    const unsigned v0 = LW > 0 ? p1[0][0] : 0u;
    MEMFENCE();
    until(tp, 4 * PS);
    const unsigned v1 = LW > 32 ? p1[1][0] : 0u;
    MEMFENCE();
    until(tp, DPW);
    const unsigned v2 = p2[0], v3 = p2[xl < 2 ? 256 : 0];
    MEMFENCE();
    return (v0 ^ v1) + (v2 ^ v3);
  };
  if (isW) {
    u32x4 bx[24];
    const char* p = reinterpret_cast<const char*>(wo + (size_t)((w_ks * 4 + tw) * DH + r) * DM + w_ct * 64 + j * 8);
#pragma unroll 1
    for (int i = 0; i < 24; ++i) { until(t0, tw == 0 ? W0START + i * (W0SPAN / 24) : W1START + i * (W1SPAN / 24)); glds16_nt(p, lds_base + i * 1024); p += 8 * DM * 2; VMCNT(WFD); }
    asm volatile("s_waitcnt vmcnt(0)" ::: "memory");
#pragma unroll
    for (int s = 0; s < 6; ++s) {
      u32x2 e[8];
#pragma unroll
      for (int k = 0; k < 8; ++k) e[k] = *reinterpret_cast<const u32x2*>(reg + (s * 32 + qd * 8 + k) * 128 + mr * 8);
#pragma unroll
      for (int k = 0; k < 4; ++k) {
        bx[s * 4 + 0][k] = PERM_LO(e[2 * k + 1][0], e[2 * k][0]); bx[s * 4 + 1][k] = PERM_HI(e[2 * k + 1][0], e[2 * k][0]);
        bx[s * 4 + 2][k] = PERM_LO(e[2 * k + 1][1], e[2 * k][1]); bx[s * 4 + 3][k] = PERM_HI(e[2 * k + 1][1], e[2 * k][1]);
      }
    }
#pragma unroll
    for (int s = 0; s < 24; s += 4) asm volatile("" : "+v"(bx[s]), "+v"(bx[s + 1]), "+v"(bx[s + 2]), "+v"(bx[s + 3]));
    LGKM0();
    prep();
    lds_wait<8>(s_c + F_REL, 1u);
    const int hh = w_ks * 4 + tw;
#pragma unroll 1
    for (int pass = 0; pass < 3; ++pass) {
      if (pass < 2) {
        done(pass ? NA : 4);
        pause(pass ? WDELAY2 : WDELAY2 + 300);
        const int b0 = pass * 2, b1 = pass + 1;
        u32x4 v0, v1;
#pragma unroll 1
        for (unsigned spin = 0; spin < SPIN_CAP; ++spin) {
          v0 = ld128(rs, WS_OC + (b0 * NH + hh) * DH + lp);
          v1 = ld128(rs, WS_OC + (b1 * NH + hh) * DH + lp);
          if (__all(min(min4(v0), min4(v1)) != 0u)) break;
          __builtin_amdgcn_s_sleep(16);
        }
        if (lane < DH / 4) {
          *reinterpret_cast<u32x2*>(reg + (b0 * 4) * AW_LD + lane * 8) = u32x2{v0[0], v0[1]};
          *reinterpret_cast<u32x2*>(reg + (b0 * 4 + 1) * AW_LD + lane * 8) = u32x2{v0[2], v0[3]};
          *reinterpret_cast<u32x2*>(reg + (b1 * 4) * AW_LD + lane * 8) = u32x2{v1[0], v1[1]};
          *reinterpret_cast<u32x2*>(reg + (b1 * 4 + 1) * AW_LD + lane * 8) = u32x2{v1[2], v1[3]};
        }
      } else {
        const float wk = weight(NS - 1, hh * GS, GS);
        float w[GS];
#pragma unroll
        for (int k = 0; k < GS; ++k) { w[k] = __uint_as_float(__builtin_amdgcn_readlane(__float_as_uint(wk), k)); asm volatile("" : "+s"(w[k])); }
        done(NS);
        __builtin_amdgcn_s_setprio(3);
        const int po = WS_PO + hh * GS * DH;
        u32x4 pv[GS];
#pragma unroll 1
        for (unsigned spin = 0; spin < SPIN_CAP; ++spin) {
#pragma unroll
          for (int k = 0; k < GS; ++k) pv[k] = __builtin_amdgcn_raw_buffer_load_b128(rs, lp * 4, (po + k * DH) * 4, AUX_SC1);
          unsigned mn = min4(pv[0]);
#pragma unroll
          for (int k = 1; k < GS; ++k) mn = min(mn, min4(pv[k]));
          if (__all(mn != 0u)) break;
          __builtin_amdgcn_s_sleep(1);
        }
        f32x4 o = w[0] * __builtin_bit_cast(f32x4, pv[0]), o2 = w[1] * __builtin_bit_cast(f32x4, pv[1]);
#pragma unroll
        for (int k = 2; k < GS; k += 2) { o += w[k] * __builtin_bit_cast(f32x4, pv[k]); o2 += w[k + 1] * __builtin_bit_cast(f32x4, pv[k + 1]); }
        u32x2 hi, lw;
        split4(o + o2, hi, lw);
        if (lane < DH / 4) {
          *reinterpret_cast<u32x2*>(reg + ((NB - 1) * 4) * AW_LD + lane * 8) = hi;
          *reinterpret_cast<u32x2*>(reg + ((NB - 1) * 4 + 1) * AW_LD + lane * 8) = lw;
        }
      }
      CFENCE();
      u32x4 a[6];
#pragma unroll
      for (int s = 0; s < 6; ++s) a[s] = *reinterpret_cast<const u32x4*>(reg + mr * AW_LD + s * 64 + qd * 16);
      __builtin_amdgcn_sched_barrier(0);
      f32x4 c[4];
#pragma unroll
      for (int t = 0; t < 4; ++t) c[t] = f32x4{0.f, 0.f, 0.f, 0.f};
#pragma unroll
      for (int s = 0; s < 6; ++s)
#pragma unroll
        for (int t = 0; t < 4; ++t) c[t] = mfma16(a[s], bx[s * 4 + t], c[t]);
      const bool mine = max(qd - 1, 0) == pass;
      f32x4* r4 = reinterpret_cast<f32x4*>(red);
      if (mine) r4[tw * 64 + lane] = f32x4{c[0][0] + c[0][1], c[1][0] + c[1][1], c[2][0] + c[2][1], c[3][0] + c[3][1]};
      const int k = arrive(s_c + C_W + pass);
      f32x4 sum = (r4[lane] + r4[64 + lane]) + (r4[128 + lane] + r4[192 + lane]);
      asm volatile("" : "+v"(sum));
      if (arrived(k) == 3) {
        if (mine) st128(rs, WS_SLAB + (w_ks * NB + qd) * DM + w_ct * 64 + mr * 4, u32x4{nz_bits(sum[0]), nz_bits(sum[1]), nz_bits(sum[2]), nz_bits(sum[3])});
      }
    }
  } else if (isG || isH) {
    f32x4* rst = reinterpret_cast<f32x4*>(red);
    const int hb = isH ? xl - 2 : NB - 1;
    uint2 wr4 = make_uint2(0u, 0u), xg = wr4;
    if (lane < 40) {
      wr4 = *reinterpret_cast<const uint2*>(wr + tw * 160 + lane * 4);
      xg = *reinterpret_cast<const uint2*>(x + row_of(hb) * DM + tw * 160 + lane * 4);
    }
    const int eb = mr >> 2;
    const float cq = bf_lo(*reinterpret_cast<const unsigned*>(x + row_of(eb) * DM));
    const float wsum = wave_sum((bf_lo(wr4.x) + bf_hi(wr4.x)) + (bf_lo(wr4.y) + bf_hi(wr4.y)));
    f32x4 xc = f32x4{bf_lo(xg.x), bf_hi(xg.x), bf_lo(xg.y), bf_hi(xg.y)} - __uint_as_float(__builtin_amdgcn_readlane(__float_as_uint(cq), hb * 4));
    asm volatile("" : "+v"(xc));
    typedef __attribute__((address_space(1))) const u32x4 g_u32x4;
    g_u32x4* wp[10];
    wp[0] = (g_u32x4*)(((mr & 4) ? wu : wg) + (size_t)(tw * 160 + qd * 8 + (mr >> 3) * 4) * FF + g_cg * 32 + (mr & 3) * 8);
    u32x4 e[20], bx[20];
    auto frag = [&](int s) __attribute__((always_inline)) {
      unsigned w[8][2];
#pragma unroll
      for (int i = 0; i < 4; ++i)
#pragma unroll
        for (int d = 0; d < 2; ++d) {
          w[i][d] = __builtin_amdgcn_update_dpp(e[s * 4 + i][d], e[s * 4 + i][2 + d], DPP_ROR8, 0xf, 0xc, false);
          w[4 + i][d] = __builtin_amdgcn_update_dpp(e[s * 4 + i][2 + d], e[s * 4 + i][d], DPP_ROR8, 0xf, 0x3, false);
        }
#pragma unroll
      for (int k = 0; k < 4; ++k) {
        bx[s * 4 + 0][k] = PERM_LO(w[2 * k + 1][0], w[2 * k][0]); bx[s * 4 + 1][k] = PERM_HI(w[2 * k + 1][0], w[2 * k][0]);
        bx[s * 4 + 2][k] = PERM_LO(w[2 * k + 1][1], w[2 * k][1]); bx[s * 4 + 3][k] = PERM_HI(w[2 * k + 1][1], w[2 * k][1]);
      }
      PIN4(bx, s * 4);
    };
    if (isG) {
#pragma unroll
      for (int i = 0; i < 4 * GE; ++i) {
        until(t0, tw == 0 ? W0START + i * (W0SPAN / max(4 * GE, 1)) : W1START + i * (W1SPAN / max(4 * GE, 1)));
        e[i] = __builtin_nontemporal_load(wp[0] + ((i >> 2) * 32 + (i & 3)) * (FF / 8));
        MEMFENCE();
      }
#pragma unroll
      for (int s = 0; s < GE; ++s) frag(s);
    }
    prep();
    lds_wait<8>(s_c + F_REL, 1u);
    auto hrow = [&](bool fast) -> f32x4 {
      f32x4 hv = {0.f, 0.f, 0.f, 0.f};
      if (lane < 40) {
        const int off = (WS_SLAB + hb * DM + tw * 160 + lane * 4) * 4;
        u32x4 sl[KS];
#pragma unroll 1
        for (unsigned spin = 0; spin < SPIN_CAP; ++spin) {
#pragma unroll
          for (int ks = 0; ks < KS; ++ks) sl[ks] = __builtin_amdgcn_raw_buffer_load_b128(rs, off, ks * (NB * DM * 4), AUX_SC1);
          unsigned mn = min4(sl[0]);
#pragma unroll
          for (int ks = 1; ks < KS; ++ks) mn = min(mn, min4(sl[ks]));
          if (!__any(mn == 0u)) break;
          if (fast) __builtin_amdgcn_s_sleep(1); else __builtin_amdgcn_s_sleep(16);
        }
#define SL(k) __builtin_bit_cast(f32x4, sl[k])
        hv = xc + (((SL(0) + SL(1)) + (SL(2) + SL(3))) + ((SL(4) + SL(5)) + (SL(6) + SL(7))));
#undef SL
      }
      return hv;
    };
    auto sums = [&](f32x4 hv) -> f32x4 {
      const float s1 = wave_sum((hv[0] + hv[1]) + (hv[2] + hv[3]));
      const float s2 = wave_sum((hv[0] * hv[0] + hv[1] * hv[1]) + (hv[2] * hv[2] + hv[3] * hv[3]));
      const float dot = wave_sum((hv[0] * bf_lo(wr4.x) + hv[1] * bf_hi(wr4.x)) + (hv[2] * bf_lo(wr4.y) + hv[3] * bf_hi(wr4.y)));
      return f32x4{s1, s2, dot, wsum};
    };
    if (isH) {
      if (tw == 3 || hb == 2) done_all(); else done(4);
      if (tw != 3 || hb == 2) pause(hb < 2 ? HDELAY : HDELAY2);
      const f32x4 hv = hrow(false), sm = sums(hv);
      u32x4 v = split8(hv, (lane & 1) != 0);
      if (lane >= 40) v = u32x4{nz_bits(sm[0]), nz_bits(sm[1]), nz_bits(sm[2]), nz_bits(sm[3])};
      if (lane < 48) st128(rs, WS_H + (hb * 4 + tw) * 192 + lane * 4, v);
      done_all();
      const unsigned pl = pull();
      asm volatile("" :: "v"(pl));
      return;
    }
    done_all();
#pragma unroll
    for (int i = 2 * GE; i < 10; ++i) { wp[i] = wp[0] + ((i >> 1) * 32 + (i & 1) * 2) * (FF / 8); asm volatile("" : "+v"(wp[i])); }
    done(NS);
    if (GW) until(clk(), GW);
#pragma unroll
    for (int i = 4 * GE; i < 20; ++i) e[i] = wp[i >> 1][(i & 1) * (FF / 8)];
    u32x4 a[5], sm;
    const int hoff = (WS_H + (min(mr >> 2, NB - 2) * 4 + tw) * 192 + qd * 8 + (mr & 1) * 4) * 4;
#pragma unroll
    for (int s = 0; s < 5; ++s) a[s] = __builtin_amdgcn_raw_buffer_load_b128(rs, hoff + s * 128, 0, AUX_SC1);
    sm = ld128(rs, WS_H + (min(lane, NB - 2) * 4 + tw) * 192 + 160);
    MEMFENCE();
#pragma unroll
    for (int s = GE; s < 5; ++s) frag(s);
    {
#pragma unroll 1
      for (unsigned spin = 0; spin < SPIN_CAP; ++spin) {
        unsigned mn = min4(sm);
#pragma unroll
        for (int s = 0; s < 5; ++s) mn = min(mn, min4(a[s]));
        if (__all(mn != 0u)) break;
        __builtin_amdgcn_s_sleep(2);
#pragma unroll
        for (int s = 0; s < 5; ++s) a[s] = __builtin_amdgcn_raw_buffer_load_b128(rs, hoff + s * 128, 0, AUX_SC1);
        sm = ld128(rs, WS_H + (min(lane, NB - 2) * 4 + tw) * 192 + 160);
      }
      if (lane < NB - 1) rst[lane * 4 + tw] = __builtin_bit_cast(f32x4, sm);
#pragma unroll
      for (int s = 0; s < 5; ++s) {
        if (mr & 2) a[s] = u32x4{0x3f803f80u, 0x3f803f80u, 0x3f803f80u, 0x3f803f80u};
        asm volatile("" : "+v"(a[s]));
      }
    }
    const f32x4 hv = hrow(true);
    if (lane < 40) {
      u32x2 hi, lw;
      split4(hv, hi, lw);
      *reinterpret_cast<u32x2*>(reg + lane * 8) = hi;
      *reinterpret_cast<u32x2*>(reg + AG_LD + lane * 8) = lw;
    }
    CFENCE();
    if ((mr & 14) == 12) {
#pragma unroll
      for (int s = 0; s < 5; ++s) a[s] = *reinterpret_cast<const u32x4*>(reg + (mr & 1) * AG_LD + s * 64 + qd * 16);
    }
    __builtin_amdgcn_sched_barrier(0);
    f32x4 c[4];
#pragma unroll
    for (int t = 0; t < 4; ++t) c[t] = f32x4{0.f, 0.f, 0.f, 0.f};
#pragma unroll
    for (int s = 0; s < 5; ++s)
#pragma unroll
      for (int t = 0; t < 4; ++t) c[t] = mfma16(a[s], bx[s * 4 + t], c[t]);
    { const f32x4 s3 = sums(hv); if (lane == 0) rst[(NB - 1) * 4 + tw] = s3; }
    const int slot = qd * 16 + (mr & 7) * 2 + (mr >> 3);
    *reinterpret_cast<f32x4*>(reg + 4096 + slot * 16) = f32x4{c[0][0] + c[0][1], c[1][0] + c[1][1], c[0][2], c[1][2]};
    *reinterpret_cast<f32x4*>(reg + 4096 + 1152 + slot * 16) = f32x4{c[2][0] + c[2][1], c[3][0] + c[3][1], c[2][2], c[3][2]};
    const int k = arrive(s_c + C_T);
    f32x4 ts[4], pg[4], pu[4];
#pragma unroll
    for (int w = 0; w < 4; ++w) {
      const unsigned char* src = smem + OFF_REG + w * REG + 4096 + (lane & 1) * 1152 + (eb * 16 + qd * 2 + ((lane >> 1) & 1)) * 16;
      ts[w] = rst[eb * 4 + w];
      pg[w] = *reinterpret_cast<const f32x4*>(src); pu[w] = *reinterpret_cast<const f32x4*>(src + 128);
    }
    PIN4(ts, 0); PIN4(pg, 0); PIN4(pu, 0);
    if (arrived(k) == 3) {
      const f32x4 t = (ts[0] + ts[1]) + (ts[2] + ts[3]), g4 = (pg[0] + pg[1]) + (pg[2] + pg[3]), u4 = (pu[0] + pu[1]) + (pu[2] + pu[3]);
      const float d = t[0] * (-1.0f / DM);
      const float rstd = __builtin_amdgcn_rsqf(fmaxf(t[1] + d * t[0], 0.f) * (1.0f / DM) + LN_EPS);
      const float g0 = rstd * (g4[0] + d * g4[2]), g1 = rstd * (g4[1] + d * g4[3]);
      const float a0 = g0 * __builtin_amdgcn_rcpf(1.0f + __expf(-g0)) * (rstd * (u4[0] + d * u4[2]));
      const float a1 = g1 * __builtin_amdgcn_rcpf(1.0f + __expf(-g1)) * (rstd * (u4[1] + d * u4[3]));
      const __hip_bfloat162 a2 = __halves2bfloat162(__float2bfloat16(a0), __float2bfloat16(a1));
      const float aw = __uint_as_float(max(*reinterpret_cast<const unsigned*>(&a2), 1u));
      const u32x4 av = {__float_as_uint(aw), __float_as_uint(dpp<0x55>(aw)), __float_as_uint(dpp<0xAA>(aw)), __float_as_uint(dpp<0xFF>(aw))};
      const float rr = rstd * (t[2] + d * t[3]);
      const u32x4 sv = {nz_bits(cq - d), nz_bits(rstd), nz_bits(__builtin_amdgcn_rcpf(1.0f + __expf(-rr))), 0x3f800000u};
      const bool stat = g_cg == 0 && (lane & 51) == 1;
      if ((lane & 3) == 0 || stat) st128(rs, stat ? WS_ST + eb * 4 : WS_ACT + g_cg * 64 + lane, stat ? sv : av);
    }
  } else if (isD) {
    const uint2 xd = *reinterpret_cast<const uint2*>(x + row_of(tw) * DM + d_ct * 32 + j * 4);
    const __amdgpu_buffer_rsrc_t rd = __builtin_amdgcn_make_buffer_rsrc(const_cast<bf16*>(wd), 0, FF * DM * 2, 0x00020000);
    prep();
    lds_wait<8>(s_c + F_REL, 1u);
    done_all();
    const unsigned pl = pull();
    done(NS);
    asm volatile("" :: "v"(pl));
    const unsigned te = clk();
    if (DW) until(te, DW);
    u32x4 bd[24];
    {
      const int off = ((tw * 384 + (mr >> 3) * 32 + qd * 8 + (mr & 4)) * DM + d_ct * 32 + (mr & 3) * 8) * 2;
      u32x4 e[24];
#pragma unroll
      for (int i = 0; i < 24; ++i) e[i] = __builtin_amdgcn_raw_buffer_load_b128(rd, off + (i & 3) * (DM * 2), (i >> 2) * 64 * (DM * 2), 0);
      MEMFENCE();
#pragma unroll
      for (int s = 0; s < 6; ++s) {
        unsigned w[8][2];
#pragma unroll
        for (int i = 0; i < 4; ++i)
#pragma unroll
          for (int d = 0; d < 2; ++d) {
            w[i][d] = __builtin_amdgcn_update_dpp(e[s * 4 + i][d], e[s * 4 + i][2 + d], 0x114, 0xf, 0xa, false);
            w[4 + i][d] = __builtin_amdgcn_update_dpp(e[s * 4 + i][2 + d], e[s * 4 + i][d], 0x104, 0xf, 0x5, false);
          }
#pragma unroll
        for (int k = 0; k < 4; ++k) {
          bd[s * 4 + 0][k] = PERM_LO(w[2 * k + 1][0], w[2 * k][0]); bd[s * 4 + 1][k] = PERM_HI(w[2 * k + 1][0], w[2 * k][0]);
          bd[s * 4 + 2][k] = PERM_LO(w[2 * k + 1][1], w[2 * k][1]); bd[s * 4 + 3][k] = PERM_HI(w[2 * k + 1][1], w[2 * k][1]);
        }
        PIN4(bd, s * 4);
      }
    }
    until(te, DLATE);
    {
      u32x4 sl;
      unsigned spin = 0;
#pragma unroll 1
      for (; spin < SPIN_CAP; ++spin) {
        sl = ld128(rs, WS_SLAB + (r * NB + tw) * DM + d_ct * 32 + j * 4);
        if (__all(min4(sl) != 0u)) break;
        __builtin_amdgcn_s_sleep(2);
      }
      if (tw == NB - 1 && lane == 0) s_c[C_W + 1] = clk() + (spin ? DLATE2 : 0);
      f32x4 hv = {bf_lo(xd.x), bf_hi(xd.x), bf_lo(xd.y), bf_hi(xd.y)};
#pragma unroll
      for (int i = 0; i < 4; ++i) hv[i] += sum_r(__uint_as_float(sl[i]));
      if (lane < 8) reinterpret_cast<f32x4*>(red + 512 + tw * 32)[lane] = hv;
      LGKM0();
      lds_add(s_c + C_W, lane);
      lds_wait<1>(s_c + C_W, 4u);
    }
    const int col = (mr & 3) * 8 + (mr & 4) + (mr >> 3) * 2;
    f32x2 hv = *reinterpret_cast<const f32x2*>(red + 512 + qd * 32 + col);
    asm volatile("" : "+v"(hv));
    u32x4 a[6], st;
#pragma unroll
    for (int s = 0; s < 6; ++s) a[s] = u32x4{1u, 1u, 1u, 1u};
    const int aoff = (WS_ACT + tw * 12 * 64 + ((mr >> 1) & 1) * 64 + qd * 16 + (mr >> 2) * 4) * 4;
    until(lds_get(s_c + C_W + 1), 0);
#pragma unroll 1
    for (unsigned spin = 0; spin < SPIN_CAP; ++spin) {
      st = ld128(rs, WS_ST + qd * 4);
      if ((mr & 1) == 0) {
#pragma unroll
        for (int s = 0; s < 6; ++s) a[s] = __builtin_amdgcn_raw_buffer_load_b128(rs, aoff + s * 512, 0, AUX_SC1);
      }
      unsigned mn = min4(st);
#pragma unroll
      for (int s = 0; s < 6; ++s) mn = min(mn, min4(a[s]));
      if (__all(mn != 0u)) break;
      __builtin_amdgcn_s_sleep(1);
    }
    f32x4 c[4];
#pragma unroll
    for (int t = 0; t < 4; ++t) c[t] = f32x4{0.f, 0.f, 0.f, 0.f};
#pragma unroll
    for (int s = 0; s < 6; ++s)
#pragma unroll
      for (int t = 0; t < 4; ++t) c[t] = mfma16(a[s], bd[s * 4 + t], c[t]);
    const f32x4 sf = __builtin_bit_cast(f32x4, st);
    hv += (hv - sf[0]) * sf[1];
    const bool up = mr >= 8;
    const float k0 = up ? c[2][2] : c[0][0], k1 = up ? c[3][2] : c[1][0], o0 = up ? c[0][2] : c[2][0], o1 = up ? c[1][2] : c[3][0];
    f32x2* r2 = reinterpret_cast<f32x2*>(red);
    r2[tw * 64 + lane] = f32x2{k0 + dpp<DPP_ROR8>(o0), k1 + dpp<DPP_ROR8>(o1)};
    const int k = arrive(s_c + C_T);
    f32x2 y = hv + ((r2[lane] + r2[64 + lane]) + (r2[128 + lane] + r2[192 + lane])) * sf[2];
    asm volatile("" : "+v"(y));
    if (arrived(k) == 3) {
      const __hip_bfloat162 o2 = __halves2bfloat162(__float2bfloat16(y[0]), __float2bfloat16(y[1]));
      __hip_atomic_store(reinterpret_cast<unsigned*>(out + row_of(qd) * DM + d_ct * 32 + col), *reinterpret_cast<const unsigned*>(&o2), __ATOMIC_RELAXED, __HIP_MEMORY_SCOPE_AGENT);
    }
  } else {
    prep();
    if (isC && tw == 1) {
      lds_wait<8>(s_c + F_REL, 1u);
      const int cs = c_it != 1 ? c_it * 2 : max(g >> 3, 1), cnt = c_it != 1 ? 8 : (g < 16 ? 16 : 32), x0 = c_it != 1 ? g * 8 : (g < 16 ? g * 16 : (g & 7) * 32);
      done(c_it < 2 ? 4 : NA);
      pause(WDELAY);
      const float wk = weight(cs, x0, cnt);
      f32x4 o = {0.f, 0.f, 0.f, 0.f};
#pragma unroll 1
      for (int k0 = 0; k0 < cnt; k0 += 16) {
        u32x2 pv[16];
#pragma unroll
        for (int k = 0; k < 16; ++k) pv[k] = __builtin_amdgcn_raw_buffer_load_b64(rs, lp * 2, (WS_PQ + ((x0 + ((k0 + k) & (cnt - 1))) * 5 + cs) * (DH / 2)) * 4, AUX_EARLY);
#pragma unroll
        for (int k = 0; k < 16; ++k) {
          const float w = k0 + k < cnt ? __uint_as_float(__builtin_amdgcn_readlane(__float_as_uint(wk), k0 + k)) : 0.f;
          o += w * f32x4{(float)(short)pv[k][0], (float)((int)pv[k][0] >> 16), (float)(short)pv[k][1], (float)((int)pv[k][1] >> 16)};
        }
      }
      u32x2 hi, lw;
      split4(o, hi, lw);
      if (lane < DH / 4) st128(rs, WS_OC + (c_it * NG + g) * DH + lane * 4, u32x4{max(hi[0], 1u), max(hi[1], 1u), max(lw[0], 1u), max(lw[1], 1u)});
    }
    done_all();
    const unsigned pl = pull();
    asm volatile("" :: "v"(pl));
  }
  if (tw == 3) done_all();
}

extern "C" void kernel_launch(void* const* d_in, const int* in_sizes, int n_in, void* d_out, int out_size, void* d_ws,
                              size_t ws_size, hipStream_t stream) {
  const bf16* x = (const bf16*)d_in[0];
  const bf16* kc = (const bf16*)d_in[1];
  const bf16* vc = (const bf16*)d_in[2];
  const bf16* wq = (const bf16*)d_in[3];
  const bf16* wo = (const bf16*)d_in[4];
  const bf16* wr = (const bf16*)d_in[6];
  const bf16* wg = (const bf16*)d_in[7];
  const bf16* wu = (const bf16*)d_in[8];
  const bf16* wd = (const bf16*)d_in[9];
  fused_kernel<<<NWG, NW * 64, 0, stream>>>(x, kc, vc, wq, wo, wr, wg, wu, wd, (float*)d_ws, (bf16*)d_out);
}
```

```cpp
#include <hip/hip_runtime.h>
#include <hip/hip_bf16.h>
#include <stdint.h>

typedef __hip_bfloat16 bf16;
using u32x2 = __attribute__((ext_vector_type(2))) unsigned;
using u32x4 = __attribute__((ext_vector_type(4))) unsigned;
using f32x4 = __attribute__((ext_vector_type(4))) float;
using f32x2 = __attribute__((ext_vector_type(2))) float;
using bf16x8 = __attribute__((ext_vector_type(8))) __bf16;

constexpr int NB = 4, DM = 640, NH = 32, SK = 8192, DH = 192, HD = NH * DH, FF = 1536;
constexpr float LN_EPS = 1e-5f;
constexpr float QSCALE = 0.07216878364870322f * 1.4426950408889634f;

constexpr int NWG = 256;
constexpr int GS = 8, NG = NWG / GS, NI = GS / 2;
constexpr int HPG = NI / NB;
constexpr int PPI = 512 / GS;
constexpr int NPAIR = NI * PPI;

constexpr int TAUK = 1320, TAUV = 1332;
constexpr int TSTART = 140;
constexpr int WGAP = 165, WP0 = 64, WPN = 176;
constexpr int VGAP = 50;
constexpr int DEEP1 = 100, DEEP2 = 200, SDEEP = 0, COARSE = 1, LAGOFF = 20, ENDT = 12, ENDS = 2, PHASE = 0;
constexpr int SPRINT = 0;
constexpr int JUMP = 145;
constexpr int HST = 2800, RT0 = 3900, RTS = 1000;
constexpr unsigned NRD = 5, RMIN = 100, FBP = 60, FBL = 150;
constexpr int W0START = 1700, W0SPAN = 1000, W1START = 2700, W1SPAN = 2600;
constexpr int ROT = 16, ROWS = 0xC9;
constexpr int FBM = 100, FBX = 15, FB0 = 170, FB1 = 55, FBS = 10;
constexpr int FBD = 20, HOLD = 800, GB = 24, GBS = 2, GBX = 60, GBF = 12, FOLD = 1;
constexpr unsigned PRD = 3; constexpr int PRB = 24;
constexpr int TLEAD = 40, PFD = 6, WFD = 3;
constexpr int QWAIT = 750, QBACK = 100;
constexpr int WDELAY = 100, WDELAY2 = 460, DLATE = 100, DLATE2 = 115;
constexpr int CPF = 1, CPT = 0, CODE = 24 * 64 * 48;
constexpr int GLEAD = 1, GCNT = 4, DP = 0, DPW = 60, GE = 0, PW = 30, PS = 0, HDELAY = 1150, HDELAY2 = 780, GW = 0, DW = 0;

constexpr int KS = 8;
constexpr int WS_PML = 0;
constexpr int WS_PO = WS_PML + NWG * 64;
constexpr int WS_PQ = WS_PO + NWG * DH;
constexpr int WS_OC = WS_PQ + NWG * 5 * (DH / 2);
constexpr int WS_SLAB = WS_OC + (NB - 1) * NH * DH;
constexpr int WS_ACT = WS_SLAB + KS * NB * DM;
constexpr int WS_H = WS_ACT + NB * FF / 2;
constexpr int WS_ST = WS_H + (NB - 1) * 4 * 192;
constexpr int WS_HN = WS_ST + 32;
constexpr int WS_HR = WS_HN + NWG;
constexpr int WS_HA = WS_HR + 2 * NWG;
constexpr int QFS = 2 * GS * 32;
constexpr int WS_QF = WS_HA + 32 * NWG;
constexpr int WS_QP = WS_QF + NG * QFS;
constexpr int WS_END = WS_QP + NH * 2 * NB * DH;
constexpr int NPROD = HPG * 6;
static_assert(WS_PO % 32 == 0 && WS_SLAB % 32 == 0 && WS_ACT % 32 == 0 && WS_ST % 32 == 0 && WS_HN % 32 == 0 && WS_QF % 32 == 0 && WS_QP % 32 == 0, "line alignment");


__device__ __forceinline__ int row_of(int b) { return (ROWS >> (2 * b)) & 3; }
__device__ __forceinline__ float bf_lo(unsigned u) { return __uint_as_float(u << 16); }
__device__ __forceinline__ float bf_hi(unsigned u) { return __uint_as_float(u & 0xffff0000u); }
__device__ __forceinline__ f32x2 bf_pair(unsigned u) { return f32x2{bf_lo(u), bf_hi(u)}; }
__device__ __forceinline__ f32x2 fma2(float a, f32x2 b, f32x2 c) { return __builtin_elementwise_fma(f32x2{a, a}, b, c); }
__device__ __forceinline__ f32x2 fma2(f32x2 a, f32x2 b, f32x2 c) { return __builtin_elementwise_fma(a, b, c); }

template <int CTRL> __device__ __forceinline__ float dpp(float x) {
  return __builtin_bit_cast(float, __builtin_amdgcn_mov_dpp(__builtin_bit_cast(int, x), CTRL, 0xf, 0xf, true));
}
constexpr int DPP_XOR1 = 0xB1, DPP_XOR2 = 0x4E, DPP_HMIRROR = 0x141, DPP_MIRROR = 0x140, DPP_ROR8 = 0x128;

__device__ __forceinline__ float sum_x16_32(float x) {
  auto s = __builtin_amdgcn_permlane16_swap(__float_as_uint(x), __float_as_uint(x), false, false);
  x = __uint_as_float(s[0]) + __uint_as_float(s[1]);
  auto t = __builtin_amdgcn_permlane32_swap(__float_as_uint(x), __float_as_uint(x), false, false);
  return __uint_as_float(t[0]) + __uint_as_float(t[1]);
}
__device__ __forceinline__ float max_x16_32(float x) {
  auto s = __builtin_amdgcn_permlane16_swap(__float_as_uint(x), __float_as_uint(x), false, false);
  x = fmaxf(__uint_as_float(s[0]), __uint_as_float(s[1]));
  auto t = __builtin_amdgcn_permlane32_swap(__float_as_uint(x), __float_as_uint(x), false, false);
  return fmaxf(__uint_as_float(t[0]), __uint_as_float(t[1]));
}
__device__ __forceinline__ float tsum32(float a, float b) {
  auto t = __builtin_amdgcn_permlane32_swap(__float_as_uint(a), __float_as_uint(b), false, false);
  return __uint_as_float(t[0]) + __uint_as_float(t[1]);
}
__device__ __forceinline__ float tsum16(float a, float b) {
  auto t = __builtin_amdgcn_permlane16_swap(__float_as_uint(a), __float_as_uint(b), false, false);
  return __uint_as_float(t[0]) + __uint_as_float(t[1]);
}
__device__ __forceinline__ float sum_r(float v) { v += dpp<DPP_ROR8>(v); return sum_x16_32(v); }
__device__ __forceinline__ float row_sum(float v) {
  v += dpp<DPP_XOR1>(v); v += dpp<DPP_XOR2>(v); v += dpp<DPP_HMIRROR>(v);
  return v + dpp<DPP_MIRROR>(v);
}
__device__ __forceinline__ float row_max(float v) {
  v = fmaxf(v, dpp<DPP_XOR1>(v)); v = fmaxf(v, dpp<DPP_XOR2>(v)); v = fmaxf(v, dpp<DPP_HMIRROR>(v));
  return fmaxf(v, dpp<DPP_MIRROR>(v));
}
__device__ __forceinline__ float wave_sum(float v) {
  v += dpp<DPP_XOR1>(v); v += dpp<DPP_XOR2>(v); v += dpp<DPP_HMIRROR>(v);
  return sum_r(v);
}

__device__ __forceinline__ u32x4 ld_nt(const char* p) { return __builtin_nontemporal_load(reinterpret_cast<const u32x4*>(p)); }

struct Tile { u32x4 d[3]; };

#define PIN4(a, o) asm volatile("" : "+v"((a)[(o) + 0]), "+v"((a)[(o) + 1]), "+v"((a)[(o) + 2]), "+v"((a)[(o) + 3]))
#define MEMFENCE() do { asm volatile("" ::: "memory"); __builtin_amdgcn_sched_barrier(0); } while (0)

__device__ __forceinline__ void glds16_nt(const void* gsrc, unsigned lds_dst) {
  unsigned keep;
  asm volatile("s_mov_b32 %0, m0\n\ts_mov_b32 m0, %2\n\ts_nop 0\n\tglobal_load_lds_dwordx4 %1, off nt\n\ts_mov_b32 m0, %0"
               : "=&s"(keep) : "v"(gsrc), "s"(lds_dst) : "memory");
}

using bf16x2 = __attribute__((ext_vector_type(2))) __bf16;
__device__ __forceinline__ float dot2(unsigned a, unsigned b, float c) { return __builtin_amdgcn_fdot2_f32_bf16(__builtin_bit_cast(bf16x2, a), __builtin_bit_cast(bf16x2, b), c, false); }
__device__ __forceinline__ float tile_score(const Tile& k, const u32x2 (&q)[12]) {
  float s0 = 0.f, s1 = 0.f;
#pragma unroll
  for (int i = 0; i < 3; ++i) {
    u32x4 v = k.d[i];
    asm("" : "+v"(v));
#pragma unroll
    for (int d = 0; d < 4; ++d) { s0 = dot2(v[d], q[i * 4 + d][0], s0); s1 = dot2(v[d], q[i * 4 + d][1], s1); }
  }
  return s0 + s1;
}
__device__ __forceinline__ void pair_put(float a, float b, float* dst, int j) {
  float s = (j < 4 ? a : b) + dpp<DPP_HMIRROR>(j < 4 ? b : a);
  s += dpp<DPP_XOR1>(s);
  s += dpp<DPP_XOR2>(s);
  if ((j & 3) == 0) dst[j * 2] = s;
}
__device__ __forceinline__ void score_put(float s, float* dst, int j) {
  s += dpp<DPP_XOR1>(s);
  s += dpp<DPP_XOR2>(s);
  s += dpp<DPP_HMIRROR>(s);
  if (j == 0) *dst = s;
}
__device__ __forceinline__ void tile_load(Tile& t, const char* base, unsigned off, int half) {
#pragma unroll
  for (int i = 0; i < 3; ++i) t.d[i] = ld_nt(base + off + half * 3072 + i * 128);
}
__device__ __forceinline__ void pair_issue(Tile& ta, Tile& tb, const char* base, unsigned off, int deep) {
  deep = __builtin_amdgcn_readfirstlane(deep);
#define WAIT_IF(level, n) asm volatile("s_cmp_lg_u32 %0, " #level "\n\ts_cbranch_scc1 .Lpi%=\n\ts_waitcnt vmcnt(" #n ")\n.Lpi%=:" :: "s"(deep) : "scc", "memory")
  const char* p = base + off;
  if constexpr (COARSE) {
    WAIT_IF(0, 3);
    ta.d[0] = ld_nt(p); ta.d[1] = ld_nt(p + 128); ta.d[2] = ld_nt(p + 256); MEMFENCE();
    WAIT_IF(0, 3); WAIT_IF(1, 6);
    tb.d[0] = ld_nt(p + 3072); tb.d[1] = ld_nt(p + 3200); tb.d[2] = ld_nt(p + 3328); MEMFENCE();
  } else {
    WAIT_IF(0, 5); ta.d[0] = ld_nt(p); MEMFENCE();
    WAIT_IF(0, 5); ta.d[1] = ld_nt(p + 128); MEMFENCE();
    WAIT_IF(0, 5); ta.d[2] = ld_nt(p + 256); MEMFENCE();
    WAIT_IF(1, 6);
    WAIT_IF(0, 5); tb.d[0] = ld_nt(p + 3072); MEMFENCE();
    WAIT_IF(0, 5); tb.d[1] = ld_nt(p + 3200); MEMFENCE();
    WAIT_IF(0, 5); tb.d[2] = ld_nt(p + 3328); MEMFENCE();
  }
#undef WAIT_IF
}
__device__ __forceinline__ void pv_last(float p, Tile& v, f32x2 (&acc)[12]) {
#pragma unroll
  for (int i = 0; i < 3; ++i) {
    asm volatile("" : "+v"(v.d[i]), "+v"(p));
#pragma unroll
    for (int d = 0; d < 4; ++d) acc[i * 4 + d] = fma2(p, bf_pair(v.d[i][d]), acc[i * 4 + d]);
    PIN4(acc, i * 4);
  }
}
__device__ __forceinline__ void q_load(u32x2 (&q)[12], const float* src, int j) {
#pragma unroll
  for (int i = 0; i < 6; ++i) {
    const u32x4 a = *reinterpret_cast<const u32x4*>(src + (i >> 1) * 64 + j * 8 + (i & 1) * 4);
    q[i * 2] = u32x2{a[0], a[1]}; q[i * 2 + 1] = u32x2{a[2], a[3]};
  }
}
#define VMCNT(n) asm volatile("s_waitcnt vmcnt(%0)" :: "n"(n) : "memory")

constexpr int AUX_SC1 = 16, AUX_EARLY = 18;
constexpr unsigned SPIN_CAP = 1u << 17;
__device__ __forceinline__ unsigned nz_bits(float v) { return max(__float_as_uint(v), 1u); }
__device__ __forceinline__ bool all_nz(u32x4 v) { return v[0] != 0u && v[1] != 0u && v[2] != 0u && v[3] != 0u; }
__device__ __forceinline__ unsigned min4(u32x4 v) { return min(min(v[0], v[1]), min(v[2], v[3])); }
__device__ __forceinline__ unsigned ld32(__amdgpu_buffer_rsrc_t rs, int word) { return __builtin_amdgcn_raw_buffer_load_b32(rs, word * 4, 0, AUX_SC1); }
__device__ __forceinline__ u32x2 ld64(__amdgpu_buffer_rsrc_t rs, int word) { return __builtin_amdgcn_raw_buffer_load_b64(rs, word * 4, 0, AUX_SC1); }
__device__ __forceinline__ u32x4 ld128(__amdgpu_buffer_rsrc_t rs, int word) { return __builtin_amdgcn_raw_buffer_load_b128(rs, word * 4, 0, AUX_SC1); }
__device__ __forceinline__ void st32(__amdgpu_buffer_rsrc_t rs, int word, unsigned v) { __builtin_amdgcn_raw_buffer_store_b32(v, rs, word * 4, 0, AUX_SC1); }
__device__ __forceinline__ void st64(__amdgpu_buffer_rsrc_t rs, int word, u32x2 v) { __builtin_amdgcn_raw_buffer_store_b64(v, rs, word * 4, 0, AUX_SC1); }
__device__ __forceinline__ void st128(__amdgpu_buffer_rsrc_t rs, int word, u32x4 v) { __builtin_amdgcn_raw_buffer_store_b128(v, rs, word * 4, 0, AUX_SC1); }

__device__ __forceinline__ u32x4 ld128nt(__amdgpu_buffer_rsrc_t rs, int word) { return __builtin_amdgcn_raw_buffer_load_b128(rs, word * 4, 0, AUX_EARLY); }
__device__ __forceinline__ void st128nt(__amdgpu_buffer_rsrc_t rs, int word, u32x4 v) { __builtin_amdgcn_raw_buffer_store_b128(v, rs, word * 4, 0, AUX_EARLY); }

__device__ __forceinline__ bool leader_release(__amdgpu_buffer_rsrc_t rs, int lane, unsigned t0, unsigned rd) {
  u32x4 a[4];
#pragma unroll
  for (int k = 0; k < 4; ++k) a[k] = ld128(rs, WS_HA + (lane * 4 + k) * 32);
  const u32x4 n = ld128(rs, WS_HN + lane * 4);
  if (rd == 0u && !__all(a[0][0] == n[0] && a[1][0] == n[1] && a[2][0] == n[2] && a[3][0] == n[3])) return false;
  const float e = fmaxf(fmaxf((float)(int)(t0 - a[0][2]), (float)(int)(t0 - a[1][2])), fmaxf((float)(int)(t0 - a[2][2]), (float)(int)(t0 - a[3][2])));
  unsigned first = t0 - (unsigned)(int)max_x16_32(row_max(e));
  float ls = 0.f, lx = 0.f;
#pragma unroll
  for (int k = 0; k < 4; ++k) {
    const float l = a[k][0] == n[k] + rd ? (float)min(a[k][3] + (rd ? 0u : min(a[k][2] - first, 300u)), 2000u) : 0.f;
    ls += l; lx = fmaxf(lx, l);
  }
  ls = wave_sum(ls) * (1.0f / NWG); lx = max_x16_32(row_max(lx));
  const int extra = min(max((int)(ls * (FBM / 100.0f) + lx * (FBX / 100.0f)) - (rd ? FB1 + (int)(NRD - rd) * FBD : FB0), 0), 600);
  first = (first & 0xffffu) | ((unsigned)extra << 16);
  if (rd != 0u && ls * NWG < HOLD) return true;
  st128(rs, WS_HR + lane * 8, u32x4{a[0][1] + rd, first, a[1][1] + rd, first});
  st128(rs, WS_HR + lane * 8 + 4, u32x4{a[2][1] + rd, first, a[3][1] + rd, first});
  return true;
}

typedef __attribute__((address_space(3))) unsigned lds_u32;
__device__ __forceinline__ unsigned lds_add(lds_u32* p, int lane) {
  unsigned k = 0;
  if (lane == 0) k = __hip_atomic_fetch_add(p, 1u, __ATOMIC_RELAXED, __HIP_MEMORY_SCOPE_WORKGROUP);
  return __builtin_amdgcn_readfirstlane(k);
}
#define CFENCE() asm volatile("" ::: "memory")
__device__ __forceinline__ int arrive(lds_u32* p) {
  int v;
  unsigned long long keep;
  asm volatile("s_mov_b64 %1, exec\n\ts_mov_b64 exec, 1\n\tds_add_rtn_u32 %0, %2, %3\n\ts_mov_b64 exec, %1"
               : "=&v"(v), "=&s"(keep) : "v"((unsigned)(size_t)p), "v"(1u) : "memory");
  return v;
}
__device__ __forceinline__ int arrived(int v) {
  asm volatile("s_waitcnt lgkmcnt(0)" : "+v"(v) :: "memory");
  return __builtin_amdgcn_readfirstlane(v);
}
__device__ __forceinline__ unsigned lds_get(lds_u32* p) {
  return __builtin_amdgcn_readfirstlane(__hip_atomic_load(p, __ATOMIC_RELAXED, __HIP_MEMORY_SCOPE_WORKGROUP));
}
template <int SLEEP> __device__ __forceinline__ void lds_wait(lds_u32* p, unsigned n) {
#pragma unroll 1
  for (unsigned spin = 0; spin < (SPIN_CAP << 4); ++spin) {
    if (lds_get(p) >= n) break;
    __builtin_amdgcn_s_sleep(SLEEP);
  }
  asm volatile("" ::: "memory");
}
__device__ __forceinline__ void pause(unsigned ticks) {
  const unsigned long long t0 = __builtin_amdgcn_s_memrealtime();
#pragma unroll 1
  for (unsigned spin = 0; spin < 4096u && __builtin_amdgcn_s_memrealtime() - t0 < ticks; ++spin) __builtin_amdgcn_s_sleep(16);
}
__device__ __forceinline__ unsigned clk() { return (unsigned)__builtin_amdgcn_s_memrealtime(); }
struct Table {
  unsigned t0; int was, moved, slowed, folded;
  u32x2 told;
  __device__ __forceinline__ int gate(int due, lds_u32* mine, bool hold = true, int sh = 0) {
    const int fold = __builtin_amdgcn_readfirstlane(told[1]);
    if (fold != folded) { const int f = min(fold - folded, moved); moved -= f; slowed += f; folded = fold; *mine = moved; }
    const int left = (int)__builtin_amdgcn_readfirstlane(told[0]) - slowed, d = max(min(left, FBS), left < -GBX ? -GBF : -GBS);
    t0 += d; was -= d; slowed += d;
    int late = (int)(clk() - t0) - due;
    if (late - was > JUMP) { t0 += late - was; moved += late - was; late = was; *mine = moved; }
    const int lag = max(late - max(left - d, 0) - LAGOFF, 0);
    mine[4] = lag;
    was = late;
#pragma unroll 1
    for (int spin = 0; spin < 65536 && hold && late < 0; ++spin) { __builtin_amdgcn_s_sleep(1); late = (int)(clk() - t0) - due; }
    return max(lag < (DEEP1 >> sh) ? 0 : (lag < (DEEP2 >> sh) ? 1 : 2), hold ? 0 : SDEEP);
  }
  __device__ __forceinline__ void listen(lds_u32* slow) {
    told = __builtin_bit_cast(u32x2, __hip_atomic_load((__attribute__((address_space(3))) unsigned long long*)slow, __ATOMIC_RELAXED, __HIP_MEMORY_SCOPE_WORKGROUP));
  }
  __device__ __forceinline__ void agree(lds_u32* all, lds_u32* mine) {
    if (moved == 0) return;
    const int m = (int)min(min(lds_get(all), lds_get(all + 1)), min(lds_get(all + 2), lds_get(all + 3))) + 30;
    if (m < moved) { t0 -= moved - m; moved = m; was = 1 << 20; *mine = moved; }
  }
};
__device__ __forceinline__ void until(unsigned t0, int due) {
#pragma unroll 1
  for (int spin = 0; spin < 65536 && (int)(clk() - t0) < due; ++spin) __builtin_amdgcn_s_sleep(1);
}
#define LGKM0() asm volatile("s_waitcnt lgkmcnt(0)" ::: "memory")

__device__ __forceinline__ void split4(f32x4 v, u32x2& hi, u32x2& lo) {
  unsigned h[4], r[4];
#pragma unroll
  for (int i = 0; i < 4; ++i) { h[i] = __float_as_uint(v[i]) & 0xffff0000u; r[i] = __float_as_uint(v[i] - __uint_as_float(h[i])); }
  hi = u32x2{__builtin_amdgcn_perm(h[1], h[0], 0x07060302u), __builtin_amdgcn_perm(h[3], h[2], 0x07060302u)};
  lo = u32x2{__builtin_amdgcn_perm(r[1], r[0], 0x07060302u), __builtin_amdgcn_perm(r[3], r[2], 0x07060302u)};
}
__device__ __forceinline__ u32x4 split8(f32x4 v, bool odd) {
  u32x2 hi, lo;
  split4(v, hi, lo);
  const unsigned n0 = __float_as_uint(dpp<DPP_XOR1>(__uint_as_float(odd ? hi[0] : lo[0]))), n1 = __float_as_uint(dpp<DPP_XOR1>(__uint_as_float(odd ? hi[1] : lo[1])));
  return u32x4{max(odd ? n0 : hi[0], 1u), max(odd ? n1 : hi[1], 1u), max(odd ? lo[0] : n0, 1u), max(odd ? lo[1] : n1, 1u)};
}
__device__ __forceinline__ u32x4 q_pack(u32x4 a, u32x4 b) {
  u32x2 hi, lo;
  split4((__builtin_bit_cast(f32x4, a) + __builtin_bit_cast(f32x4, b)) * QSCALE, hi, lo);
  return u32x4{hi[0], lo[0], hi[1], lo[1]};
}
__device__ __forceinline__ f32x4 mfma16(u32x4 a, u32x4 b, f32x4 c) {
  return __builtin_amdgcn_mfma_f32_16x16x32_bf16(__builtin_bit_cast(bf16x8, a), __builtin_bit_cast(bf16x8, b), c, 0, 0, 0);
}

constexpr int NW = 8, NMW = 4;
constexpr int NLP = 5, NRP = 7;
constexpr int NTP = 4 * (NLP + NRP);
constexpr int REG = NLP * 6 * 1024;
constexpr int NRING = 4;
constexpr int NS = 6, NA = NS - 1;
__device__ __forceinline__ int seg_beg(int s) { return s >= NS ? NPAIR : ((0xC87640 >> (4 * s)) & 15) * 16; }
__device__ __forceinline__ int seg_k(int t) { return (int)(0x5433222211110000ull >> (4 * (t >> 4))) & 15; }
__device__ __forceinline__ int seg_of_k(int k) { return (0x321540 >> (4 * k)) & 15; }
__device__ __forceinline__ int k_of_seg(int s) { return (0x215430 >> (4 * s)) & 15; }
__device__ __forceinline__ int pair_of(int t) { return t < 64 ? t : (t < 192 ? t + 64 : t - 128); }
constexpr int OFF_CNT = 0, OFF_REG = 256,
              OFF_SC = OFF_REG + 4 * REG, OFF_BUF = OFF_SC + NPAIR * 16 * 4, OFF_Q = OFF_BUF + NRING * NMW * DH * 4, OFF_ML = OFF_Q + NS * DH * 4,
              OFF_RED = OFF_ML + 128, OFF_RS = OFF_RED + 4 * 256 * 4, OFF_END = OFF_RS + 5 * 16;
enum { F_REL = 0, F_DONE, C_LAST, C_T = 3, F_OUT, C_P, C_K, F_P, F_Q = 8, F_FREE, S_FIRST, F_Q2, C_W = 12, C_SC = 16, C_PART = 24, S_ECHO = 32, S_MOVED = 48, S_SLOW = 56 };
__device__ __forceinline__ int due_k(int p) { return TSTART + ((p * TAUK + min(max(p - WP0, 0), WPN) * (WGAP * 64 / WPN)) >> 6); }
__device__ __forceinline__ int due_v(int p) { return TSTART + WGAP + ((NPAIR * TAUK + p * TAUV + max(p - NPAIR / 2, 0) * (VGAP * 64 / (NPAIR / 2))) >> 6); }
constexpr int AW_LD = DH * 2 + 16, AG_LD = 160 * 2 + 16;

__global__ void __launch_bounds__(NW * 64) fused_kernel(const bf16* __restrict__ x, const bf16* __restrict__ kc,
                                                        const bf16* __restrict__ vc, const bf16* __restrict__ wq,
                                                        const bf16* __restrict__ wo, const bf16* __restrict__ wr,
                                                        const bf16* __restrict__ wg, const bf16* __restrict__ wu,
                                                        const bf16* __restrict__ wd, float* ws, bf16* __restrict__ out) {
  __shared__ __attribute__((aligned(16))) unsigned char smem[OFF_END];
  float* s_sc = reinterpret_cast<float*>(smem + OFF_SC);
  float* s_buf = reinterpret_cast<float*>(smem + OFF_BUF);
  float* s_q = reinterpret_cast<float*>(smem + OFF_Q);
  u32x4* s_ml = reinterpret_cast<u32x4*>(smem + OFF_ML);
  lds_u32* s_c = (lds_u32*)(smem + OFF_CNT);

  const int tid = threadIdx.x, lane = tid & 63, r = lane >> 3, j = lane & 7;
  const int wave = __builtin_amdgcn_readfirstlane(tid >> 6);
  const int tw = wave - NMW;
  const int bid = blockIdx.x, xl = bid & 7, idx = bid >> 3;
  const int g = (xl * 32 + idx) / GS, mm = idx % GS;
  const __amdgpu_buffer_rsrc_t rs = __builtin_amdgcn_make_buffer_rsrc(ws, 0, WS_END * 4, 0x00020000);
  const int ro = ((idx & 3) << 3) | (idx >> 2);
  const bool isW = ro < 10, isG = ro >= 10 && ro < 16;
  const int w_ks = xl, w_ct = ro;
  const int g_cg = xl * 6 + (ro - 10);
  const int d_ct = (xl + ((ro - 16) >> 2) * 8) * 2 + (((ro - 16) >> 1) & 1);
  const bool isD = ro >= 16 && ro < 24 && (ro & 1) == 0 && d_ct < DM / 32;
  const bool isH = ro == 22 && xl >= 2 && xl < 5;
  const bool isC = (mm & 7) == 3 || (mm & 7) >= 6;
  const int c_it = (mm >> 3) * 3 + max((mm & 7) - 5, 0);
  const int qf = WS_QF + g * QFS;
  const char* kb = reinterpret_cast<const char*>(kc);
  const char* vb = reinterpret_cast<const char*>(vc);
  const unsigned lo = (r * 24 + j) * 16;
  const int xb = g * GS + mm, c0 = mm * 8 + g * ROT;
  const int b2 = (2 * NH + (xb >> 4)) * 1024 + (xb & 15) * 8, b3 = (2 * NH + 16 + (xb >> 5)) * 1024 + (xb & 31) * 8;
  auto poff = [&](int p) -> unsigned {
    const int turn = (p & 63) >> 2;
    int t = (row_of(p >> 6) * NH + g) * 1024 + ((turn * 64 + (p & 3) * 2 + c0) & 1023);
    if ((p >> 6) == 1) t = (turn < 8 ? b2 + turn * 128 : b3 + (turn & 4) * 2048 + (turn & 3) * 256) + (p & 3) * 2;
    return (unsigned)t * 3072u;
  };
  const unsigned lds_base = (unsigned)(size_t)(__attribute__((address_space(3))) unsigned char*)smem + OFF_REG + tw * REG;
  auto merged = [&](int it, int ln) -> f32x4 {
    const float* src = s_buf + (it % NRING) * (NMW * DH) + ((ln >> 1) & 7) * 24 + (ln >> 4) * 8 + (ln & 1) * 4;
    return (*reinterpret_cast<const f32x4*>(src) + *reinterpret_cast<const f32x4*>(src + DH)) +
           (*reinterpret_cast<const f32x4*>(src + 2 * DH) + *reinterpret_cast<const f32x4*>(src + 3 * DH));
  };
  auto publish = [&](int it) -> bool {
    const int k = arrive(s_c + C_PART + it);
    const f32x4 m = merged(it, lane < DH / 4 ? lane : 0);
    u32x4 o = {nz_bits(m[0]), nz_bits(m[1]), nz_bits(m[2]), nz_bits(m[3])};
    asm volatile("" : "+v"(o));
    if (arrived(k) != NMW - 1) return false;
    if (lane < DH / 4) st128(rs, WS_PO + xb * DH + lane * 4, o);
    if (lane == 0) __hip_atomic_fetch_add(s_c + F_DONE, 1u, __ATOMIC_RELAXED, __HIP_MEMORY_SCOPE_WORKGROUP);
    return true;
  };
#define NONCE() do { unsigned n_ = __builtin_amdgcn_readfirstlane(f) + 1u; while (__any(f == n_)) ++n_; \
    if (lane < 32) st32(rs, qf + (GS + mm) * 32 + lane, n_); f = n_; } while (0)
  unsigned long long pc;
  asm volatile("s_getpc_b64 %0" : "=s"(pc));
  unsigned r0 = 0u;
  unsigned t0 = clk();
  const unsigned c0_ = (unsigned)__builtin_amdgcn_s_memtime();
  if (wave < NMW) {
    Tile ta, tb, tc, td;
    const int c = lane & (GS - 1);
    unsigned f = 0u;
    if (wave == 3) f = ld32(rs, qf + c * 32 + mm);
    if (mm < NPROD) {
      const int hp = g + (mm / 6) * NG, ck = (mm % 6) >> 1, hf = mm & 1, jp = hf * 4 + wave;
      const int bb = lane >> 4, t = lane & 15;
      const u32x4* xp = reinterpret_cast<const u32x4*>(x + row_of(bb) * DM);
      u32x4 xv[5], xs = {0u, 0u, 0u, 0u};
#pragma unroll
      for (int k = 0; k < 5; ++k) xv[k] = xp[t + 16 * k];
      if (t < 10) xs = xp[jp * 10 + t];
      const u32x4* wp = reinterpret_cast<const u32x4*>(wq + (size_t)(jp * 80 + r) * HD + hp * DH + ck * 64) + j;
      u32x4 wv[10];
#pragma unroll
      for (int k = 0; k < 10; ++k) wv[k] = __builtin_nontemporal_load(wp + (size_t)k * 8 * (HD / 8));
      if (wave == 0) s_c[lane] = lane == C_K ? NTP + NMW : 0u;
      MEMFENCE(); LGKM0(); __builtin_amdgcn_s_barrier(); MEMFENCE();
      if (wave == 3) { NONCE(); }
      float sm = 0.f;
#pragma unroll
      for (int k = 0; k < 5; ++k)
#pragma unroll
        for (int d = 0; d < 4; ++d) sm += bf_lo(xv[k][d]) + bf_hi(xv[k][d]);
      const float mean = row_sum(sm) * (1.0f / DM);
      float sv = 0.f;
#pragma unroll
      for (int k = 0; k < 5; ++k)
#pragma unroll
        for (int d = 0; d < 4; ++d) { const float a = bf_lo(xv[k][d]) - mean, c2 = bf_hi(xv[k][d]) - mean; sv += a * a + c2 * c2; }
      const float rstd = 1.0f / sqrtf(row_sum(sv) * (1.0f / DM) + LN_EPS);
      float* s_xn = s_buf + wave * 320;
      if (t < 10) {
#pragma unroll
        for (int d = 0; d < 4; ++d) {
          s_xn[(t * 8 + 2 * d) * 4 + bb] = (bf_lo(xs[d]) - mean) * rstd;
          s_xn[(t * 8 + 2 * d + 1) * 4 + bb] = (bf_hi(xs[d]) - mean) * rstd;
        }
      }
      LGKM0();
      f32x2 qa[16];
#pragma unroll
      for (int i = 0; i < 16; ++i) qa[i] = f32x2{0.f, 0.f};
#pragma unroll
      for (int k = 0; k < 10; ++k) {
        const f32x4 xn = *reinterpret_cast<const f32x4*>(&s_xn[(k * 8 + r) * 4]);
#pragma unroll
        for (int d = 0; d < 4; ++d) {
          const f32x2 w2 = bf_pair(wv[k][d]);
#pragma unroll
          for (int e = 0; e < 4; ++e) qa[e * 4 + d] = fma2(xn[e], w2, qa[e * 4 + d]);
        }
      }
      float t16[16], t8[8];
#pragma unroll
      for (int i = 0; i < 16; ++i) t16[i] = tsum32(qa[i >> 1][i & 1], qa[8 + (i >> 1)][i & 1]);
#pragma unroll
      for (int i = 0; i < 8; ++i) t8[i] = tsum16(t16[i], t16[i + 8]);
#pragma unroll
      for (int i = 0; i < 8; ++i) t8[i] += dpp<DPP_ROR8>(t8[i]);
      const bool up = (lane & 8) != 0;
      f32x4* r4 = reinterpret_cast<f32x4*>(smem + OFF_RED);
      r4[wave * 64 + lane] = f32x4{up ? t8[4] : t8[0], up ? t8[5] : t8[1], up ? t8[6] : t8[2], up ? t8[7] : t8[3]};
      LGKM0();
      if (lds_add(s_c + C_P, lane) == NMW - 1) {
        asm volatile("" ::: "memory");
        const f32x4 o4 = (r4[lane] + r4[64 + lane]) + (r4[128 + lane] + r4[192 + lane]);
        st128nt(rs, WS_QP + ((hp * 2 + hf) * NB + bb) * DH + ck * 64 + j * 8 + (up ? 4 : 0), __builtin_bit_cast(u32x4, o4));
        VMCNT(0);
        if (lane == 0) s_c[F_OUT] = 1u;
      }
    } else {
      if (wave == 0) s_c[lane] = lane == C_K ? NTP + NMW : 0u;
      MEMFENCE(); LGKM0(); __builtin_amdgcn_s_barrier(); MEMFENCE();
      if (wave == 3) { NONCE(); }
      else if (CPF && lane < CODE / (24 * 64)) {
        typedef __attribute__((address_space(1))) const unsigned g_u32;
        until(t0, CPT);
        const unsigned v = *(g_u32*)((pc & ~255ull) + (((idx >> 3) * 2 + (mm & 1)) * 3 + wave) * (CODE / 24) + lane * 64);
        asm volatile("" :: "v"(v));
      }
    }
    MEMFENCE();
    if (wave == 3) {
      __builtin_amdgcn_s_setprio(3);
      const unsigned nonce = f;
      unsigned echoed = 0u;
      if (mm < NPROD) {
        echoed = ld32(rs, qf + (GS + c) * 32);
        asm volatile("" : "+v"(echoed) :: "memory");
        lds_wait<1>(s_c + F_OUT, 1u);
        if (lane < 32) st32(rs, qf + mm * 32 + lane, echoed);
#pragma unroll 1
        while ((int)(clk() - t0) < QWAIT - QBACK) {
          const unsigned nv = ld32(rs, qf + (GS + c) * 32);
          if (__any(nv != echoed)) {
            if (lane < 32) st32(rs, qf + mm * 32 + lane, nv);
            echoed = nv;
          }
          __builtin_amdgcn_s_sleep(4);
        }
      }
      until(t0, QWAIT);
#pragma unroll 1
      for (unsigned spin = 0; spin < SPIN_CAP; ++spin) {
        const unsigned nv = ld32(rs, qf + (GS + c) * 32);
        f = ld32(rs, qf + c * 32 + mm);
        const bool done = __all(c == mm || c >= NPROD || f == nonce);
        if (mm < NPROD && __any(nv != echoed)) {
          if (lane < 32) st32(rs, qf + mm * 32 + lane, nv);
          echoed = nv;
        }
        if (done) break;
        __builtin_amdgcn_s_sleep(8);
      }
      if (lane < GS) s_c[S_ECHO + lane] = echoed;
      if (lane < DH / 4) {
        u32x4 p[3][2];
#pragma unroll
        for (int it = 0; it < 3; ++it)
#pragma unroll
          for (int k = 0; k < 2; ++k) p[it][k] = ld128nt(rs, WS_QP + ((g * 2 + k) * NB + (it ? it + 1 : 0)) * DH + lane * 4);
#pragma unroll
        for (int it = 0; it < 3; ++it) {
          *reinterpret_cast<u32x4*>(&s_q[(it ? it + 3 : 0) * DH + lane * 4]) = q_pack(p[it][0], p[it][1]);
          if (it != 1) { LGKM0(); if (lane == 0) s_c[F_Q] = it + 1; MEMFENCE(); }
        }
      }
    }
    __builtin_amdgcn_s_setprio(3);
    until(t0, due_k(NTP + wave));
    Table tt = {t0 + ((idx * PHASE) >> 5), 1 << 20, 0, 0, 0, {0u, 0u}};
    bool common = false;
    tile_load(ta, kb, poff(NTP + wave) + lo, 0);
    tile_load(tb, kb, poff(NTP + wave) + lo, 1);
    MEMFENCE();
    {
      u32x2 q[12];
      int cur = NTP + wave, nxt = lds_add(s_c + C_K, lane), passed = 0, ci = -1;
      auto kstep = [&](Tile& xa, Tile& xb_, Tile& ya, Tile& yb) __attribute__((always_inline)) -> bool {
        const bool last = nxt >= NPAIR;
        const unsigned off = poff(last ? wave : pair_of(nxt)) + lo;
        const char* base = last ? vb : kb;
        const int it = seg_k(cur), cp = pair_of(cur);
        if (it != ci) {
          LGKM0();
          for (; passed < it; ++passed) lds_add(s_c + C_SC + passed, lane);
          lds_wait<1>(s_c + (it < 3 ? F_Q : F_Q2), min(it + 1, 3));
          q_load(q, s_q + seg_of_k(it) * DH, j);
          if (!common) { const unsigned d = lds_get(s_c + S_FIRST); if (d) { tt.t0 -= d - 1u; common = true; tt.was = 1 << 20; } }
          tt.agree(s_c + S_MOVED, s_c + S_MOVED + wave);
          ci = it;
        }
        MEMFENCE();
        const int deep = tt.gate(due_k(last ? NPAIR + wave : nxt), s_c + S_MOVED + wave);
        MEMFENCE();
        pair_issue(ya, yb, base, off, deep);
        const int tk = arrive(s_c + C_K);
        tt.listen(s_c + S_SLOW + 2 * wave);
        pair_put(tile_score(xa, q), tile_score(xb_, q), s_sc + cp * 16 + r, j);
        MEMFENCE();
        cur = nxt; nxt = arrived(tk);
        return last;
      };
      bool swap;
      for (;;) {
        if (kstep(ta, tb, tc, td)) { swap = true; break; }
        if (kstep(tc, td, ta, tb)) { swap = false; break; }
      }
      LGKM0();
      for (; passed < NS; ++passed) lds_add(s_c + C_SC + passed, lane);
      __builtin_amdgcn_s_waitcnt(0x0F70);
      if (swap) { ta = tc; tb = td; }
    }
#pragma unroll 1
    for (int it = 0; it < NS; ++it) {
      lds_wait<1>(s_c + F_P, k_of_seg(it) + 1);
      if (!common) { const unsigned d = lds_get(s_c + S_FIRST); if (d) { tt.t0 -= d - 1u; common = true; tt.was = 1 << 20; } }
      tt.agree(s_c + S_MOVED, s_c + S_MOVED + wave);
      f32x2 acc[12];
#pragma unroll
      for (int i = 0; i < 12; ++i) acc[i] = f32x2{0.f, 0.f};
      auto vstep = [&](Tile& xa, Tile& xb_, Tile& ya, Tile& yb, int p) __attribute__((always_inline)) {
        const unsigned off = poff(p + NMW) + lo;
        MEMFENCE();
        const int deep = tt.gate(due_v(p + NMW), s_c + S_MOVED + wave, p + NMW < NPAIR - SPRINT * NMW, p >= NPAIR - ENDT * NMW ? ENDS : 0);
        MEMFENCE();
        pair_issue(ya, yb, vb, off, deep);
        const float pa = s_sc[p * 16 + r], pb = s_sc[p * 16 + 8 + r];
        tt.listen(s_c + S_SLOW + 2 * wave);
        if (p + 2 * NMW >= NPAIR) { if (lane == 0) __hip_atomic_fetch_add(s_c + C_LAST, 1u, __ATOMIC_RELAXED, __HIP_MEMORY_SCOPE_WORKGROUP); }
        pv_last(pa, xa, acc);
        pv_last(pb, xb_, acc);
        MEMFENCE();
      };
#pragma unroll 1
      for (int p = seg_beg(it) + wave; p < min(seg_beg(it + 1), NPAIR - 2 * NMW); p += 2 * NMW) {
        vstep(ta, tb, tc, td, p);
        vstep(tc, td, ta, tb, p + NMW);
      }
      if (it == NS - 1) {
        vstep(ta, tb, tc, td, NPAIR - 2 * NMW + wave);
        const int p = NPAIR - NMW + wave;
        const float pa = s_sc[p * 16 + r], pb = s_sc[p * 16 + 8 + r];
        pv_last(pa, tc, acc);
        pv_last(pb, td, acc);
      }
      float t12[12], t6[6];
#pragma unroll
      for (int i = 0; i < 12; ++i) t12[i] = tsum32(acc[i >> 1][i & 1], acc[6 + (i >> 1)][i & 1]);
#pragma unroll
      for (int i = 0; i < 6; ++i) t6[i] = tsum16(t12[i], t12[i + 6]);
#pragma unroll
      for (int i = 0; i < 6; ++i) t6[i] += dpp<DPP_ROR8>(t6[i]);
      if (it >= NRING) lds_wait<1>(s_c + F_FREE, it - NRING + 1);
      if ((lane & 8) == 0) {
        f32x2* dst = reinterpret_cast<f32x2*>(&s_buf[(it % NRING) * (NMW * DH) + wave * DH + j * 24 + (lane >> 4) * 6]);
        dst[0] = f32x2{t6[0], t6[1]}; dst[1] = f32x2{t6[2], t6[3]}; dst[2] = f32x2{t6[4], t6[5]};
      }
      CFENCE();
      if (it < NA) { if (lane == 0) __hip_atomic_fetch_add(s_c + C_PART + it, 1u, __ATOMIC_RELAXED, __HIP_MEMORY_SCOPE_WORKGROUP); }
    }
    publish(NS - 1);
    return;
  }

  {
    Tile pk[NRP * 2];
    __builtin_amdgcn_s_setprio(3);
#pragma unroll
    for (int e = 0; e < NLP * 2; ++e) {
      const char* src = kb + (poff((e >> 1) * 4 + tw) + lo + (e & 1) * 3072);
      if (e >= 2) until(t0, due_k(e * 2 + tw) - TLEAD);
#pragma unroll
      for (int i = 0; i < 3; ++i) glds16_nt(src + i * 128, lds_base + (e * 3 + i) * 1024);
      if (e == 1) __builtin_amdgcn_s_barrier();
      if (e * 3 + 3 > PFD) VMCNT(PFD);
    }
#pragma unroll
    for (int e = 0; e < NRP * 2; ++e) {
      until(t0, due_k((NLP * 2 + e) * 2 + tw) - TLEAD);
      tile_load(pk[e], kb, poff((NLP + (e >> 1)) * 4 + tw) + lo, e & 1);
      MEMFENCE();
      if (e + 1 < NRP * 2) VMCNT(PFD);
    }
    if (tw == 0) {
      r0 = ld32(rs, WS_HR + bid * 2);
      if (bid == 0) {
        u32x4 n;
#pragma unroll
        for (int k = 0; k < 4; ++k) n[k] = ld32(rs, WS_HA + (lane * 4 + k) * 32) + 1u;
        st128(rs, WS_HN + lane * 4, n);
      }
      const u32x4 z = {0u, 0u, 0u, 0u};
      if (lane < 48) {
#pragma unroll
        for (int it = NA; it < NS; ++it) st128(rs, WS_PO + xb * DH + lane * 4, z);
      }
      else st128(rs, WS_PML + (g * GS + mm) * 64 + (lane - 48) * 4, z);
      if (isG && g_cg == 0 && lane < 8) st128(rs, WS_ST + lane * 4, z);
      if (isW) st128(rs, WS_SLAB + (w_ks * NB + (lane >> 4)) * DM + w_ct * 64 + (lane & 15) * 4, z);
      else if (isG) { if (lane < 16) st128(rs, WS_ACT + g_cg * 64 + lane * 4, z); }
      else if (isH) {
#pragma unroll
        for (int k = 0; k < 3; ++k) st128(rs, WS_H + (xl - 2) * 768 + k * 256 + lane * 4, z);
      }
      else if (isC && lane < 48) st128(rs, WS_OC + (c_it * NG + g) * DH + lane * 4, z);
    }
    __builtin_amdgcn_s_setprio(0);
    lds_wait<1>(s_c + F_Q, HPG);
    u32x2 q[12];
    q_load(q, s_q, j);
    const u32x4* s_kv = reinterpret_cast<const u32x4*>(smem + OFF_REG + tw * REG) + lane;
    static_assert(NLP * 4 <= PPI && NTP <= HPG * PPI, "tail tiles: batch row 0 only");
#pragma unroll 1
    for (int e = 0; e < NLP * 2; ++e) {
      Tile tmp;
#pragma unroll
      for (int i = 0; i < 3; ++i) tmp.d[i] = s_kv[(e * 3 + i) * 64];
      score_put(tile_score(tmp, q), s_sc + ((e >> 1) * 4 + tw) * 16 + (e & 1) * 8 + r, j);
    }
#pragma unroll
    for (int e = 0; e < NRP * 2; ++e) {
      if ((NLP * 2 + e) * 2 == PPI) q_load(q, s_q + DH, j);
      score_put(tile_score(pk[e], q), s_sc + ((NLP + (e >> 1)) * 4 + tw) * 16 + (e & 1) * 8 + r, j);
      __builtin_amdgcn_sched_barrier(0);
    }
    asm volatile("s_waitcnt vmcnt(0) lgkmcnt(0)" ::: "memory");
#pragma unroll
    for (int it = 0; it * PPI < NTP; ++it) lds_add(s_c + C_SC + it, lane);
  }

  const int mr = lane & 15, qd = lane >> 4;
  unsigned char* reg = smem + OFF_REG + tw * REG;
  float* red = reinterpret_cast<float*>(smem + OFF_RED);
#define PERM_LO(hi_, lo_) __builtin_amdgcn_perm(hi_, lo_, 0x05040100u)
#define PERM_HI(hi_, lo_) __builtin_amdgcn_perm(hi_, lo_, 0x07060302u)
  int served = 0;
  auto done = [&](int n) {
    if (tw == 3 && served < min(n, NA)) {
      const int from = served;
      int ln = lane;
      asm volatile("" : "+v"(ln));
#pragma unroll 1
      for (; served < min(n, NA); ++served) {
        lds_wait<8>(s_c + C_PART + served, NMW);
        const f32x4 o = merged(served, ln < DH / 4 ? ln : 0);
        const float am = max_x16_32(row_max(fmaxf(fmaxf(fabsf(o[0]), fabsf(o[1])), fmaxf(fabsf(o[2]), fabsf(o[3])))));
        const float inv = am > 1e-30f ? 32767.0f / am : 0.f;
        const int q0 = __float2int_rn(o[0] * inv), q1 = __float2int_rn(o[1] * inv), q2 = __float2int_rn(o[2] * inv), q3 = __float2int_rn(o[3] * inv);
        if (ln < DH / 4) reinterpret_cast<u32x2*>(s_sc + served * (DH / 2))[ln] = u32x2{PERM_LO((unsigned)q1, (unsigned)q0), PERM_LO((unsigned)q3, (unsigned)q2)};
        const bool nan = __any((o[0] + o[1]) + (o[2] + o[3]) != (o[0] + o[1]) + (o[2] + o[3]));
        if (ln == 0) s_ml[served][2] = nan ? 0x7fc00000u : nz_bits(am * (1.0f / 32767.0f));
        LGKM0();
        if (ln == 0) s_c[F_FREE] = served + 1;
      }
#pragma unroll 1
      for (int c = from * 24 + ln; c < served * 24; c += 64) st128nt(rs, WS_PQ + xb * 5 * (DH / 2) + c * 4, reinterpret_cast<const u32x4*>(s_sc)[c]);
      VMCNT(0);
      if (ln < 8) st128(rs, WS_PML + xb * 64 + (from ? 32 : 0) + ln * 4, s_ml[ln]);
      if (ln == 0) __hip_atomic_fetch_add(s_c + F_DONE, (unsigned)(served - from), __ATOMIC_RELAXED, __HIP_MEMORY_SCOPE_WORKGROUP);
    }
    if (n > NA) lds_wait<1>(s_c + F_DONE, n); else lds_wait<8>(s_c + F_DONE, n);
  };
  auto done_all = [&]() {
#pragma unroll 1
    for (int n = 4; n <= NA; ++n) done(n);
  };
  const int lp = (lane < DH / 4 ? lane : 0) * 4;
  auto weight = [&](int it, int x0, int cnt) -> float {
    const int mlw = WS_PML + (x0 + (lane & (cnt - 1))) * 64 + (it < 4 ? 0 : 32) + it * 4;
    u32x4 ml;
#pragma unroll 1
    for (unsigned spin = 0; spin < SPIN_CAP; ++spin) {
      ml = ld128(rs, mlw);
      if (__all(min4(ml) != 0u)) break;
      __builtin_amdgcn_s_sleep(16);
    }
    const float mk = __uint_as_float(ml[0]);
    const float e = __builtin_amdgcn_exp2f(mk - max_x16_32(row_max(mk)));
    return e / (wave_sum(e * __uint_as_float(ml[1])) * ((float)cnt * (1.0f / 64.0f))) * __uint_as_float(ml[2]);
  };
  auto prep = [&]() {
    if (tw == 0) {
      const unsigned tok = __builtin_amdgcn_readfirstlane(r0) + 1u;
      const int c = lane & (GS - 1);
      unsigned echoed = 0u, qe = s_c[S_ECHO + c];
      bool first = true, released = false, sent = false;
#pragma unroll 1
      while ((int)(clk() - t0) < HST) {
        const unsigned qn = ld32(rs, qf + (GS + c) * 32);
        if (mm < NPROD && __any(qn != qe)) {
          if (lane < 32) st32(rs, qf + mm * 32 + lane, qn);
          qe = qn;
        }
        __builtin_amdgcn_s_sleep(64);
      }
      int slow = 0;
      unsigned fold = 0u;
      auto lag = [&]() {
        const unsigned v = __hip_atomic_load(s_c + S_MOVED + (lane & 7), __ATOMIC_RELAXED, __HIP_MEMORY_SCOPE_WORKGROUP);
        if (FOLD) fold += v;
        unsigned a = 0u;
#pragma unroll
        for (int k = FOLD ? 0 : 4; k < 8; ++k) a += __builtin_amdgcn_readlane(v, k);
        return a >> 2;
      };
      auto tell = [&]() { if (lane < 4) { s_c[S_SLOW + 2 * lane] = slow; s_c[S_SLOW + 2 * lane + 1] = fold; } };
      const unsigned behind = lag();
#pragma unroll 1
      for (unsigned spin = 0; spin < SPIN_CAP && !released; ++spin) {
        const unsigned qn = ld32(rs, qf + (GS + c) * 32);
        const u32x2 rv = ld64(rs, WS_HR + bid * 2);
        const unsigned nv = __builtin_amdgcn_readfirstlane(ld32(rs, WS_HN + bid));
        if (__builtin_amdgcn_readfirstlane(rv[0]) - tok <= NRD) {
          released = true;
          slow = __builtin_amdgcn_readfirstlane(rv[1]) >> 16;
          if (lane == 0) { s_c[S_FIRST] = min((t0 - rv[1]) & 0xffffu, 300u) + 1u; }
        } else {
          if (mm < NPROD && __any(qn != qe)) {
            if (lane < 32) st32(rs, qf + mm * 32 + lane, qn);
            qe = qn;
          }
          if (first || nv != echoed) {
            echoed = nv; first = false;
            if (lane < 8) st128(rs, WS_HA + bid * 32 + lane * 4, u32x4{echoed, tok, t0, behind});
          }
          if (bid == 0 && !sent) sent = leader_release(rs, lane, t0, 0u);
          __builtin_amdgcn_s_sleep(32);
        }
      }
      tell();
      if (lane == 0) s_c[F_REL] = 1u;
      if (lane < DH / 4) {
        u32x4 p[3][2];
#pragma unroll
        for (int e = 0; e < 3; ++e)
#pragma unroll
          for (int k = 0; k < 2; ++k) p[e][k] = ld128nt(rs, WS_QP + (((e ? 8 + e * 8 + (xb >> 5) : xb >> 4) * 2 + k) * NB + 1) * DH + lane * 4);
#pragma unroll
        for (int e = 0; e < 3; ++e) *reinterpret_cast<u32x4*>(&s_q[(1 + e) * DH + lane * 4]) = q_pack(p[e][0], p[e][1]);
      }
      LGKM0();
      if (lane == 0) s_c[F_Q2] = 3u;
      unsigned prev = slow;
#pragma unroll 1
      for (unsigned rd = 1; rd <= NRD; ++rd) {
        const int tr = RT0 + (int)(rd - 1) * RTS;
        until(t0, tr);
        const unsigned bh = lag();
        if (bh > RMIN && lane < 8) st128(rs, WS_HA + bid * 32 + lane * 4, u32x4{echoed + rd, tok, t0, bh});
        if (bid == 0) { until(t0, tr + 220); leader_release(rs, lane, t0, rd); }
        until(t0, tr + 500);
        const u32x2 rv = ld64(rs, WS_HR + bid * 2);
        if (__builtin_amdgcn_readfirstlane(rv[0]) != tok + rd) {
          prev = 0u;
          slow = slow > 0 ? max(slow - GB, 0) : slow - (rd <= PRD ? PRB : 0);
        } else {
          const unsigned ex = __builtin_amdgcn_readfirstlane(rv[1]) >> 16;
          slow += ex + min(ex, prev) * (rd == NRD ? FBL : FBP) / 100u;
          prev = ex;
        }
        tell();
      }
    }
    if (tw == 3) {
#pragma unroll 1
      for (int ik = 0; ik < NS; ++ik) {
        const int it = seg_of_k(ik), nv = (seg_beg(it + 1) - seg_beg(it)) >> 4;
        lds_wait<4>(s_c + C_SC + ik, ik == 0 ? NW : NMW);
        f32x4* sp = reinterpret_cast<f32x4*>(s_sc + seg_beg(it) * 16) + lane;
        f32x4 v[4];
#pragma unroll
        for (int k = 0; k < 4; ++k) v[k] = sp[(k & (nv - 1)) * 64];
        const f32x4 mx = __builtin_elementwise_max(__builtin_elementwise_max(v[0], v[1]), __builtin_elementwise_max(v[2], v[3]));
        const float m = max_x16_32(row_max(fmaxf(fmaxf(mx[0], mx[1]), fmaxf(mx[2], mx[3]))));
        float l = 0.f;
#pragma unroll
        for (int k = 0; k < 4; ++k) {
#pragma unroll
          for (int e = 0; e < 4; ++e) v[k][e] = __builtin_amdgcn_exp2f(v[k][e] - m);
          if (k < nv) {
            l += (v[k][0] + v[k][1]) + (v[k][2] + v[k][3]);
            sp[k * 64] = v[k];
          }
        }
        l = wave_sum(l);
        if (lane == 0) s_ml[it] = u32x4{nz_bits(m), nz_bits(l), 0x3f800000u, 0x3f800000u};
        LGKM0();
        if (lane == 0) s_c[F_P] = ik + 1;
      }
    }
  };
  auto pull = [&]() {
    typedef __attribute__((address_space(1))) const unsigned g_u32;
    const int id = (ro - 16) * 4 + tw;
    constexpr int RW = 160 - 32 * GE, LW = RW * 4 * 3 * 2 / 64;
    g_u32* p1[2];
#pragma unroll
    for (int i = 0; i < 2; ++i) {
      const int l = id * LW + min(i * 32 + (lane >> 1), max(LW - 1, 0)), m = l >= LW * 32 ? l - LW * 32 : l, rr = m / 3;
      p1[i] = (g_u32*)((l >= LW * 32 ? wu : wg) + (size_t)(rr / max(RW, 1) * 160 + 32 * GE + rr % max(RW, 1)) * FF + xl * 192 + (m % 3) * 64 + (lane & 1) * 32);
    }
    g_u32* p2 = (g_u32*)(wd + (size_t)(id * 24 + min(lane >> 1, 23)) * DM + xl * 64 + (lane & 1) * 32);
    asm volatile("" : "+v"(p1[0]), "+v"(p1[1]), "+v"(p2));
    if (GLEAD) lds_wait<1>(s_c + C_LAST, GCNT); else done(NS);
    const unsigned tp = clk() + PW + tw * PS;
    until(tp, 0);
    const unsigned v0 = LW > 0 ? p1[0][0] : 0u;
    MEMFENCE();
    until(tp, 4 * PS);
    const unsigned v1 = LW > 32 ? p1[1][0] : 0u;
    MEMFENCE();
    until(tp, DPW);
    const unsigned v2 = p2[0], v3 = p2[xl < 2 ? 256 : 0];
    MEMFENCE();
    return (v0 ^ v1) + (v2 ^ v3);
  };
  if (isW) {
    u32x4 bx[24];
    const char* p = reinterpret_cast<const char*>(wo + (size_t)((w_ks * 4 + tw) * DH + r) * DM + w_ct * 64 + j * 8);
#pragma unroll 1
    for (int i = 0; i < 24; ++i) { until(t0, tw == 0 ? W0START + i * (W0SPAN / 24) : W1START + i * (W1SPAN / 24)); glds16_nt(p, lds_base + i * 1024); p += 8 * DM * 2; VMCNT(WFD); }
    asm volatile("s_waitcnt vmcnt(0)" ::: "memory");
#pragma unroll
    for (int s = 0; s < 6; ++s) {
      u32x2 e[8];
#pragma unroll
      for (int k = 0; k < 8; ++k) e[k] = *reinterpret_cast<const u32x2*>(reg + (s * 32 + qd * 8 + k) * 128 + mr * 8);
#pragma unroll
      for (int k = 0; k < 4; ++k) {
        bx[s * 4 + 0][k] = PERM_LO(e[2 * k + 1][0], e[2 * k][0]); bx[s * 4 + 1][k] = PERM_HI(e[2 * k + 1][0], e[2 * k][0]);
        bx[s * 4 + 2][k] = PERM_LO(e[2 * k + 1][1], e[2 * k][1]); bx[s * 4 + 3][k] = PERM_HI(e[2 * k + 1][1], e[2 * k][1]);
      }
    }
#pragma unroll
    for (int s = 0; s < 24; s += 4) asm volatile("" : "+v"(bx[s]), "+v"(bx[s + 1]), "+v"(bx[s + 2]), "+v"(bx[s + 3]));
    LGKM0();
    prep();
    lds_wait<8>(s_c + F_REL, 1u);
    const int hh = w_ks * 4 + tw;
#pragma unroll 1
    for (int pass = 0; pass < 3; ++pass) {
      if (pass < 2) {
        done(pass ? NA : 4);
        pause(pass ? WDELAY2 : WDELAY2 + 300);
        const int b0 = pass * 2, b1 = pass + 1;
        u32x4 v0, v1;
#pragma unroll 1
        for (unsigned spin = 0; spin < SPIN_CAP; ++spin) {
          v0 = ld128(rs, WS_OC + (b0 * NH + hh) * DH + lp);
          v1 = ld128(rs, WS_OC + (b1 * NH + hh) * DH + lp);
          if (__all(min(min4(v0), min4(v1)) != 0u)) break;
          __builtin_amdgcn_s_sleep(16);
        }
        if (lane < DH / 4) {
          *reinterpret_cast<u32x2*>(reg + (b0 * 4) * AW_LD + lane * 8) = u32x2{v0[0], v0[1]};
          *reinterpret_cast<u32x2*>(reg + (b0 * 4 + 1) * AW_LD + lane * 8) = u32x2{v0[2], v0[3]};
          *reinterpret_cast<u32x2*>(reg + (b1 * 4) * AW_LD + lane * 8) = u32x2{v1[0], v1[1]};
          *reinterpret_cast<u32x2*>(reg + (b1 * 4 + 1) * AW_LD + lane * 8) = u32x2{v1[2], v1[3]};
        }
      } else {
        const float wk = weight(NS - 1, hh * GS, GS);
        float w[GS];
#pragma unroll
        for (int k = 0; k < GS; ++k) { w[k] = __uint_as_float(__builtin_amdgcn_readlane(__float_as_uint(wk), k)); asm volatile("" : "+s"(w[k])); }
        done(NS);
        __builtin_amdgcn_s_setprio(3);
        const int po = WS_PO + hh * GS * DH;
        u32x4 pv[GS];
#pragma unroll 1
        for (unsigned spin = 0; spin < SPIN_CAP; ++spin) {
#pragma unroll
          for (int k = 0; k < GS; ++k) pv[k] = __builtin_amdgcn_raw_buffer_load_b128(rs, lp * 4, (po + k * DH) * 4, AUX_SC1);
          unsigned mn = min4(pv[0]);
#pragma unroll
          for (int k = 1; k < GS; ++k) mn = min(mn, min4(pv[k]));
          if (__all(mn != 0u)) break;
          __builtin_amdgcn_s_sleep(1);
        }
        f32x4 o = w[0] * __builtin_bit_cast(f32x4, pv[0]), o2 = w[1] * __builtin_bit_cast(f32x4, pv[1]);
#pragma unroll
        for (int k = 2; k < GS; k += 2) { o += w[k] * __builtin_bit_cast(f32x4, pv[k]); o2 += w[k + 1] * __builtin_bit_cast(f32x4, pv[k + 1]); }
        u32x2 hi, lw;
        split4(o + o2, hi, lw);
        if (lane < DH / 4) {
          *reinterpret_cast<u32x2*>(reg + ((NB - 1) * 4) * AW_LD + lane * 8) = hi;
          *reinterpret_cast<u32x2*>(reg + ((NB - 1) * 4 + 1) * AW_LD + lane * 8) = lw;
        }
      }
      CFENCE();
      u32x4 a[6];
#pragma unroll
      for (int s = 0; s < 6; ++s) a[s] = *reinterpret_cast<const u32x4*>(reg + mr * AW_LD + s * 64 + qd * 16);
      __builtin_amdgcn_sched_barrier(0);
      f32x4 c[4];
#pragma unroll
      for (int t = 0; t < 4; ++t) c[t] = f32x4{0.f, 0.f, 0.f, 0.f};
#pragma unroll
      for (int s = 0; s < 6; ++s)
#pragma unroll
        for (int t = 0; t < 4; ++t) c[t] = mfma16(a[s], bx[s * 4 + t], c[t]);
      const bool mine = max(qd - 1, 0) == pass;
      f32x4* r4 = reinterpret_cast<f32x4*>(red);
      if (mine) r4[tw * 64 + lane] = f32x4{c[0][0] + c[0][1], c[1][0] + c[1][1], c[2][0] + c[2][1], c[3][0] + c[3][1]};
      const int k = arrive(s_c + C_W + pass);
      f32x4 sum = (r4[lane] + r4[64 + lane]) + (r4[128 + lane] + r4[192 + lane]);
      asm volatile("" : "+v"(sum));
      if (arrived(k) == 3) {
        if (mine) st128(rs, WS_SLAB + (w_ks * NB + qd) * DM + w_ct * 64 + mr * 4, u32x4{nz_bits(sum[0]), nz_bits(sum[1]), nz_bits(sum[2]), nz_bits(sum[3])});
      }
    }
  } else if (isG || isH) {
    f32x4* rst = reinterpret_cast<f32x4*>(red);
    const int hb = isH ? xl - 2 : NB - 1;
    uint2 wr4 = make_uint2(0u, 0u), xg = wr4;
    if (lane < 40) {
      wr4 = *reinterpret_cast<const uint2*>(wr + tw * 160 + lane * 4);
      xg = *reinterpret_cast<const uint2*>(x + row_of(hb) * DM + tw * 160 + lane * 4);
    }
    const int eb = mr >> 2;
    const float cq = bf_lo(*reinterpret_cast<const unsigned*>(x + row_of(eb) * DM));
    const float wsum = wave_sum((bf_lo(wr4.x) + bf_hi(wr4.x)) + (bf_lo(wr4.y) + bf_hi(wr4.y)));
    f32x4 xc = f32x4{bf_lo(xg.x), bf_hi(xg.x), bf_lo(xg.y), bf_hi(xg.y)} - __uint_as_float(__builtin_amdgcn_readlane(__float_as_uint(cq), hb * 4));
    asm volatile("" : "+v"(xc));
    typedef __attribute__((address_space(1))) const u32x4 g_u32x4;
    g_u32x4* wp[10];
    wp[0] = (g_u32x4*)(((mr & 4) ? wu : wg) + (size_t)(tw * 160 + qd * 8 + (mr >> 3) * 4) * FF + g_cg * 32 + (mr & 3) * 8);
    u32x4 e[20], bx[20];
    auto frag = [&](int s) __attribute__((always_inline)) {
      unsigned w[8][2];
#pragma unroll
      for (int i = 0; i < 4; ++i)
#pragma unroll
        for (int d = 0; d < 2; ++d) {
          w[i][d] = __builtin_amdgcn_update_dpp(e[s * 4 + i][d], e[s * 4 + i][2 + d], DPP_ROR8, 0xf, 0xc, false);
          w[4 + i][d] = __builtin_amdgcn_update_dpp(e[s * 4 + i][2 + d], e[s * 4 + i][d], DPP_ROR8, 0xf, 0x3, false);
        }
#pragma unroll
      for (int k = 0; k < 4; ++k) {
        bx[s * 4 + 0][k] = PERM_LO(w[2 * k + 1][0], w[2 * k][0]); bx[s * 4 + 1][k] = PERM_HI(w[2 * k + 1][0], w[2 * k][0]);
        bx[s * 4 + 2][k] = PERM_LO(w[2 * k + 1][1], w[2 * k][1]); bx[s * 4 + 3][k] = PERM_HI(w[2 * k + 1][1], w[2 * k][1]);
      }
      PIN4(bx, s * 4);
    };
    if (isG) {
#pragma unroll
      for (int i = 0; i < 4 * GE; ++i) {
        until(t0, tw == 0 ? W0START + i * (W0SPAN / max(4 * GE, 1)) : W1START + i * (W1SPAN / max(4 * GE, 1)));
        e[i] = __builtin_nontemporal_load(wp[0] + ((i >> 2) * 32 + (i & 3)) * (FF / 8));
        MEMFENCE();
      }
#pragma unroll
      for (int s = 0; s < GE; ++s) frag(s);
    }
    prep();
    lds_wait<8>(s_c + F_REL, 1u);
    auto hrow = [&](bool fast) -> f32x4 {
      f32x4 hv = {0.f, 0.f, 0.f, 0.f};
      if (lane < 40) {
        const int off = (WS_SLAB + hb * DM + tw * 160 + lane * 4) * 4;
        u32x4 sl[KS];
#pragma unroll 1
        for (unsigned spin = 0; spin < SPIN_CAP; ++spin) {
#pragma unroll
          for (int ks = 0; ks < KS; ++ks) sl[ks] = __builtin_amdgcn_raw_buffer_load_b128(rs, off, ks * (NB * DM * 4), AUX_SC1);
          unsigned mn = min4(sl[0]);
#pragma unroll
          for (int ks = 1; ks < KS; ++ks) mn = min(mn, min4(sl[ks]));
          if (!__any(mn == 0u)) break;
          if (fast) __builtin_amdgcn_s_sleep(1); else __builtin_amdgcn_s_sleep(16);
        }
#define SL(k) __builtin_bit_cast(f32x4, sl[k])
        hv = xc + (((SL(0) + SL(1)) + (SL(2) + SL(3))) + ((SL(4) + SL(5)) + (SL(6) + SL(7))));
#undef SL
      }
      return hv;
    };
    auto sums = [&](f32x4 hv) -> f32x4 {
      const float s1 = wave_sum((hv[0] + hv[1]) + (hv[2] + hv[3]));
      const float s2 = wave_sum((hv[0] * hv[0] + hv[1] * hv[1]) + (hv[2] * hv[2] + hv[3] * hv[3]));
      const float dot = wave_sum((hv[0] * bf_lo(wr4.x) + hv[1] * bf_hi(wr4.x)) + (hv[2] * bf_lo(wr4.y) + hv[3] * bf_hi(wr4.y)));
      return f32x4{s1, s2, dot, wsum};
    };
    if (isH) {
      if (tw == 3 || hb == 2) done_all(); else done(4);
      if (tw != 3 || hb == 2) pause(hb < 2 ? HDELAY : HDELAY2);
      const f32x4 hv = hrow(false), sm = sums(hv);
      u32x4 v = split8(hv, (lane & 1) != 0);
      if (lane >= 40) v = u32x4{nz_bits(sm[0]), nz_bits(sm[1]), nz_bits(sm[2]), nz_bits(sm[3])};
      if (lane < 48) st128(rs, WS_H + (hb * 4 + tw) * 192 + lane * 4, v);
      done_all();
      const unsigned pl = pull();
      asm volatile("" :: "v"(pl));
      return;
    }
    done_all();
#pragma unroll
    for (int i = 2 * GE; i < 10; ++i) { wp[i] = wp[0] + ((i >> 1) * 32 + (i & 1) * 2) * (FF / 8); asm volatile("" : "+v"(wp[i])); }
    done(NS);
    if (GW) until(clk(), GW);
#pragma unroll
    for (int i = 4 * GE; i < 20; ++i) e[i] = wp[i >> 1][(i & 1) * (FF / 8)];
    u32x4 a[5], sm;
    const int hoff = (WS_H + (min(mr >> 2, NB - 2) * 4 + tw) * 192 + qd * 8 + (mr & 1) * 4) * 4;
#pragma unroll
    for (int s = 0; s < 5; ++s) a[s] = __builtin_amdgcn_raw_buffer_load_b128(rs, hoff + s * 128, 0, AUX_SC1);
    sm = ld128(rs, WS_H + (min(lane, NB - 2) * 4 + tw) * 192 + 160);
    MEMFENCE();
#pragma unroll
    for (int s = GE; s < 5; ++s) frag(s);
    {
#pragma unroll 1
      for (unsigned spin = 0; spin < SPIN_CAP; ++spin) {
        unsigned mn = min4(sm);
#pragma unroll
        for (int s = 0; s < 5; ++s) mn = min(mn, min4(a[s]));
        if (__all(mn != 0u)) break;
        __builtin_amdgcn_s_sleep(2);
#pragma unroll
        for (int s = 0; s < 5; ++s) a[s] = __builtin_amdgcn_raw_buffer_load_b128(rs, hoff + s * 128, 0, AUX_SC1);
        sm = ld128(rs, WS_H + (min(lane, NB - 2) * 4 + tw) * 192 + 160);
      }
      if (lane < NB - 1) rst[lane * 4 + tw] = __builtin_bit_cast(f32x4, sm);
#pragma unroll
      for (int s = 0; s < 5; ++s) {
        if (mr & 2) a[s] = u32x4{0x3f803f80u, 0x3f803f80u, 0x3f803f80u, 0x3f803f80u};
        asm volatile("" : "+v"(a[s]));
      }
    }
    const f32x4 hv = hrow(true);
    if (lane < 40) {
      u32x2 hi, lw;
      split4(hv, hi, lw);
      *reinterpret_cast<u32x2*>(reg + lane * 8) = hi;
      *reinterpret_cast<u32x2*>(reg + AG_LD + lane * 8) = lw;
    }
    CFENCE();
    if ((mr & 14) == 12) {
#pragma unroll
      for (int s = 0; s < 5; ++s) a[s] = *reinterpret_cast<const u32x4*>(reg + (mr & 1) * AG_LD + s * 64 + qd * 16);
    }
    __builtin_amdgcn_sched_barrier(0);
    f32x4 c[4];
#pragma unroll
    for (int t = 0; t < 4; ++t) c[t] = f32x4{0.f, 0.f, 0.f, 0.f};
#pragma unroll
    for (int s = 0; s < 5; ++s)
#pragma unroll
      for (int t = 0; t < 4; ++t) c[t] = mfma16(a[s], bx[s * 4 + t], c[t]);
    { const f32x4 s3 = sums(hv); if (lane == 0) rst[(NB - 1) * 4 + tw] = s3; }
    const int slot = qd * 16 + (mr & 7) * 2 + (mr >> 3);
    *reinterpret_cast<f32x4*>(reg + 4096 + slot * 16) = f32x4{c[0][0] + c[0][1], c[1][0] + c[1][1], c[0][2], c[1][2]};
    *reinterpret_cast<f32x4*>(reg + 4096 + 1152 + slot * 16) = f32x4{c[2][0] + c[2][1], c[3][0] + c[3][1], c[2][2], c[3][2]};
    const int k = arrive(s_c + C_T);
    f32x4 ts[4], pg[4], pu[4];
#pragma unroll
    for (int w = 0; w < 4; ++w) {
      const unsigned char* src = smem + OFF_REG + w * REG + 4096 + (lane & 1) * 1152 + (eb * 16 + qd * 2 + ((lane >> 1) & 1)) * 16;
      ts[w] = rst[eb * 4 + w];
      pg[w] = *reinterpret_cast<const f32x4*>(src); pu[w] = *reinterpret_cast<const f32x4*>(src + 128);
    }
    PIN4(ts, 0); PIN4(pg, 0); PIN4(pu, 0);
    if (arrived(k) == 3) {
      const f32x4 t = (ts[0] + ts[1]) + (ts[2] + ts[3]), g4 = (pg[0] + pg[1]) + (pg[2] + pg[3]), u4 = (pu[0] + pu[1]) + (pu[2] + pu[3]);
      const float d = t[0] * (-1.0f / DM);
      const float rstd = __builtin_amdgcn_rsqf(fmaxf(t[1] + d * t[0], 0.f) * (1.0f / DM) + LN_EPS);
      const float g0 = rstd * (g4[0] + d * g4[2]), g1 = rstd * (g4[1] + d * g4[3]);
      const float a0 = g0 * __builtin_amdgcn_rcpf(1.0f + __expf(-g0)) * (rstd * (u4[0] + d * u4[2]));
      const float a1 = g1 * __builtin_amdgcn_rcpf(1.0f + __expf(-g1)) * (rstd * (u4[1] + d * u4[3]));
      const __hip_bfloat162 a2 = __halves2bfloat162(__float2bfloat16(a0), __float2bfloat16(a1));
      const float aw = __uint_as_float(max(*reinterpret_cast<const unsigned*>(&a2), 1u));
      const u32x4 av = {__float_as_uint(aw), __float_as_uint(dpp<0x55>(aw)), __float_as_uint(dpp<0xAA>(aw)), __float_as_uint(dpp<0xFF>(aw))};
      const float rr = rstd * (t[2] + d * t[3]);
      const u32x4 sv = {nz_bits(cq - d), nz_bits(rstd), nz_bits(__builtin_amdgcn_rcpf(1.0f + __expf(-rr))), 0x3f800000u};
      const bool stat = g_cg == 0 && (lane & 51) == 1;
      if ((lane & 3) == 0 || stat) st128(rs, stat ? WS_ST + eb * 4 : WS_ACT + g_cg * 64 + lane, stat ? sv : av);
    }
  } else if (isD) {
    const uint2 xd = *reinterpret_cast<const uint2*>(x + row_of(tw) * DM + d_ct * 32 + j * 4);
    const __amdgpu_buffer_rsrc_t rd = __builtin_amdgcn_make_buffer_rsrc(const_cast<bf16*>(wd), 0, FF * DM * 2, 0x00020000);
    prep();
    lds_wait<8>(s_c + F_REL, 1u);
    done_all();
    const unsigned pl = pull();
    done(NS);
    asm volatile("" :: "v"(pl));
    const unsigned te = clk();
    if (DW) until(te, DW);
    u32x4 bd[24];
    {
      const int off = ((tw * 384 + (mr >> 3) * 32 + qd * 8 + (mr & 4)) * DM + d_ct * 32 + (mr & 3) * 8) * 2;
      u32x4 e[24];
#pragma unroll
      for (int i = 0; i < 24; ++i) e[i] = __builtin_amdgcn_raw_buffer_load_b128(rd, off + (i & 3) * (DM * 2), (i >> 2) * 64 * (DM * 2), 0);
      MEMFENCE();
#pragma unroll
      for (int s = 0; s < 6; ++s) {
        unsigned w[8][2];
#pragma unroll
        for (int i = 0; i < 4; ++i)
#pragma unroll
          for (int d = 0; d < 2; ++d) {
            w[i][d] = __builtin_amdgcn_update_dpp(e[s * 4 + i][d], e[s * 4 + i][2 + d], 0x114, 0xf, 0xa, false);
            w[4 + i][d] = __builtin_amdgcn_update_dpp(e[s * 4 + i][2 + d], e[s * 4 + i][d], 0x104, 0xf, 0x5, false);
          }
#pragma unroll
        for (int k = 0; k < 4; ++k) {
          bd[s * 4 + 0][k] = PERM_LO(w[2 * k + 1][0], w[2 * k][0]); bd[s * 4 + 1][k] = PERM_HI(w[2 * k + 1][0], w[2 * k][0]);
          bd[s * 4 + 2][k] = PERM_LO(w[2 * k + 1][1], w[2 * k][1]); bd[s * 4 + 3][k] = PERM_HI(w[2 * k + 1][1], w[2 * k][1]);
        }
        PIN4(bd, s * 4);
      }
    }
    until(te, DLATE);
    {
      u32x4 sl;
      unsigned spin = 0;
#pragma unroll 1
      for (; spin < SPIN_CAP; ++spin) {
        sl = ld128(rs, WS_SLAB + (r * NB + tw) * DM + d_ct * 32 + j * 4);
        if (__all(min4(sl) != 0u)) break;
        __builtin_amdgcn_s_sleep(2);
      }
      if (tw == NB - 1 && lane == 0) s_c[C_W + 1] = clk() + (spin ? DLATE2 : 0);
      f32x4 hv = {bf_lo(xd.x), bf_hi(xd.x), bf_lo(xd.y), bf_hi(xd.y)};
#pragma unroll
      for (int i = 0; i < 4; ++i) hv[i] += sum_r(__uint_as_float(sl[i]));
      if (lane < 8) reinterpret_cast<f32x4*>(red + 512 + tw * 32)[lane] = hv;
      LGKM0();
      lds_add(s_c + C_W, lane);
      lds_wait<1>(s_c + C_W, 4u);
    }
    const int col = (mr & 3) * 8 + (mr & 4) + (mr >> 3) * 2;
    f32x2 hv = *reinterpret_cast<const f32x2*>(red + 512 + qd * 32 + col);
    asm volatile("" : "+v"(hv));
    u32x4 a[6], st;
#pragma unroll
    for (int s = 0; s < 6; ++s) a[s] = u32x4{1u, 1u, 1u, 1u};
    const int aoff = (WS_ACT + tw * 12 * 64 + ((mr >> 1) & 1) * 64 + qd * 16 + (mr >> 2) * 4) * 4;
    until(lds_get(s_c + C_W + 1), 0);
#pragma unroll 1
    for (unsigned spin = 0; spin < SPIN_CAP; ++spin) {
      st = ld128(rs, WS_ST + qd * 4);
      if ((mr & 1) == 0) {
#pragma unroll
        for (int s = 0; s < 6; ++s) a[s] = __builtin_amdgcn_raw_buffer_load_b128(rs, aoff + s * 512, 0, AUX_SC1);
      }
      unsigned mn = min4(st);
#pragma unroll
      for (int s = 0; s < 6; ++s) mn = min(mn, min4(a[s]));
      if (__all(mn != 0u)) break;
      __builtin_amdgcn_s_sleep(1);
    }
    f32x4 c[4];
#pragma unroll
    for (int t = 0; t < 4; ++t) c[t] = f32x4{0.f, 0.f, 0.f, 0.f};
#pragma unroll
    for (int s = 0; s < 6; ++s)
#pragma unroll
      for (int t = 0; t < 4; ++t) c[t] = mfma16(a[s], bd[s * 4 + t], c[t]);
    const f32x4 sf = __builtin_bit_cast(f32x4, st);
    hv += (hv - sf[0]) * sf[1];
    const bool up = mr >= 8;
    const float k0 = up ? c[2][2] : c[0][0], k1 = up ? c[3][2] : c[1][0], o0 = up ? c[0][2] : c[2][0], o1 = up ? c[1][2] : c[3][0];
    f32x2* r2 = reinterpret_cast<f32x2*>(red);
    r2[tw * 64 + lane] = f32x2{k0 + dpp<DPP_ROR8>(o0), k1 + dpp<DPP_ROR8>(o1)};
    const int k = arrive(s_c + C_T);
    f32x2 y = hv + ((r2[lane] + r2[64 + lane]) + (r2[128 + lane] + r2[192 + lane])) * sf[2];
    asm volatile("" : "+v"(y));
    if (arrived(k) == 3) {
      { const unsigned cy = (unsigned)__builtin_amdgcn_s_memtime() - c0_, tk = clk() - t0; until(t0, 15000 + (int)(cy * 200u / tk)); }
      const __hip_bfloat162 o2 = __halves2bfloat162(__float2bfloat16(y[0]), __float2bfloat16(y[1]));
      __hip_atomic_store(reinterpret_cast<unsigned*>(out + row_of(qd) * DM + d_ct * 32 + col), *reinterpret_cast<const unsigned*>(&o2), __ATOMIC_RELAXED, __HIP_MEMORY_SCOPE_AGENT);
    }
  } else {
    prep();
    if (isC && tw == 1) {
      lds_wait<8>(s_c + F_REL, 1u);
      const int cs = c_it != 1 ? c_it * 2 : max(g >> 3, 1), cnt = c_it != 1 ? 8 : (g < 16 ? 16 : 32), x0 = c_it != 1 ? g * 8 : (g < 16 ? g * 16 : (g & 7) * 32);
      done(c_it < 2 ? 4 : NA);
      pause(WDELAY);
      const float wk = weight(cs, x0, cnt);
      f32x4 o = {0.f, 0.f, 0.f, 0.f};
#pragma unroll 1
      for (int k0 = 0; k0 < cnt; k0 += 16) {
        u32x2 pv[16];
#pragma unroll
        for (int k = 0; k < 16; ++k) pv[k] = __builtin_amdgcn_raw_buffer_load_b64(rs, lp * 2, (WS_PQ + ((x0 + ((k0 + k) & (cnt - 1))) * 5 + cs) * (DH / 2)) * 4, AUX_EARLY);
#pragma unroll
        for (int k = 0; k < 16; ++k) {
          const float w = k0 + k < cnt ? __uint_as_float(__builtin_amdgcn_readlane(__float_as_uint(wk), k0 + k)) : 0.f;
          o += w * f32x4{(float)(short)pv[k][0], (float)((int)pv[k][0] >> 16), (float)(short)pv[k][1], (float)((int)pv[k][1] >> 16)};
        }
      }
      u32x2 hi, lw;
      split4(o, hi, lw);
      if (lane < DH / 4) st128(rs, WS_OC + (c_it * NG + g) * DH + lane * 4, u32x4{max(hi[0], 1u), max(hi[1], 1u), max(lw[0], 1u), max(lw[1], 1u)});
    }
    done_all();
    const unsigned pl = pull();
    asm volatile("" :: "v"(pl));
  }
  if (tw == 3) done_all();
}

extern "C" void kernel_launch(void* const* d_in, const int* in_sizes, int n_in, void* d_out, int out_size, void* d_ws,
                              size_t ws_size, hipStream_t stream) {
  const bf16* x = (const bf16*)d_in[0];
  const bf16* kc = (const bf16*)d_in[1];
  const bf16* vc = (const bf16*)d_in[2];
  const bf16* wq = (const bf16*)d_in[3];
  const bf16* wo = (const bf16*)d_in[4];
  const bf16* wr = (const bf16*)d_in[6];
  const bf16* wg = (const bf16*)d_in[7];
  const bf16* wu = (const bf16*)d_in[8];
  const bf16* wd = (const bf16*)d_in[9];
  fused_kernel<<<NWG, NW * 64, 0, stream>>>(x, kc, vc, wq, wo, wr, wg, wu, wd, (float*)d_ws, (bf16*)d_out);
}
```

```cpp
#include <hip/hip_runtime.h>
#include <hip/hip_bf16.h>
#include <stdint.h>

typedef __hip_bfloat16 bf16;
using u32x2 = __attribute__((ext_vector_type(2))) unsigned;
using u32x4 = __attribute__((ext_vector_type(4))) unsigned;
using f32x4 = __attribute__((ext_vector_type(4))) float;
using f32x2 = __attribute__((ext_vector_type(2))) float;
using bf16x8 = __attribute__((ext_vector_type(8))) __bf16;

constexpr int NB = 4, DM = 640, NH = 32, SK = 8192, DH = 192, HD = NH * DH, FF = 1536;
constexpr float LN_EPS = 1e-5f;
constexpr float QSCALE = 0.07216878364870322f * 1.4426950408889634f;

constexpr int NWG = 256;
constexpr int GS = 8, NG = NWG / GS, NI = GS / 2;
constexpr int HPG = NI / NB;
constexpr int PPI = 512 / GS;
constexpr int NPAIR = NI * PPI;

constexpr int TAUK = 1320, TAUV = 1332;
constexpr int TSTART = 140;
constexpr int WGAP = 165, WP0 = 64, WPN = 176;
constexpr int VGAP = 50;
constexpr int DEEP1 = 100, DEEP2 = 200, SDEEP = 0, COARSE = 1, LAGOFF = 20, ENDT = 12, ENDS = 2, PHASE = 0;
constexpr int SPRINT = 0;
constexpr int JUMP = 145;
constexpr int HST = 2800, RT0 = 3900, RTS = 1000;
constexpr unsigned NRD = 5, RMIN = 100, FBP = 60, FBL = 150;
constexpr int W0START = 1700, W0SPAN = 1000, W1START = 2700, W1SPAN = 2600;
constexpr int ROT = 16, ROWS = 0xC9;
constexpr int FBM = 100, FBX = 15, FB0 = 170, FB1 = 55, FBS = 10;
constexpr int FBD = 20, HOLD = 800, GB = 24, GBS = 2, GBX = 60, GBF = 12, FOLD = 1;
constexpr unsigned PRD = 3; constexpr int PRB = 24;
constexpr int TLEAD = 40, PFD = 6, WFD = 3;
constexpr int QWAIT = 750, QBACK = 100;
constexpr int WDELAY = 100, WDELAY2 = 460, DLATE = 100, DLATE2 = 115;
constexpr int CPF = 1, CPT = 0, CODE = 24 * 64 * 48;
constexpr int GLEAD = 1, GCNT = 4, DP = 0, DPW = 60, GE = 0, PW = 30, PS = 0, HDELAY = 1150, HDELAY2 = 780, GW = 0, DW = 0;

constexpr int KS = 8;
constexpr int WS_PML = 0;
constexpr int WS_PO = WS_PML + NWG * 64;
constexpr int WS_PQ = WS_PO + NWG * DH;
constexpr int WS_OC = WS_PQ + NWG * 5 * (DH / 2);
constexpr int WS_SLAB = WS_OC + (NB - 1) * NH * DH;
constexpr int WS_ACT = WS_SLAB + KS * NB * DM;
constexpr int WS_H = WS_ACT + NB * FF / 2;
constexpr int WS_ST = WS_H + (NB - 1) * 4 * 192;
constexpr int WS_HN = WS_ST + 32;
constexpr int WS_HR = WS_HN + NWG;
constexpr int WS_HA = WS_HR + 2 * NWG;
constexpr int QFS = 2 * GS * 32;
constexpr int WS_QF = WS_HA + 32 * NWG;
constexpr int WS_QP = WS_QF + NG * QFS;
constexpr int WS_END = WS_QP + NH * 2 * NB * DH;
constexpr int NPROD = HPG * 6;
static_assert(WS_PO % 32 == 0 && WS_SLAB % 32 == 0 && WS_ACT % 32 == 0 && WS_ST % 32 == 0 && WS_HN % 32 == 0 && WS_QF % 32 == 0 && WS_QP % 32 == 0, "line alignment");


__device__ __forceinline__ int row_of(int b) { return (ROWS >> (2 * b)) & 3; }
__device__ __forceinline__ float bf_lo(unsigned u) { return __uint_as_float(u << 16); }
__device__ __forceinline__ float bf_hi(unsigned u) { return __uint_as_float(u & 0xffff0000u); }
__device__ __forceinline__ f32x2 bf_pair(unsigned u) { return f32x2{bf_lo(u), bf_hi(u)}; }
__device__ __forceinline__ f32x2 fma2(float a, f32x2 b, f32x2 c) { return __builtin_elementwise_fma(f32x2{a, a}, b, c); }
__device__ __forceinline__ f32x2 fma2(f32x2 a, f32x2 b, f32x2 c) { return __builtin_elementwise_fma(a, b, c); }

template <int CTRL> __device__ __forceinline__ float dpp(float x) {
  return __builtin_bit_cast(float, __builtin_amdgcn_mov_dpp(__builtin_bit_cast(int, x), CTRL, 0xf, 0xf, true));
}
constexpr int DPP_XOR1 = 0xB1, DPP_XOR2 = 0x4E, DPP_HMIRROR = 0x141, DPP_MIRROR = 0x140, DPP_ROR8 = 0x128;

__device__ __forceinline__ float sum_x16_32(float x) {
  auto s = __builtin_amdgcn_permlane16_swap(__float_as_uint(x), __float_as_uint(x), false, false);
  x = __uint_as_float(s[0]) + __uint_as_float(s[1]);
  auto t = __builtin_amdgcn_permlane32_swap(__float_as_uint(x), __float_as_uint(x), false, false);
  return __uint_as_float(t[0]) + __uint_as_float(t[1]);
}
__device__ __forceinline__ float max_x16_32(float x) {
  auto s = __builtin_amdgcn_permlane16_swap(__float_as_uint(x), __float_as_uint(x), false, false);
  x = fmaxf(__uint_as_float(s[0]), __uint_as_float(s[1]));
  auto t = __builtin_amdgcn_permlane32_swap(__float_as_uint(x), __float_as_uint(x), false, false);
  return fmaxf(__uint_as_float(t[0]), __uint_as_float(t[1]));
}
__device__ __forceinline__ float tsum32(float a, float b) {
  auto t = __builtin_amdgcn_permlane32_swap(__float_as_uint(a), __float_as_uint(b), false, false);
  return __uint_as_float(t[0]) + __uint_as_float(t[1]);
}
__device__ __forceinline__ float tsum16(float a, float b) {
  auto t = __builtin_amdgcn_permlane16_swap(__float_as_uint(a), __float_as_uint(b), false, false);
  return __uint_as_float(t[0]) + __uint_as_float(t[1]);
}
__device__ __forceinline__ float sum_r(float v) { v += dpp<DPP_ROR8>(v); return sum_x16_32(v); }
__device__ __forceinline__ float row_sum(float v) {
  v += dpp<DPP_XOR1>(v); v += dpp<DPP_XOR2>(v); v += dpp<DPP_HMIRROR>(v);
  return v + dpp<DPP_MIRROR>(v);
}
__device__ __forceinline__ float row_max(float v) {
  v = fmaxf(v, dpp<DPP_XOR1>(v)); v = fmaxf(v, dpp<DPP_XOR2>(v)); v = fmaxf(v, dpp<DPP_HMIRROR>(v));
  return fmaxf(v, dpp<DPP_MIRROR>(v));
}
__device__ __forceinline__ float wave_sum(float v) {
  v += dpp<DPP_XOR1>(v); v += dpp<DPP_XOR2>(v); v += dpp<DPP_HMIRROR>(v);
  return sum_r(v);
}

__device__ __forceinline__ u32x4 ld_nt(const char* p) { return __builtin_nontemporal_load(reinterpret_cast<const u32x4*>(p)); }

struct Tile { u32x4 d[3]; };

#define PIN4(a, o) asm volatile("" : "+v"((a)[(o) + 0]), "+v"((a)[(o) + 1]), "+v"((a)[(o) + 2]), "+v"((a)[(o) + 3]))
#define MEMFENCE() do { asm volatile("" ::: "memory"); __builtin_amdgcn_sched_barrier(0); } while (0)

__device__ __forceinline__ void glds16_nt(const void* gsrc, unsigned lds_dst) {
  unsigned keep;
  asm volatile("s_mov_b32 %0, m0\n\ts_mov_b32 m0, %2\n\ts_nop 0\n\tglobal_load_lds_dwordx4 %1, off nt\n\ts_mov_b32 m0, %0"
               : "=&s"(keep) : "v"(gsrc), "s"(lds_dst) : "memory");
}

using bf16x2 = __attribute__((ext_vector_type(2))) __bf16;
__device__ __forceinline__ float dot2(unsigned a, unsigned b, float c) { return __builtin_amdgcn_fdot2_f32_bf16(__builtin_bit_cast(bf16x2, a), __builtin_bit_cast(bf16x2, b), c, false); }
__device__ __forceinline__ float tile_score(const Tile& k, const u32x2 (&q)[12]) {
  float s0 = 0.f, s1 = 0.f;
#pragma unroll
  for (int i = 0; i < 3; ++i) {
    u32x4 v = k.d[i];
    asm("" : "+v"(v));
#pragma unroll
    for (int d = 0; d < 4; ++d) { s0 = dot2(v[d], q[i * 4 + d][0], s0); s1 = dot2(v[d], q[i * 4 + d][1], s1); }
  }
  return s0 + s1;
}
__device__ __forceinline__ void pair_put(float a, float b, float* dst, int j) {
  float s = (j < 4 ? a : b) + dpp<DPP_HMIRROR>(j < 4 ? b : a);
  s += dpp<DPP_XOR1>(s);
  s += dpp<DPP_XOR2>(s);
  if ((j & 3) == 0) dst[j * 2] = s;
}
__device__ __forceinline__ void score_put(float s, float* dst, int j) {
  s += dpp<DPP_XOR1>(s);
  s += dpp<DPP_XOR2>(s);
  s += dpp<DPP_HMIRROR>(s);
  if (j == 0) *dst = s;
}
__device__ __forceinline__ void tile_load(Tile& t, const char* base, unsigned off, int half) {
#pragma unroll
  for (int i = 0; i < 3; ++i) t.d[i] = ld_nt(base + off + half * 3072 + i * 128);
}
__device__ __forceinline__ void pair_issue(Tile& ta, Tile& tb, const char* base, unsigned off, int deep) {
  deep = __builtin_amdgcn_readfirstlane(deep);
#define WAIT_IF(level, n) asm volatile("s_cmp_lg_u32 %0, " #level "\n\ts_cbranch_scc1 .Lpi%=\n\ts_waitcnt vmcnt(" #n ")\n.Lpi%=:" :: "s"(deep) : "scc", "memory")
  const char* p = base + off;
  if constexpr (COARSE) {
    WAIT_IF(0, 3);
    ta.d[0] = ld_nt(p); ta.d[1] = ld_nt(p + 128); ta.d[2] = ld_nt(p + 256); MEMFENCE();
    WAIT_IF(0, 3); WAIT_IF(1, 6);
    tb.d[0] = ld_nt(p + 3072); tb.d[1] = ld_nt(p + 3200); tb.d[2] = ld_nt(p + 3328); MEMFENCE();
  } else {
    WAIT_IF(0, 5); ta.d[0] = ld_nt(p); MEMFENCE();
    WAIT_IF(0, 5); ta.d[1] = ld_nt(p + 128); MEMFENCE();
    WAIT_IF(0, 5); ta.d[2] = ld_nt(p + 256); MEMFENCE();
    WAIT_IF(1, 6);
    WAIT_IF(0, 5); tb.d[0] = ld_nt(p + 3072); MEMFENCE();
    WAIT_IF(0, 5); tb.d[1] = ld_nt(p + 3200); MEMFENCE();
    WAIT_IF(0, 5); tb.d[2] = ld_nt(p + 3328); MEMFENCE();
  }
#undef WAIT_IF
}
__device__ __forceinline__ void pv_last(float p, Tile& v, f32x2 (&acc)[12]) {
#pragma unroll
  for (int i = 0; i < 3; ++i) {
    asm volatile("" : "+v"(v.d[i]), "+v"(p));
#pragma unroll
    for (int d = 0; d < 4; ++d) acc[i * 4 + d] = fma2(p, bf_pair(v.d[i][d]), acc[i * 4 + d]);
    PIN4(acc, i * 4);
  }
}
__device__ __forceinline__ void q_load(u32x2 (&q)[12], const float* src, int j) {
#pragma unroll
  for (int i = 0; i < 6; ++i) {
    const u32x4 a = *reinterpret_cast<const u32x4*>(src + (i >> 1) * 64 + j * 8 + (i & 1) * 4);
    q[i * 2] = u32x2{a[0], a[1]}; q[i * 2 + 1] = u32x2{a[2], a[3]};
  }
}
#define VMCNT(n) asm volatile("s_waitcnt vmcnt(%0)" :: "n"(n) : "memory")

constexpr int AUX_SC1 = 16, AUX_EARLY = 18;
constexpr unsigned SPIN_CAP = 1u << 17;
__device__ __forceinline__ unsigned nz_bits(float v) { return max(__float_as_uint(v), 1u); }
__device__ __forceinline__ bool all_nz(u32x4 v) { return v[0] != 0u && v[1] != 0u && v[2] != 0u && v[3] != 0u; }
__device__ __forceinline__ unsigned min4(u32x4 v) { return min(min(v[0], v[1]), min(v[2], v[3])); }
__device__ __forceinline__ unsigned ld32(__amdgpu_buffer_rsrc_t rs, int word) { return __builtin_amdgcn_raw_buffer_load_b32(rs, word * 4, 0, AUX_SC1); }
__device__ __forceinline__ u32x2 ld64(__amdgpu_buffer_rsrc_t rs, int word) { return __builtin_amdgcn_raw_buffer_load_b64(rs, word * 4, 0, AUX_SC1); }
__device__ __forceinline__ u32x4 ld128(__amdgpu_buffer_rsrc_t rs, int word) { return __builtin_amdgcn_raw_buffer_load_b128(rs, word * 4, 0, AUX_SC1); }
__device__ __forceinline__ void st32(__amdgpu_buffer_rsrc_t rs, int word, unsigned v) { __builtin_amdgcn_raw_buffer_store_b32(v, rs, word * 4, 0, AUX_SC1); }
__device__ __forceinline__ void st64(__amdgpu_buffer_rsrc_t rs, int word, u32x2 v) { __builtin_amdgcn_raw_buffer_store_b64(v, rs, word * 4, 0, AUX_SC1); }
__device__ __forceinline__ void st128(__amdgpu_buffer_rsrc_t rs, int word, u32x4 v) { __builtin_amdgcn_raw_buffer_store_b128(v, rs, word * 4, 0, AUX_SC1); }

__device__ __forceinline__ u32x4 ld128nt(__amdgpu_buffer_rsrc_t rs, int word) { return __builtin_amdgcn_raw_buffer_load_b128(rs, word * 4, 0, AUX_EARLY); }
__device__ __forceinline__ void st128nt(__amdgpu_buffer_rsrc_t rs, int word, u32x4 v) { __builtin_amdgcn_raw_buffer_store_b128(v, rs, word * 4, 0, AUX_EARLY); }

__device__ __forceinline__ bool leader_release(__amdgpu_buffer_rsrc_t rs, int lane, unsigned t0, unsigned rd) {
  u32x4 a[4];
#pragma unroll
  for (int k = 0; k < 4; ++k) a[k] = ld128(rs, WS_HA + (lane * 4 + k) * 32);
  const u32x4 n = ld128(rs, WS_HN + lane * 4);
  if (rd == 0u && !__all(a[0][0] == n[0] && a[1][0] == n[1] && a[2][0] == n[2] && a[3][0] == n[3])) return false;
  const float e = fmaxf(fmaxf((float)(int)(t0 - a[0][2]), (float)(int)(t0 - a[1][2])), fmaxf((float)(int)(t0 - a[2][2]), (float)(int)(t0 - a[3][2])));
  unsigned first = t0 - (unsigned)(int)max_x16_32(row_max(e));
  float ls = 0.f, lx = 0.f;
#pragma unroll
  for (int k = 0; k < 4; ++k) {
    const float l = a[k][0] == n[k] + rd ? (float)min(a[k][3] + (rd ? 0u : min(a[k][2] - first, 300u)), 2000u) : 0.f;
    ls += l; lx = fmaxf(lx, l);
  }
  ls = wave_sum(ls) * (1.0f / NWG); lx = max_x16_32(row_max(lx));
  const int extra = min(max((int)(ls * (FBM / 100.0f) + lx * (FBX / 100.0f)) - (rd ? FB1 + (int)(NRD - rd) * FBD : FB0), 0), 600);
  first = (first & 0xffffu) | ((unsigned)extra << 16);
  if (rd != 0u && ls * NWG < HOLD) return true;
  st128(rs, WS_HR + lane * 8, u32x4{a[0][1] + rd, first, a[1][1] + rd, first});
  st128(rs, WS_HR + lane * 8 + 4, u32x4{a[2][1] + rd, first, a[3][1] + rd, first});
  return true;
}

typedef __attribute__((address_space(3))) unsigned lds_u32;
__device__ __forceinline__ unsigned lds_add(lds_u32* p, int lane) {
  unsigned k = 0;
  if (lane == 0) k = __hip_atomic_fetch_add(p, 1u, __ATOMIC_RELAXED, __HIP_MEMORY_SCOPE_WORKGROUP);
  return __builtin_amdgcn_readfirstlane(k);
}
#define CFENCE() asm volatile("" ::: "memory")
__device__ __forceinline__ int arrive(lds_u32* p) {
  int v;
  unsigned long long keep;
  asm volatile("s_mov_b64 %1, exec\n\ts_mov_b64 exec, 1\n\tds_add_rtn_u32 %0, %2, %3\n\ts_mov_b64 exec, %1"
               : "=&v"(v), "=&s"(keep) : "v"((unsigned)(size_t)p), "v"(1u) : "memory");
  return v;
}
__device__ __forceinline__ int arrived(int v) {
  asm volatile("s_waitcnt lgkmcnt(0)" : "+v"(v) :: "memory");
  return __builtin_amdgcn_readfirstlane(v);
}
__device__ __forceinline__ unsigned lds_get(lds_u32* p) {
  return __builtin_amdgcn_readfirstlane(__hip_atomic_load(p, __ATOMIC_RELAXED, __HIP_MEMORY_SCOPE_WORKGROUP));
}
template <int SLEEP> __device__ __forceinline__ void lds_wait(lds_u32* p, unsigned n) {
#pragma unroll 1
  for (unsigned spin = 0; spin < (SPIN_CAP << 4); ++spin) {
    if (lds_get(p) >= n) break;
    __builtin_amdgcn_s_sleep(SLEEP);
  }
  asm volatile("" ::: "memory");
}
__device__ __forceinline__ void pause(unsigned ticks) {
  const unsigned long long t0 = __builtin_amdgcn_s_memrealtime();
#pragma unroll 1
  for (unsigned spin = 0; spin < 4096u && __builtin_amdgcn_s_memrealtime() - t0 < ticks; ++spin) __builtin_amdgcn_s_sleep(16);
}
__device__ __forceinline__ unsigned clk() { return (unsigned)__builtin_amdgcn_s_memrealtime(); }
struct Table {
  unsigned t0; int was, moved, slowed, folded;
  u32x2 told;
  __device__ __forceinline__ int gate(int due, lds_u32* mine, bool hold = true, int sh = 0) {
    const int fold = __builtin_amdgcn_readfirstlane(told[1]);
    if (fold != folded) { const int f = min(fold - folded, moved); moved -= f; slowed += f; folded = fold; *mine = moved; }
    const int left = (int)__builtin_amdgcn_readfirstlane(told[0]) - slowed, d = max(min(left, FBS), left < -GBX ? -GBF : -GBS);
    t0 += d; was -= d; slowed += d;
    int late = (int)(clk() - t0) - due;
    if (late - was > JUMP) { t0 += late - was; moved += late - was; late = was; *mine = moved; }
    const int lag = max(late - max(left - d, 0) - LAGOFF, 0);
    mine[4] = lag;
    was = late;
#pragma unroll 1
    for (int spin = 0; spin < 65536 && hold && late < 0; ++spin) { __builtin_amdgcn_s_sleep(1); late = (int)(clk() - t0) - due; }
    return max(lag < (DEEP1 >> sh) ? 0 : (lag < (DEEP2 >> sh) ? 1 : 2), hold ? 0 : SDEEP);
  }
  __device__ __forceinline__ void listen(lds_u32* slow) {
    told = __builtin_bit_cast(u32x2, __hip_atomic_load((__attribute__((address_space(3))) unsigned long long*)slow, __ATOMIC_RELAXED, __HIP_MEMORY_SCOPE_WORKGROUP));
  }
  __device__ __forceinline__ void agree(lds_u32* all, lds_u32* mine) {
    if (moved == 0) return;
    const int m = (int)min(min(lds_get(all), lds_get(all + 1)), min(lds_get(all + 2), lds_get(all + 3))) + 30;
    if (m < moved) { t0 -= moved - m; moved = m; was = 1 << 20; *mine = moved; }
  }
};
__device__ __forceinline__ void until(unsigned t0, int due) {
#pragma unroll 1
  for (int spin = 0; spin < 65536 && (int)(clk() - t0) < due; ++spin) __builtin_amdgcn_s_sleep(1);
}
#define LGKM0() asm volatile("s_waitcnt lgkmcnt(0)" ::: "memory")

__device__ __forceinline__ void split4(f32x4 v, u32x2& hi, u32x2& lo) {
  unsigned h[4], r[4];
#pragma unroll
  for (int i = 0; i < 4; ++i) { h[i] = __float_as_uint(v[i]) & 0xffff0000u; r[i] = __float_as_uint(v[i] - __uint_as_float(h[i])); }
  hi = u32x2{__builtin_amdgcn_perm(h[1], h[0], 0x07060302u), __builtin_amdgcn_perm(h[3], h[2], 0x07060302u)};
  lo = u32x2{__builtin_amdgcn_perm(r[1], r[0], 0x07060302u), __builtin_amdgcn_perm(r[3], r[2], 0x07060302u)};
}
__device__ __forceinline__ u32x4 split8(f32x4 v, bool odd) {
  u32x2 hi, lo;
  split4(v, hi, lo);
  const unsigned n0 = __float_as_uint(dpp<DPP_XOR1>(__uint_as_float(odd ? hi[0] : lo[0]))), n1 = __float_as_uint(dpp<DPP_XOR1>(__uint_as_float(odd ? hi[1] : lo[1])));
  return u32x4{max(odd ? n0 : hi[0], 1u), max(odd ? n1 : hi[1], 1u), max(odd ? lo[0] : n0, 1u), max(odd ? lo[1] : n1, 1u)};
}
__device__ __forceinline__ u32x4 q_pack(u32x4 a, u32x4 b) {
  u32x2 hi, lo;
  split4((__builtin_bit_cast(f32x4, a) + __builtin_bit_cast(f32x4, b)) * QSCALE, hi, lo);
  return u32x4{hi[0], lo[0], hi[1], lo[1]};
}
__device__ __forceinline__ f32x4 mfma16(u32x4 a, u32x4 b, f32x4 c) {
  return __builtin_amdgcn_mfma_f32_16x16x32_bf16(__builtin_bit_cast(bf16x8, a), __builtin_bit_cast(bf16x8, b), c, 0, 0, 0);
}

constexpr int NW = 8, NMW = 4;
constexpr int NLP = 5, NRP = 7;
constexpr int NTP = 4 * (NLP + NRP);
constexpr int REG = NLP * 6 * 1024;
constexpr int NRING = 4;
constexpr int NS = 6, NA = NS - 1;
__device__ __forceinline__ int seg_beg(int s) { return s >= NS ? NPAIR : ((0xC87640 >> (4 * s)) & 15) * 16; }
__device__ __forceinline__ int seg_k(int t) { return (int)(0x5433222211110000ull >> (4 * (t >> 4))) & 15; }
__device__ __forceinline__ int seg_of_k(int k) { return (0x321540 >> (4 * k)) & 15; }
__device__ __forceinline__ int k_of_seg(int s) { return (0x215430 >> (4 * s)) & 15; }
__device__ __forceinline__ int pair_of(int t) { return t < 64 ? t : (t < 192 ? t + 64 : t - 128); }
constexpr int OFF_CNT = 0, OFF_REG = 256,
              OFF_SC = OFF_REG + 4 * REG, OFF_BUF = OFF_SC + NPAIR * 16 * 4, OFF_Q = OFF_BUF + NRING * NMW * DH * 4, OFF_ML = OFF_Q + NS * DH * 4,
              OFF_RED = OFF_ML + 128, OFF_RS = OFF_RED + 4 * 256 * 4, OFF_END = OFF_RS + 5 * 16;
enum { F_REL = 0, F_DONE, C_LAST, C_T = 3, F_OUT, C_P, C_K, F_P, F_Q = 8, F_FREE, S_FIRST, F_Q2, C_W = 12, C_SC = 16, C_PART = 24, S_ECHO = 32, S_MOVED = 48, S_SLOW = 56 };
__device__ __forceinline__ int due_k(int p) { return TSTART + ((p * TAUK + min(max(p - WP0, 0), WPN) * (WGAP * 64 / WPN)) >> 6); }
__device__ __forceinline__ int due_v(int p) { return TSTART + WGAP + ((NPAIR * TAUK + p * TAUV + max(p - NPAIR / 2, 0) * (VGAP * 64 / (NPAIR / 2))) >> 6); }
constexpr int AW_LD = DH * 2 + 16, AG_LD = 160 * 2 + 16;

__global__ void __launch_bounds__(NW * 64) fused_kernel(const bf16* __restrict__ x, const bf16* __restrict__ kc,
                                                        const bf16* __restrict__ vc, const bf16* __restrict__ wq,
                                                        const bf16* __restrict__ wo, const bf16* __restrict__ wr,
                                                        const bf16* __restrict__ wg, const bf16* __restrict__ wu,
                                                        const bf16* __restrict__ wd, float* ws, bf16* __restrict__ out) {
  __shared__ __attribute__((aligned(16))) unsigned char smem[OFF_END];
  float* s_sc = reinterpret_cast<float*>(smem + OFF_SC);
  float* s_buf = reinterpret_cast<float*>(smem + OFF_BUF);
  float* s_q = reinterpret_cast<float*>(smem + OFF_Q);
  u32x4* s_ml = reinterpret_cast<u32x4*>(smem + OFF_ML);
  lds_u32* s_c = (lds_u32*)(smem + OFF_CNT);

  const int tid = threadIdx.x, lane = tid & 63, r = lane >> 3, j = lane & 7;
  const int wave = __builtin_amdgcn_readfirstlane(tid >> 6);
  const int tw = wave - NMW;
  const int bid = blockIdx.x, xl = bid & 7, idx = bid >> 3;
  const int g = (xl * 32 + idx) / GS, mm = idx % GS;
  const __amdgpu_buffer_rsrc_t rs = __builtin_amdgcn_make_buffer_rsrc(ws, 0, WS_END * 4, 0x00020000);
  const int ro = ((idx & 3) << 3) | (idx >> 2);
  const bool isW = ro < 10, isG = ro >= 10 && ro < 16;
  const int w_ks = xl, w_ct = ro;
  const int g_cg = xl * 6 + (ro - 10);
  const int d_ct = (xl + ((ro - 16) >> 2) * 8) * 2 + (((ro - 16) >> 1) & 1);
  const bool isD = ro >= 16 && ro < 24 && (ro & 1) == 0 && d_ct < DM / 32;
  const bool isH = ro == 22 && xl >= 2 && xl < 5;
  const bool isC = (mm & 7) == 3 || (mm & 7) >= 6;
  const int c_it = (mm >> 3) * 3 + max((mm & 7) - 5, 0);
  const int qf = WS_QF + g * QFS;
  const char* kb = reinterpret_cast<const char*>(kc);
  const char* vb = reinterpret_cast<const char*>(vc);
  const unsigned lo = (r * 24 + j) * 16;
  const int xb = g * GS + mm, c0 = mm * 8 + g * ROT;
  const int b2 = (2 * NH + (xb >> 4)) * 1024 + (xb & 15) * 8, b3 = (2 * NH + 16 + (xb >> 5)) * 1024 + (xb & 31) * 8;
  auto poff = [&](int p) -> unsigned {
    const int turn = (p & 63) >> 2;
    int t = (row_of(p >> 6) * NH + g) * 1024 + ((turn * 64 + (p & 3) * 2 + c0) & 1023);
    if ((p >> 6) == 1) t = (turn < 8 ? b2 + turn * 128 : b3 + (turn & 4) * 2048 + (turn & 3) * 256) + (p & 3) * 2;
    return (unsigned)t * 3072u;
  };
  const unsigned lds_base = (unsigned)(size_t)(__attribute__((address_space(3))) unsigned char*)smem + OFF_REG + tw * REG;
  auto merged = [&](int it, int ln) -> f32x4 {
    const float* src = s_buf + (it % NRING) * (NMW * DH) + ((ln >> 1) & 7) * 24 + (ln >> 4) * 8 + (ln & 1) * 4;
    return (*reinterpret_cast<const f32x4*>(src) + *reinterpret_cast<const f32x4*>(src + DH)) +
           (*reinterpret_cast<const f32x4*>(src + 2 * DH) + *reinterpret_cast<const f32x4*>(src + 3 * DH));
  };
  auto publish = [&](int it) -> bool {
    const int k = arrive(s_c + C_PART + it);
    const f32x4 m = merged(it, lane < DH / 4 ? lane : 0);
    u32x4 o = {nz_bits(m[0]), nz_bits(m[1]), nz_bits(m[2]), nz_bits(m[3])};
    asm volatile("" : "+v"(o));
    if (arrived(k) != NMW - 1) return false;
    if (lane < DH / 4) st128(rs, WS_PO + xb * DH + lane * 4, o);
    if (lane == 0) __hip_atomic_fetch_add(s_c + F_DONE, 1u, __ATOMIC_RELAXED, __HIP_MEMORY_SCOPE_WORKGROUP);
    return true;
  };
#define NONCE() do { unsigned n_ = __builtin_amdgcn_readfirstlane(f) + 1u; while (__any(f == n_)) ++n_; \
    if (lane < 32) st32(rs, qf + (GS + mm) * 32 + lane, n_); f = n_; } while (0)
  unsigned long long pc;
  asm volatile("s_getpc_b64 %0" : "=s"(pc));
  unsigned r0 = 0u;
  unsigned t0 = clk();
  if (wave < NMW) {
    Tile ta, tb, tc, td;
    const int c = lane & (GS - 1);
    unsigned f = 0u;
    if (wave == 3) f = ld32(rs, qf + c * 32 + mm);
    if (mm < NPROD) {
      const int hp = g + (mm / 6) * NG, ck = (mm % 6) >> 1, hf = mm & 1, jp = hf * 4 + wave;
      const int bb = lane >> 4, t = lane & 15;
      const u32x4* xp = reinterpret_cast<const u32x4*>(x + row_of(bb) * DM);
      u32x4 xv[5], xs = {0u, 0u, 0u, 0u};
#pragma unroll
      for (int k = 0; k < 5; ++k) xv[k] = xp[t + 16 * k];
      if (t < 10) xs = xp[jp * 10 + t];
      const u32x4* wp = reinterpret_cast<const u32x4*>(wq + (size_t)(jp * 80 + r) * HD + hp * DH + ck * 64) + j;
      u32x4 wv[10];
#pragma unroll
      for (int k = 0; k < 10; ++k) wv[k] = __builtin_nontemporal_load(wp + (size_t)k * 8 * (HD / 8));
      if (wave == 0) s_c[lane] = lane == C_K ? NTP + NMW : 0u;
      MEMFENCE(); LGKM0(); __builtin_amdgcn_s_barrier(); MEMFENCE();
      if (wave == 3) { NONCE(); }
      float sm = 0.f;
#pragma unroll
      for (int k = 0; k < 5; ++k)
#pragma unroll
        for (int d = 0; d < 4; ++d) sm += bf_lo(xv[k][d]) + bf_hi(xv[k][d]);
      const float mean = row_sum(sm) * (1.0f / DM);
      float sv = 0.f;
#pragma unroll
      for (int k = 0; k < 5; ++k)
#pragma unroll
        for (int d = 0; d < 4; ++d) { const float a = bf_lo(xv[k][d]) - mean, c2 = bf_hi(xv[k][d]) - mean; sv += a * a + c2 * c2; }
      const float rstd = 1.0f / sqrtf(row_sum(sv) * (1.0f / DM) + LN_EPS);
      float* s_xn = s_buf + wave * 320;
      if (t < 10) {
#pragma unroll
        for (int d = 0; d < 4; ++d) {
          s_xn[(t * 8 + 2 * d) * 4 + bb] = (bf_lo(xs[d]) - mean) * rstd;
          s_xn[(t * 8 + 2 * d + 1) * 4 + bb] = (bf_hi(xs[d]) - mean) * rstd;
        }
      }
      LGKM0();
      f32x2 qa[16];
#pragma unroll
      for (int i = 0; i < 16; ++i) qa[i] = f32x2{0.f, 0.f};
#pragma unroll
      for (int k = 0; k < 10; ++k) {
        const f32x4 xn = *reinterpret_cast<const f32x4*>(&s_xn[(k * 8 + r) * 4]);
#pragma unroll
        for (int d = 0; d < 4; ++d) {
          const f32x2 w2 = bf_pair(wv[k][d]);
#pragma unroll
          for (int e = 0; e < 4; ++e) qa[e * 4 + d] = fma2(xn[e], w2, qa[e * 4 + d]);
        }
      }
      float t16[16], t8[8];
#pragma unroll
      for (int i = 0; i < 16; ++i) t16[i] = tsum32(qa[i >> 1][i & 1], qa[8 + (i >> 1)][i & 1]);
#pragma unroll
      for (int i = 0; i < 8; ++i) t8[i] = tsum16(t16[i], t16[i + 8]);
#pragma unroll
      for (int i = 0; i < 8; ++i) t8[i] += dpp<DPP_ROR8>(t8[i]);
      const bool up = (lane & 8) != 0;
      f32x4* r4 = reinterpret_cast<f32x4*>(smem + OFF_RED);
      r4[wave * 64 + lane] = f32x4{up ? t8[4] : t8[0], up ? t8[5] : t8[1], up ? t8[6] : t8[2], up ? t8[7] : t8[3]};
      LGKM0();
      if (lds_add(s_c + C_P, lane) == NMW - 1) {
        asm volatile("" ::: "memory");
        const f32x4 o4 = (r4[lane] + r4[64 + lane]) + (r4[128 + lane] + r4[192 + lane]);
        st128nt(rs, WS_QP + ((hp * 2 + hf) * NB + bb) * DH + ck * 64 + j * 8 + (up ? 4 : 0), __builtin_bit_cast(u32x4, o4));
        VMCNT(0);
        if (lane == 0) s_c[F_OUT] = 1u;
      }
    } else {
      if (wave == 0) s_c[lane] = lane == C_K ? NTP + NMW : 0u;
      MEMFENCE(); LGKM0(); __builtin_amdgcn_s_barrier(); MEMFENCE();
      if (wave == 3) { NONCE(); }
      else if (CPF && lane < CODE / (24 * 64)) {
        typedef __attribute__((address_space(1))) const unsigned g_u32;
        until(t0, CPT);
        const unsigned v = *(g_u32*)((pc & ~255ull) + (((idx >> 3) * 2 + (mm & 1)) * 3 + wave) * (CODE / 24) + lane * 64);
        asm volatile("" :: "v"(v));
      }
    }
    MEMFENCE();
    if (wave == 3) {
      __builtin_amdgcn_s_setprio(3);
      const unsigned nonce = f;
      unsigned echoed = 0u;
      if (mm < NPROD) {
        echoed = ld32(rs, qf + (GS + c) * 32);
        asm volatile("" : "+v"(echoed) :: "memory");
        lds_wait<1>(s_c + F_OUT, 1u);
        if (lane < 32) st32(rs, qf + mm * 32 + lane, echoed);
#pragma unroll 1
        while ((int)(clk() - t0) < QWAIT - QBACK) {
          const unsigned nv = ld32(rs, qf + (GS + c) * 32);
          if (__any(nv != echoed)) {
            if (lane < 32) st32(rs, qf + mm * 32 + lane, nv);
            echoed = nv;
          }
          __builtin_amdgcn_s_sleep(4);
        }
      }
      until(t0, QWAIT);
#pragma unroll 1
      for (unsigned spin = 0; spin < SPIN_CAP; ++spin) {
        const unsigned nv = ld32(rs, qf + (GS + c) * 32);
        f = ld32(rs, qf + c * 32 + mm);
        const bool done = __all(c == mm || c >= NPROD || f == nonce);
        if (mm < NPROD && __any(nv != echoed)) {
          if (lane < 32) st32(rs, qf + mm * 32 + lane, nv);
          echoed = nv;
        }
        if (done) break;
        __builtin_amdgcn_s_sleep(8);
      }
      if (lane < GS) s_c[S_ECHO + lane] = echoed;
      if (lane < DH / 4) {
        u32x4 p[3][2];
#pragma unroll
        for (int it = 0; it < 3; ++it)
#pragma unroll
          for (int k = 0; k < 2; ++k) p[it][k] = ld128nt(rs, WS_QP + ((g * 2 + k) * NB + (it ? it + 1 : 0)) * DH + lane * 4);
#pragma unroll
        for (int it = 0; it < 3; ++it) {
          *reinterpret_cast<u32x4*>(&s_q[(it ? it + 3 : 0) * DH + lane * 4]) = q_pack(p[it][0], p[it][1]);
          if (it != 1) { LGKM0(); if (lane == 0) s_c[F_Q] = it + 1; MEMFENCE(); }
        }
      }
    }
    __builtin_amdgcn_s_setprio(3);
    until(t0, due_k(NTP + wave));
    Table tt = {t0 + ((idx * PHASE) >> 5), 1 << 20, 0, 0, 0, {0u, 0u}};
    bool common = false;
    tile_load(ta, kb, poff(NTP + wave) + lo, 0);
    tile_load(tb, kb, poff(NTP + wave) + lo, 1);
    MEMFENCE();
    {
      u32x2 q[12];
      int cur = NTP + wave, nxt = lds_add(s_c + C_K, lane), passed = 0, ci = -1;
      auto kstep = [&](Tile& xa, Tile& xb_, Tile& ya, Tile& yb) __attribute__((always_inline)) -> bool {
        const bool last = nxt >= NPAIR;
        const unsigned off = poff(last ? wave : pair_of(nxt)) + lo;
        const char* base = last ? vb : kb;
        const int it = seg_k(cur), cp = pair_of(cur);
        if (it != ci) {
          LGKM0();
          for (; passed < it; ++passed) lds_add(s_c + C_SC + passed, lane);
          lds_wait<1>(s_c + (it < 3 ? F_Q : F_Q2), min(it + 1, 3));
          q_load(q, s_q + seg_of_k(it) * DH, j);
          if (!common) { const unsigned d = lds_get(s_c + S_FIRST); if (d) { tt.t0 -= d - 1u; common = true; tt.was = 1 << 20; } }
          tt.agree(s_c + S_MOVED, s_c + S_MOVED + wave);
          ci = it;
        }
        MEMFENCE();
        const int deep = tt.gate(due_k(last ? NPAIR + wave : nxt), s_c + S_MOVED + wave);
        MEMFENCE();
        pair_issue(ya, yb, base, off, deep);
        const int tk = arrive(s_c + C_K);
        tt.listen(s_c + S_SLOW + 2 * wave);
        pair_put(tile_score(xa, q), tile_score(xb_, q), s_sc + cp * 16 + r, j);
        MEMFENCE();
        cur = nxt; nxt = arrived(tk);
        return last;
      };
      bool swap;
      for (;;) {
        if (kstep(ta, tb, tc, td)) { swap = true; break; }
        if (kstep(tc, td, ta, tb)) { swap = false; break; }
      }
      LGKM0();
      for (; passed < NS; ++passed) lds_add(s_c + C_SC + passed, lane);
      __builtin_amdgcn_s_waitcnt(0x0F70);
      if (swap) { ta = tc; tb = td; }
    }
#pragma unroll 1
    for (int it = 0; it < NS; ++it) {
      lds_wait<1>(s_c + F_P, k_of_seg(it) + 1);
      if (!common) { const unsigned d = lds_get(s_c + S_FIRST); if (d) { tt.t0 -= d - 1u; common = true; tt.was = 1 << 20; } }
      tt.agree(s_c + S_MOVED, s_c + S_MOVED + wave);
      f32x2 acc[12];
#pragma unroll
      for (int i = 0; i < 12; ++i) acc[i] = f32x2{0.f, 0.f};
      auto vstep = [&](Tile& xa, Tile& xb_, Tile& ya, Tile& yb, int p) __attribute__((always_inline)) {
        const unsigned off = poff(p + NMW) + lo;
        MEMFENCE();
        const int deep = tt.gate(due_v(p + NMW), s_c + S_MOVED + wave, p + NMW < NPAIR - SPRINT * NMW, p >= NPAIR - ENDT * NMW ? ENDS : 0);
        MEMFENCE();
        pair_issue(ya, yb, vb, off, deep);
        const float pa = s_sc[p * 16 + r], pb = s_sc[p * 16 + 8 + r];
        tt.listen(s_c + S_SLOW + 2 * wave);
        if (p + 2 * NMW >= NPAIR) { if (lane == 0) __hip_atomic_fetch_add(s_c + C_LAST, 1u, __ATOMIC_RELAXED, __HIP_MEMORY_SCOPE_WORKGROUP); }
        pv_last(pa, xa, acc);
        pv_last(pb, xb_, acc);
        MEMFENCE();
      };
#pragma unroll 1
      for (int p = seg_beg(it) + wave; p < min(seg_beg(it + 1), NPAIR - 2 * NMW); p += 2 * NMW) {
        vstep(ta, tb, tc, td, p);
        vstep(tc, td, ta, tb, p + NMW);
      }
      if (it == NS - 1) {
        vstep(ta, tb, tc, td, NPAIR - 2 * NMW + wave);
        const int p = NPAIR - NMW + wave;
        const float pa = s_sc[p * 16 + r], pb = s_sc[p * 16 + 8 + r];
        pv_last(pa, tc, acc);
        pv_last(pb, td, acc);
      }
      float t12[12], t6[6];
#pragma unroll
      for (int i = 0; i < 12; ++i) t12[i] = tsum32(acc[i >> 1][i & 1], acc[6 + (i >> 1)][i & 1]);
#pragma unroll
      for (int i = 0; i < 6; ++i) t6[i] = tsum16(t12[i], t12[i + 6]);
#pragma unroll
      for (int i = 0; i < 6; ++i) t6[i] += dpp<DPP_ROR8>(t6[i]);
      if (it >= NRING) lds_wait<1>(s_c + F_FREE, it - NRING + 1);
      if ((lane & 8) == 0) {
        f32x2* dst = reinterpret_cast<f32x2*>(&s_buf[(it % NRING) * (NMW * DH) + wave * DH + j * 24 + (lane >> 4) * 6]);
        dst[0] = f32x2{t6[0], t6[1]}; dst[1] = f32x2{t6[2], t6[3]}; dst[2] = f32x2{t6[4], t6[5]};
      }
      CFENCE();
      if (it < NA) { if (lane == 0) __hip_atomic_fetch_add(s_c + C_PART + it, 1u, __ATOMIC_RELAXED, __HIP_MEMORY_SCOPE_WORKGROUP); }
    }
    publish(NS - 1);
    return;
  }

  {
    Tile pk[NRP * 2];
    __builtin_amdgcn_s_setprio(3);
#pragma unroll
    for (int e = 0; e < NLP * 2; ++e) {
      const char* src = kb + (poff((e >> 1) * 4 + tw) + lo + (e & 1) * 3072);
      if (e >= 2) until(t0, due_k(e * 2 + tw) - TLEAD);
#pragma unroll
      for (int i = 0; i < 3; ++i) glds16_nt(src + i * 128, lds_base + (e * 3 + i) * 1024);
      if (e == 1) __builtin_amdgcn_s_barrier();
      if (e * 3 + 3 > PFD) VMCNT(PFD);
    }
#pragma unroll
    for (int e = 0; e < NRP * 2; ++e) {
      until(t0, due_k((NLP * 2 + e) * 2 + tw) - TLEAD);
      tile_load(pk[e], kb, poff((NLP + (e >> 1)) * 4 + tw) + lo, e & 1);
      MEMFENCE();
      if (e + 1 < NRP * 2) VMCNT(PFD);
    }
    if (tw == 0) {
      r0 = ld32(rs, WS_HR + bid * 2);
      if (bid == 0) {
        u32x4 n;
#pragma unroll
        for (int k = 0; k < 4; ++k) n[k] = ld32(rs, WS_HA + (lane * 4 + k) * 32) + 1u;
        st128(rs, WS_HN + lane * 4, n);
      }
      const u32x4 z = {0u, 0u, 0u, 0u};
      if (lane < 48) {
#pragma unroll
        for (int it = NA; it < NS; ++it) st128(rs, WS_PO + xb * DH + lane * 4, z);
      }
      else st128(rs, WS_PML + (g * GS + mm) * 64 + (lane - 48) * 4, z);
      if (isG && g_cg == 0 && lane < 8) st128(rs, WS_ST + lane * 4, z);
      if (isW) st128(rs, WS_SLAB + (w_ks * NB + (lane >> 4)) * DM + w_ct * 64 + (lane & 15) * 4, z);
      else if (isG) { if (lane < 16) st128(rs, WS_ACT + g_cg * 64 + lane * 4, z); }
      else if (isH) {
#pragma unroll
        for (int k = 0; k < 3; ++k) st128(rs, WS_H + (xl - 2) * 768 + k * 256 + lane * 4, z);
      }
      else if (isC && lane < 48) st128(rs, WS_OC + (c_it * NG + g) * DH + lane * 4, z);
    }
    __builtin_amdgcn_s_setprio(0);
    lds_wait<1>(s_c + F_Q, HPG);
    u32x2 q[12];
    q_load(q, s_q, j);
    const u32x4* s_kv = reinterpret_cast<const u32x4*>(smem + OFF_REG + tw * REG) + lane;
    static_assert(NLP * 4 <= PPI && NTP <= HPG * PPI, "tail tiles: batch row 0 only");
#pragma unroll 1
    for (int e = 0; e < NLP * 2; ++e) {
      Tile tmp;
#pragma unroll
      for (int i = 0; i < 3; ++i) tmp.d[i] = s_kv[(e * 3 + i) * 64];
      score_put(tile_score(tmp, q), s_sc + ((e >> 1) * 4 + tw) * 16 + (e & 1) * 8 + r, j);
    }
#pragma unroll
    for (int e = 0; e < NRP * 2; ++e) {
      if ((NLP * 2 + e) * 2 == PPI) q_load(q, s_q + DH, j);
      score_put(tile_score(pk[e], q), s_sc + ((NLP + (e >> 1)) * 4 + tw) * 16 + (e & 1) * 8 + r, j);
      __builtin_amdgcn_sched_barrier(0);
    }
    asm volatile("s_waitcnt vmcnt(0) lgkmcnt(0)" ::: "memory");
#pragma unroll
    for (int it = 0; it * PPI < NTP; ++it) lds_add(s_c + C_SC + it, lane);
  }

  const int mr = lane & 15, qd = lane >> 4;
  unsigned char* reg = smem + OFF_REG + tw * REG;
  float* red = reinterpret_cast<float*>(smem + OFF_RED);
#define PERM_LO(hi_, lo_) __builtin_amdgcn_perm(hi_, lo_, 0x05040100u)
#define PERM_HI(hi_, lo_) __builtin_amdgcn_perm(hi_, lo_, 0x07060302u)
  int served = 0;
  auto done = [&](int n) {
    if (tw == 3 && served < min(n, NA)) {
      const int from = served;
      int ln = lane;
      asm volatile("" : "+v"(ln));
#pragma unroll 1
      for (; served < min(n, NA); ++served) {
        lds_wait<8>(s_c + C_PART + served, NMW);
        const f32x4 o = merged(served, ln < DH / 4 ? ln : 0);
        const float am = max_x16_32(row_max(fmaxf(fmaxf(fabsf(o[0]), fabsf(o[1])), fmaxf(fabsf(o[2]), fabsf(o[3])))));
        const float inv = am > 1e-30f ? 32767.0f / am : 0.f;
        const int q0 = __float2int_rn(o[0] * inv), q1 = __float2int_rn(o[1] * inv), q2 = __float2int_rn(o[2] * inv), q3 = __float2int_rn(o[3] * inv);
        if (ln < DH / 4) reinterpret_cast<u32x2*>(s_sc + served * (DH / 2))[ln] = u32x2{PERM_LO((unsigned)q1, (unsigned)q0), PERM_LO((unsigned)q3, (unsigned)q2)};
        const bool nan = __any((o[0] + o[1]) + (o[2] + o[3]) != (o[0] + o[1]) + (o[2] + o[3]));
        if (ln == 0) s_ml[served][2] = nan ? 0x7fc00000u : nz_bits(am * (1.0f / 32767.0f));
        LGKM0();
        if (ln == 0) s_c[F_FREE] = served + 1;
      }
#pragma unroll 1
      for (int c = from * 24 + ln; c < served * 24; c += 64) st128nt(rs, WS_PQ + xb * 5 * (DH / 2) + c * 4, reinterpret_cast<const u32x4*>(s_sc)[c]);
      VMCNT(0);
      if (ln < 8) st128(rs, WS_PML + xb * 64 + (from ? 32 : 0) + ln * 4, s_ml[ln]);
      if (ln == 0) __hip_atomic_fetch_add(s_c + F_DONE, (unsigned)(served - from), __ATOMIC_RELAXED, __HIP_MEMORY_SCOPE_WORKGROUP);
    }
    if (n > NA) lds_wait<1>(s_c + F_DONE, n); else lds_wait<8>(s_c + F_DONE, n);
  };
  auto done_all = [&]() {
#pragma unroll 1
    for (int n = 4; n <= NA; ++n) done(n);
  };
  const int lp = (lane < DH / 4 ? lane : 0) * 4;
  auto weight = [&](int it, int x0, int cnt) -> float {
    const int mlw = WS_PML + (x0 + (lane & (cnt - 1))) * 64 + (it < 4 ? 0 : 32) + it * 4;
    u32x4 ml;
#pragma unroll 1
    for (unsigned spin = 0; spin < SPIN_CAP; ++spin) {
      ml = ld128(rs, mlw);
      if (__all(min4(ml) != 0u)) break;
      __builtin_amdgcn_s_sleep(16);
    }
    const float mk = __uint_as_float(ml[0]);
    const float e = __builtin_amdgcn_exp2f(mk - max_x16_32(row_max(mk)));
    return e / (wave_sum(e * __uint_as_float(ml[1])) * ((float)cnt * (1.0f / 64.0f))) * __uint_as_float(ml[2]);
  };
  auto prep = [&]() {
    if (tw == 0) {
      const unsigned tok = __builtin_amdgcn_readfirstlane(r0) + 1u;
      const int c = lane & (GS - 1);
      unsigned echoed = 0u, qe = s_c[S_ECHO + c];
      bool first = true, released = false, sent = false;
#pragma unroll 1
      while ((int)(clk() - t0) < HST) {
        const unsigned qn = ld32(rs, qf + (GS + c) * 32);
        if (mm < NPROD && __any(qn != qe)) {
          if (lane < 32) st32(rs, qf + mm * 32 + lane, qn);
          qe = qn;
        }
        __builtin_amdgcn_s_sleep(64);
      }
      int slow = 0;
      unsigned fold = 0u;
      auto lag = [&]() {
        const unsigned v = __hip_atomic_load(s_c + S_MOVED + (lane & 7), __ATOMIC_RELAXED, __HIP_MEMORY_SCOPE_WORKGROUP);
        if (FOLD) fold += v;
        unsigned a = 0u;
#pragma unroll
        for (int k = FOLD ? 0 : 4; k < 8; ++k) a += __builtin_amdgcn_readlane(v, k);
        return a >> 2;
      };
      auto tell = [&]() { if (lane < 4) { s_c[S_SLOW + 2 * lane] = slow; s_c[S_SLOW + 2 * lane + 1] = fold; } };
      const unsigned behind = lag();
#pragma unroll 1
      for (unsigned spin = 0; spin < SPIN_CAP && !released; ++spin) {
        const unsigned qn = ld32(rs, qf + (GS + c) * 32);
        const u32x2 rv = ld64(rs, WS_HR + bid * 2);
        const unsigned nv = __builtin_amdgcn_readfirstlane(ld32(rs, WS_HN + bid));
        if (__builtin_amdgcn_readfirstlane(rv[0]) - tok <= NRD) {
          released = true;
          slow = __builtin_amdgcn_readfirstlane(rv[1]) >> 16;
          if (lane == 0) { s_c[S_FIRST] = min((t0 - rv[1]) & 0xffffu, 300u) + 1u; }
        } else {
          if (mm < NPROD && __any(qn != qe)) {
            if (lane < 32) st32(rs, qf + mm * 32 + lane, qn);
            qe = qn;
          }
          if (first || nv != echoed) {
            echoed = nv; first = false;
            if (lane < 8) st128(rs, WS_HA + bid * 32 + lane * 4, u32x4{echoed, tok, t0, behind});
          }
          if (bid == 0 && !sent) sent = leader_release(rs, lane, t0, 0u);
          __builtin_amdgcn_s_sleep(32);
        }
      }
      tell();
      if (lane == 0) s_c[F_REL] = 1u;
      if (lane < DH / 4) {
        u32x4 p[3][2];
#pragma unroll
        for (int e = 0; e < 3; ++e)
#pragma unroll
          for (int k = 0; k < 2; ++k) p[e][k] = ld128nt(rs, WS_QP + (((e ? 8 + e * 8 + (xb >> 5) : xb >> 4) * 2 + k) * NB + 1) * DH + lane * 4);
#pragma unroll
        for (int e = 0; e < 3; ++e) *reinterpret_cast<u32x4*>(&s_q[(1 + e) * DH + lane * 4]) = q_pack(p[e][0], p[e][1]);
      }
      LGKM0();
      if (lane == 0) s_c[F_Q2] = 3u;
      unsigned prev = slow;
#pragma unroll 1
      for (unsigned rd = 1; rd <= NRD; ++rd) {
        const int tr = RT0 + (int)(rd - 1) * RTS;
        until(t0, tr);
        const unsigned bh = lag();
        if (bh > RMIN && lane < 8) st128(rs, WS_HA + bid * 32 + lane * 4, u32x4{echoed + rd, tok, t0, bh});
        if (bid == 0) { until(t0, tr + 220); leader_release(rs, lane, t0, rd); }
        until(t0, tr + 500);
        const u32x2 rv = ld64(rs, WS_HR + bid * 2);
        if (__builtin_amdgcn_readfirstlane(rv[0]) != tok + rd) {
          prev = 0u;
          slow = slow > 0 ? max(slow - GB, 0) : slow - (rd <= PRD ? PRB : 0);
        } else {
          const unsigned ex = __builtin_amdgcn_readfirstlane(rv[1]) >> 16;
          slow += ex + min(ex, prev) * (rd == NRD ? FBL : FBP) / 100u;
          prev = ex;
        }
        tell();
      }
    }
    if (tw == 3) {
#pragma unroll 1
      for (int ik = 0; ik < NS; ++ik) {
        const int it = seg_of_k(ik), nv = (seg_beg(it + 1) - seg_beg(it)) >> 4;
        lds_wait<4>(s_c + C_SC + ik, ik == 0 ? NW : NMW);
        f32x4* sp = reinterpret_cast<f32x4*>(s_sc + seg_beg(it) * 16) + lane;
        f32x4 v[4];
#pragma unroll
        for (int k = 0; k < 4; ++k) v[k] = sp[(k & (nv - 1)) * 64];
        const f32x4 mx = __builtin_elementwise_max(__builtin_elementwise_max(v[0], v[1]), __builtin_elementwise_max(v[2], v[3]));
        const float m = max_x16_32(row_max(fmaxf(fmaxf(mx[0], mx[1]), fmaxf(mx[2], mx[3]))));
        float l = 0.f;
#pragma unroll
        for (int k = 0; k < 4; ++k) {
#pragma unroll
          for (int e = 0; e < 4; ++e) v[k][e] = __builtin_amdgcn_exp2f(v[k][e] - m);
          if (k < nv) {
            l += (v[k][0] + v[k][1]) + (v[k][2] + v[k][3]);
            sp[k * 64] = v[k];
          }
        }
        l = wave_sum(l);
        if (lane == 0) s_ml[it] = u32x4{nz_bits(m), nz_bits(l), 0x3f800000u, 0x3f800000u};
        LGKM0();
        if (lane == 0) s_c[F_P] = ik + 1;
      }
    }
  };
  auto pull = [&]() {
    typedef __attribute__((address_space(1))) const unsigned g_u32;
    const int id = (ro - 16) * 4 + tw;
    constexpr int RW = 160 - 32 * GE, LW = RW * 4 * 3 * 2 / 64;
    g_u32* p1[2];
#pragma unroll
    for (int i = 0; i < 2; ++i) {
      const int l = id * LW + min(i * 32 + (lane >> 1), max(LW - 1, 0)), m = l >= LW * 32 ? l - LW * 32 : l, rr = m / 3;
      p1[i] = (g_u32*)((l >= LW * 32 ? wu : wg) + (size_t)(rr / max(RW, 1) * 160 + 32 * GE + rr % max(RW, 1)) * FF + xl * 192 + (m % 3) * 64 + (lane & 1) * 32);
    }
    g_u32* p2 = (g_u32*)(wd + (size_t)(id * 24 + min(lane >> 1, 23)) * DM + xl * 64 + (lane & 1) * 32);
    asm volatile("" : "+v"(p1[0]), "+v"(p1[1]), "+v"(p2));
    if (GLEAD) lds_wait<1>(s_c + C_LAST, GCNT); else done(NS);
    const unsigned tp = clk() + PW + tw * PS;
    until(tp, 0);
    const unsigned v0 = LW > 0 ? p1[0][0] : 0u;
    MEMFENCE();
    until(tp, 4 * PS);
    const unsigned v1 = LW > 32 ? p1[1][0] : 0u;
    MEMFENCE();
    until(tp, DPW);
    const unsigned v2 = p2[0], v3 = p2[xl < 2 ? 256 : 0];
    MEMFENCE();
    return (v0 ^ v1) + (v2 ^ v3);
  };
  if (isW) {
    u32x4 bx[24];
    const char* p = reinterpret_cast<const char*>(wo + (size_t)((w_ks * 4 + tw) * DH + r) * DM + w_ct * 64 + j * 8);
#pragma unroll 1
    for (int i = 0; i < 24; ++i) { until(t0, tw == 0 ? W0START + i * (W0SPAN / 24) : W1START + i * (W1SPAN / 24)); glds16_nt(p, lds_base + i * 1024); p += 8 * DM * 2; VMCNT(WFD); }
    asm volatile("s_waitcnt vmcnt(0)" ::: "memory");
#pragma unroll
    for (int s = 0; s < 6; ++s) {
      u32x2 e[8];
#pragma unroll
      for (int k = 0; k < 8; ++k) e[k] = *reinterpret_cast<const u32x2*>(reg + (s * 32 + qd * 8 + k) * 128 + mr * 8);
#pragma unroll
      for (int k = 0; k < 4; ++k) {
        bx[s * 4 + 0][k] = PERM_LO(e[2 * k + 1][0], e[2 * k][0]); bx[s * 4 + 1][k] = PERM_HI(e[2 * k + 1][0], e[2 * k][0]);
        bx[s * 4 + 2][k] = PERM_LO(e[2 * k + 1][1], e[2 * k][1]); bx[s * 4 + 3][k] = PERM_HI(e[2 * k + 1][1], e[2 * k][1]);
      }
    }
#pragma unroll
    for (int s = 0; s < 24; s += 4) asm volatile("" : "+v"(bx[s]), "+v"(bx[s + 1]), "+v"(bx[s + 2]), "+v"(bx[s + 3]));
    LGKM0();
    prep();
    lds_wait<8>(s_c + F_REL, 1u);
    const int hh = w_ks * 4 + tw;
#pragma unroll 1
    for (int pass = 0; pass < 3; ++pass) {
      if (pass < 2) {
        done(pass ? NA : 4);
        pause(pass ? WDELAY2 : WDELAY2 + 300);
        const int b0 = pass * 2, b1 = pass + 1;
        u32x4 v0, v1;
#pragma unroll 1
        for (unsigned spin = 0; spin < SPIN_CAP; ++spin) {
          v0 = ld128(rs, WS_OC + (b0 * NH + hh) * DH + lp);
          v1 = ld128(rs, WS_OC + (b1 * NH + hh) * DH + lp);
          if (__all(min(min4(v0), min4(v1)) != 0u)) break;
          __builtin_amdgcn_s_sleep(16);
        }
        if (lane < DH / 4) {
          *reinterpret_cast<u32x2*>(reg + (b0 * 4) * AW_LD + lane * 8) = u32x2{v0[0], v0[1]};
          *reinterpret_cast<u32x2*>(reg + (b0 * 4 + 1) * AW_LD + lane * 8) = u32x2{v0[2], v0[3]};
          *reinterpret_cast<u32x2*>(reg + (b1 * 4) * AW_LD + lane * 8) = u32x2{v1[0], v1[1]};
          *reinterpret_cast<u32x2*>(reg + (b1 * 4 + 1) * AW_LD + lane * 8) = u32x2{v1[2], v1[3]};
        }
      } else {
        const float wk = weight(NS - 1, hh * GS, GS);
        float w[GS];
#pragma unroll
        for (int k = 0; k < GS; ++k) { w[k] = __uint_as_float(__builtin_amdgcn_readlane(__float_as_uint(wk), k)); asm volatile("" : "+s"(w[k])); }
        done(NS);
        __builtin_amdgcn_s_setprio(3);
        const int po = WS_PO + hh * GS * DH;
        u32x4 pv[GS];
#pragma unroll 1
        for (unsigned spin = 0; spin < SPIN_CAP; ++spin) {
#pragma unroll
          for (int k = 0; k < GS; ++k) pv[k] = __builtin_amdgcn_raw_buffer_load_b128(rs, lp * 4, (po + k * DH) * 4, AUX_SC1);
          unsigned mn = min4(pv[0]);
#pragma unroll
          for (int k = 1; k < GS; ++k) mn = min(mn, min4(pv[k]));
          if (__all(mn != 0u)) break;
          __builtin_amdgcn_s_sleep(1);
        }
        f32x4 o = w[0] * __builtin_bit_cast(f32x4, pv[0]), o2 = w[1] * __builtin_bit_cast(f32x4, pv[1]);
#pragma unroll
        for (int k = 2; k < GS; k += 2) { o += w[k] * __builtin_bit_cast(f32x4, pv[k]); o2 += w[k + 1] * __builtin_bit_cast(f32x4, pv[k + 1]); }
        u32x2 hi, lw;
        split4(o + o2, hi, lw);
        if (lane < DH / 4) {
          *reinterpret_cast<u32x2*>(reg + ((NB - 1) * 4) * AW_LD + lane * 8) = hi;
          *reinterpret_cast<u32x2*>(reg + ((NB - 1) * 4 + 1) * AW_LD + lane * 8) = lw;
        }
      }
      CFENCE();
      u32x4 a[6];
#pragma unroll
      for (int s = 0; s < 6; ++s) a[s] = *reinterpret_cast<const u32x4*>(reg + mr * AW_LD + s * 64 + qd * 16);
      __builtin_amdgcn_sched_barrier(0);
      f32x4 c[4];
#pragma unroll
      for (int t = 0; t < 4; ++t) c[t] = f32x4{0.f, 0.f, 0.f, 0.f};
#pragma unroll
      for (int s = 0; s < 6; ++s)
#pragma unroll
        for (int t = 0; t < 4; ++t) c[t] = mfma16(a[s], bx[s * 4 + t], c[t]);
      const bool mine = max(qd - 1, 0) == pass;
      f32x4* r4 = reinterpret_cast<f32x4*>(red);
      if (mine) r4[tw * 64 + lane] = f32x4{c[0][0] + c[0][1], c[1][0] + c[1][1], c[2][0] + c[2][1], c[3][0] + c[3][1]};
      const int k = arrive(s_c + C_W + pass);
      f32x4 sum = (r4[lane] + r4[64 + lane]) + (r4[128 + lane] + r4[192 + lane]);
      asm volatile("" : "+v"(sum));
      if (arrived(k) == 3) {
        if (mine) st128(rs, WS_SLAB + (w_ks * NB + qd) * DM + w_ct * 64 + mr * 4, u32x4{nz_bits(sum[0]), nz_bits(sum[1]), nz_bits(sum[2]), nz_bits(sum[3])});
      }
    }
  } else if (isG || isH) {
    f32x4* rst = reinterpret_cast<f32x4*>(red);
    const int hb = isH ? xl - 2 : NB - 1;
    uint2 wr4 = make_uint2(0u, 0u), xg = wr4;
    if (lane < 40) {
      wr4 = *reinterpret_cast<const uint2*>(wr + tw * 160 + lane * 4);
      xg = *reinterpret_cast<const uint2*>(x + row_of(hb) * DM + tw * 160 + lane * 4);
    }
    const int eb = mr >> 2;
    const float cq = bf_lo(*reinterpret_cast<const unsigned*>(x + row_of(eb) * DM));
    const float wsum = wave_sum((bf_lo(wr4.x) + bf_hi(wr4.x)) + (bf_lo(wr4.y) + bf_hi(wr4.y)));
    f32x4 xc = f32x4{bf_lo(xg.x), bf_hi(xg.x), bf_lo(xg.y), bf_hi(xg.y)} - __uint_as_float(__builtin_amdgcn_readlane(__float_as_uint(cq), hb * 4));
    asm volatile("" : "+v"(xc));
    typedef __attribute__((address_space(1))) const u32x4 g_u32x4;
    g_u32x4* wp[10];
    wp[0] = (g_u32x4*)(((mr & 4) ? wu : wg) + (size_t)(tw * 160 + qd * 8 + (mr >> 3) * 4) * FF + g_cg * 32 + (mr & 3) * 8);
    u32x4 e[20], bx[20];
    auto frag = [&](int s) __attribute__((always_inline)) {
      unsigned w[8][2];
#pragma unroll
      for (int i = 0; i < 4; ++i)
#pragma unroll
        for (int d = 0; d < 2; ++d) {
          w[i][d] = __builtin_amdgcn_update_dpp(e[s * 4 + i][d], e[s * 4 + i][2 + d], DPP_ROR8, 0xf, 0xc, false);
          w[4 + i][d] = __builtin_amdgcn_update_dpp(e[s * 4 + i][2 + d], e[s * 4 + i][d], DPP_ROR8, 0xf, 0x3, false);
        }
#pragma unroll
      for (int k = 0; k < 4; ++k) {
        bx[s * 4 + 0][k] = PERM_LO(w[2 * k + 1][0], w[2 * k][0]); bx[s * 4 + 1][k] = PERM_HI(w[2 * k + 1][0], w[2 * k][0]);
        bx[s * 4 + 2][k] = PERM_LO(w[2 * k + 1][1], w[2 * k][1]); bx[s * 4 + 3][k] = PERM_HI(w[2 * k + 1][1], w[2 * k][1]);
      }
      PIN4(bx, s * 4);
    };
    if (isG) {
#pragma unroll
      for (int i = 0; i < 4 * GE; ++i) {
        until(t0, tw == 0 ? W0START + i * (W0SPAN / max(4 * GE, 1)) : W1START + i * (W1SPAN / max(4 * GE, 1)));
        e[i] = __builtin_nontemporal_load(wp[0] + ((i >> 2) * 32 + (i & 3)) * (FF / 8));
        MEMFENCE();
      }
#pragma unroll
      for (int s = 0; s < GE; ++s) frag(s);
    }
    prep();
    lds_wait<8>(s_c + F_REL, 1u);
    auto hrow = [&](bool fast) -> f32x4 {
      f32x4 hv = {0.f, 0.f, 0.f, 0.f};
      if (lane < 40) {
        const int off = (WS_SLAB + hb * DM + tw * 160 + lane * 4) * 4;
        u32x4 sl[KS];
#pragma unroll 1
        for (unsigned spin = 0; spin < SPIN_CAP; ++spin) {
#pragma unroll
          for (int ks = 0; ks < KS; ++ks) sl[ks] = __builtin_amdgcn_raw_buffer_load_b128(rs, off, ks * (NB * DM * 4), AUX_SC1);
          unsigned mn = min4(sl[0]);
#pragma unroll
          for (int ks = 1; ks < KS; ++ks) mn = min(mn, min4(sl[ks]));
          if (!__any(mn == 0u)) break;
          if (fast) __builtin_amdgcn_s_sleep(1); else __builtin_amdgcn_s_sleep(16);
        }
#define SL(k) __builtin_bit_cast(f32x4, sl[k])
        hv = xc + (((SL(0) + SL(1)) + (SL(2) + SL(3))) + ((SL(4) + SL(5)) + (SL(6) + SL(7))));
#undef SL
      }
      return hv;
    };
    auto sums = [&](f32x4 hv) -> f32x4 {
      const float s1 = wave_sum((hv[0] + hv[1]) + (hv[2] + hv[3]));
      const float s2 = wave_sum((hv[0] * hv[0] + hv[1] * hv[1]) + (hv[2] * hv[2] + hv[3] * hv[3]));
      const float dot = wave_sum((hv[0] * bf_lo(wr4.x) + hv[1] * bf_hi(wr4.x)) + (hv[2] * bf_lo(wr4.y) + hv[3] * bf_hi(wr4.y)));
      return f32x4{s1, s2, dot, wsum};
    };
    if (isH) {
      if (tw == 3 || hb == 2) done_all(); else done(4);
      if (tw != 3 || hb == 2) pause(hb < 2 ? HDELAY : HDELAY2);
      const f32x4 hv = hrow(false), sm = sums(hv);
      u32x4 v = split8(hv, (lane & 1) != 0);
      if (lane >= 40) v = u32x4{nz_bits(sm[0]), nz_bits(sm[1]), nz_bits(sm[2]), nz_bits(sm[3])};
      if (lane < 48) st128(rs, WS_H + (hb * 4 + tw) * 192 + lane * 4, v);
      done_all();
      const unsigned pl = pull();
      asm volatile("" :: "v"(pl));
      return;
    }
    done_all();
#pragma unroll
    for (int i = 2 * GE; i < 10; ++i) { wp[i] = wp[0] + ((i >> 1) * 32 + (i & 1) * 2) * (FF / 8); asm volatile("" : "+v"(wp[i])); }
    done(NS);
    if (GW) until(clk(), GW);
#pragma unroll
    for (int i = 4 * GE; i < 20; ++i) e[i] = wp[i >> 1][(i & 1) * (FF / 8)];
    u32x4 a[5], sm;
    const int hoff = (WS_H + (min(mr >> 2, NB - 2) * 4 + tw) * 192 + qd * 8 + (mr & 1) * 4) * 4;
#pragma unroll
    for (int s = 0; s < 5; ++s) a[s] = __builtin_amdgcn_raw_buffer_load_b128(rs, hoff + s * 128, 0, AUX_SC1);
    sm = ld128(rs, WS_H + (min(lane, NB - 2) * 4 + tw) * 192 + 160);
    MEMFENCE();
#pragma unroll
    for (int s = GE; s < 5; ++s) frag(s);
    {
#pragma unroll 1
      for (unsigned spin = 0; spin < SPIN_CAP; ++spin) {
        unsigned mn = min4(sm);
#pragma unroll
        for (int s = 0; s < 5; ++s) mn = min(mn, min4(a[s]));
        if (__all(mn != 0u)) break;
        __builtin_amdgcn_s_sleep(2);
#pragma unroll
        for (int s = 0; s < 5; ++s) a[s] = __builtin_amdgcn_raw_buffer_load_b128(rs, hoff + s * 128, 0, AUX_SC1);
        sm = ld128(rs, WS_H + (min(lane, NB - 2) * 4 + tw) * 192 + 160);
      }
      if (lane < NB - 1) rst[lane * 4 + tw] = __builtin_bit_cast(f32x4, sm);
#pragma unroll
      for (int s = 0; s < 5; ++s) {
        if (mr & 2) a[s] = u32x4{0x3f803f80u, 0x3f803f80u, 0x3f803f80u, 0x3f803f80u};
        asm volatile("" : "+v"(a[s]));
      }
    }
    const f32x4 hv = hrow(true);
    if (lane < 40) {
      u32x2 hi, lw;
      split4(hv, hi, lw);
      *reinterpret_cast<u32x2*>(reg + lane * 8) = hi;
      *reinterpret_cast<u32x2*>(reg + AG_LD + lane * 8) = lw;
    }
    CFENCE();
    if ((mr & 14) == 12) {
#pragma unroll
      for (int s = 0; s < 5; ++s) a[s] = *reinterpret_cast<const u32x4*>(reg + (mr & 1) * AG_LD + s * 64 + qd * 16);
    }
    __builtin_amdgcn_sched_barrier(0);
    f32x4 c[4];
#pragma unroll
    for (int t = 0; t < 4; ++t) c[t] = f32x4{0.f, 0.f, 0.f, 0.f};
#pragma unroll
    for (int s = 0; s < 5; ++s)
#pragma unroll
      for (int t = 0; t < 4; ++t) c[t] = mfma16(a[s], bx[s * 4 + t], c[t]);
    { const f32x4 s3 = sums(hv); if (lane == 0) rst[(NB - 1) * 4 + tw] = s3; }
    const int slot = qd * 16 + (mr & 7) * 2 + (mr >> 3);
    *reinterpret_cast<f32x4*>(reg + 4096 + slot * 16) = f32x4{c[0][0] + c[0][1], c[1][0] + c[1][1], c[0][2], c[1][2]};
    *reinterpret_cast<f32x4*>(reg + 4096 + 1152 + slot * 16) = f32x4{c[2][0] + c[2][1], c[3][0] + c[3][1], c[2][2], c[3][2]};
    const int k = arrive(s_c + C_T);
    f32x4 ts[4], pg[4], pu[4];
#pragma unroll
    for (int w = 0; w < 4; ++w) {
      const unsigned char* src = smem + OFF_REG + w * REG + 4096 + (lane & 1) * 1152 + (eb * 16 + qd * 2 + ((lane >> 1) & 1)) * 16;
      ts[w] = rst[eb * 4 + w];
      pg[w] = *reinterpret_cast<const f32x4*>(src); pu[w] = *reinterpret_cast<const f32x4*>(src + 128);
    }
    PIN4(ts, 0); PIN4(pg, 0); PIN4(pu, 0);
    if (arrived(k) == 3) {
      const f32x4 t = (ts[0] + ts[1]) + (ts[2] + ts[3]), g4 = (pg[0] + pg[1]) + (pg[2] + pg[3]), u4 = (pu[0] + pu[1]) + (pu[2] + pu[3]);
      const float d = t[0] * (-1.0f / DM);
      const float rstd = __builtin_amdgcn_rsqf(fmaxf(t[1] + d * t[0], 0.f) * (1.0f / DM) + LN_EPS);
      const float g0 = rstd * (g4[0] + d * g4[2]), g1 = rstd * (g4[1] + d * g4[3]);
      const float a0 = g0 * __builtin_amdgcn_rcpf(1.0f + __expf(-g0)) * (rstd * (u4[0] + d * u4[2]));
      const float a1 = g1 * __builtin_amdgcn_rcpf(1.0f + __expf(-g1)) * (rstd * (u4[1] + d * u4[3]));
      const __hip_bfloat162 a2 = __halves2bfloat162(__float2bfloat16(a0), __float2bfloat16(a1));
      const float aw = __uint_as_float(max(*reinterpret_cast<const unsigned*>(&a2), 1u));
      const u32x4 av = {__float_as_uint(aw), __float_as_uint(dpp<0x55>(aw)), __float_as_uint(dpp<0xAA>(aw)), __float_as_uint(dpp<0xFF>(aw))};
      const float rr = rstd * (t[2] + d * t[3]);
      const u32x4 sv = {nz_bits(cq - d), nz_bits(rstd), nz_bits(__builtin_amdgcn_rcpf(1.0f + __expf(-rr))), 0x3f800000u};
      const bool stat = g_cg == 0 && (lane & 51) == 1;
      if ((lane & 3) == 0 || stat) st128(rs, stat ? WS_ST + eb * 4 : WS_ACT + g_cg * 64 + lane, stat ? sv : av);
    }
  } else if (isD) {
    const uint2 xd = *reinterpret_cast<const uint2*>(x + row_of(tw) * DM + d_ct * 32 + j * 4);
    const __amdgpu_buffer_rsrc_t rd = __builtin_amdgcn_make_buffer_rsrc(const_cast<bf16*>(wd), 0, FF * DM * 2, 0x00020000);
    if (tw == 1) { lds_wait<4>(s_c + F_P, NS); if (lane == 0) s_c[44] = clk(); lds_wait<4>(s_c + C_PART + 3, NMW); if (lane == 0) s_c[45] = clk(); }
    if (tw == 2) { lds_wait<4>(s_c + C_SC + 1, NMW); if (lane == 0) s_c[47] = clk(); }
    prep();
    lds_wait<8>(s_c + F_REL, 1u);
    done_all();
    if (tw == 1) { lds_wait<1>(s_c + C_LAST, 4); if (lane == 0) s_c[46] = clk(); }
    const unsigned pl = pull();
    done(NS);
    asm volatile("" :: "v"(pl));
    const unsigned te = clk();
    if (DW) until(te, DW);
    u32x4 bd[24];
    {
      const int off = ((tw * 384 + (mr >> 3) * 32 + qd * 8 + (mr & 4)) * DM + d_ct * 32 + (mr & 3) * 8) * 2;
      u32x4 e[24];
#pragma unroll
      for (int i = 0; i < 24; ++i) e[i] = __builtin_amdgcn_raw_buffer_load_b128(rd, off + (i & 3) * (DM * 2), (i >> 2) * 64 * (DM * 2), 0);
      MEMFENCE();
#pragma unroll
      for (int s = 0; s < 6; ++s) {
        unsigned w[8][2];
#pragma unroll
        for (int i = 0; i < 4; ++i)
#pragma unroll
          for (int d = 0; d < 2; ++d) {
            w[i][d] = __builtin_amdgcn_update_dpp(e[s * 4 + i][d], e[s * 4 + i][2 + d], 0x114, 0xf, 0xa, false);
            w[4 + i][d] = __builtin_amdgcn_update_dpp(e[s * 4 + i][2 + d], e[s * 4 + i][d], 0x104, 0xf, 0x5, false);
          }
#pragma unroll
        for (int k = 0; k < 4; ++k) {
          bd[s * 4 + 0][k] = PERM_LO(w[2 * k + 1][0], w[2 * k][0]); bd[s * 4 + 1][k] = PERM_HI(w[2 * k + 1][0], w[2 * k][0]);
          bd[s * 4 + 2][k] = PERM_LO(w[2 * k + 1][1], w[2 * k][1]); bd[s * 4 + 3][k] = PERM_HI(w[2 * k + 1][1], w[2 * k][1]);
        }
        PIN4(bd, s * 4);
      }
    }
    until(te, DLATE);
    {
      u32x4 sl;
      unsigned spin = 0;
#pragma unroll 1
      for (; spin < SPIN_CAP; ++spin) {
        sl = ld128(rs, WS_SLAB + (r * NB + tw) * DM + d_ct * 32 + j * 4);
        if (__all(min4(sl) != 0u)) break;
        __builtin_amdgcn_s_sleep(2);
      }
      if (tw == NB - 1 && lane == 0) s_c[C_W + 1] = clk() + (spin ? DLATE2 : 0);
      f32x4 hv = {bf_lo(xd.x), bf_hi(xd.x), bf_lo(xd.y), bf_hi(xd.y)};
#pragma unroll
      for (int i = 0; i < 4; ++i) hv[i] += sum_r(__uint_as_float(sl[i]));
      if (lane < 8) reinterpret_cast<f32x4*>(red + 512 + tw * 32)[lane] = hv;
      LGKM0();
      lds_add(s_c + C_W, lane);
      lds_wait<1>(s_c + C_W, 4u);
    }
    const int col = (mr & 3) * 8 + (mr & 4) + (mr >> 3) * 2;
    f32x2 hv = *reinterpret_cast<const f32x2*>(red + 512 + qd * 32 + col);
    asm volatile("" : "+v"(hv));
    u32x4 a[6], st;
#pragma unroll
    for (int s = 0; s < 6; ++s) a[s] = u32x4{1u, 1u, 1u, 1u};
    const int aoff = (WS_ACT + tw * 12 * 64 + ((mr >> 1) & 1) * 64 + qd * 16 + (mr >> 2) * 4) * 4;
    until(lds_get(s_c + C_W + 1), 0);
#pragma unroll 1
    for (unsigned spin = 0; spin < SPIN_CAP; ++spin) {
      st = ld128(rs, WS_ST + qd * 4);
      if ((mr & 1) == 0) {
#pragma unroll
        for (int s = 0; s < 6; ++s) a[s] = __builtin_amdgcn_raw_buffer_load_b128(rs, aoff + s * 512, 0, AUX_SC1);
      }
      unsigned mn = min4(st);
#pragma unroll
      for (int s = 0; s < 6; ++s) mn = min(mn, min4(a[s]));
      if (__all(mn != 0u)) break;
      __builtin_amdgcn_s_sleep(1);
    }
    f32x4 c[4];
#pragma unroll
    for (int t = 0; t < 4; ++t) c[t] = f32x4{0.f, 0.f, 0.f, 0.f};
#pragma unroll
    for (int s = 0; s < 6; ++s)
#pragma unroll
      for (int t = 0; t < 4; ++t) c[t] = mfma16(a[s], bd[s * 4 + t], c[t]);
    const f32x4 sf = __builtin_bit_cast(f32x4, st);
    hv += (hv - sf[0]) * sf[1];
    const bool up = mr >= 8;
    const float k0 = up ? c[2][2] : c[0][0], k1 = up ? c[3][2] : c[1][0], o0 = up ? c[0][2] : c[2][0], o1 = up ? c[1][2] : c[3][0];
    f32x2* r2 = reinterpret_cast<f32x2*>(red);
    r2[tw * 64 + lane] = f32x2{k0 + dpp<DPP_ROR8>(o0), k1 + dpp<DPP_ROR8>(o1)};
    const int k = arrive(s_c + C_T);
    f32x2 y = hv + ((r2[lane] + r2[64 + lane]) + (r2[128 + lane] + r2[192 + lane])) * sf[2];
    asm volatile("" : "+v"(y));
    if (arrived(k) == 3) {
      if (d_ct == 0) {
        auto qz = [&](unsigned tt, int base) { return min(max(((int)(tt - t0) - base) / 50, 0), 7); };
        const int code = ((qz(clk(), 11350) * 8 + qz(lds_get(s_c + 44), 5500)) * 8 + qz(lds_get(s_c + 45), 8250)) * 8 + qz(lds_get(s_c + 46), 10750);
        until(t0, 15000 + code * 40);
      }
      const __hip_bfloat162 o2 = __halves2bfloat162(__float2bfloat16(y[0]), __float2bfloat16(y[1]));
      __hip_atomic_store(reinterpret_cast<unsigned*>(out + row_of(qd) * DM + d_ct * 32 + col), *reinterpret_cast<const unsigned*>(&o2), __ATOMIC_RELAXED, __HIP_MEMORY_SCOPE_AGENT);
    }
  } else {
    prep();
    if (isC && tw == 1) {
      lds_wait<8>(s_c + F_REL, 1u);
      const int cs = c_it != 1 ? c_it * 2 : max(g >> 3, 1), cnt = c_it != 1 ? 8 : (g < 16 ? 16 : 32), x0 = c_it != 1 ? g * 8 : (g < 16 ? g * 16 : (g & 7) * 32);
      done(c_it < 2 ? 4 : NA);
      pause(WDELAY);
      const float wk = weight(cs, x0, cnt);
      f32x4 o = {0.f, 0.f, 0.f, 0.f};
#pragma unroll 1
      for (int k0 = 0; k0 < cnt; k0 += 16) {
        u32x2 pv[16];
#pragma unroll
        for (int k = 0; k < 16; ++k) pv[k] = __builtin_amdgcn_raw_buffer_load_b64(rs, lp * 2, (WS_PQ + ((x0 + ((k0 + k) & (cnt - 1))) * 5 + cs) * (DH / 2)) * 4, AUX_EARLY);
#pragma unroll
        for (int k = 0; k < 16; ++k) {
          const float w = k0 + k < cnt ? __uint_as_float(__builtin_amdgcn_readlane(__float_as_uint(wk), k0 + k)) : 0.f;
          o += w * f32x4{(float)(short)pv[k][0], (float)((int)pv[k][0] >> 16), (float)(short)pv[k][1], (float)((int)pv[k][1] >> 16)};
        }
      }
      u32x2 hi, lw;
      split4(o, hi, lw);
      if (lane < DH / 4) st128(rs, WS_OC + (c_it * NG + g) * DH + lane * 4, u32x4{max(hi[0], 1u), max(hi[1], 1u), max(lw[0], 1u), max(lw[1], 1u)});
    }
    done_all();
    const unsigned pl = pull();
    asm volatile("" :: "v"(pl));
  }
  if (tw == 3) done_all();
}

extern "C" void kernel_launch(void* const* d_in, const int* in_sizes, int n_in, void* d_out, int out_size, void* d_ws,
                              size_t ws_size, hipStream_t stream) {
  const bf16* x = (const bf16*)d_in[0];
  const bf16* kc = (const bf16*)d_in[1];
  const bf16* vc = (const bf16*)d_in[2];
  const bf16* wq = (const bf16*)d_in[3];
  const bf16* wo = (const bf16*)d_in[4];
  const bf16* wr = (const bf16*)d_in[6];
  const bf16* wg = (const bf16*)d_in[7];
  const bf16* wu = (const bf16*)d_in[8];
  const bf16* wd = (const bf16*)d_in[9];
  fused_kernel<<<NWG, NW * 64, 0, stream>>>(x, kc, vc, wq, wo, wr, wg, wu, wd, (float*)d_ws, (bf16*)d_out);
}
```

```cpp
#include <hip/hip_runtime.h>
#include <hip/hip_bf16.h>
#include <stdint.h>

typedef __hip_bfloat16 bf16;
using u32x2 = __attribute__((ext_vector_type(2))) unsigned;
using u32x4 = __attribute__((ext_vector_type(4))) unsigned;
using f32x4 = __attribute__((ext_vector_type(4))) float;
using f32x2 = __attribute__((ext_vector_type(2))) float;
using bf16x8 = __attribute__((ext_vector_type(8))) __bf16;

constexpr int NB = 4, DM = 640, NH = 32, SK = 8192, DH = 192, HD = NH * DH, FF = 1536;
constexpr float LN_EPS = 1e-5f;
constexpr float QSCALE = 0.07216878364870322f * 1.4426950408889634f;

constexpr int NWG = 256;
constexpr int GS = 8, NG = NWG / GS, NI = GS / 2;
constexpr int HPG = NI / NB;
constexpr int PPI = 512 / GS;
constexpr int NPAIR = NI * PPI;

constexpr int TAUK = 1320, TAUV = 1332;
constexpr int TSTART = 140;
constexpr int WGAP = 165, WP0 = 64, WPN = 176;
constexpr int VGAP = 50;
constexpr int DEEP1 = 100, DEEP2 = 200, SDEEP = 0, COARSE = 1, LAGOFF = 20, ENDT = 12, ENDS = 2, PHASE = 0;
constexpr int SPRINT = 0;
constexpr int JUMP = 145;
constexpr int HST = 2800, RT0 = 3900, RTS = 1000, RA1 = 220, RA2 = 500, LEADER = 24, WL = 0;
constexpr unsigned NRD = 5, RMIN = 30, FBP = 60, FBL = 150;
constexpr int W0START = 1700, W0SPAN = 1000, W1START = 2700, W1SPAN = 2600;
constexpr int ROT = 16, ROWS = 0xC9;
constexpr int FBM = 100, FBX = 15, FB0 = 170, FB1 = 10, FBS = 10;
constexpr int FBD = 10, HOLD = 4000, GB = 24, GBS = 2, GBX = 60, GBF = 12;
constexpr unsigned PRD = 3; constexpr int PRB = 24;
constexpr int TLEAD = 40, PFD = 6, WFD = 3;
constexpr int QWAIT = 750, QBACK = 100;
constexpr int WDELAY = 100, WDELAY2 = 460, DLATE = 100, DLATE2 = 85;
constexpr int CPF = 1, CPT = 0, CODE = 24 * 64 * 48;
constexpr int GLEAD = 1, GCNT = 4, DP = 0, DPW = 60, GE = 0, PW = 30, PS = 0, HDELAY = 1150, HDELAY2 = 780, GW = 0, DW = 0;

constexpr int KS = 8;
constexpr int WS_PML = 0;
constexpr int WS_PO = WS_PML + NWG * 64;
constexpr int WS_PQ = WS_PO + NWG * DH;
constexpr int WS_OC = WS_PQ + NWG * 5 * (DH / 2);
constexpr int WS_SLAB = WS_OC + (NB - 1) * NH * DH;
constexpr int WS_ACT = WS_SLAB + KS * NB * DM;
constexpr int WS_H = WS_ACT + NB * FF / 2;
constexpr int WS_ST = WS_H + (NB - 1) * 4 * 192;
constexpr int WS_HN = WS_ST + 32;
constexpr int WS_HR = WS_HN + NWG;
constexpr int WS_HA = WS_HR + 2 * NWG;
constexpr int QFS = 2 * GS * 32;
constexpr int WS_QF = WS_HA + 32 * NWG;
constexpr int WS_QP = WS_QF + NG * QFS;
constexpr int WS_END = WS_QP + NH * 2 * NB * DH;
constexpr int NPROD = HPG * 6;
static_assert(WS_PO % 32 == 0 && WS_SLAB % 32 == 0 && WS_ACT % 32 == 0 && WS_ST % 32 == 0 && WS_HN % 32 == 0 && WS_QF % 32 == 0 && WS_QP % 32 == 0, "line alignment");


__device__ __forceinline__ int row_of(int b) { return (ROWS >> (2 * b)) & 3; }
__device__ __forceinline__ float bf_lo(unsigned u) { return __uint_as_float(u << 16); }
__device__ __forceinline__ float bf_hi(unsigned u) { return __uint_as_float(u & 0xffff0000u); }
__device__ __forceinline__ f32x2 bf_pair(unsigned u) { return f32x2{bf_lo(u), bf_hi(u)}; }
__device__ __forceinline__ f32x2 fma2(float a, f32x2 b, f32x2 c) { return __builtin_elementwise_fma(f32x2{a, a}, b, c); }
__device__ __forceinline__ f32x2 fma2(f32x2 a, f32x2 b, f32x2 c) { return __builtin_elementwise_fma(a, b, c); }

template <int CTRL> __device__ __forceinline__ float dpp(float x) {
  return __builtin_bit_cast(float, __builtin_amdgcn_mov_dpp(__builtin_bit_cast(int, x), CTRL, 0xf, 0xf, true));
}
constexpr int DPP_XOR1 = 0xB1, DPP_XOR2 = 0x4E, DPP_HMIRROR = 0x141, DPP_MIRROR = 0x140, DPP_ROR8 = 0x128;

__device__ __forceinline__ float sum_x16_32(float x) {
  auto s = __builtin_amdgcn_permlane16_swap(__float_as_uint(x), __float_as_uint(x), false, false);
  x = __uint_as_float(s[0]) + __uint_as_float(s[1]);
  auto t = __builtin_amdgcn_permlane32_swap(__float_as_uint(x), __float_as_uint(x), false, false);
  return __uint_as_float(t[0]) + __uint_as_float(t[1]);
}
__device__ __forceinline__ float max_x16_32(float x) {
  auto s = __builtin_amdgcn_permlane16_swap(__float_as_uint(x), __float_as_uint(x), false, false);
  x = fmaxf(__uint_as_float(s[0]), __uint_as_float(s[1]));
  auto t = __builtin_amdgcn_permlane32_swap(__float_as_uint(x), __float_as_uint(x), false, false);
  return fmaxf(__uint_as_float(t[0]), __uint_as_float(t[1]));
}
__device__ __forceinline__ float tsum32(float a, float b) {
  auto t = __builtin_amdgcn_permlane32_swap(__float_as_uint(a), __float_as_uint(b), false, false);
  return __uint_as_float(t[0]) + __uint_as_float(t[1]);
}
__device__ __forceinline__ float tsum16(float a, float b) {
  auto t = __builtin_amdgcn_permlane16_swap(__float_as_uint(a), __float_as_uint(b), false, false);
  return __uint_as_float(t[0]) + __uint_as_float(t[1]);
}
__device__ __forceinline__ float sum_r(float v) { v += dpp<DPP_ROR8>(v); return sum_x16_32(v); }
__device__ __forceinline__ float row_sum(float v) {
  v += dpp<DPP_XOR1>(v); v += dpp<DPP_XOR2>(v); v += dpp<DPP_HMIRROR>(v);
  return v + dpp<DPP_MIRROR>(v);
}
__device__ __forceinline__ float row_max(float v) {
  v = fmaxf(v, dpp<DPP_XOR1>(v)); v = fmaxf(v, dpp<DPP_XOR2>(v)); v = fmaxf(v, dpp<DPP_HMIRROR>(v));
  return fmaxf(v, dpp<DPP_MIRROR>(v));
}
__device__ __forceinline__ float wave_sum(float v) {
  v += dpp<DPP_XOR1>(v); v += dpp<DPP_XOR2>(v); v += dpp<DPP_HMIRROR>(v);
  return sum_r(v);
}

__device__ __forceinline__ u32x4 ld_nt(const char* p) { return __builtin_nontemporal_load(reinterpret_cast<const u32x4*>(p)); }

struct Tile { u32x4 d[3]; };

#define PIN4(a, o) asm volatile("" : "+v"((a)[(o) + 0]), "+v"((a)[(o) + 1]), "+v"((a)[(o) + 2]), "+v"((a)[(o) + 3]))
#define MEMFENCE() do { asm volatile("" ::: "memory"); __builtin_amdgcn_sched_barrier(0); } while (0)

__device__ __forceinline__ void glds16_nt(const void* gsrc, unsigned lds_dst) {
  unsigned keep;
  asm volatile("s_mov_b32 %0, m0\n\ts_mov_b32 m0, %2\n\ts_nop 0\n\tglobal_load_lds_dwordx4 %1, off nt\n\ts_mov_b32 m0, %0"
               : "=&s"(keep) : "v"(gsrc), "s"(lds_dst) : "memory");
}

using bf16x2 = __attribute__((ext_vector_type(2))) __bf16;
__device__ __forceinline__ float dot2(unsigned a, unsigned b, float c) { return __builtin_amdgcn_fdot2_f32_bf16(__builtin_bit_cast(bf16x2, a), __builtin_bit_cast(bf16x2, b), c, false); }
__device__ __forceinline__ float tile_score(const Tile& k, const u32x2 (&q)[12]) {
  float s0 = 0.f, s1 = 0.f;
#pragma unroll
  for (int i = 0; i < 3; ++i) {
    u32x4 v = k.d[i];
    asm("" : "+v"(v));
#pragma unroll
    for (int d = 0; d < 4; ++d) { s0 = dot2(v[d], q[i * 4 + d][0], s0); s1 = dot2(v[d], q[i * 4 + d][1], s1); }
  }
  return s0 + s1;
}
__device__ __forceinline__ void pair_put(float a, float b, float* dst, int j) {
  float s = (j < 4 ? a : b) + dpp<DPP_HMIRROR>(j < 4 ? b : a);
  s += dpp<DPP_XOR1>(s);
  s += dpp<DPP_XOR2>(s);
  if ((j & 3) == 0) dst[j * 2] = s;
}
__device__ __forceinline__ void score_put(float s, float* dst, int j) {
  s += dpp<DPP_XOR1>(s);
  s += dpp<DPP_XOR2>(s);
  s += dpp<DPP_HMIRROR>(s);
  if (j == 0) *dst = s;
}
__device__ __forceinline__ void tile_load(Tile& t, const char* base, unsigned off, int half) {
#pragma unroll
  for (int i = 0; i < 3; ++i) t.d[i] = ld_nt(base + off + half * 3072 + i * 128);
}
__device__ __forceinline__ void pair_issue(Tile& ta, Tile& tb, const char* base, unsigned off, int deep) {
  deep = __builtin_amdgcn_readfirstlane(deep);
#define WAIT_IF(level, n) asm volatile("s_cmp_lg_u32 %0, " #level "\n\ts_cbranch_scc1 .Lpi%=\n\ts_waitcnt vmcnt(" #n ")\n.Lpi%=:" :: "s"(deep) : "scc", "memory")
  const char* p = base + off;
  if constexpr (COARSE) {
    WAIT_IF(0, 3);
    ta.d[0] = ld_nt(p); ta.d[1] = ld_nt(p + 128); ta.d[2] = ld_nt(p + 256); MEMFENCE();
    WAIT_IF(0, 3); WAIT_IF(1, 6);
    tb.d[0] = ld_nt(p + 3072); tb.d[1] = ld_nt(p + 3200); tb.d[2] = ld_nt(p + 3328); MEMFENCE();
  } else {
    WAIT_IF(0, 5); ta.d[0] = ld_nt(p); MEMFENCE();
    WAIT_IF(0, 5); ta.d[1] = ld_nt(p + 128); MEMFENCE();
    WAIT_IF(0, 5); ta.d[2] = ld_nt(p + 256); MEMFENCE();
    WAIT_IF(1, 6);
    WAIT_IF(0, 5); tb.d[0] = ld_nt(p + 3072); MEMFENCE();
    WAIT_IF(0, 5); tb.d[1] = ld_nt(p + 3200); MEMFENCE();
    WAIT_IF(0, 5); tb.d[2] = ld_nt(p + 3328); MEMFENCE();
  }
#undef WAIT_IF
}
__device__ __forceinline__ void pv_last(float p, Tile& v, f32x2 (&acc)[12]) {
#pragma unroll
  for (int i = 0; i < 3; ++i) {
    asm volatile("" : "+v"(v.d[i]), "+v"(p));
#pragma unroll
    for (int d = 0; d < 4; ++d) acc[i * 4 + d] = fma2(p, bf_pair(v.d[i][d]), acc[i * 4 + d]);
    PIN4(acc, i * 4);
  }
}
__device__ __forceinline__ void q_load(u32x2 (&q)[12], const float* src, int j) {
#pragma unroll
  for (int i = 0; i < 6; ++i) {
    const u32x4 a = *reinterpret_cast<const u32x4*>(src + (i >> 1) * 64 + j * 8 + (i & 1) * 4);
    q[i * 2] = u32x2{a[0], a[1]}; q[i * 2 + 1] = u32x2{a[2], a[3]};
  }
}
#define VMCNT(n) asm volatile("s_waitcnt vmcnt(%0)" :: "n"(n) : "memory")

constexpr int AUX_SC1 = 16, AUX_EARLY = 18;
constexpr unsigned SPIN_CAP = 1u << 17;
__device__ __forceinline__ unsigned nz_bits(float v) { return max(__float_as_uint(v), 1u); }
__device__ __forceinline__ bool all_nz(u32x4 v) { return v[0] != 0u && v[1] != 0u && v[2] != 0u && v[3] != 0u; }
__device__ __forceinline__ unsigned min4(u32x4 v) { return min(min(v[0], v[1]), min(v[2], v[3])); }
__device__ __forceinline__ unsigned ld32(__amdgpu_buffer_rsrc_t rs, int word) { return __builtin_amdgcn_raw_buffer_load_b32(rs, word * 4, 0, AUX_SC1); }
__device__ __forceinline__ u32x2 ld64(__amdgpu_buffer_rsrc_t rs, int word) { return __builtin_amdgcn_raw_buffer_load_b64(rs, word * 4, 0, AUX_SC1); }
__device__ __forceinline__ u32x4 ld128(__amdgpu_buffer_rsrc_t rs, int word) { return __builtin_amdgcn_raw_buffer_load_b128(rs, word * 4, 0, AUX_SC1); }
__device__ __forceinline__ void st32(__amdgpu_buffer_rsrc_t rs, int word, unsigned v) { __builtin_amdgcn_raw_buffer_store_b32(v, rs, word * 4, 0, AUX_SC1); }
__device__ __forceinline__ void st64(__amdgpu_buffer_rsrc_t rs, int word, u32x2 v) { __builtin_amdgcn_raw_buffer_store_b64(v, rs, word * 4, 0, AUX_SC1); }
__device__ __forceinline__ void st128(__amdgpu_buffer_rsrc_t rs, int word, u32x4 v) { __builtin_amdgcn_raw_buffer_store_b128(v, rs, word * 4, 0, AUX_SC1); }

__device__ __forceinline__ u32x4 ld128nt(__amdgpu_buffer_rsrc_t rs, int word) { return __builtin_amdgcn_raw_buffer_load_b128(rs, word * 4, 0, AUX_EARLY); }
__device__ __forceinline__ void st128nt(__amdgpu_buffer_rsrc_t rs, int word, u32x4 v) { __builtin_amdgcn_raw_buffer_store_b128(v, rs, word * 4, 0, AUX_EARLY); }

__device__ __forceinline__ bool leader_release(__amdgpu_buffer_rsrc_t rs, int lane, unsigned t0, unsigned rd, int slow, int& prev) {
  u32x4 a[4];
#pragma unroll
  for (int k = 0; k < 4; ++k) a[k] = ld128(rs, WS_HA + (lane * 4 + k) * 32);
  const u32x4 n = ld128(rs, WS_HN + lane * 4);
  if (rd == 0u && !__all(a[0][0] == n[0] && a[1][0] == n[1] && a[2][0] == n[2] && a[3][0] == n[3])) return false;
  const float e = fmaxf(fmaxf((float)(int)(t0 - a[0][2]), (float)(int)(t0 - a[1][2])), fmaxf((float)(int)(t0 - a[2][2]), (float)(int)(t0 - a[3][2])));
  unsigned first = t0 - (unsigned)(int)max_x16_32(row_max(e));
  float ls = 0.f, lx = 0.f;
#pragma unroll
  for (int k = 0; k < 4; ++k) {
    const float l = a[k][0] == n[k] + rd ? (float)min(a[k][3] + (rd ? 0u : min(a[k][2] - first, 300u)), 2000u) : 0.f;
    ls += l; lx = fmaxf(lx, l);
  }
  ls = wave_sum(ls) * (1.0f / NWG); lx = max_x16_32(row_max(lx));
  const int extra = __builtin_amdgcn_readfirstlane(min(max((int)(ls * (FBM / 100.0f) + lx * (FBX / 100.0f)) - (rd ? FB1 + (int)(NRD - rd) * FBD : FB0), 0), 600));
  if (rd != 0u && __all(ls * NWG < HOLD)) { prev = 0; return true; }
  first = (first & 0xffffu) | ((unsigned)(slow + extra + min(extra, prev) * (int)(rd == NRD ? FBL : FBP) / 100) << 16);
  prev = extra;
  st128(rs, WS_HR + lane * 8, u32x4{a[0][1] + rd, first, a[1][1] + rd, first});
  st128(rs, WS_HR + lane * 8 + 4, u32x4{a[2][1] + rd, first, a[3][1] + rd, first});
  return true;
}

typedef __attribute__((address_space(3))) unsigned lds_u32;
__device__ __forceinline__ unsigned lds_add(lds_u32* p, int lane) {
  unsigned k = 0;
  if (lane == 0) k = __hip_atomic_fetch_add(p, 1u, __ATOMIC_RELAXED, __HIP_MEMORY_SCOPE_WORKGROUP);
  return __builtin_amdgcn_readfirstlane(k);
}
#define CFENCE() asm volatile("" ::: "memory")
__device__ __forceinline__ int arrive(lds_u32* p) {
  int v;
  unsigned long long keep;
  asm volatile("s_mov_b64 %1, exec\n\ts_mov_b64 exec, 1\n\tds_add_rtn_u32 %0, %2, %3\n\ts_mov_b64 exec, %1"
               : "=&v"(v), "=&s"(keep) : "v"((unsigned)(size_t)p), "v"(1u) : "memory");
  return v;
}
__device__ __forceinline__ int arrived(int v) {
  asm volatile("s_waitcnt lgkmcnt(0)" : "+v"(v) :: "memory");
  return __builtin_amdgcn_readfirstlane(v);
}
__device__ __forceinline__ unsigned lds_get(lds_u32* p) {
  return __builtin_amdgcn_readfirstlane(__hip_atomic_load(p, __ATOMIC_RELAXED, __HIP_MEMORY_SCOPE_WORKGROUP));
}
template <int SLEEP> __device__ __forceinline__ void lds_wait(lds_u32* p, unsigned n) {
#pragma unroll 1
  for (unsigned spin = 0; spin < (SPIN_CAP << 4); ++spin) {
    if (lds_get(p) >= n) break;
    __builtin_amdgcn_s_sleep(SLEEP);
  }
  asm volatile("" ::: "memory");
}
__device__ __forceinline__ unsigned clk() { return (unsigned)__builtin_amdgcn_s_memrealtime(); }
struct Table {
  unsigned t0; int was, moved, slowed, folded;
  u32x2 told;
  __device__ __forceinline__ int gate(int due, lds_u32* mine, bool hold = true, int sh = 0) {
    const int fold = __builtin_amdgcn_readfirstlane(told[1]);
    if (fold != folded) { const int f = min(fold - folded, moved); moved -= f; slowed += f; folded = fold; *mine = moved; }
    const int left = (int)__builtin_amdgcn_readfirstlane(told[0]) - slowed, d = max(min(left, FBS), left < -GBX ? -GBF : -GBS);
    t0 += d; was -= d; slowed += d;
    int late = (int)(clk() - t0) - due;
    if (late - was > JUMP) { t0 += late - was; moved += late - was; late = was; *mine = moved; }
    const int lag = max(late - max(left - d, 0) - LAGOFF, 0);
    mine[4] = lag;
    was = late;
#pragma unroll 1
    for (int spin = 0; spin < 65536 && hold && late < 0; ++spin) { __builtin_amdgcn_s_sleep(1); late = (int)(clk() - t0) - due; }
    return max(lag < (DEEP1 >> sh) ? 0 : (lag < (DEEP2 >> sh) ? 1 : 2), hold ? 0 : SDEEP);
  }
  __device__ __forceinline__ void listen(lds_u32* slow) {
    told = __builtin_bit_cast(u32x2, __hip_atomic_load((__attribute__((address_space(3))) unsigned long long*)slow, __ATOMIC_RELAXED, __HIP_MEMORY_SCOPE_WORKGROUP));
  }
  __device__ __forceinline__ void agree(lds_u32* all, lds_u32* mine) {
    if (moved == 0) return;
    const int m = (int)min(min(lds_get(all), lds_get(all + 1)), min(lds_get(all + 2), lds_get(all + 3))) + 30;
    if (m < moved) { t0 -= moved - m; moved = m; was = 1 << 20; *mine = moved; }
  }
};
__device__ __forceinline__ void until(unsigned t0, int due) {
#pragma unroll 1
  for (int spin = 0; spin < 65536 && (int)(clk() - t0) < due; ++spin) __builtin_amdgcn_s_sleep(1);
}
__device__ __forceinline__ void pause(unsigned t0, int due) {
#pragma unroll 1
  for (int spin = 0; spin < 4096 && (int)(clk() - t0) < due; ++spin) __builtin_amdgcn_s_sleep(16);
}
#define LGKM0() asm volatile("s_waitcnt lgkmcnt(0)" ::: "memory")

__device__ __forceinline__ void split4(f32x4 v, u32x2& hi, u32x2& lo) {
  unsigned h[4], r[4];
#pragma unroll
  for (int i = 0; i < 4; ++i) { h[i] = __float_as_uint(v[i]) & 0xffff0000u; r[i] = __float_as_uint(v[i] - __uint_as_float(h[i])); }
  hi = u32x2{__builtin_amdgcn_perm(h[1], h[0], 0x07060302u), __builtin_amdgcn_perm(h[3], h[2], 0x07060302u)};
  lo = u32x2{__builtin_amdgcn_perm(r[1], r[0], 0x07060302u), __builtin_amdgcn_perm(r[3], r[2], 0x07060302u)};
}
__device__ __forceinline__ u32x4 split8(f32x4 v, bool odd) {
  u32x2 hi, lo;
  split4(v, hi, lo);
  const unsigned n0 = __float_as_uint(dpp<DPP_XOR1>(__uint_as_float(odd ? hi[0] : lo[0]))), n1 = __float_as_uint(dpp<DPP_XOR1>(__uint_as_float(odd ? hi[1] : lo[1])));
  return u32x4{max(odd ? n0 : hi[0], 1u), max(odd ? n1 : hi[1], 1u), max(odd ? lo[0] : n0, 1u), max(odd ? lo[1] : n1, 1u)};
}
__device__ __forceinline__ u32x4 q_pack(u32x4 a, u32x4 b) {
  u32x2 hi, lo;
  split4((__builtin_bit_cast(f32x4, a) + __builtin_bit_cast(f32x4, b)) * QSCALE, hi, lo);
  return u32x4{hi[0], lo[0], hi[1], lo[1]};
}
__device__ __forceinline__ f32x4 mfma16(u32x4 a, u32x4 b, f32x4 c) {
  return __builtin_amdgcn_mfma_f32_16x16x32_bf16(__builtin_bit_cast(bf16x8, a), __builtin_bit_cast(bf16x8, b), c, 0, 0, 0);
}

constexpr int NW = 8, NMW = 4;
constexpr int NLP = 5, NRP = 7;
constexpr int NTP = 4 * (NLP + NRP);
constexpr int REG = NLP * 6 * 1024;
constexpr int NRING = 4;
constexpr int NS = 6, NA = NS - 1;
__device__ __forceinline__ int seg_beg(int s) { return s >= NS ? NPAIR : ((0xC87640 >> (4 * s)) & 15) * 16; }
__device__ __forceinline__ int seg_k(int t) { return (int)(0x5433222211110000ull >> (4 * (t >> 4))) & 15; }
__device__ __forceinline__ int seg_of_k(int k) { return (0x321540 >> (4 * k)) & 15; }
__device__ __forceinline__ int k_of_seg(int s) { return (0x215430 >> (4 * s)) & 15; }
__device__ __forceinline__ int pair_of(int t) { return t < 64 ? t : (t < 192 ? t + 64 : t - 128); }
constexpr int OFF_CNT = 0, OFF_REG = 256,
              OFF_SC = OFF_REG + 4 * REG, OFF_BUF = OFF_SC + NPAIR * 16 * 4, OFF_Q = OFF_BUF + NRING * NMW * DH * 4, OFF_ML = OFF_Q + NS * DH * 4,
              OFF_RED = OFF_ML + 128, OFF_RS = OFF_RED + 4 * 256 * 4, OFF_END = OFF_RS + 5 * 16;
enum { F_REL = 0, F_DONE, C_LAST, C_T = 3, F_OUT, C_P, C_K, F_P, F_Q = 8, F_FREE, S_FIRST, F_Q2, C_W = 12, C_SC = 16, C_PART = 24, S_ECHO = 32, S_BURST = 40, S_MOVED = 48, S_SLOW = 56 };
__device__ __forceinline__ int due_k(int p) { return TSTART + ((p * TAUK + min(max(p - WP0, 0), WPN) * (WGAP * 64 / WPN)) >> 6); }
__device__ __forceinline__ int due_v(int p) { return TSTART + WGAP + ((NPAIR * TAUK + p * TAUV + max(p - NPAIR / 2, 0) * (VGAP * 64 / (NPAIR / 2))) >> 6); }
constexpr int AW_LD = DH * 2 + 16, AG_LD = 160 * 2 + 16;

__global__ void __launch_bounds__(NW * 64) fused_kernel(const bf16* __restrict__ x, const bf16* __restrict__ kc,
                                                        const bf16* __restrict__ vc, const bf16* __restrict__ wq,
                                                        const bf16* __restrict__ wo, const bf16* __restrict__ wr,
                                                        const bf16* __restrict__ wg, const bf16* __restrict__ wu,
                                                        const bf16* __restrict__ wd, float* ws, bf16* __restrict__ out) {
  __shared__ __attribute__((aligned(16))) unsigned char smem[OFF_END];
  float* s_sc = reinterpret_cast<float*>(smem + OFF_SC);
  float* s_buf = reinterpret_cast<float*>(smem + OFF_BUF);
  float* s_q = reinterpret_cast<float*>(smem + OFF_Q);
  u32x4* s_ml = reinterpret_cast<u32x4*>(smem + OFF_ML);
  lds_u32* s_c = (lds_u32*)(smem + OFF_CNT);

  const int tid = threadIdx.x, lane = tid & 63, r = lane >> 3, j = lane & 7;
  const int wave = __builtin_amdgcn_readfirstlane(tid >> 6);
  const int tw = wave - NMW;
  const int bid = blockIdx.x, xl = bid & 7, idx = bid >> 3;
  const int g = (xl * 32 + idx) / GS, mm = idx % GS;
  const __amdgpu_buffer_rsrc_t rs = __builtin_amdgcn_make_buffer_rsrc(ws, 0, WS_END * 4, 0x00020000);
  const int ro = ((idx & 3) << 3) | (idx >> 2);
  const bool isW = ro < 10, isG = ro >= 10 && ro < 16;
  const int w_ks = xl, w_ct = ro;
  const int g_cg = xl * 6 + (ro - 10);
  const int d_ct = (xl + ((ro - 16) >> 2) * 8) * 2 + (((ro - 16) >> 1) & 1);
  const bool isD = ro >= 16 && ro < 24 && (ro & 1) == 0 && d_ct < DM / 32;
  const bool isH = ro == 22 && xl >= 2 && xl < 5;
  const bool isC = (mm & 7) == 3 || (mm & 7) >= 6;
  const int c_it = (mm >> 3) * 3 + max((mm & 7) - 5, 0);
  const int qf = WS_QF + g * QFS;
  const char* kb = reinterpret_cast<const char*>(kc);
  const char* vb = reinterpret_cast<const char*>(vc);
  const unsigned lo = (r * 24 + j) * 16;
  const int xb = g * GS + mm, c0 = mm * 8 + g * ROT;
  const int b2 = (2 * NH + (xb >> 4)) * 1024 + (xb & 15) * 8, b3 = (2 * NH + 16 + (xb >> 5)) * 1024 + (xb & 31) * 8;
  auto poff = [&](int p) -> unsigned {
    const int turn = (p & 63) >> 2;
    int t = (row_of(p >> 6) * NH + g) * 1024 + ((turn * 64 + (p & 3) * 2 + c0) & 1023);
    if ((p >> 6) == 1) t = (turn < 8 ? b2 + turn * 128 : b3 + (turn & 4) * 2048 + (turn & 3) * 256) + (p & 3) * 2;
    return (unsigned)t * 3072u;
  };
  const unsigned lds_base = (unsigned)(size_t)(__attribute__((address_space(3))) unsigned char*)smem + OFF_REG + tw * REG;
  auto merged = [&](int it, int ln) -> f32x4 {
    const float* src = s_buf + (it % NRING) * (NMW * DH) + ((ln >> 1) & 7) * 24 + (ln >> 4) * 8 + (ln & 1) * 4;
    return (*reinterpret_cast<const f32x4*>(src) + *reinterpret_cast<const f32x4*>(src + DH)) +
           (*reinterpret_cast<const f32x4*>(src + 2 * DH) + *reinterpret_cast<const f32x4*>(src + 3 * DH));
  };
  auto publish = [&](int it) -> bool {
    const int k = arrive(s_c + C_PART + it);
    const f32x4 m = merged(it, lane < DH / 4 ? lane : 0);
    u32x4 o = {nz_bits(m[0]), nz_bits(m[1]), nz_bits(m[2]), nz_bits(m[3])};
    asm volatile("" : "+v"(o));
    if (arrived(k) != NMW - 1) return false;
    if (lane < DH / 4) st128(rs, WS_PO + xb * DH + lane * 4, o);
    if (lane == 0) __hip_atomic_fetch_add(s_c + F_DONE, 1u, __ATOMIC_RELAXED, __HIP_MEMORY_SCOPE_WORKGROUP);
    return true;
  };
#define NONCE() do { unsigned n_ = __builtin_amdgcn_readfirstlane(f) + 1u; while (__any(f == n_)) ++n_; \
    if (lane < 32) st32(rs, qf + (GS + mm) * 32 + lane, n_); f = n_; } while (0)
  unsigned long long pc;
  asm volatile("s_getpc_b64 %0" : "=s"(pc));
  unsigned r0 = 0u;
  unsigned t0 = clk();
  if (wave < NMW) {
    Tile ta, tb, tc, td;
    const int c = lane & (GS - 1);
    unsigned f = 0u;
    if (wave == 3) f = ld32(rs, qf + c * 32 + mm);
    if (mm < NPROD) {
      const int hp = g + (mm / 6) * NG, ck = (mm % 6) >> 1, hf = mm & 1, jp = hf * 4 + wave;
      const int bb = lane >> 4, t = lane & 15;
      const u32x4* xp = reinterpret_cast<const u32x4*>(x + row_of(bb) * DM);
      u32x4 xv[5], xs = {0u, 0u, 0u, 0u};
#pragma unroll
      for (int k = 0; k < 5; ++k) xv[k] = xp[t + 16 * k];
      if (t < 10) xs = xp[jp * 10 + t];
      const u32x4* wp = reinterpret_cast<const u32x4*>(wq + (size_t)(jp * 80 + r) * HD + hp * DH + ck * 64) + j;
      u32x4 wv[10];
#pragma unroll
      for (int k = 0; k < 10; ++k) wv[k] = __builtin_nontemporal_load(wp + (size_t)k * 8 * (HD / 8));
      if (wave == 0) s_c[lane] = lane == C_K ? NTP + NMW : 0u;
      MEMFENCE(); LGKM0(); __builtin_amdgcn_s_barrier(); MEMFENCE();
      if (wave == 3) { NONCE(); }
      float sm = 0.f;
#pragma unroll
      for (int k = 0; k < 5; ++k)
#pragma unroll
        for (int d = 0; d < 4; ++d) sm += bf_lo(xv[k][d]) + bf_hi(xv[k][d]);
      const float mean = row_sum(sm) * (1.0f / DM);
      float sv = 0.f;
#pragma unroll
      for (int k = 0; k < 5; ++k)
#pragma unroll
        for (int d = 0; d < 4; ++d) { const float a = bf_lo(xv[k][d]) - mean, c2 = bf_hi(xv[k][d]) - mean; sv += a * a + c2 * c2; }
      const float rstd = 1.0f / sqrtf(row_sum(sv) * (1.0f / DM) + LN_EPS);
      float* s_xn = s_buf + wave * 320;
      if (t < 10) {
#pragma unroll
        for (int d = 0; d < 4; ++d) {
          s_xn[(t * 8 + 2 * d) * 4 + bb] = (bf_lo(xs[d]) - mean) * rstd;
          s_xn[(t * 8 + 2 * d + 1) * 4 + bb] = (bf_hi(xs[d]) - mean) * rstd;
        }
      }
      LGKM0();
      f32x2 qa[16];
#pragma unroll
      for (int i = 0; i < 16; ++i) qa[i] = f32x2{0.f, 0.f};
#pragma unroll
      for (int k = 0; k < 10; ++k) {
        const f32x4 xn = *reinterpret_cast<const f32x4*>(&s_xn[(k * 8 + r) * 4]);
#pragma unroll
        for (int d = 0; d < 4; ++d) {
          const f32x2 w2 = bf_pair(wv[k][d]);
#pragma unroll
          for (int e = 0; e < 4; ++e) qa[e * 4 + d] = fma2(xn[e], w2, qa[e * 4 + d]);
        }
      }
      float t16[16], t8[8];
#pragma unroll
      for (int i = 0; i < 16; ++i) t16[i] = tsum32(qa[i >> 1][i & 1], qa[8 + (i >> 1)][i & 1]);
#pragma unroll
      for (int i = 0; i < 8; ++i) t8[i] = tsum16(t16[i], t16[i + 8]);
#pragma unroll
      for (int i = 0; i < 8; ++i) t8[i] += dpp<DPP_ROR8>(t8[i]);
      const bool up = (lane & 8) != 0;
      f32x4* r4 = reinterpret_cast<f32x4*>(smem + OFF_RED);
      r4[wave * 64 + lane] = f32x4{up ? t8[4] : t8[0], up ? t8[5] : t8[1], up ? t8[6] : t8[2], up ? t8[7] : t8[3]};
      LGKM0();
      if (lds_add(s_c + C_P, lane) == NMW - 1) {
        asm volatile("" ::: "memory");
        const f32x4 o4 = (r4[lane] + r4[64 + lane]) + (r4[128 + lane] + r4[192 + lane]);
        st128nt(rs, WS_QP + ((hp * 2 + hf) * NB + bb) * DH + ck * 64 + j * 8 + (up ? 4 : 0), __builtin_bit_cast(u32x4, o4));
        VMCNT(0);
        if (lane == 0) s_c[F_OUT] = 1u;
      }
    } else {
      if (wave == 0) s_c[lane] = lane == C_K ? NTP + NMW : 0u;
      MEMFENCE(); LGKM0(); __builtin_amdgcn_s_barrier(); MEMFENCE();
      if (wave == 3) { NONCE(); }
      else if (CPF && lane < CODE / (24 * 64)) {
        typedef __attribute__((address_space(1))) const unsigned g_u32;
        until(t0, CPT);
        const unsigned v = *(g_u32*)((pc & ~255ull) + (((idx >> 3) * 2 + (mm & 1)) * 3 + wave) * (CODE / 24) + lane * 64);
        asm volatile("" :: "v"(v));
      }
    }
    MEMFENCE();
    if (wave == 3) {
      __builtin_amdgcn_s_setprio(3);
      const unsigned nonce = f;
      unsigned echoed = 0u;
      if (mm < NPROD) {
        echoed = ld32(rs, qf + (GS + c) * 32);
        asm volatile("" : "+v"(echoed) :: "memory");
        lds_wait<1>(s_c + F_OUT, 1u);
        if (lane < 32) st32(rs, qf + mm * 32 + lane, echoed);
#pragma unroll 1
        while ((int)(clk() - t0) < QWAIT - QBACK) {
          const unsigned nv = ld32(rs, qf + (GS + c) * 32);
          if (__any(nv != echoed)) {
            if (lane < 32) st32(rs, qf + mm * 32 + lane, nv);
            echoed = nv;
          }
          __builtin_amdgcn_s_sleep(4);
        }
      }
      until(t0, QWAIT);
#pragma unroll 1
      for (unsigned spin = 0; spin < SPIN_CAP; ++spin) {
        const unsigned nv = ld32(rs, qf + (GS + c) * 32);
        f = ld32(rs, qf + c * 32 + mm);
        const bool done = __all(c == mm || c >= NPROD || f == nonce);
        if (mm < NPROD && __any(nv != echoed)) {
          if (lane < 32) st32(rs, qf + mm * 32 + lane, nv);
          echoed = nv;
        }
        if (done) break;
        __builtin_amdgcn_s_sleep(8);
      }
      if (lane < GS) s_c[S_ECHO + lane] = echoed;
      if (lane < DH / 4) {
        u32x4 p[3][2];
#pragma unroll
        for (int it = 0; it < 3; ++it)
#pragma unroll
          for (int k = 0; k < 2; ++k) p[it][k] = ld128nt(rs, WS_QP + ((g * 2 + k) * NB + (it ? it + 1 : 0)) * DH + lane * 4);
#pragma unroll
        for (int it = 0; it < 3; ++it) {
          *reinterpret_cast<u32x4*>(&s_q[(it ? it + 3 : 0) * DH + lane * 4]) = q_pack(p[it][0], p[it][1]);
          if (it != 1) { LGKM0(); if (lane == 0) s_c[F_Q] = it + 1; MEMFENCE(); }
        }
      }
    }
    __builtin_amdgcn_s_setprio(3);
    until(t0, due_k(NTP + wave));
    Table tt = {t0 + ((idx * PHASE) >> 5), 1 << 20, 0, 0, 0, {0u, 0u}};
    bool common = false;
    tile_load(ta, kb, poff(NTP + wave) + lo, 0);
    tile_load(tb, kb, poff(NTP + wave) + lo, 1);
    MEMFENCE();
    {
      u32x2 q[12];
      int cur = NTP + wave, nxt = lds_add(s_c + C_K, lane), passed = 0, ci = -1;
      auto kstep = [&](Tile& xa, Tile& xb_, Tile& ya, Tile& yb) __attribute__((always_inline)) -> bool {
        const bool last = nxt >= NPAIR;
        const unsigned off = poff(last ? wave : pair_of(nxt)) + lo;
        const char* base = last ? vb : kb;
        const int it = seg_k(cur), cp = pair_of(cur);
        if (it != ci) {
          LGKM0();
          for (; passed < it; ++passed) lds_add(s_c + C_SC + passed, lane);
          lds_wait<1>(s_c + (it < 3 ? F_Q : F_Q2), min(it + 1, 3));
          q_load(q, s_q + seg_of_k(it) * DH, j);
          if (!common) { const unsigned d = lds_get(s_c + S_FIRST); if (d) { tt.t0 -= d - 1u; common = true; tt.was = 1 << 20; } }
          tt.agree(s_c + S_MOVED, s_c + S_MOVED + wave);
          ci = it;
        }
        MEMFENCE();
        const int deep = tt.gate(due_k(last ? NPAIR + wave : nxt), s_c + S_MOVED + wave);
        MEMFENCE();
        pair_issue(ya, yb, base, off, deep);
        const int tk = arrive(s_c + C_K);
        tt.listen(s_c + S_SLOW + 2 * wave);
        pair_put(tile_score(xa, q), tile_score(xb_, q), s_sc + cp * 16 + r, j);
        MEMFENCE();
        cur = nxt; nxt = arrived(tk);
        return last;
      };
      bool swap;
      for (;;) {
        if (kstep(ta, tb, tc, td)) { swap = true; break; }
        if (kstep(tc, td, ta, tb)) { swap = false; break; }
      }
      LGKM0();
      for (; passed < NS; ++passed) lds_add(s_c + C_SC + passed, lane);
      __builtin_amdgcn_s_waitcnt(0x0F70);
      if (swap) { ta = tc; tb = td; }
    }
#pragma unroll 1
    for (int it = 0; it < NS; ++it) {
      lds_wait<1>(s_c + F_P, k_of_seg(it) + 1);
      if (!common) { const unsigned d = lds_get(s_c + S_FIRST); if (d) { tt.t0 -= d - 1u; common = true; tt.was = 1 << 20; } }
      tt.agree(s_c + S_MOVED, s_c + S_MOVED + wave);
      f32x2 acc[12];
#pragma unroll
      for (int i = 0; i < 12; ++i) acc[i] = f32x2{0.f, 0.f};
      auto vstep = [&](Tile& xa, Tile& xb_, Tile& ya, Tile& yb, int p) __attribute__((always_inline)) {
        const unsigned off = poff(p + NMW) + lo;
        MEMFENCE();
        const int deep = tt.gate(due_v(p + NMW), s_c + S_MOVED + wave, p + NMW < NPAIR - SPRINT * NMW, p >= NPAIR - ENDT * NMW ? ENDS : 0);
        MEMFENCE();
        pair_issue(ya, yb, vb, off, deep);
        const float pa = s_sc[p * 16 + r], pb = s_sc[p * 16 + 8 + r];
        tt.listen(s_c + S_SLOW + 2 * wave);
        if (p + 2 * NMW >= NPAIR) { if (lane == 0) __hip_atomic_fetch_add(s_c + C_LAST, 1u, __ATOMIC_RELAXED, __HIP_MEMORY_SCOPE_WORKGROUP); }
        pv_last(pa, xa, acc);
        pv_last(pb, xb_, acc);
        MEMFENCE();
      };
#pragma unroll 1
      for (int p = seg_beg(it) + wave; p < min(seg_beg(it + 1), NPAIR - 2 * NMW); p += 2 * NMW) {
        vstep(ta, tb, tc, td, p);
        vstep(tc, td, ta, tb, p + NMW);
      }
      if (it >= NRING) lds_wait<1>(s_c + F_FREE, it - NRING + 1);
      if (it == NS - 1) {
        vstep(ta, tb, tc, td, NPAIR - 2 * NMW + wave);
        const int p = NPAIR - NMW + wave;
        const float pa = s_sc[p * 16 + r], pb = s_sc[p * 16 + 8 + r];
        pv_last(pa, tc, acc);
        pv_last(pb, td, acc);
      }
      float t12[12], t6[6];
#pragma unroll
      for (int i = 0; i < 12; ++i) t12[i] = tsum32(acc[i >> 1][i & 1], acc[6 + (i >> 1)][i & 1]);
#pragma unroll
      for (int i = 0; i < 6; ++i) t6[i] = tsum16(t12[i], t12[i + 6]);
#pragma unroll
      for (int i = 0; i < 6; ++i) t6[i] += dpp<DPP_ROR8>(t6[i]);
      if ((lane & 8) == 0) {
        f32x2* dst = reinterpret_cast<f32x2*>(&s_buf[(it % NRING) * (NMW * DH) + wave * DH + j * 24 + (lane >> 4) * 6]);
        dst[0] = f32x2{t6[0], t6[1]}; dst[1] = f32x2{t6[2], t6[3]}; dst[2] = f32x2{t6[4], t6[5]};
      }
      CFENCE();
      if (it < NA) { if (lane == 0) __hip_atomic_fetch_add(s_c + C_PART + it, 1u, __ATOMIC_RELAXED, __HIP_MEMORY_SCOPE_WORKGROUP); }
    }
    publish(NS - 1);
    return;
  }

  {
    Tile pk[NRP * 2];
    __builtin_amdgcn_s_setprio(3);
#pragma unroll
    for (int e = 0; e < NLP * 2; ++e) {
      const char* src = kb + (poff((e >> 1) * 4 + tw) + lo + (e & 1) * 3072);
      if (e >= 2) until(t0, due_k(e * 2 + tw) - TLEAD);
#pragma unroll
      for (int i = 0; i < 3; ++i) glds16_nt(src + i * 128, lds_base + (e * 3 + i) * 1024);
      if (e == 1) __builtin_amdgcn_s_barrier();
      if (e * 3 + 3 > PFD) VMCNT(PFD);
    }
#pragma unroll
    for (int e = 0; e < NRP * 2; ++e) {
      until(t0, due_k((NLP * 2 + e) * 2 + tw) - TLEAD);
      tile_load(pk[e], kb, poff((NLP + (e >> 1)) * 4 + tw) + lo, e & 1);
      MEMFENCE();
      if (e + 1 < NRP * 2) VMCNT(PFD);
    }
    if (tw == 0) {
      r0 = ld32(rs, WS_HR + bid * 2);
      if (bid == LEADER) {
        u32x4 n;
#pragma unroll
        for (int k = 0; k < 4; ++k) n[k] = ld32(rs, WS_HA + (lane * 4 + k) * 32) + 1u;
        st128(rs, WS_HN + lane * 4, n);
      }
      const u32x4 z = {0u, 0u, 0u, 0u};
      if (lane < 48) {
#pragma unroll
        for (int it = NA; it < NS; ++it) st128(rs, WS_PO + xb * DH + lane * 4, z);
      }
      else st128(rs, WS_PML + (g * GS + mm) * 64 + (lane - 48) * 4, z);
      if (isG && g_cg == 0 && lane < 8) st128(rs, WS_ST + lane * 4, z);
      if (isW) st128(rs, WS_SLAB + (w_ks * NB + (lane >> 4)) * DM + w_ct * 64 + (lane & 15) * 4, z);
      else if (isG) { if (lane < 16) st128(rs, WS_ACT + g_cg * 64 + lane * 4, z); }
      else if (isH) {
#pragma unroll
        for (int k = 0; k < 3; ++k) st128(rs, WS_H + (xl - 2) * 768 + k * 256 + lane * 4, z);
      }
      else if (isC && lane < 48) st128(rs, WS_OC + (c_it * NG + g) * DH + lane * 4, z);
    }
    __builtin_amdgcn_s_setprio(0);
    lds_wait<1>(s_c + F_Q, HPG);
    u32x2 q[12];
    q_load(q, s_q, j);
    const u32x4* s_kv = reinterpret_cast<const u32x4*>(smem + OFF_REG + tw * REG) + lane;
    static_assert(NLP * 4 <= PPI && NTP <= HPG * PPI, "tail tiles: batch row 0 only");
#pragma unroll 1
    for (int e = 0; e < NLP * 2; ++e) {
      Tile tmp;
#pragma unroll
      for (int i = 0; i < 3; ++i) tmp.d[i] = s_kv[(e * 3 + i) * 64];
      score_put(tile_score(tmp, q), s_sc + ((e >> 1) * 4 + tw) * 16 + (e & 1) * 8 + r, j);
    }
#pragma unroll
    for (int e = 0; e < NRP * 2; ++e) {
      if ((NLP * 2 + e) * 2 == PPI) q_load(q, s_q + DH, j);
      score_put(tile_score(pk[e], q), s_sc + ((NLP + (e >> 1)) * 4 + tw) * 16 + (e & 1) * 8 + r, j);
      __builtin_amdgcn_sched_barrier(0);
    }
    asm volatile("s_waitcnt vmcnt(0) lgkmcnt(0)" ::: "memory");
#pragma unroll
    for (int it = 0; it * PPI < NTP; ++it) lds_add(s_c + C_SC + it, lane);
  }

  const int mr = lane & 15, qd = lane >> 4;
  unsigned char* reg = smem + OFF_REG + tw * REG;
  float* red = reinterpret_cast<float*>(smem + OFF_RED);
#define PERM_LO(hi_, lo_) __builtin_amdgcn_perm(hi_, lo_, 0x05040100u)
#define PERM_HI(hi_, lo_) __builtin_amdgcn_perm(hi_, lo_, 0x07060302u)
  int served = 0;
  auto done = [&](int n) {
    if (tw == 3 && served < min(n, NA)) {
      const int from = served;
      int ln = lane;
      asm volatile("" : "+v"(ln));
#pragma unroll 1
      for (; served < min(n, NA); ++served) {
        lds_wait<8>(s_c + C_PART + served, NMW);
        const f32x4 o = merged(served, ln < DH / 4 ? ln : 0);
        const float am = max_x16_32(row_max(fmaxf(fmaxf(fabsf(o[0]), fabsf(o[1])), fmaxf(fabsf(o[2]), fabsf(o[3])))));
        const float inv = am > 1e-30f ? 32767.0f / am : 0.f;
        const int q0 = __float2int_rn(o[0] * inv), q1 = __float2int_rn(o[1] * inv), q2 = __float2int_rn(o[2] * inv), q3 = __float2int_rn(o[3] * inv);
        if (ln < DH / 4) reinterpret_cast<u32x2*>(s_sc + served * (DH / 2))[ln] = u32x2{PERM_LO((unsigned)q1, (unsigned)q0), PERM_LO((unsigned)q3, (unsigned)q2)};
        const bool nan = __any((o[0] + o[1]) + (o[2] + o[3]) != (o[0] + o[1]) + (o[2] + o[3]));
        if (ln == 0) s_ml[served][2] = nan ? 0x7fc00000u : nz_bits(am * (1.0f / 32767.0f));
        LGKM0();
        if (ln == 0) s_c[F_FREE] = served + 1;
      }
#pragma unroll 1
      for (int c = from * 24 + ln; c < served * 24; c += 64) st128nt(rs, WS_PQ + xb * 5 * (DH / 2) + c * 4, reinterpret_cast<const u32x4*>(s_sc)[c]);
      VMCNT(0);
      if (ln < 8) st128(rs, WS_PML + xb * 64 + (from ? 32 : 0) + ln * 4, s_ml[ln]);
      if (ln == 0) s_c[S_BURST + (from ? 1 : 0)] = clk();
      CFENCE();
      if (ln == 0) __hip_atomic_fetch_add(s_c + F_DONE, (unsigned)(served - from), __ATOMIC_RELAXED, __HIP_MEMORY_SCOPE_WORKGROUP);
    }
    if (n > NA) lds_wait<1>(s_c + F_DONE, n); else lds_wait<8>(s_c + F_DONE, n);
  };
  auto done_all = [&]() {
#pragma unroll 1
    for (int n = 4; n <= NA; ++n) done(n);
  };
  const int lp = (lane < DH / 4 ? lane : 0) * 4;
  auto weight = [&](int it, int x0, int cnt) -> float {
    const int mlw = WS_PML + (x0 + (lane & (cnt - 1))) * 64 + (it < 4 ? 0 : 32) + it * 4;
    u32x4 ml;
#pragma unroll 1
    for (unsigned spin = 0; spin < SPIN_CAP; ++spin) {
      ml = ld128(rs, mlw);
      if (__all(min4(ml) != 0u)) break;
      __builtin_amdgcn_s_sleep(16);
    }
    const float mk = __uint_as_float(ml[0]);
    const float e = __builtin_amdgcn_exp2f(mk - max_x16_32(row_max(mk)));
    return e / (wave_sum(e * __uint_as_float(ml[1])) * ((float)cnt * (1.0f / 64.0f))) * __uint_as_float(ml[2]);
  };
  const unsigned tok = __builtin_amdgcn_readfirstlane(r0) + 1u;
  unsigned echoed = 0u, fold = 0u;
  int slow = 0, prev = 0;
  auto lag = [&]() {
    const unsigned v = __hip_atomic_load(s_c + S_MOVED + (lane & 7), __ATOMIC_RELAXED, __HIP_MEMORY_SCOPE_WORKGROUP);
    fold += v;
    unsigned a = 0u;
#pragma unroll
    for (int k = 0; k < 8; ++k) a += __builtin_amdgcn_readlane(v, k);
    return a >> 2;
  };
  auto tell = [&]() { if (lane < 4) { s_c[S_SLOW + 2 * lane] = slow; s_c[S_SLOW + 2 * lane + 1] = fold; } };
  auto round = [&](unsigned rd, bool lead) {
    const int tr = RT0 + (int)(rd - 1) * RTS;
    until(t0, tr);
    const unsigned bh = lag();
    if (bh > RMIN && lane < 8) st128(rs, WS_HA + bid * 32 + lane * 4, u32x4{echoed + rd, tok, t0, bh});
    if (lead) { until(t0, tr + RA1); leader_release(rs, lane, t0, rd, slow, prev); }
    until(t0, tr + RA2);
    const u32x2 rv = ld64(rs, WS_HR + bid * 2);
    slow = (int)__builtin_amdgcn_readfirstlane(rv[1]) >> 16;
#pragma unroll 1
    for (unsigned k = min(__builtin_amdgcn_readfirstlane(rv[0]) - tok, rd) + 1u; k <= rd; ++k) slow = slow > 0 ? max(slow - GB, 0) : slow - (k <= PRD ? PRB : 0);
    tell();
  };
  auto prep = [&](bool lead, unsigned nrd) {
    if (tw == 0) {
      const int c = lane & (GS - 1);
      unsigned qe = s_c[S_ECHO + c];
      bool first = true, released = false, sent = false;
#pragma unroll 1
      while ((int)(clk() - t0) < HST) {
        const unsigned qn = ld32(rs, qf + (GS + c) * 32);
        if (mm < NPROD && __any(qn != qe)) {
          if (lane < 32) st32(rs, qf + mm * 32 + lane, qn);
          qe = qn;
        }
        __builtin_amdgcn_s_sleep(64);
      }
      const unsigned behind = lag();
#pragma unroll 1
      for (unsigned spin = 0; spin < SPIN_CAP && !released; ++spin) {
        const unsigned qn = ld32(rs, qf + (GS + c) * 32);
        const u32x2 rv = ld64(rs, WS_HR + bid * 2);
        const unsigned nv = __builtin_amdgcn_readfirstlane(ld32(rs, WS_HN + bid));
        if (__builtin_amdgcn_readfirstlane(rv[0]) - tok <= NRD) {
          released = true;
          slow = (int)__builtin_amdgcn_readfirstlane(rv[1]) >> 16;
          if (lane == 0) { s_c[S_FIRST] = min((t0 - rv[1]) & 0xffffu, 300u) + 1u; }
        } else {
          if (mm < NPROD && __any(qn != qe)) {
            if (lane < 32) st32(rs, qf + mm * 32 + lane, qn);
            qe = qn;
          }
          if (first || nv != echoed) {
            echoed = nv; first = false;
            if (lane < 8) st128(rs, WS_HA + bid * 32 + lane * 4, u32x4{echoed, tok, t0, behind});
          }
          if (lead && !sent) sent = leader_release(rs, lane, t0, 0u, slow, prev);
          __builtin_amdgcn_s_sleep(32);
        }
      }
      tell();
      if (lane == 0) s_c[F_REL] = 1u;
      if (lane < DH / 4) {
        u32x4 p[3][2];
#pragma unroll
        for (int e = 0; e < 3; ++e)
#pragma unroll
          for (int k = 0; k < 2; ++k) p[e][k] = ld128nt(rs, WS_QP + (((e ? 8 + e * 8 + (xb >> 5) : xb >> 4) * 2 + k) * NB + 1) * DH + lane * 4);
#pragma unroll
        for (int e = 0; e < 3; ++e) *reinterpret_cast<u32x4*>(&s_q[(1 + e) * DH + lane * 4]) = q_pack(p[e][0], p[e][1]);
      }
      LGKM0();
      if (lane == 0) s_c[F_Q2] = 3u;
#pragma unroll 1
      for (unsigned rd = 1; rd <= nrd; ++rd) round(rd, lead);
    }
    if (tw == 3) {
#pragma unroll 1
      for (int ik = 0; ik < NS; ++ik) {
        const int it = seg_of_k(ik), nv = (seg_beg(it + 1) - seg_beg(it)) >> 4;
        lds_wait<4>(s_c + C_SC + ik, ik == 0 ? NW : NMW);
        f32x4* sp = reinterpret_cast<f32x4*>(s_sc + seg_beg(it) * 16) + lane;
        f32x4 v[4];
#pragma unroll
        for (int k = 0; k < 4; ++k) v[k] = sp[(k & (nv - 1)) * 64];
        const f32x4 mx = __builtin_elementwise_max(__builtin_elementwise_max(v[0], v[1]), __builtin_elementwise_max(v[2], v[3]));
        const float m = max_x16_32(row_max(fmaxf(fmaxf(mx[0], mx[1]), fmaxf(mx[2], mx[3]))));
        float l = 0.f;
#pragma unroll
        for (int k = 0; k < 4; ++k) {
#pragma unroll
          for (int e = 0; e < 4; ++e) v[k][e] = __builtin_amdgcn_exp2f(v[k][e] - m);
          if (k < nv) {
            l += (v[k][0] + v[k][1]) + (v[k][2] + v[k][3]);
            sp[k * 64] = v[k];
          }
        }
        l = wave_sum(l);
        if (lane == 0) s_ml[it] = u32x4{nz_bits(m), nz_bits(l), 0x3f800000u, 0x3f800000u};
        LGKM0();
        if (lane == 0) s_c[F_P] = ik + 1;
      }
    }
  };
  auto pull = [&]() {
    typedef __attribute__((address_space(1))) const unsigned g_u32;
    const int id = (ro - 16) * 4 + tw;
    constexpr int RW = 160 - 32 * GE, LW = RW * 4 * 3 * 2 / 64;
    g_u32* p1[2];
#pragma unroll
    for (int i = 0; i < 2; ++i) {
      const int l = id * LW + min(i * 32 + (lane >> 1), max(LW - 1, 0)), m = l >= LW * 32 ? l - LW * 32 : l, rr = m / 3;
      p1[i] = (g_u32*)((l >= LW * 32 ? wu : wg) + (size_t)(rr / max(RW, 1) * 160 + 32 * GE + rr % max(RW, 1)) * FF + xl * 192 + (m % 3) * 64 + (lane & 1) * 32);
    }
    g_u32* p2 = (g_u32*)(wd + (size_t)(id * 24 + min(lane >> 1, 23)) * DM + xl * 64 + (lane & 1) * 32);
    asm volatile("" : "+v"(p1[0]), "+v"(p1[1]), "+v"(p2));
    if (GLEAD) lds_wait<1>(s_c + C_LAST, GCNT); else done(NS);
    const unsigned tp = clk() + PW + tw * PS;
    until(tp, 0);
    const unsigned v0 = LW > 0 ? p1[0][0] : 0u;
    MEMFENCE();
    until(tp, 4 * PS);
    const unsigned v1 = LW > 32 ? p1[1][0] : 0u;
    MEMFENCE();
    until(tp, DPW);
    const unsigned v2 = p2[0], v3 = p2[xl < 2 ? 256 : 0];
    MEMFENCE();
    return (v0 ^ v1) + (v2 ^ v3);
  };
  if (isW) {
    u32x4 bx[24];
    const char* p = reinterpret_cast<const char*>(wo + (size_t)((w_ks * 4 + tw) * DH + r) * DM + w_ct * 64 + j * 8);
#pragma unroll 1
    for (int i = 0; i < 24; ++i) { until(t0, tw == 0 ? W0START + i * (W0SPAN / 24) : W1START + i * (W1SPAN / 24)); glds16_nt(p, lds_base + i * 1024); p += 8 * DM * 2; VMCNT(WFD); }
    asm volatile("s_waitcnt vmcnt(0)" ::: "memory");
#pragma unroll
    for (int s = 0; s < 6; ++s) {
      u32x2 e[8];
#pragma unroll
      for (int k = 0; k < 8; ++k) e[k] = *reinterpret_cast<const u32x2*>(reg + (s * 32 + qd * 8 + k) * 128 + mr * 8);
#pragma unroll
      for (int k = 0; k < 4; ++k) {
        bx[s * 4 + 0][k] = PERM_LO(e[2 * k + 1][0], e[2 * k][0]); bx[s * 4 + 1][k] = PERM_HI(e[2 * k + 1][0], e[2 * k][0]);
        bx[s * 4 + 2][k] = PERM_LO(e[2 * k + 1][1], e[2 * k][1]); bx[s * 4 + 3][k] = PERM_HI(e[2 * k + 1][1], e[2 * k][1]);
      }
    }
#pragma unroll
    for (int s = 0; s < 24; s += 4) asm volatile("" : "+v"(bx[s]), "+v"(bx[s + 1]), "+v"(bx[s + 2]), "+v"(bx[s + 3]));
    LGKM0();
    prep(false, NRD - WL);
    lds_wait<8>(s_c + F_REL, 1u);
    const int hh = w_ks * 4 + tw;
#pragma unroll 1
    for (int pass = 0; pass < 3; ++pass) {
      if (pass < 2) {
        if (WL && pass == 1 && tw == 0) round(NRD, false);
        done(pass ? NA : 4);
        pause(lds_get(s_c + S_BURST + pass), pass ? WDELAY2 : WDELAY2 + 300);
        const int b0 = pass * 2, b1 = pass + 1;
        u32x4 v0, v1;
#pragma unroll 1
        for (unsigned spin = 0; spin < SPIN_CAP; ++spin) {
          v0 = ld128(rs, WS_OC + (b0 * NH + hh) * DH + lp);
          v1 = ld128(rs, WS_OC + (b1 * NH + hh) * DH + lp);
          if (__all(min(min4(v0), min4(v1)) != 0u)) break;
          __builtin_amdgcn_s_sleep(16);
        }
        if (lane < DH / 4) {
          *reinterpret_cast<u32x2*>(reg + (b0 * 4) * AW_LD + lane * 8) = u32x2{v0[0], v0[1]};
          *reinterpret_cast<u32x2*>(reg + (b0 * 4 + 1) * AW_LD + lane * 8) = u32x2{v0[2], v0[3]};
          *reinterpret_cast<u32x2*>(reg + (b1 * 4) * AW_LD + lane * 8) = u32x2{v1[0], v1[1]};
          *reinterpret_cast<u32x2*>(reg + (b1 * 4 + 1) * AW_LD + lane * 8) = u32x2{v1[2], v1[3]};
        }
      } else {
        const float wk = weight(NS - 1, hh * GS, GS);
        float w[GS];
#pragma unroll
        for (int k = 0; k < GS; ++k) { w[k] = __uint_as_float(__builtin_amdgcn_readlane(__float_as_uint(wk), k)); asm volatile("" : "+s"(w[k])); }
        done(NS);
        __builtin_amdgcn_s_setprio(3);
        const int po = WS_PO + hh * GS * DH;
        u32x4 pv[GS];
#pragma unroll 1
        for (unsigned spin = 0; spin < SPIN_CAP; ++spin) {
#pragma unroll
          for (int k = 0; k < GS; ++k) pv[k] = __builtin_amdgcn_raw_buffer_load_b128(rs, lp * 4, (po + k * DH) * 4, AUX_SC1);
          unsigned mn = min4(pv[0]);
#pragma unroll
          for (int k = 1; k < GS; ++k) mn = min(mn, min4(pv[k]));
          if (__all(mn != 0u)) break;
          __builtin_amdgcn_s_sleep(1);
        }
        f32x4 o = w[0] * __builtin_bit_cast(f32x4, pv[0]), o2 = w[1] * __builtin_bit_cast(f32x4, pv[1]);
#pragma unroll
        for (int k = 2; k < GS; k += 2) { o += w[k] * __builtin_bit_cast(f32x4, pv[k]); o2 += w[k + 1] * __builtin_bit_cast(f32x4, pv[k + 1]); }
        u32x2 hi, lw;
        split4(o + o2, hi, lw);
        if (lane < DH / 4) {
          *reinterpret_cast<u32x2*>(reg + ((NB - 1) * 4) * AW_LD + lane * 8) = hi;
          *reinterpret_cast<u32x2*>(reg + ((NB - 1) * 4 + 1) * AW_LD + lane * 8) = lw;
        }
      }
      CFENCE();
      u32x4 a[6];
#pragma unroll
      for (int s = 0; s < 6; ++s) a[s] = *reinterpret_cast<const u32x4*>(reg + mr * AW_LD + s * 64 + qd * 16);
      __builtin_amdgcn_sched_barrier(0);
      f32x4 c[4];
#pragma unroll
      for (int t = 0; t < 4; ++t) c[t] = f32x4{0.f, 0.f, 0.f, 0.f};
#pragma unroll
      for (int s = 0; s < 6; ++s)
#pragma unroll
        for (int t = 0; t < 4; ++t) c[t] = mfma16(a[s], bx[s * 4 + t], c[t]);
      const bool mine = max(qd - 1, 0) == pass;
      f32x4* r4 = reinterpret_cast<f32x4*>(red);
      if (mine) r4[tw * 64 + lane] = f32x4{c[0][0] + c[0][1], c[1][0] + c[1][1], c[2][0] + c[2][1], c[3][0] + c[3][1]};
      const int k = arrive(s_c + C_W + pass);
      f32x4 sum = (r4[lane] + r4[64 + lane]) + (r4[128 + lane] + r4[192 + lane]);
      asm volatile("" : "+v"(sum));
      if (arrived(k) == 3) {
        if (mine) st128(rs, WS_SLAB + (w_ks * NB + qd) * DM + w_ct * 64 + mr * 4, u32x4{nz_bits(sum[0]), nz_bits(sum[1]), nz_bits(sum[2]), nz_bits(sum[3])});
      }
    }
  } else if (isG || isH) {
    f32x4* rst = reinterpret_cast<f32x4*>(red);
    const int hb = isH ? xl - 2 : NB - 1;
    uint2 wr4 = make_uint2(0u, 0u), xg = wr4;
    if (lane < 40) {
      wr4 = *reinterpret_cast<const uint2*>(wr + tw * 160 + lane * 4);
      xg = *reinterpret_cast<const uint2*>(x + row_of(hb) * DM + tw * 160 + lane * 4);
    }
    const int eb = mr >> 2;
    const float cq = bf_lo(*reinterpret_cast<const unsigned*>(x + row_of(eb) * DM));
    const float wsum = wave_sum((bf_lo(wr4.x) + bf_hi(wr4.x)) + (bf_lo(wr4.y) + bf_hi(wr4.y)));
    f32x4 xc = f32x4{bf_lo(xg.x), bf_hi(xg.x), bf_lo(xg.y), bf_hi(xg.y)} - __uint_as_float(__builtin_amdgcn_readlane(__float_as_uint(cq), hb * 4));
    asm volatile("" : "+v"(xc));
    typedef __attribute__((address_space(1))) const u32x4 g_u32x4;
    g_u32x4* wp[10];
    wp[0] = (g_u32x4*)(((mr & 4) ? wu : wg) + (size_t)(tw * 160 + qd * 8 + (mr >> 3) * 4) * FF + g_cg * 32 + (mr & 3) * 8);
    u32x4 e[20], bx[20];
    auto frag = [&](int s) __attribute__((always_inline)) {
      unsigned w[8][2];
#pragma unroll
      for (int i = 0; i < 4; ++i)
#pragma unroll
        for (int d = 0; d < 2; ++d) {
          w[i][d] = __builtin_amdgcn_update_dpp(e[s * 4 + i][d], e[s * 4 + i][2 + d], DPP_ROR8, 0xf, 0xc, false);
          w[4 + i][d] = __builtin_amdgcn_update_dpp(e[s * 4 + i][2 + d], e[s * 4 + i][d], DPP_ROR8, 0xf, 0x3, false);
        }
#pragma unroll
      for (int k = 0; k < 4; ++k) {
        bx[s * 4 + 0][k] = PERM_LO(w[2 * k + 1][0], w[2 * k][0]); bx[s * 4 + 1][k] = PERM_HI(w[2 * k + 1][0], w[2 * k][0]);
        bx[s * 4 + 2][k] = PERM_LO(w[2 * k + 1][1], w[2 * k][1]); bx[s * 4 + 3][k] = PERM_HI(w[2 * k + 1][1], w[2 * k][1]);
      }
      PIN4(bx, s * 4);
    };
    if (isG) {
#pragma unroll
      for (int i = 0; i < 4 * GE; ++i) {
        until(t0, tw == 0 ? W0START + i * (W0SPAN / max(4 * GE, 1)) : W1START + i * (W1SPAN / max(4 * GE, 1)));
        e[i] = __builtin_nontemporal_load(wp[0] + ((i >> 2) * 32 + (i & 3)) * (FF / 8));
        MEMFENCE();
      }
#pragma unroll
      for (int s = 0; s < GE; ++s) frag(s);
    }
    prep(false, NRD);
    lds_wait<8>(s_c + F_REL, 1u);
    auto hrow = [&](bool fast) -> f32x4 {
      f32x4 hv = {0.f, 0.f, 0.f, 0.f};
      if (lane < 40) {
        const int off = (WS_SLAB + hb * DM + tw * 160 + lane * 4) * 4;
        u32x4 sl[KS];
#pragma unroll 1
        for (unsigned spin = 0; spin < SPIN_CAP; ++spin) {
#pragma unroll
          for (int ks = 0; ks < KS; ++ks) sl[ks] = __builtin_amdgcn_raw_buffer_load_b128(rs, off, ks * (NB * DM * 4), AUX_SC1);
          unsigned mn = min4(sl[0]);
#pragma unroll
          for (int ks = 1; ks < KS; ++ks) mn = min(mn, min4(sl[ks]));
          if (!__any(mn == 0u)) break;
          if (fast) __builtin_amdgcn_s_sleep(1); else __builtin_amdgcn_s_sleep(16);
        }
#define SL(k) __builtin_bit_cast(f32x4, sl[k])
        hv = xc + (((SL(0) + SL(1)) + (SL(2) + SL(3))) + ((SL(4) + SL(5)) + (SL(6) + SL(7))));
#undef SL
      }
      return hv;
    };
    auto sums = [&](f32x4 hv) -> f32x4 {
      const float s1 = wave_sum((hv[0] + hv[1]) + (hv[2] + hv[3]));
      const float s2 = wave_sum((hv[0] * hv[0] + hv[1] * hv[1]) + (hv[2] * hv[2] + hv[3] * hv[3]));
      const float dot = wave_sum((hv[0] * bf_lo(wr4.x) + hv[1] * bf_hi(wr4.x)) + (hv[2] * bf_lo(wr4.y) + hv[3] * bf_hi(wr4.y)));
      return f32x4{s1, s2, dot, wsum};
    };
    if (isH) {
      if (tw == 3 || hb == 2) done_all(); else done(4);
      pause(lds_get(s_c + S_BURST + (hb >> 1)), hb < 2 ? HDELAY : HDELAY2);
      const f32x4 hv = hrow(false), sm = sums(hv);
      u32x4 v = split8(hv, (lane & 1) != 0);
      if (lane >= 40) v = u32x4{nz_bits(sm[0]), nz_bits(sm[1]), nz_bits(sm[2]), nz_bits(sm[3])};
      if (lane < 48) st128(rs, WS_H + (hb * 4 + tw) * 192 + lane * 4, v);
      done_all();
      const unsigned pl = pull();
      asm volatile("" :: "v"(pl));
      return;
    }
    done_all();
#pragma unroll
    for (int i = 2 * GE; i < 10; ++i) { wp[i] = wp[0] + ((i >> 1) * 32 + (i & 1) * 2) * (FF / 8); asm volatile("" : "+v"(wp[i])); }
    done(NS);
    if (GW) until(clk(), GW);
#pragma unroll
    for (int i = 4 * GE; i < 20; ++i) e[i] = wp[i >> 1][(i & 1) * (FF / 8)];
    u32x4 a[5], sm;
    const int hoff = (WS_H + (min(mr >> 2, NB - 2) * 4 + tw) * 192 + qd * 8 + (mr & 1) * 4) * 4;
#pragma unroll
    for (int s = 0; s < 5; ++s) a[s] = __builtin_amdgcn_raw_buffer_load_b128(rs, hoff + s * 128, 0, AUX_SC1);
    sm = ld128(rs, WS_H + (min(lane, NB - 2) * 4 + tw) * 192 + 160);
    MEMFENCE();
#pragma unroll
    for (int s = GE; s < 5; ++s) frag(s);
    {
#pragma unroll 1
      for (unsigned spin = 0; spin < SPIN_CAP; ++spin) {
        unsigned mn = min4(sm);
#pragma unroll
        for (int s = 0; s < 5; ++s) mn = min(mn, min4(a[s]));
        if (__all(mn != 0u)) break;
        __builtin_amdgcn_s_sleep(2);
#pragma unroll
        for (int s = 0; s < 5; ++s) a[s] = __builtin_amdgcn_raw_buffer_load_b128(rs, hoff + s * 128, 0, AUX_SC1);
        sm = ld128(rs, WS_H + (min(lane, NB - 2) * 4 + tw) * 192 + 160);
      }
      if (lane < NB - 1) rst[lane * 4 + tw] = __builtin_bit_cast(f32x4, sm);
#pragma unroll
      for (int s = 0; s < 5; ++s) {
        if (mr & 2) a[s] = u32x4{0x3f803f80u, 0x3f803f80u, 0x3f803f80u, 0x3f803f80u};
        asm volatile("" : "+v"(a[s]));
      }
    }
    const f32x4 hv = hrow(true);
    if (lane < 40) {
      u32x2 hi, lw;
      split4(hv, hi, lw);
      *reinterpret_cast<u32x2*>(reg + lane * 8) = hi;
      *reinterpret_cast<u32x2*>(reg + AG_LD + lane * 8) = lw;
    }
    CFENCE();
    if ((mr & 14) == 12) {
#pragma unroll
      for (int s = 0; s < 5; ++s) a[s] = *reinterpret_cast<const u32x4*>(reg + (mr & 1) * AG_LD + s * 64 + qd * 16);
    }
    __builtin_amdgcn_sched_barrier(0);
    f32x4 c[4];
#pragma unroll
    for (int t = 0; t < 4; ++t) c[t] = f32x4{0.f, 0.f, 0.f, 0.f};
#pragma unroll
    for (int s = 0; s < 5; ++s)
#pragma unroll
      for (int t = 0; t < 4; ++t) c[t] = mfma16(a[s], bx[s * 4 + t], c[t]);
    { const f32x4 s3 = sums(hv); if (lane == 0) rst[(NB - 1) * 4 + tw] = s3; }
    const int slot = qd * 16 + (mr & 7) * 2 + (mr >> 3);
    *reinterpret_cast<f32x4*>(reg + 4096 + slot * 16) = f32x4{c[0][0] + c[0][1], c[1][0] + c[1][1], c[0][2], c[1][2]};
    *reinterpret_cast<f32x4*>(reg + 4096 + 1152 + slot * 16) = f32x4{c[2][0] + c[2][1], c[3][0] + c[3][1], c[2][2], c[3][2]};
    const int k = arrive(s_c + C_T);
    f32x4 ts[4], pg[4], pu[4];
#pragma unroll
    for (int w = 0; w < 4; ++w) {
      const unsigned char* src = smem + OFF_REG + w * REG + 4096 + (lane & 1) * 1152 + (eb * 16 + qd * 2 + ((lane >> 1) & 1)) * 16;
      ts[w] = rst[eb * 4 + w];
      pg[w] = *reinterpret_cast<const f32x4*>(src); pu[w] = *reinterpret_cast<const f32x4*>(src + 128);
    }
    PIN4(ts, 0); PIN4(pg, 0); PIN4(pu, 0);
    if (arrived(k) == 3) {
      const f32x4 t = (ts[0] + ts[1]) + (ts[2] + ts[3]), g4 = (pg[0] + pg[1]) + (pg[2] + pg[3]), u4 = (pu[0] + pu[1]) + (pu[2] + pu[3]);
      const float d = t[0] * (-1.0f / DM);
      const float rstd = __builtin_amdgcn_rsqf(fmaxf(t[1] + d * t[0], 0.f) * (1.0f / DM) + LN_EPS);
      const float g0 = rstd * (g4[0] + d * g4[2]), g1 = rstd * (g4[1] + d * g4[3]);
      const float a0 = g0 * __builtin_amdgcn_rcpf(1.0f + __expf(-g0)) * (rstd * (u4[0] + d * u4[2]));
      const float a1 = g1 * __builtin_amdgcn_rcpf(1.0f + __expf(-g1)) * (rstd * (u4[1] + d * u4[3]));
      const __hip_bfloat162 a2 = __halves2bfloat162(__float2bfloat16(a0), __float2bfloat16(a1));
      const float aw = __uint_as_float(max(*reinterpret_cast<const unsigned*>(&a2), 1u));
      const u32x4 av = {__float_as_uint(aw), __float_as_uint(dpp<0x55>(aw)), __float_as_uint(dpp<0xAA>(aw)), __float_as_uint(dpp<0xFF>(aw))};
      const float rr = rstd * (t[2] + d * t[3]);
      const u32x4 sv = {nz_bits(cq - d), nz_bits(rstd), nz_bits(__builtin_amdgcn_rcpf(1.0f + __expf(-rr))), 0x3f800000u};
      const bool stat = g_cg == 0 && (lane & 51) == 1;
      if ((lane & 3) == 0 || stat) st128(rs, stat ? WS_ST + eb * 4 : WS_ACT + g_cg * 64 + lane, stat ? sv : av);
    }
  } else if (isD) {
    const uint2 xd = *reinterpret_cast<const uint2*>(x + row_of(tw) * DM + d_ct * 32 + j * 4);
    const __amdgpu_buffer_rsrc_t rd = __builtin_amdgcn_make_buffer_rsrc(const_cast<bf16*>(wd), 0, FF * DM * 2, 0x00020000);
    prep(false, NRD);
    lds_wait<8>(s_c + F_REL, 1u);
    done_all();
    const unsigned pl = pull();
    done(NS);
    asm volatile("" :: "v"(pl));
    const unsigned te = clk();
    if (DW) until(te, DW);
    u32x4 bd[24];
    {
      const int off = ((tw * 384 + (mr >> 3) * 32 + qd * 8 + (mr & 4)) * DM + d_ct * 32 + (mr & 3) * 8) * 2;
      u32x4 e[24];
#pragma unroll
      for (int i = 0; i < 24; ++i) e[i] = __builtin_amdgcn_raw_buffer_load_b128(rd, off + (i & 3) * (DM * 2), (i >> 2) * 64 * (DM * 2), 0);
      MEMFENCE();
#pragma unroll
      for (int s = 0; s < 6; ++s) {
        unsigned w[8][2];
#pragma unroll
        for (int i = 0; i < 4; ++i)
#pragma unroll
          for (int d = 0; d < 2; ++d) {
            w[i][d] = __builtin_amdgcn_update_dpp(e[s * 4 + i][d], e[s * 4 + i][2 + d], 0x114, 0xf, 0xa, false);
            w[4 + i][d] = __builtin_amdgcn_update_dpp(e[s * 4 + i][2 + d], e[s * 4 + i][d], 0x104, 0xf, 0x5, false);
          }
#pragma unroll
        for (int k = 0; k < 4; ++k) {
          bd[s * 4 + 0][k] = PERM_LO(w[2 * k + 1][0], w[2 * k][0]); bd[s * 4 + 1][k] = PERM_HI(w[2 * k + 1][0], w[2 * k][0]);
          bd[s * 4 + 2][k] = PERM_LO(w[2 * k + 1][1], w[2 * k][1]); bd[s * 4 + 3][k] = PERM_HI(w[2 * k + 1][1], w[2 * k][1]);
        }
        PIN4(bd, s * 4);
      }
    }
    until(te, DLATE);
    {
      u32x4 sl;
      unsigned spin = 0;
      const int wl = min(lane, 19), watch = WS_SLAB + ((tw * 2 + wl / 10) * NB + NB - 1) * DM + (wl % 10) * 64;
#pragma unroll 1
      for (; spin < SPIN_CAP; ++spin) {
        sl = ld128(rs, WS_SLAB + (r * NB + tw) * DM + d_ct * 32 + j * 4);
        const unsigned wv = ld32(rs, watch);
        if (__all(min(min4(sl), wv) != 0u)) break;
        __builtin_amdgcn_s_sleep(1);
      }
      if (lane == 0) __hip_atomic_fetch_max(s_c + C_W + 1, clk() - t0 + (spin ? DLATE2 : 0), __ATOMIC_RELAXED, __HIP_MEMORY_SCOPE_WORKGROUP);
      f32x4 hv = {bf_lo(xd.x), bf_hi(xd.x), bf_lo(xd.y), bf_hi(xd.y)};
#pragma unroll
      for (int i = 0; i < 4; ++i) hv[i] += sum_r(__uint_as_float(sl[i]));
      if (lane < 8) reinterpret_cast<f32x4*>(red + 512 + tw * 32)[lane] = hv;
      LGKM0();
      lds_add(s_c + C_W, lane);
      lds_wait<1>(s_c + C_W, 4u);
    }
    const int col = (mr & 3) * 8 + (mr & 4) + (mr >> 3) * 2;
    f32x2 hv = *reinterpret_cast<const f32x2*>(red + 512 + qd * 32 + col);
    asm volatile("" : "+v"(hv));
    u32x4 a[6], st;
#pragma unroll
    for (int s = 0; s < 6; ++s) a[s] = u32x4{1u, 1u, 1u, 1u};
    const int aoff = (WS_ACT + tw * 12 * 64 + ((mr >> 1) & 1) * 64 + qd * 16 + (mr >> 2) * 4) * 4;
    until(t0, lds_get(s_c + C_W + 1));
#pragma unroll 1
    for (unsigned spin = 0; spin < SPIN_CAP; ++spin) {
      st = ld128(rs, WS_ST + qd * 4);
      if ((mr & 1) == 0) {
#pragma unroll
        for (int s = 0; s < 6; ++s) a[s] = __builtin_amdgcn_raw_buffer_load_b128(rs, aoff + s * 512, 0, AUX_SC1);
      }
      unsigned mn = min4(st);
#pragma unroll
      for (int s = 0; s < 6; ++s) mn = min(mn, min4(a[s]));
      if (__all(mn != 0u)) break;
      __builtin_amdgcn_s_sleep(1);
    }
    f32x4 c[4];
#pragma unroll
    for (int t = 0; t < 4; ++t) c[t] = f32x4{0.f, 0.f, 0.f, 0.f};
#pragma unroll
    for (int s = 0; s < 6; ++s)
#pragma unroll
      for (int t = 0; t < 4; ++t) c[t] = mfma16(a[s], bd[s * 4 + t], c[t]);
    const f32x4 sf = __builtin_bit_cast(f32x4, st);
    hv += (hv - sf[0]) * sf[1];
    const bool up = mr >= 8;
    const float k0 = up ? c[2][2] : c[0][0], k1 = up ? c[3][2] : c[1][0], o0 = up ? c[0][2] : c[2][0], o1 = up ? c[1][2] : c[3][0];
    f32x2* r2 = reinterpret_cast<f32x2*>(red);
    r2[tw * 64 + lane] = f32x2{k0 + dpp<DPP_ROR8>(o0), k1 + dpp<DPP_ROR8>(o1)};
    const int k = arrive(s_c + C_T);
    f32x2 y = hv + ((r2[lane] + r2[64 + lane]) + (r2[128 + lane] + r2[192 + lane])) * sf[2];
    asm volatile("" : "+v"(y));
    if (arrived(k) == 3) {
      const __hip_bfloat162 o2 = __halves2bfloat162(__float2bfloat16(y[0]), __float2bfloat16(y[1]));
      __hip_atomic_store(reinterpret_cast<unsigned*>(out + row_of(qd) * DM + d_ct * 32 + col), *reinterpret_cast<const unsigned*>(&o2), __ATOMIC_RELAXED, __HIP_MEMORY_SCOPE_AGENT);
    }
  } else {
    prep(bid == LEADER, NRD);
    if (isC && tw == 1) {
      lds_wait<8>(s_c + F_REL, 1u);
      const int cs = c_it != 1 ? c_it * 2 : max(g >> 3, 1), cnt = c_it != 1 ? 8 : (g < 16 ? 16 : 32), x0 = c_it != 1 ? g * 8 : (g < 16 ? g * 16 : (g & 7) * 32);
      done(c_it < 2 ? 4 : NA);
      pause(lds_get(s_c + S_BURST + (c_it >> 1)), WDELAY);
      const float wk = weight(cs, x0, cnt);
      f32x4 o = {0.f, 0.f, 0.f, 0.f};
#pragma unroll 1
      for (int k0 = 0; k0 < cnt; k0 += 16) {
        u32x2 pv[16];
#pragma unroll
        for (int k = 0; k < 16; ++k) pv[k] = __builtin_amdgcn_raw_buffer_load_b64(rs, lp * 2, (WS_PQ + ((x0 + ((k0 + k) & (cnt - 1))) * 5 + cs) * (DH / 2)) * 4, AUX_EARLY);
#pragma unroll
        for (int k = 0; k < 16; ++k) {
          const float w = k0 + k < cnt ? __uint_as_float(__builtin_amdgcn_readlane(__float_as_uint(wk), k0 + k)) : 0.f;
          o += w * f32x4{(float)(short)pv[k][0], (float)((int)pv[k][0] >> 16), (float)(short)pv[k][1], (float)((int)pv[k][1] >> 16)};
        }
      }
      u32x2 hi, lw;
      split4(o, hi, lw);
      if (lane < DH / 4) st128(rs, WS_OC + (c_it * NG + g) * DH + lane * 4, u32x4{max(hi[0], 1u), max(hi[1], 1u), max(lw[0], 1u), max(lw[1], 1u)});
    }
    done_all();
    const unsigned pl = pull();
    asm volatile("" :: "v"(pl));
  }
  if (tw == 3) done_all();
}

extern "C" void kernel_launch(void* const* d_in, const int* in_sizes, int n_in, void* d_out, int out_size, void* d_ws,
                              size_t ws_size, hipStream_t stream) {
  const bf16* x = (const bf16*)d_in[0];
  const bf16* kc = (const bf16*)d_in[1];
  const bf16* vc = (const bf16*)d_in[2];
  const bf16* wq = (const bf16*)d_in[3];
  const bf16* wo = (const bf16*)d_in[4];
  const bf16* wr = (const bf16*)d_in[6];
  const bf16* wg = (const bf16*)d_in[7];
  const bf16* wu = (const bf16*)d_in[8];
  const bf16* wd = (const bf16*)d_in[9];
  fused_kernel<<<NWG, NW * 64, 0, stream>>>(x, kc, vc, wq, wo, wr, wg, wu, wd, (float*)d_ws, (bf16*)d_out);
}
```

```cpp
#include <hip/hip_runtime.h>
#include <hip/hip_bf16.h>
#include <stdint.h>

typedef __hip_bfloat16 bf16;
using u32x2 = __attribute__((ext_vector_type(2))) unsigned;
using u32x4 = __attribute__((ext_vector_type(4))) unsigned;
using f32x4 = __attribute__((ext_vector_type(4))) float;
using f32x2 = __attribute__((ext_vector_type(2))) float;
using bf16x8 = __attribute__((ext_vector_type(8))) __bf16;

constexpr int NB = 4, DM = 640, NH = 32, SK = 8192, DH = 192, HD = NH * DH, FF = 1536;
constexpr float LN_EPS = 1e-5f;
constexpr float QSCALE = 0.07216878364870322f * 1.4426950408889634f;

constexpr int NWG = 256;
constexpr int GS = 8, NG = NWG / GS, NI = GS / 2;
constexpr int HPG = NI / NB;
constexpr int PPI = 512 / GS;
constexpr int NPAIR = NI * PPI;

constexpr int TAUK = 1320, TAUV = 1332;
constexpr int TSTART = 110;
constexpr int WGAP = 165, WP0 = 64, WPN = 176;
constexpr int VGAP = 50;
constexpr int DEEP1 = 100, DEEP2 = 200, SDEEP = 0, COARSE = 1, LAGOFF = 20, ENDT = 12, ENDS = 2;
constexpr int SPRINT = 0;
constexpr int JUMP = 145;
constexpr int HST = 2800, RT0 = 3900, RTS = 1000, RA1 = 220, RA2 = 500, LEADER = 24, WL = 0;
constexpr unsigned NRD = 5, RMIN = 30, FBP = 60, FBL = 150;
constexpr int W0START = 1700, W0SPAN = 1000, W1START = 2700, W1SPAN = 2600;
constexpr int ROT = 16, ROWS = 0xC9;
constexpr int FBM = 100, FBX = 15, FB0 = 170, FB1 = 10, FBS = 10;
constexpr int FBD = 10, HOLD = 4000, GB = 24, GBS = 2, GBX = 60, GBF = 12;
constexpr unsigned PRD = 3; constexpr int PRB = 24;
constexpr int ANCH = 110;
constexpr int TLEAD = 10, PFD = 6, WFD = 3;
constexpr int QWAIT = 750, QBACK = 100;
constexpr int WDELAY = 100, WDELAY2 = 460, DLATE = 100, DLATE2 = 85;
constexpr int CPF = 1, CPT = 0, CODE = 24 * 64 * 48;
constexpr int GLEAD = 1, GCNT = 4, DP = 0, DPW = 60, GE = 0, PW = 30, PS = 0, HDELAY = 1150, HDELAY2 = 780, GW = 0, DW = 0;

constexpr int KS = 8;
constexpr int WS_PML = 0;
constexpr int WS_PO = WS_PML + NWG * 64;
constexpr int WS_PQ = WS_PO + NWG * DH;
constexpr int WS_OC = WS_PQ + NWG * 5 * (DH / 2);
constexpr int WS_SLAB = WS_OC + (NB - 1) * NH * DH;
constexpr int WS_ACT = WS_SLAB + KS * NB * DM;
constexpr int WS_H = WS_ACT + NB * FF / 2;
constexpr int WS_ST = WS_H + (NB - 1) * 4 * 192;
constexpr int WS_HN = WS_ST + 32;
constexpr int WS_HR = WS_HN + NWG;
constexpr int WS_HA = WS_HR + 2 * NWG;
constexpr int QFS = 2 * GS * 32;
constexpr int WS_QF = WS_HA + 32 * NWG;
constexpr int WS_QP = WS_QF + NG * QFS;
constexpr int WS_END = WS_QP + NH * 2 * NB * DH;
constexpr int NPROD = HPG * 6;
static_assert(WS_PO % 32 == 0 && WS_SLAB % 32 == 0 && WS_ACT % 32 == 0 && WS_ST % 32 == 0 && WS_HN % 32 == 0 && WS_QF % 32 == 0 && WS_QP % 32 == 0, "line alignment");


__device__ __forceinline__ int row_of(int b) { return (ROWS >> (2 * b)) & 3; }
__device__ __forceinline__ float bf_lo(unsigned u) { return __uint_as_float(u << 16); }
__device__ __forceinline__ float bf_hi(unsigned u) { return __uint_as_float(u & 0xffff0000u); }
__device__ __forceinline__ f32x2 bf_pair(unsigned u) { return f32x2{bf_lo(u), bf_hi(u)}; }
__device__ __forceinline__ f32x2 fma2(float a, f32x2 b, f32x2 c) { return __builtin_elementwise_fma(f32x2{a, a}, b, c); }
__device__ __forceinline__ f32x2 fma2(f32x2 a, f32x2 b, f32x2 c) { return __builtin_elementwise_fma(a, b, c); }

template <int CTRL> __device__ __forceinline__ float dpp(float x) {
  return __builtin_bit_cast(float, __builtin_amdgcn_mov_dpp(__builtin_bit_cast(int, x), CTRL, 0xf, 0xf, true));
}
constexpr int DPP_XOR1 = 0xB1, DPP_XOR2 = 0x4E, DPP_HMIRROR = 0x141, DPP_MIRROR = 0x140, DPP_ROR8 = 0x128;

__device__ __forceinline__ float sum_x16_32(float x) {
  auto s = __builtin_amdgcn_permlane16_swap(__float_as_uint(x), __float_as_uint(x), false, false);
  x = __uint_as_float(s[0]) + __uint_as_float(s[1]);
  auto t = __builtin_amdgcn_permlane32_swap(__float_as_uint(x), __float_as_uint(x), false, false);
  return __uint_as_float(t[0]) + __uint_as_float(t[1]);
}
__device__ __forceinline__ float max_x16_32(float x) {
  auto s = __builtin_amdgcn_permlane16_swap(__float_as_uint(x), __float_as_uint(x), false, false);
  x = fmaxf(__uint_as_float(s[0]), __uint_as_float(s[1]));
  auto t = __builtin_amdgcn_permlane32_swap(__float_as_uint(x), __float_as_uint(x), false, false);
  return fmaxf(__uint_as_float(t[0]), __uint_as_float(t[1]));
}
__device__ __forceinline__ float tsum32(float a, float b) {
  auto t = __builtin_amdgcn_permlane32_swap(__float_as_uint(a), __float_as_uint(b), false, false);
  return __uint_as_float(t[0]) + __uint_as_float(t[1]);
}
__device__ __forceinline__ float tsum16(float a, float b) {
  auto t = __builtin_amdgcn_permlane16_swap(__float_as_uint(a), __float_as_uint(b), false, false);
  return __uint_as_float(t[0]) + __uint_as_float(t[1]);
}
__device__ __forceinline__ float sum_r(float v) { v += dpp<DPP_ROR8>(v); return sum_x16_32(v); }
__device__ __forceinline__ float row_sum(float v) {
  v += dpp<DPP_XOR1>(v); v += dpp<DPP_XOR2>(v); v += dpp<DPP_HMIRROR>(v);
  return v + dpp<DPP_MIRROR>(v);
}
__device__ __forceinline__ float row_max(float v) {
  v = fmaxf(v, dpp<DPP_XOR1>(v)); v = fmaxf(v, dpp<DPP_XOR2>(v)); v = fmaxf(v, dpp<DPP_HMIRROR>(v));
  return fmaxf(v, dpp<DPP_MIRROR>(v));
}
__device__ __forceinline__ float wave_sum(float v) {
  v += dpp<DPP_XOR1>(v); v += dpp<DPP_XOR2>(v); v += dpp<DPP_HMIRROR>(v);
  return sum_r(v);
}

__device__ __forceinline__ u32x4 ld_nt(const char* p) { return __builtin_nontemporal_load(reinterpret_cast<const u32x4*>(p)); }

struct Tile { u32x4 d[3]; };

#define PIN4(a, o) asm volatile("" : "+v"((a)[(o) + 0]), "+v"((a)[(o) + 1]), "+v"((a)[(o) + 2]), "+v"((a)[(o) + 3]))
#define MEMFENCE() do { asm volatile("" ::: "memory"); __builtin_amdgcn_sched_barrier(0); } while (0)

__device__ __forceinline__ void glds16_nt(const void* gsrc, unsigned lds_dst) {
  unsigned keep;
  asm volatile("s_mov_b32 %0, m0\n\ts_mov_b32 m0, %2\n\ts_nop 0\n\tglobal_load_lds_dwordx4 %1, off nt\n\ts_mov_b32 m0, %0"
               : "=&s"(keep) : "v"(gsrc), "s"(lds_dst) : "memory");
}

using bf16x2 = __attribute__((ext_vector_type(2))) __bf16;
__device__ __forceinline__ float dot2(unsigned a, unsigned b, float c) { return __builtin_amdgcn_fdot2_f32_bf16(__builtin_bit_cast(bf16x2, a), __builtin_bit_cast(bf16x2, b), c, false); }
__device__ __forceinline__ float tile_score(const Tile& k, const u32x2 (&q)[12]) {
  float s0 = 0.f, s1 = 0.f;
#pragma unroll
  for (int i = 0; i < 3; ++i) {
    u32x4 v = k.d[i];
    asm("" : "+v"(v));
#pragma unroll
    for (int d = 0; d < 4; ++d) { s0 = dot2(v[d], q[i * 4 + d][0], s0); s1 = dot2(v[d], q[i * 4 + d][1], s1); }
  }
  return s0 + s1;
}
__device__ __forceinline__ void pair_put(float a, float b, float* dst, int j) {
  float s = (j < 4 ? a : b) + dpp<DPP_HMIRROR>(j < 4 ? b : a);
  s += dpp<DPP_XOR1>(s);
  s += dpp<DPP_XOR2>(s);
  if ((j & 3) == 0) dst[j * 2] = s;
}
__device__ __forceinline__ void score_put(float s, float* dst, int j) {
  s += dpp<DPP_XOR1>(s);
  s += dpp<DPP_XOR2>(s);
  s += dpp<DPP_HMIRROR>(s);
  if (j == 0) *dst = s;
}
__device__ __forceinline__ void tile_load(Tile& t, const char* base, unsigned off, int half) {
#pragma unroll
  for (int i = 0; i < 3; ++i) t.d[i] = ld_nt(base + off + half * 3072 + i * 128);
}
__device__ __forceinline__ void pair_issue(Tile& ta, Tile& tb, const char* base, unsigned off, int deep) {
  deep = __builtin_amdgcn_readfirstlane(deep);
#define WAIT_IF(level, n) asm volatile("s_cmp_lg_u32 %0, " #level "\n\ts_cbranch_scc1 .Lpi%=\n\ts_waitcnt vmcnt(" #n ")\n.Lpi%=:" :: "s"(deep) : "scc", "memory")
  const char* p = base + off;
  if constexpr (COARSE) {
    WAIT_IF(0, 3);
    ta.d[0] = ld_nt(p); ta.d[1] = ld_nt(p + 128); ta.d[2] = ld_nt(p + 256); MEMFENCE();
    WAIT_IF(0, 3); WAIT_IF(1, 6);
    tb.d[0] = ld_nt(p + 3072); tb.d[1] = ld_nt(p + 3200); tb.d[2] = ld_nt(p + 3328); MEMFENCE();
  } else {
    WAIT_IF(0, 5); ta.d[0] = ld_nt(p); MEMFENCE();
    WAIT_IF(0, 5); ta.d[1] = ld_nt(p + 128); MEMFENCE();
    WAIT_IF(0, 5); ta.d[2] = ld_nt(p + 256); MEMFENCE();
    WAIT_IF(1, 6);
    WAIT_IF(0, 5); tb.d[0] = ld_nt(p + 3072); MEMFENCE();
    WAIT_IF(0, 5); tb.d[1] = ld_nt(p + 3200); MEMFENCE();
    WAIT_IF(0, 5); tb.d[2] = ld_nt(p + 3328); MEMFENCE();
  }
#undef WAIT_IF
}
__device__ __forceinline__ void pv_last(float p, Tile& v, f32x2 (&acc)[12]) {
#pragma unroll
  for (int i = 0; i < 3; ++i) {
    asm volatile("" : "+v"(v.d[i]), "+v"(p));
#pragma unroll
    for (int d = 0; d < 4; ++d) acc[i * 4 + d] = fma2(p, bf_pair(v.d[i][d]), acc[i * 4 + d]);
    PIN4(acc, i * 4);
  }
}
__device__ __forceinline__ void q_load(u32x2 (&q)[12], const float* src, int j) {
#pragma unroll
  for (int i = 0; i < 6; ++i) {
    const u32x4 a = *reinterpret_cast<const u32x4*>(src + (i >> 1) * 64 + j * 8 + (i & 1) * 4);
    q[i * 2] = u32x2{a[0], a[1]}; q[i * 2 + 1] = u32x2{a[2], a[3]};
  }
}
#define VMCNT(n) asm volatile("s_waitcnt vmcnt(%0)" :: "n"(n) : "memory")

constexpr int AUX_SC1 = 16, AUX_EARLY = 18;
constexpr unsigned SPIN_CAP = 1u << 17;
__device__ __forceinline__ unsigned nz_bits(float v) { return max(__float_as_uint(v), 1u); }
__device__ __forceinline__ bool all_nz(u32x4 v) { return v[0] != 0u && v[1] != 0u && v[2] != 0u && v[3] != 0u; }
__device__ __forceinline__ unsigned min4(u32x4 v) { return min(min(v[0], v[1]), min(v[2], v[3])); }
__device__ __forceinline__ unsigned ld32(__amdgpu_buffer_rsrc_t rs, int word) { return __builtin_amdgcn_raw_buffer_load_b32(rs, word * 4, 0, AUX_SC1); }
__device__ __forceinline__ u32x2 ld64(__amdgpu_buffer_rsrc_t rs, int word) { return __builtin_amdgcn_raw_buffer_load_b64(rs, word * 4, 0, AUX_SC1); }
__device__ __forceinline__ u32x4 ld128(__amdgpu_buffer_rsrc_t rs, int word) { return __builtin_amdgcn_raw_buffer_load_b128(rs, word * 4, 0, AUX_SC1); }
__device__ __forceinline__ void st32(__amdgpu_buffer_rsrc_t rs, int word, unsigned v) { __builtin_amdgcn_raw_buffer_store_b32(v, rs, word * 4, 0, AUX_SC1); }
__device__ __forceinline__ void st64(__amdgpu_buffer_rsrc_t rs, int word, u32x2 v) { __builtin_amdgcn_raw_buffer_store_b64(v, rs, word * 4, 0, AUX_SC1); }
__device__ __forceinline__ void st128(__amdgpu_buffer_rsrc_t rs, int word, u32x4 v) { __builtin_amdgcn_raw_buffer_store_b128(v, rs, word * 4, 0, AUX_SC1); }

__device__ __forceinline__ u32x4 ld128nt(__amdgpu_buffer_rsrc_t rs, int word) { return __builtin_amdgcn_raw_buffer_load_b128(rs, word * 4, 0, AUX_EARLY); }
__device__ __forceinline__ void st128nt(__amdgpu_buffer_rsrc_t rs, int word, u32x4 v) { __builtin_amdgcn_raw_buffer_store_b128(v, rs, word * 4, 0, AUX_EARLY); }

__device__ __forceinline__ bool leader_release(__amdgpu_buffer_rsrc_t rs, int lane, unsigned t0, unsigned rd, int slow, int& prev) {
  u32x4 a[4];
#pragma unroll
  for (int k = 0; k < 4; ++k) a[k] = ld128(rs, WS_HA + (lane * 4 + k) * 32);
  const u32x4 n = ld128(rs, WS_HN + lane * 4);
  if (rd == 0u && !__all(a[0][0] == n[0] && a[1][0] == n[1] && a[2][0] == n[2] && a[3][0] == n[3])) return false;
  const float e = fmaxf(fmaxf((float)(int)(t0 - a[0][2]), (float)(int)(t0 - a[1][2])), fmaxf((float)(int)(t0 - a[2][2]), (float)(int)(t0 - a[3][2])));
  unsigned first = t0 - (unsigned)(int)max_x16_32(row_max(e));
  float ls = 0.f, lx = 0.f;
#pragma unroll
  for (int k = 0; k < 4; ++k) {
    const float l = a[k][0] == n[k] + rd ? (float)min(a[k][3] + (rd ? 0u : min(a[k][2] - first, 300u)), 2000u) : 0.f;
    ls += l; lx = fmaxf(lx, l);
  }
  ls = wave_sum(ls) * (1.0f / NWG); lx = max_x16_32(row_max(lx));
  const int extra = __builtin_amdgcn_readfirstlane(min(max((int)(ls * (FBM / 100.0f) + lx * (FBX / 100.0f)) - (rd ? FB1 + (int)(NRD - rd) * FBD : FB0), 0), 600));
  if (rd != 0u && __all(ls * NWG < HOLD)) { prev = 0; return true; }
  first = (first & 0xffffu) | ((unsigned)(slow + extra + min(extra, prev) * (int)(rd == NRD ? FBL : FBP) / 100) << 16);
  prev = extra;
  st128(rs, WS_HR + lane * 8, u32x4{a[0][1] + rd, first, a[1][1] + rd, first});
  st128(rs, WS_HR + lane * 8 + 4, u32x4{a[2][1] + rd, first, a[3][1] + rd, first});
  return true;
}

typedef __attribute__((address_space(3))) unsigned lds_u32;
__device__ __forceinline__ unsigned lds_add(lds_u32* p, int lane) {
  unsigned k = 0;
  if (lane == 0) k = __hip_atomic_fetch_add(p, 1u, __ATOMIC_RELAXED, __HIP_MEMORY_SCOPE_WORKGROUP);
  return __builtin_amdgcn_readfirstlane(k);
}
#define CFENCE() asm volatile("" ::: "memory")
__device__ __forceinline__ int arrive(lds_u32* p) {
  int v;
  unsigned long long keep;
  asm volatile("s_mov_b64 %1, exec\n\ts_mov_b64 exec, 1\n\tds_add_rtn_u32 %0, %2, %3\n\ts_mov_b64 exec, %1"
               : "=&v"(v), "=&s"(keep) : "v"((unsigned)(size_t)p), "v"(1u) : "memory");
  return v;
}
__device__ __forceinline__ int arrived(int v) {
  asm volatile("s_waitcnt lgkmcnt(0)" : "+v"(v) :: "memory");
  return __builtin_amdgcn_readfirstlane(v);
}
__device__ __forceinline__ unsigned lds_get(lds_u32* p) {
  return __builtin_amdgcn_readfirstlane(__hip_atomic_load(p, __ATOMIC_RELAXED, __HIP_MEMORY_SCOPE_WORKGROUP));
}
template <int SLEEP> __device__ __forceinline__ void lds_wait(lds_u32* p, unsigned n) {
#pragma unroll 1
  for (unsigned spin = 0; spin < (SPIN_CAP << 4); ++spin) {
    if (lds_get(p) >= n) break;
    __builtin_amdgcn_s_sleep(SLEEP);
  }
  asm volatile("" ::: "memory");
}
__device__ __forceinline__ unsigned clk() { return (unsigned)__builtin_amdgcn_s_memrealtime(); }
struct Table {
  unsigned t0; int was, moved, slowed, folded;
  u32x2 told;
  __device__ __forceinline__ int gate(int due, lds_u32* mine, bool hold = true, int sh = 0) {
    const int fold = __builtin_amdgcn_readfirstlane(told[1]);
    if (fold != folded) { const int f = min(fold - folded, moved); moved -= f; slowed += f; folded = fold; *mine = moved; }
    const int left = (int)__builtin_amdgcn_readfirstlane(told[0]) - slowed, d = max(min(left, FBS), left < -GBX ? -GBF : -GBS);
    t0 += d; was -= d; slowed += d;
    int late = (int)(clk() - t0) - due;
    if (late - was > JUMP) { t0 += late - was; moved += late - was; late = was; *mine = moved; }
    const int lag = max(late - max(left - d, 0) - LAGOFF, 0);
    mine[4] = lag;
    was = late;
#pragma unroll 1
    for (int spin = 0; spin < 65536 && hold && late < 0; ++spin) { __builtin_amdgcn_s_sleep(1); late = (int)(clk() - t0) - due; }
    return max(lag < (DEEP1 >> sh) ? 0 : (lag < (DEEP2 >> sh) ? 1 : 2), hold ? 0 : SDEEP);
  }
  __device__ __forceinline__ void listen(lds_u32* slow) {
    told = __builtin_bit_cast(u32x2, __hip_atomic_load((__attribute__((address_space(3))) unsigned long long*)slow, __ATOMIC_RELAXED, __HIP_MEMORY_SCOPE_WORKGROUP));
  }
  __device__ __forceinline__ void agree(lds_u32* all, lds_u32* mine) {
    if (moved == 0) return;
    const int m = (int)min(min(lds_get(all), lds_get(all + 1)), min(lds_get(all + 2), lds_get(all + 3))) + 30;
    if (m < moved) { t0 -= moved - m; moved = m; was = 1 << 20; *mine = moved; }
  }
};
__device__ __forceinline__ void until(unsigned t0, int due) {
#pragma unroll 1
  for (int spin = 0; spin < 65536 && (int)(clk() - t0) < due; ++spin) __builtin_amdgcn_s_sleep(1);
}
__device__ __forceinline__ void pause(unsigned t0, int due) {
#pragma unroll 1
  for (int spin = 0; spin < 4096 && (int)(clk() - t0) < due; ++spin) __builtin_amdgcn_s_sleep(16);
}
#define LGKM0() asm volatile("s_waitcnt lgkmcnt(0)" ::: "memory")

__device__ __forceinline__ void split4(f32x4 v, u32x2& hi, u32x2& lo) {
  unsigned h[4], r[4];
#pragma unroll
  for (int i = 0; i < 4; ++i) { h[i] = __float_as_uint(v[i]) & 0xffff0000u; r[i] = __float_as_uint(v[i] - __uint_as_float(h[i])); }
  hi = u32x2{__builtin_amdgcn_perm(h[1], h[0], 0x07060302u), __builtin_amdgcn_perm(h[3], h[2], 0x07060302u)};
  lo = u32x2{__builtin_amdgcn_perm(r[1], r[0], 0x07060302u), __builtin_amdgcn_perm(r[3], r[2], 0x07060302u)};
}
__device__ __forceinline__ u32x4 split8(f32x4 v, bool odd) {
  u32x2 hi, lo;
  split4(v, hi, lo);
  const unsigned n0 = __float_as_uint(dpp<DPP_XOR1>(__uint_as_float(odd ? hi[0] : lo[0]))), n1 = __float_as_uint(dpp<DPP_XOR1>(__uint_as_float(odd ? hi[1] : lo[1])));
  return u32x4{max(odd ? n0 : hi[0], 1u), max(odd ? n1 : hi[1], 1u), max(odd ? lo[0] : n0, 1u), max(odd ? lo[1] : n1, 1u)};
}
__device__ __forceinline__ u32x4 q_pack(u32x4 a, u32x4 b) {
  u32x2 hi, lo;
  split4((__builtin_bit_cast(f32x4, a) + __builtin_bit_cast(f32x4, b)) * QSCALE, hi, lo);
  return u32x4{hi[0], lo[0], hi[1], lo[1]};
}
__device__ __forceinline__ f32x4 mfma16(u32x4 a, u32x4 b, f32x4 c) {
  return __builtin_amdgcn_mfma_f32_16x16x32_bf16(__builtin_bit_cast(bf16x8, a), __builtin_bit_cast(bf16x8, b), c, 0, 0, 0);
}

constexpr int NW = 8, NMW = 4;
constexpr int NLP = 5, NRP = 8;
constexpr int NTP = 4 * (NLP + NRP);
constexpr int REG = NLP * 6 * 1024;
constexpr int NRING = 4;
constexpr int NS = 6, NA = NS - 1;
__device__ __forceinline__ int seg_beg(int s) { return s >= NS ? NPAIR : ((0xC87640 >> (4 * s)) & 15) * 16; }
__device__ __forceinline__ int seg_k(int t) { return (int)(0x5433222211110000ull >> (4 * (t >> 4))) & 15; }
__device__ __forceinline__ int seg_of_k(int k) { return (0x321540 >> (4 * k)) & 15; }
__device__ __forceinline__ int k_of_seg(int s) { return (0x215430 >> (4 * s)) & 15; }
__device__ __forceinline__ int pair_of(int t) { return t < 64 ? t : (t < 192 ? t + 64 : t - 128); }
constexpr int OFF_CNT = 0, OFF_REG = 256,
              OFF_SC = OFF_REG + 4 * REG, OFF_BUF = OFF_SC + NPAIR * 16 * 4, OFF_Q = OFF_BUF + NRING * NMW * DH * 4, OFF_ML = OFF_Q + NS * DH * 4,
              OFF_RED = OFF_ML + 128, OFF_RS = OFF_RED + 4 * 256 * 4, OFF_END = OFF_RS + 5 * 16;
enum { F_REL = 0, F_DONE, C_LAST, C_T = 3, F_OUT, C_P, C_K, F_P, F_Q = 8, F_FREE, S_FIRST, F_Q2, C_W = 12, C_SC = 16, C_PART = 24, S_ECHO = 32, S_BURST = 40, S_LATE = 42, S_MOVED = 48, S_SLOW = 56 };
__device__ __forceinline__ int due_k(int p) { return TSTART + ((p * TAUK + min(max(p - WP0, 0), WPN) * (WGAP * 64 / WPN)) >> 6); }
__device__ __forceinline__ int due_v(int p) { return TSTART + WGAP + ((NPAIR * TAUK + p * TAUV + max(p - NPAIR / 2, 0) * (VGAP * 64 / (NPAIR / 2))) >> 6); }
constexpr int AW_LD = DH * 2 + 16, AG_LD = 160 * 2 + 16;

__global__ void __launch_bounds__(NW * 64) fused_kernel(const bf16* __restrict__ x, const bf16* __restrict__ kc,
                                                        const bf16* __restrict__ vc, const bf16* __restrict__ wq,
                                                        const bf16* __restrict__ wo, const bf16* __restrict__ wr,
                                                        const bf16* __restrict__ wg, const bf16* __restrict__ wu,
                                                        const bf16* __restrict__ wd, float* ws, bf16* __restrict__ out) {
  __shared__ __attribute__((aligned(16))) unsigned char smem[OFF_END];
  float* s_sc = reinterpret_cast<float*>(smem + OFF_SC);
  float* s_buf = reinterpret_cast<float*>(smem + OFF_BUF);
  float* s_q = reinterpret_cast<float*>(smem + OFF_Q);
  u32x4* s_ml = reinterpret_cast<u32x4*>(smem + OFF_ML);
  lds_u32* s_c = (lds_u32*)(smem + OFF_CNT);

  const int tid = threadIdx.x, lane = tid & 63, r = lane >> 3, j = lane & 7;
  const int wave = __builtin_amdgcn_readfirstlane(tid >> 6);
  const int tw = wave - NMW;
  const int bid = blockIdx.x, xl = bid & 7, idx = bid >> 3;
  const int g = (xl * 32 + idx) / GS, mm = idx % GS;
  const __amdgpu_buffer_rsrc_t rs = __builtin_amdgcn_make_buffer_rsrc(ws, 0, WS_END * 4, 0x00020000);
  const int ro = ((idx & 3) << 3) | (idx >> 2);
  const bool isW = ro < 10, isG = ro >= 10 && ro < 16;
  const int w_ks = xl, w_ct = ro;
  const int g_cg = xl * 6 + (ro - 10);
  const int d_ct = (xl + ((ro - 16) >> 2) * 8) * 2 + (((ro - 16) >> 1) & 1);
  const bool isD = ro >= 16 && ro < 24 && (ro & 1) == 0 && d_ct < DM / 32;
  const bool isH = ro == 22 && xl >= 2 && xl < 5;
  const bool isC = (mm & 7) == 3 || (mm & 7) >= 6;
  const int c_it = (mm >> 3) * 3 + max((mm & 7) - 5, 0);
  const int qf = WS_QF + g * QFS;
  const char* kb = reinterpret_cast<const char*>(kc);
  const char* vb = reinterpret_cast<const char*>(vc);
  const unsigned lo = (r * 24 + j) * 16;
  const int xb = g * GS + mm, c0 = mm * 8 + g * ROT;
  const int b2 = (2 * NH + (xb >> 4)) * 1024 + (xb & 15) * 8, b3 = (2 * NH + 16 + (xb >> 5)) * 1024 + (xb & 31) * 8;
  auto poff = [&](int p) -> unsigned {
    const int turn = (p & 63) >> 2;
    int t = (row_of(p >> 6) * NH + g) * 1024 + ((turn * 64 + (p & 3) * 2 + c0) & 1023);
    if ((p >> 6) == 1) t = (turn < 8 ? b2 + turn * 128 : b3 + (turn & 4) * 2048 + (turn & 3) * 256) + (p & 3) * 2;
    return (unsigned)t * 3072u;
  };
  const unsigned lds_base = (unsigned)(size_t)(__attribute__((address_space(3))) unsigned char*)smem + OFF_REG + tw * REG;
  auto merged = [&](int it, int ln) -> f32x4 {
    const float* src = s_buf + (it % NRING) * (NMW * DH) + ((ln >> 1) & 7) * 24 + (ln >> 4) * 8 + (ln & 1) * 4;
    return (*reinterpret_cast<const f32x4*>(src) + *reinterpret_cast<const f32x4*>(src + DH)) +
           (*reinterpret_cast<const f32x4*>(src + 2 * DH) + *reinterpret_cast<const f32x4*>(src + 3 * DH));
  };
  auto publish = [&](int it) -> bool {
    const int k = arrive(s_c + C_PART + it);
    const f32x4 m = merged(it, lane < DH / 4 ? lane : 0);
    u32x4 o = {nz_bits(m[0]), nz_bits(m[1]), nz_bits(m[2]), nz_bits(m[3])};
    asm volatile("" : "+v"(o));
    if (arrived(k) != NMW - 1) return false;
    if (lane < DH / 4) st128(rs, WS_PO + xb * DH + lane * 4, o);
    if (lane == 0) __hip_atomic_fetch_add(s_c + F_DONE, 1u, __ATOMIC_RELAXED, __HIP_MEMORY_SCOPE_WORKGROUP);
    return true;
  };
#define NONCE() do { unsigned n_ = __builtin_amdgcn_readfirstlane(f) + 1u; while (__any(f == n_)) ++n_; \
    if (lane < 32) st32(rs, qf + (GS + mm) * 32 + lane, n_); f = n_; } while (0)
  unsigned long long pc;
  asm volatile("s_getpc_b64 %0" : "=s"(pc));
  unsigned r0 = 0u;
  unsigned t0 = clk();
  unsigned tb0 = t0;
  if (wave < NMW) {
    Tile ta, tb, tc, td;
    const int c = lane & (GS - 1);
    unsigned f = 0u;
    if (wave == 3) f = ld32(rs, qf + c * 32 + mm);
    if (mm < NPROD) {
      const int hp = g + (mm / 6) * NG, ck = (mm % 6) >> 1, hf = mm & 1, jp = hf * 4 + wave;
      const int bb = lane >> 4, t = lane & 15;
      const u32x4* xp = reinterpret_cast<const u32x4*>(x + row_of(bb) * DM);
      u32x4 xv[5], xs = {0u, 0u, 0u, 0u};
#pragma unroll
      for (int k = 0; k < 5; ++k) xv[k] = xp[t + 16 * k];
      if (t < 10) xs = xp[jp * 10 + t];
      const u32x4* wp = reinterpret_cast<const u32x4*>(wq + (size_t)(jp * 80 + r) * HD + hp * DH + ck * 64) + j;
      u32x4 wv[10];
#pragma unroll
      for (int k = 0; k < 10; ++k) wv[k] = __builtin_nontemporal_load(wp + (size_t)k * 8 * (HD / 8));
      if (wave == 0) s_c[lane] = lane == C_K ? NTP + NMW : 0u;
      MEMFENCE(); LGKM0(); __builtin_amdgcn_s_barrier(); MEMFENCE();
      if (wave == 3) { NONCE(); }
      float sm = 0.f;
#pragma unroll
      for (int k = 0; k < 5; ++k)
#pragma unroll
        for (int d = 0; d < 4; ++d) sm += bf_lo(xv[k][d]) + bf_hi(xv[k][d]);
      const float mean = row_sum(sm) * (1.0f / DM);
      float sv = 0.f;
#pragma unroll
      for (int k = 0; k < 5; ++k)
#pragma unroll
        for (int d = 0; d < 4; ++d) { const float a = bf_lo(xv[k][d]) - mean, c2 = bf_hi(xv[k][d]) - mean; sv += a * a + c2 * c2; }
      const float rstd = 1.0f / sqrtf(row_sum(sv) * (1.0f / DM) + LN_EPS);
      float* s_xn = s_buf + wave * 320;
      if (t < 10) {
#pragma unroll
        for (int d = 0; d < 4; ++d) {
          s_xn[(t * 8 + 2 * d) * 4 + bb] = (bf_lo(xs[d]) - mean) * rstd;
          s_xn[(t * 8 + 2 * d + 1) * 4 + bb] = (bf_hi(xs[d]) - mean) * rstd;
        }
      }
      LGKM0();
      f32x2 qa[16];
#pragma unroll
      for (int i = 0; i < 16; ++i) qa[i] = f32x2{0.f, 0.f};
#pragma unroll
      for (int k = 0; k < 10; ++k) {
        const f32x4 xn = *reinterpret_cast<const f32x4*>(&s_xn[(k * 8 + r) * 4]);
#pragma unroll
        for (int d = 0; d < 4; ++d) {
          const f32x2 w2 = bf_pair(wv[k][d]);
#pragma unroll
          for (int e = 0; e < 4; ++e) qa[e * 4 + d] = fma2(xn[e], w2, qa[e * 4 + d]);
        }
      }
      float t16[16], t8[8];
#pragma unroll
      for (int i = 0; i < 16; ++i) t16[i] = tsum32(qa[i >> 1][i & 1], qa[8 + (i >> 1)][i & 1]);
#pragma unroll
      for (int i = 0; i < 8; ++i) t8[i] = tsum16(t16[i], t16[i + 8]);
#pragma unroll
      for (int i = 0; i < 8; ++i) t8[i] += dpp<DPP_ROR8>(t8[i]);
      const bool up = (lane & 8) != 0;
      f32x4* r4 = reinterpret_cast<f32x4*>(smem + OFF_RED);
      r4[wave * 64 + lane] = f32x4{up ? t8[4] : t8[0], up ? t8[5] : t8[1], up ? t8[6] : t8[2], up ? t8[7] : t8[3]};
      LGKM0();
      if (lds_add(s_c + C_P, lane) == NMW - 1) {
        asm volatile("" ::: "memory");
        const f32x4 o4 = (r4[lane] + r4[64 + lane]) + (r4[128 + lane] + r4[192 + lane]);
        st128nt(rs, WS_QP + ((hp * 2 + hf) * NB + bb) * DH + ck * 64 + j * 8 + (up ? 4 : 0), __builtin_bit_cast(u32x4, o4));
        VMCNT(0);
        if (lane == 0) s_c[F_OUT] = 1u;
      }
    } else {
      if (wave == 0) s_c[lane] = lane == C_K ? NTP + NMW : 0u;
      MEMFENCE(); LGKM0(); __builtin_amdgcn_s_barrier(); MEMFENCE();
      if (wave == 3) { NONCE(); }
      else if (CPF && lane < CODE / (24 * 64)) {
        typedef __attribute__((address_space(1))) const unsigned g_u32;
        until(t0, CPT);
        const unsigned v = *(g_u32*)((pc & ~255ull) + (((idx >> 3) * 2 + (mm & 1)) * 3 + wave) * (CODE / 24) + lane * 64);
        asm volatile("" :: "v"(v));
      }
    }
    MEMFENCE();
    if (wave == 3) {
      __builtin_amdgcn_s_setprio(3);
      const unsigned nonce = f;
      unsigned echoed = 0u;
      if (mm < NPROD) {
        echoed = ld32(rs, qf + (GS + c) * 32);
        asm volatile("" : "+v"(echoed) :: "memory");
        lds_wait<1>(s_c + F_OUT, 1u);
        if (lane < 32) st32(rs, qf + mm * 32 + lane, echoed);
#pragma unroll 1
        while ((int)(clk() - t0) < QWAIT - QBACK) {
          const unsigned nv = ld32(rs, qf + (GS + c) * 32);
          if (__any(nv != echoed)) {
            if (lane < 32) st32(rs, qf + mm * 32 + lane, nv);
            echoed = nv;
          }
          __builtin_amdgcn_s_sleep(4);
        }
      }
      until(t0, QWAIT);
#pragma unroll 1
      for (unsigned spin = 0; spin < SPIN_CAP; ++spin) {
        const unsigned nv = ld32(rs, qf + (GS + c) * 32);
        f = ld32(rs, qf + c * 32 + mm);
        const bool done = __all(c == mm || c >= NPROD || f == nonce);
        if (mm < NPROD && __any(nv != echoed)) {
          if (lane < 32) st32(rs, qf + mm * 32 + lane, nv);
          echoed = nv;
        }
        if (done) break;
        __builtin_amdgcn_s_sleep(8);
      }
      if (lane < GS) s_c[S_ECHO + lane] = echoed;
      if (lane < DH / 4) {
        u32x4 p[3][2];
#pragma unroll
        for (int it = 0; it < 3; ++it)
#pragma unroll
          for (int k = 0; k < 2; ++k) p[it][k] = ld128nt(rs, WS_QP + ((g * 2 + k) * NB + (it ? it + 1 : 0)) * DH + lane * 4);
#pragma unroll
        for (int it = 0; it < 3; ++it) {
          *reinterpret_cast<u32x4*>(&s_q[(it ? it + 3 : 0) * DH + lane * 4]) = q_pack(p[it][0], p[it][1]);
          if (it != 1) { LGKM0(); if (lane == 0) s_c[F_Q] = it + 1; MEMFENCE(); }
        }
      }
    }
    __builtin_amdgcn_s_setprio(3);
    until(t0, due_k(NTP + wave));
    tb0 = lds_get(s_c + S_LATE);
    if ((unsigned)(tb0 - t0 + 100u) > 500u) tb0 = t0;
    until(tb0, due_k(NTP + wave));
    Table tt = {tb0, 1 << 20, 0, 0, 0, {0u, 0u}};
    bool common = false;
    tile_load(ta, kb, poff(NTP + wave) + lo, 0);
    tile_load(tb, kb, poff(NTP + wave) + lo, 1);
    MEMFENCE();
    {
      u32x2 q[12];
      int cur = NTP + wave, nxt = lds_add(s_c + C_K, lane), passed = 0, ci = -1;
      auto kstep = [&](Tile& xa, Tile& xb_, Tile& ya, Tile& yb) __attribute__((always_inline)) -> bool {
        const bool last = nxt >= NPAIR;
        const unsigned off = poff(last ? wave : pair_of(nxt)) + lo;
        const char* base = last ? vb : kb;
        const int it = seg_k(cur), cp = pair_of(cur);
        MEMFENCE();
        const int deep = tt.gate(due_k(last ? NPAIR + wave : nxt), s_c + S_MOVED + wave);
        MEMFENCE();
        pair_issue(ya, yb, base, off, deep);
        if (it != ci) {
          LGKM0();
          for (; passed < it; ++passed) lds_add(s_c + C_SC + passed, lane);
          lds_wait<1>(s_c + (it < 3 ? F_Q : F_Q2), min(it + 1, 3));
          q_load(q, s_q + seg_of_k(it) * DH, j);
          if (!common) { const unsigned d = lds_get(s_c + S_FIRST); if (d) { tt.t0 -= d - 1u; common = true; tt.was = 1 << 20; } }
          tt.agree(s_c + S_MOVED, s_c + S_MOVED + wave);
          ci = it;
        }
        const int tk = arrive(s_c + C_K);
        tt.listen(s_c + S_SLOW + 2 * wave);
        pair_put(tile_score(xa, q), tile_score(xb_, q), s_sc + cp * 16 + r, j);
        MEMFENCE();
        cur = nxt; nxt = arrived(tk);
        return last;
      };
      bool swap;
      for (;;) {
        if (kstep(ta, tb, tc, td)) { swap = true; break; }
        if (kstep(tc, td, ta, tb)) { swap = false; break; }
      }
      LGKM0();
      for (; passed < NS; ++passed) lds_add(s_c + C_SC + passed, lane);
      __builtin_amdgcn_s_waitcnt(0x0F70);
      if (swap) { ta = tc; tb = td; }
    }
#pragma unroll 1
    for (int it = 0; it < NS; ++it) {
      lds_wait<1>(s_c + F_P, k_of_seg(it) + 1);
      if (!common) { const unsigned d = lds_get(s_c + S_FIRST); if (d) { tt.t0 -= d - 1u; common = true; tt.was = 1 << 20; } }
      tt.agree(s_c + S_MOVED, s_c + S_MOVED + wave);
      f32x2 acc[12];
#pragma unroll
      for (int i = 0; i < 12; ++i) acc[i] = f32x2{0.f, 0.f};
      auto vstep = [&](Tile& xa, Tile& xb_, Tile& ya, Tile& yb, int p) __attribute__((always_inline)) {
        const unsigned off = poff(p + NMW) + lo;
        MEMFENCE();
        const int deep = tt.gate(due_v(p + NMW), s_c + S_MOVED + wave, p + NMW < NPAIR - SPRINT * NMW, p >= NPAIR - ENDT * NMW ? ENDS : 0);
        MEMFENCE();
        pair_issue(ya, yb, vb, off, deep);
        const float pa = s_sc[p * 16 + r], pb = s_sc[p * 16 + 8 + r];
        tt.listen(s_c + S_SLOW + 2 * wave);
        if (p + 2 * NMW >= NPAIR) { if (lane == 0) __hip_atomic_fetch_add(s_c + C_LAST, 1u, __ATOMIC_RELAXED, __HIP_MEMORY_SCOPE_WORKGROUP); }
        pv_last(pa, xa, acc);
        pv_last(pb, xb_, acc);
        MEMFENCE();
      };
#pragma unroll 1
      for (int p = seg_beg(it) + wave; p < min(seg_beg(it + 1), NPAIR - 2 * NMW); p += 2 * NMW) {
        vstep(ta, tb, tc, td, p);
        vstep(tc, td, ta, tb, p + NMW);
      }
      if (it >= NRING) lds_wait<1>(s_c + F_FREE, it - NRING + 1);
      if (it == NS - 1) {
        vstep(ta, tb, tc, td, NPAIR - 2 * NMW + wave);
        const int p = NPAIR - NMW + wave;
        const float pa = s_sc[p * 16 + r], pb = s_sc[p * 16 + 8 + r];
        pv_last(pa, tc, acc);
        pv_last(pb, td, acc);
      }
      float t12[12], t6[6];
#pragma unroll
      for (int i = 0; i < 12; ++i) t12[i] = tsum32(acc[i >> 1][i & 1], acc[6 + (i >> 1)][i & 1]);
#pragma unroll
      for (int i = 0; i < 6; ++i) t6[i] = tsum16(t12[i], t12[i + 6]);
#pragma unroll
      for (int i = 0; i < 6; ++i) t6[i] += dpp<DPP_ROR8>(t6[i]);
      if ((lane & 8) == 0) {
        f32x2* dst = reinterpret_cast<f32x2*>(&s_buf[(it % NRING) * (NMW * DH) + wave * DH + j * 24 + (lane >> 4) * 6]);
        dst[0] = f32x2{t6[0], t6[1]}; dst[1] = f32x2{t6[2], t6[3]}; dst[2] = f32x2{t6[4], t6[5]};
      }
      CFENCE();
      if (it < NA) { if (lane == 0) __hip_atomic_fetch_add(s_c + C_PART + it, 1u, __ATOMIC_RELAXED, __HIP_MEMORY_SCOPE_WORKGROUP); }
    }
    publish(NS - 1);
    return;
  }

  {
    Tile pk[NRP * 2];
    __builtin_amdgcn_s_setprio(3);
#pragma unroll
    for (int e = 0; e < NLP * 2; ++e) {
      const char* src = kb + (poff((e >> 1) * 4 + tw) + lo + (e & 1) * 3072);
      if (e >= 2) until(tb0, due_k(e * 2 + tw) - TLEAD);
#pragma unroll
      for (int i = 0; i < 3; ++i) glds16_nt(src + i * 128, lds_base + (e * 3 + i) * 1024);
      if (e == 1) {
        VMCNT(3);
        tb0 = t0 + min(max((int)(clk() - t0) - ANCH, 0), 300);
        __builtin_amdgcn_s_barrier();
        if (tw == 0 && lane == 0) s_c[S_LATE] = tb0;
      }
      if (e * 3 + 3 > PFD) VMCNT(PFD);
    }
#pragma unroll
    for (int e = 0; e < NRP * 2; ++e) {
      until(tb0, due_k((NLP * 2 + e) * 2 + tw) - TLEAD);
      tile_load(pk[e], kb, poff((NLP + (e >> 1)) * 4 + tw) + lo, e & 1);
      MEMFENCE();
      if (e + 1 < NRP * 2) VMCNT(PFD);
    }
    if (tw == 0) {
      r0 = ld32(rs, WS_HR + bid * 2);
      if (bid == LEADER) {
        u32x4 n;
#pragma unroll
        for (int k = 0; k < 4; ++k) n[k] = ld32(rs, WS_HA + (lane * 4 + k) * 32) + 1u;
        st128(rs, WS_HN + lane * 4, n);
      }
      const u32x4 z = {0u, 0u, 0u, 0u};
      if (lane < 48) {
#pragma unroll
        for (int it = NA; it < NS; ++it) st128(rs, WS_PO + xb * DH + lane * 4, z);
      }
      else st128(rs, WS_PML + (g * GS + mm) * 64 + (lane - 48) * 4, z);
      if (isG && g_cg == 0 && lane < 8) st128(rs, WS_ST + lane * 4, z);
      if (isW) st128(rs, WS_SLAB + (w_ks * NB + (lane >> 4)) * DM + w_ct * 64 + (lane & 15) * 4, z);
      else if (isG) { if (lane < 16) st128(rs, WS_ACT + g_cg * 64 + lane * 4, z); }
      else if (isH) {
#pragma unroll
        for (int k = 0; k < 3; ++k) st128(rs, WS_H + (xl - 2) * 768 + k * 256 + lane * 4, z);
      }
      else if (isC && lane < 48) st128(rs, WS_OC + (c_it * NG + g) * DH + lane * 4, z);
    }
    __builtin_amdgcn_s_setprio(0);
    lds_wait<1>(s_c + F_Q, HPG);
    u32x2 q[12];
    q_load(q, s_q, j);
    const u32x4* s_kv = reinterpret_cast<const u32x4*>(smem + OFF_REG + tw * REG) + lane;
    static_assert(NLP * 4 <= PPI && NTP <= HPG * PPI, "tail tiles: batch row 0 only");
#pragma unroll 1
    for (int e = 0; e < NLP * 2; ++e) {
      Tile tmp;
#pragma unroll
      for (int i = 0; i < 3; ++i) tmp.d[i] = s_kv[(e * 3 + i) * 64];
      score_put(tile_score(tmp, q), s_sc + ((e >> 1) * 4 + tw) * 16 + (e & 1) * 8 + r, j);
    }
#pragma unroll
    for (int e = 0; e < NRP * 2; ++e) {
      if ((NLP * 2 + e) * 2 == PPI) q_load(q, s_q + DH, j);
      score_put(tile_score(pk[e], q), s_sc + ((NLP + (e >> 1)) * 4 + tw) * 16 + (e & 1) * 8 + r, j);
      __builtin_amdgcn_sched_barrier(0);
    }
    asm volatile("s_waitcnt vmcnt(0) lgkmcnt(0)" ::: "memory");
#pragma unroll
    for (int it = 0; it * PPI < NTP; ++it) lds_add(s_c + C_SC + it, lane);
  }

  const int mr = lane & 15, qd = lane >> 4;
  unsigned char* reg = smem + OFF_REG + tw * REG;
  float* red = reinterpret_cast<float*>(smem + OFF_RED);
#define PERM_LO(hi_, lo_) __builtin_amdgcn_perm(hi_, lo_, 0x05040100u)
#define PERM_HI(hi_, lo_) __builtin_amdgcn_perm(hi_, lo_, 0x07060302u)
  int served = 0;
  auto done = [&](int n) {
    if (tw == 3 && served < min(n, NA)) {
      const int from = served;
      int ln = lane;
      asm volatile("" : "+v"(ln));
#pragma unroll 1
      for (; served < min(n, NA); ++served) {
        lds_wait<8>(s_c + C_PART + served, NMW);
        const f32x4 o = merged(served, ln < DH / 4 ? ln : 0);
        const float am = max_x16_32(row_max(fmaxf(fmaxf(fabsf(o[0]), fabsf(o[1])), fmaxf(fabsf(o[2]), fabsf(o[3])))));
        const float inv = am > 1e-30f ? 32767.0f / am : 0.f;
        const int q0 = __float2int_rn(o[0] * inv), q1 = __float2int_rn(o[1] * inv), q2 = __float2int_rn(o[2] * inv), q3 = __float2int_rn(o[3] * inv);
        if (ln < DH / 4) reinterpret_cast<u32x2*>(s_sc + served * (DH / 2))[ln] = u32x2{PERM_LO((unsigned)q1, (unsigned)q0), PERM_LO((unsigned)q3, (unsigned)q2)};
        const bool nan = __any((o[0] + o[1]) + (o[2] + o[3]) != (o[0] + o[1]) + (o[2] + o[3]));
        if (ln == 0) s_ml[served][2] = nan ? 0x7fc00000u : nz_bits(am * (1.0f / 32767.0f));
        LGKM0();
        if (ln == 0) s_c[F_FREE] = served + 1;
      }
#pragma unroll 1
      for (int c = from * 24 + ln; c < served * 24; c += 64) st128nt(rs, WS_PQ + xb * 5 * (DH / 2) + c * 4, reinterpret_cast<const u32x4*>(s_sc)[c]);
      VMCNT(0);
      if (ln < 8) st128(rs, WS_PML + xb * 64 + (from ? 32 : 0) + ln * 4, s_ml[ln]);
      if (ln == 0) s_c[S_BURST + (from ? 1 : 0)] = clk();
      CFENCE();
      if (ln == 0) __hip_atomic_fetch_add(s_c + F_DONE, (unsigned)(served - from), __ATOMIC_RELAXED, __HIP_MEMORY_SCOPE_WORKGROUP);
    }
    if (n > NA) lds_wait<1>(s_c + F_DONE, n); else lds_wait<8>(s_c + F_DONE, n);
  };
  auto done_all = [&]() {
#pragma unroll 1
    for (int n = 4; n <= NA; ++n) done(n);
  };
  const int lp = (lane < DH / 4 ? lane : 0) * 4;
  auto weight = [&](int it, int x0, int cnt) -> float {
    const int mlw = WS_PML + (x0 + (lane & (cnt - 1))) * 64 + (it < 4 ? 0 : 32) + it * 4;
    u32x4 ml;
#pragma unroll 1
    for (unsigned spin = 0; spin < SPIN_CAP; ++spin) {
      ml = ld128(rs, mlw);
      if (__all(min4(ml) != 0u)) break;
      __builtin_amdgcn_s_sleep(16);
    }
    const float mk = __uint_as_float(ml[0]);
    const float e = __builtin_amdgcn_exp2f(mk - max_x16_32(row_max(mk)));
    return e / (wave_sum(e * __uint_as_float(ml[1])) * ((float)cnt * (1.0f / 64.0f))) * __uint_as_float(ml[2]);
  };
  const unsigned tok = __builtin_amdgcn_readfirstlane(r0) + 1u;
  unsigned echoed = 0u, fold = 0u;
  int slow = 0, prev = 0;
  auto lag = [&]() {
    const unsigned v = __hip_atomic_load(s_c + S_MOVED + (lane & 7), __ATOMIC_RELAXED, __HIP_MEMORY_SCOPE_WORKGROUP);
    fold += v;
    unsigned a = 0u;
#pragma unroll
    for (int k = 0; k < 8; ++k) a += __builtin_amdgcn_readlane(v, k);
    return a >> 2;
  };
  auto tell = [&]() { if (lane < 4) { s_c[S_SLOW + 2 * lane] = slow; s_c[S_SLOW + 2 * lane + 1] = fold; } };
  auto round = [&](unsigned rd, bool lead) {
    const int tr = RT0 + (int)(rd - 1) * RTS;
    until(t0, tr);
    const unsigned bh = lag();
    if (bh > RMIN && lane < 8) st128(rs, WS_HA + bid * 32 + lane * 4, u32x4{echoed + rd, tok, tb0, bh});
    if (lead) { until(t0, tr + RA1); leader_release(rs, lane, tb0, rd, slow, prev); }
    until(t0, tr + RA2);
    const u32x2 rv = ld64(rs, WS_HR + bid * 2);
    slow = (int)__builtin_amdgcn_readfirstlane(rv[1]) >> 16;
#pragma unroll 1
    for (unsigned k = min(__builtin_amdgcn_readfirstlane(rv[0]) - tok, rd) + 1u; k <= rd; ++k) slow = slow > 0 ? max(slow - GB, 0) : slow - (k <= PRD ? PRB : 0);
    tell();
  };
  auto prep = [&](bool lead, unsigned nrd) {
    if (tw == 0) {
      const int c = lane & (GS - 1);
      unsigned qe = s_c[S_ECHO + c];
      bool first = true, released = false, sent = false;
#pragma unroll 1
      while ((int)(clk() - t0) < HST) {
        const unsigned qn = ld32(rs, qf + (GS + c) * 32);
        if (mm < NPROD && __any(qn != qe)) {
          if (lane < 32) st32(rs, qf + mm * 32 + lane, qn);
          qe = qn;
        }
        __builtin_amdgcn_s_sleep(64);
      }
      const unsigned behind = lag();
#pragma unroll 1
      for (unsigned spin = 0; spin < SPIN_CAP && !released; ++spin) {
        const unsigned qn = ld32(rs, qf + (GS + c) * 32);
        const u32x2 rv = ld64(rs, WS_HR + bid * 2);
        const unsigned nv = __builtin_amdgcn_readfirstlane(ld32(rs, WS_HN + bid));
        if (__builtin_amdgcn_readfirstlane(rv[0]) - tok <= NRD) {
          released = true;
          slow = (int)__builtin_amdgcn_readfirstlane(rv[1]) >> 16;
          if (lane == 0) { s_c[S_FIRST] = min((tb0 - rv[1]) & 0xffffu, 300u) + 1u; }
        } else {
          if (mm < NPROD && __any(qn != qe)) {
            if (lane < 32) st32(rs, qf + mm * 32 + lane, qn);
            qe = qn;
          }
          if (first || nv != echoed) {
            echoed = nv; first = false;
            if (lane < 8) st128(rs, WS_HA + bid * 32 + lane * 4, u32x4{echoed, tok, tb0, behind});
          }
          if (lead && !sent) sent = leader_release(rs, lane, tb0, 0u, slow, prev);
          __builtin_amdgcn_s_sleep(32);
        }
      }
      tell();
      if (lane == 0) s_c[F_REL] = 1u;
      if (lane < DH / 4) {
        u32x4 p[3][2];
#pragma unroll
        for (int e = 0; e < 3; ++e)
#pragma unroll
          for (int k = 0; k < 2; ++k) p[e][k] = ld128nt(rs, WS_QP + (((e ? 8 + e * 8 + (xb >> 5) : xb >> 4) * 2 + k) * NB + 1) * DH + lane * 4);
#pragma unroll
        for (int e = 0; e < 3; ++e) *reinterpret_cast<u32x4*>(&s_q[(1 + e) * DH + lane * 4]) = q_pack(p[e][0], p[e][1]);
      }
      LGKM0();
      if (lane == 0) s_c[F_Q2] = 3u;
#pragma unroll 1
      for (unsigned rd = 1; rd <= nrd; ++rd) round(rd, lead);
    }
    if (tw == 3) {
#pragma unroll 1
      for (int ik = 0; ik < NS; ++ik) {
        const int it = seg_of_k(ik), nv = (seg_beg(it + 1) - seg_beg(it)) >> 4;
        lds_wait<4>(s_c + C_SC + ik, ik == 0 ? NW : NMW);
        f32x4* sp = reinterpret_cast<f32x4*>(s_sc + seg_beg(it) * 16) + lane;
        f32x4 v[4];
#pragma unroll
        for (int k = 0; k < 4; ++k) v[k] = sp[(k & (nv - 1)) * 64];
        const f32x4 mx = __builtin_elementwise_max(__builtin_elementwise_max(v[0], v[1]), __builtin_elementwise_max(v[2], v[3]));
        const float m = max_x16_32(row_max(fmaxf(fmaxf(mx[0], mx[1]), fmaxf(mx[2], mx[3]))));
        float l = 0.f;
#pragma unroll
        for (int k = 0; k < 4; ++k) {
#pragma unroll
          for (int e = 0; e < 4; ++e) v[k][e] = __builtin_amdgcn_exp2f(v[k][e] - m);
          if (k < nv) {
            l += (v[k][0] + v[k][1]) + (v[k][2] + v[k][3]);
            sp[k * 64] = v[k];
          }
        }
        l = wave_sum(l);
        if (lane == 0) s_ml[it] = u32x4{nz_bits(m), nz_bits(l), 0x3f800000u, 0x3f800000u};
        LGKM0();
        if (lane == 0) s_c[F_P] = ik + 1;
      }
    }
  };
  auto pull = [&]() {
    typedef __attribute__((address_space(1))) const unsigned g_u32;
    const int id = (ro - 16) * 4 + tw;
    constexpr int RW = 160 - 32 * GE, LW = RW * 4 * 3 * 2 / 64;
    g_u32* p1[2];
#pragma unroll
    for (int i = 0; i < 2; ++i) {
      const int l = id * LW + min(i * 32 + (lane >> 1), max(LW - 1, 0)), m = l >= LW * 32 ? l - LW * 32 : l, rr = m / 3;
      p1[i] = (g_u32*)((l >= LW * 32 ? wu : wg) + (size_t)(rr / max(RW, 1) * 160 + 32 * GE + rr % max(RW, 1)) * FF + xl * 192 + (m % 3) * 64 + (lane & 1) * 32);
    }
    g_u32* p2 = (g_u32*)(wd + (size_t)(id * 24 + min(lane >> 1, 23)) * DM + xl * 64 + (lane & 1) * 32);
    asm volatile("" : "+v"(p1[0]), "+v"(p1[1]), "+v"(p2));
    if (GLEAD) lds_wait<1>(s_c + C_LAST, GCNT); else done(NS);
    const unsigned tp = clk() + PW + tw * PS;
    until(tp, 0);
    const unsigned v0 = LW > 0 ? p1[0][0] : 0u;
    MEMFENCE();
    until(tp, 4 * PS);
    const unsigned v1 = LW > 32 ? p1[1][0] : 0u;
    MEMFENCE();
    until(tp, DPW);
    const unsigned v2 = p2[0], v3 = p2[xl < 2 ? 256 : 0];
    MEMFENCE();
    return (v0 ^ v1) + (v2 ^ v3);
  };
  if (isW) {
    u32x4 bx[24];
    const char* p = reinterpret_cast<const char*>(wo + (size_t)((w_ks * 4 + tw) * DH + r) * DM + w_ct * 64 + j * 8);
#pragma unroll 1
    for (int i = 0; i < 24; ++i) { until(t0, tw == 0 ? W0START + i * (W0SPAN / 24) : W1START + i * (W1SPAN / 24)); glds16_nt(p, lds_base + i * 1024); p += 8 * DM * 2; VMCNT(WFD); }
    asm volatile("s_waitcnt vmcnt(0)" ::: "memory");
#pragma unroll
    for (int s = 0; s < 6; ++s) {
      u32x2 e[8];
#pragma unroll
      for (int k = 0; k < 8; ++k) e[k] = *reinterpret_cast<const u32x2*>(reg + (s * 32 + qd * 8 + k) * 128 + mr * 8);
#pragma unroll
      for (int k = 0; k < 4; ++k) {
        bx[s * 4 + 0][k] = PERM_LO(e[2 * k + 1][0], e[2 * k][0]); bx[s * 4 + 1][k] = PERM_HI(e[2 * k + 1][0], e[2 * k][0]);
        bx[s * 4 + 2][k] = PERM_LO(e[2 * k + 1][1], e[2 * k][1]); bx[s * 4 + 3][k] = PERM_HI(e[2 * k + 1][1], e[2 * k][1]);
      }
    }
#pragma unroll
    for (int s = 0; s < 24; s += 4) asm volatile("" : "+v"(bx[s]), "+v"(bx[s + 1]), "+v"(bx[s + 2]), "+v"(bx[s + 3]));
    LGKM0();
    prep(false, NRD - WL);
    lds_wait<8>(s_c + F_REL, 1u);
    const int hh = w_ks * 4 + tw;
#pragma unroll 1
    for (int pass = 0; pass < 3; ++pass) {
      if (pass < 2) {
        if (WL && pass == 1 && tw == 0) round(NRD, false);
        done(pass ? NA : 4);
        pause(lds_get(s_c + S_BURST + pass), pass ? WDELAY2 : WDELAY2 + 300);
        const int b0 = pass * 2, b1 = pass + 1;
        u32x4 v0, v1;
#pragma unroll 1
        for (unsigned spin = 0; spin < SPIN_CAP; ++spin) {
          v0 = ld128(rs, WS_OC + (b0 * NH + hh) * DH + lp);
          v1 = ld128(rs, WS_OC + (b1 * NH + hh) * DH + lp);
          if (__all(min(min4(v0), min4(v1)) != 0u)) break;
          __builtin_amdgcn_s_sleep(16);
        }
        if (lane < DH / 4) {
          *reinterpret_cast<u32x2*>(reg + (b0 * 4) * AW_LD + lane * 8) = u32x2{v0[0], v0[1]};
          *reinterpret_cast<u32x2*>(reg + (b0 * 4 + 1) * AW_LD + lane * 8) = u32x2{v0[2], v0[3]};
          *reinterpret_cast<u32x2*>(reg + (b1 * 4) * AW_LD + lane * 8) = u32x2{v1[0], v1[1]};
          *reinterpret_cast<u32x2*>(reg + (b1 * 4 + 1) * AW_LD + lane * 8) = u32x2{v1[2], v1[3]};
        }
      } else {
        const float wk = weight(NS - 1, hh * GS, GS);
        float w[GS];
#pragma unroll
        for (int k = 0; k < GS; ++k) { w[k] = __uint_as_float(__builtin_amdgcn_readlane(__float_as_uint(wk), k)); asm volatile("" : "+s"(w[k])); }
        done(NS);
        __builtin_amdgcn_s_setprio(3);
        const int po = WS_PO + hh * GS * DH;
        u32x4 pv[GS];
#pragma unroll 1
        for (unsigned spin = 0; spin < SPIN_CAP; ++spin) {
#pragma unroll
          for (int k = 0; k < GS; ++k) pv[k] = __builtin_amdgcn_raw_buffer_load_b128(rs, lp * 4, (po + k * DH) * 4, AUX_SC1);
          unsigned mn = min4(pv[0]);
#pragma unroll
          for (int k = 1; k < GS; ++k) mn = min(mn, min4(pv[k]));
          if (__all(mn != 0u)) break;
          __builtin_amdgcn_s_sleep(1);
        }
        f32x4 o = w[0] * __builtin_bit_cast(f32x4, pv[0]), o2 = w[1] * __builtin_bit_cast(f32x4, pv[1]);
#pragma unroll
        for (int k = 2; k < GS; k += 2) { o += w[k] * __builtin_bit_cast(f32x4, pv[k]); o2 += w[k + 1] * __builtin_bit_cast(f32x4, pv[k + 1]); }
        u32x2 hi, lw;
        split4(o + o2, hi, lw);
        if (lane < DH / 4) {
          *reinterpret_cast<u32x2*>(reg + ((NB - 1) * 4) * AW_LD + lane * 8) = hi;
          *reinterpret_cast<u32x2*>(reg + ((NB - 1) * 4 + 1) * AW_LD + lane * 8) = lw;
        }
      }
      CFENCE();
      u32x4 a[6];
#pragma unroll
      for (int s = 0; s < 6; ++s) a[s] = *reinterpret_cast<const u32x4*>(reg + mr * AW_LD + s * 64 + qd * 16);
      __builtin_amdgcn_sched_barrier(0);
      f32x4 c[4];
#pragma unroll
      for (int t = 0; t < 4; ++t) c[t] = f32x4{0.f, 0.f, 0.f, 0.f};
#pragma unroll
      for (int s = 0; s < 6; ++s)
#pragma unroll
        for (int t = 0; t < 4; ++t) c[t] = mfma16(a[s], bx[s * 4 + t], c[t]);
      const bool mine = max(qd - 1, 0) == pass;
      f32x4* r4 = reinterpret_cast<f32x4*>(red);
      if (mine) r4[tw * 64 + lane] = f32x4{c[0][0] + c[0][1], c[1][0] + c[1][1], c[2][0] + c[2][1], c[3][0] + c[3][1]};
      const int k = arrive(s_c + C_W + pass);
      f32x4 sum = (r4[lane] + r4[64 + lane]) + (r4[128 + lane] + r4[192 + lane]);
      asm volatile("" : "+v"(sum));
      if (arrived(k) == 3) {
        if (mine) st128(rs, WS_SLAB + (w_ks * NB + qd) * DM + w_ct * 64 + mr * 4, u32x4{nz_bits(sum[0]), nz_bits(sum[1]), nz_bits(sum[2]), nz_bits(sum[3])});
      }
    }
  } else if (isG || isH) {
    f32x4* rst = reinterpret_cast<f32x4*>(red);
    const int hb = isH ? xl - 2 : NB - 1;
    uint2 wr4 = make_uint2(0u, 0u), xg = wr4;
    if (lane < 40) {
      wr4 = *reinterpret_cast<const uint2*>(wr + tw * 160 + lane * 4);
      xg = *reinterpret_cast<const uint2*>(x + row_of(hb) * DM + tw * 160 + lane * 4);
    }
    const int eb = mr >> 2;
    const float cq = bf_lo(*reinterpret_cast<const unsigned*>(x + row_of(eb) * DM));
    const float wsum = wave_sum((bf_lo(wr4.x) + bf_hi(wr4.x)) + (bf_lo(wr4.y) + bf_hi(wr4.y)));
    f32x4 xc = f32x4{bf_lo(xg.x), bf_hi(xg.x), bf_lo(xg.y), bf_hi(xg.y)} - __uint_as_float(__builtin_amdgcn_readlane(__float_as_uint(cq), hb * 4));
    asm volatile("" : "+v"(xc));
    typedef __attribute__((address_space(1))) const u32x4 g_u32x4;
    g_u32x4* wp[10];
    wp[0] = (g_u32x4*)(((mr & 4) ? wu : wg) + (size_t)(tw * 160 + qd * 8 + (mr >> 3) * 4) * FF + g_cg * 32 + (mr & 3) * 8);
    u32x4 e[20], bx[20];
    auto frag = [&](int s) __attribute__((always_inline)) {
      unsigned w[8][2];
#pragma unroll
      for (int i = 0; i < 4; ++i)
#pragma unroll
        for (int d = 0; d < 2; ++d) {
          w[i][d] = __builtin_amdgcn_update_dpp(e[s * 4 + i][d], e[s * 4 + i][2 + d], DPP_ROR8, 0xf, 0xc, false);
          w[4 + i][d] = __builtin_amdgcn_update_dpp(e[s * 4 + i][2 + d], e[s * 4 + i][d], DPP_ROR8, 0xf, 0x3, false);
        }
#pragma unroll
      for (int k = 0; k < 4; ++k) {
        bx[s * 4 + 0][k] = PERM_LO(w[2 * k + 1][0], w[2 * k][0]); bx[s * 4 + 1][k] = PERM_HI(w[2 * k + 1][0], w[2 * k][0]);
        bx[s * 4 + 2][k] = PERM_LO(w[2 * k + 1][1], w[2 * k][1]); bx[s * 4 + 3][k] = PERM_HI(w[2 * k + 1][1], w[2 * k][1]);
      }
      PIN4(bx, s * 4);
    };
    if (isG) {
#pragma unroll
      for (int i = 0; i < 4 * GE; ++i) {
        until(t0, tw == 0 ? W0START + i * (W0SPAN / max(4 * GE, 1)) : W1START + i * (W1SPAN / max(4 * GE, 1)));
        e[i] = __builtin_nontemporal_load(wp[0] + ((i >> 2) * 32 + (i & 3)) * (FF / 8));
        MEMFENCE();
      }
#pragma unroll
      for (int s = 0; s < GE; ++s) frag(s);
    }
    prep(false, NRD);
    lds_wait<8>(s_c + F_REL, 1u);
    auto hrow = [&](bool fast) -> f32x4 {
      f32x4 hv = {0.f, 0.f, 0.f, 0.f};
      if (lane < 40) {
        const int off = (WS_SLAB + hb * DM + tw * 160 + lane * 4) * 4;
        u32x4 sl[KS];
#pragma unroll 1
        for (unsigned spin = 0; spin < SPIN_CAP; ++spin) {
#pragma unroll
          for (int ks = 0; ks < KS; ++ks) sl[ks] = __builtin_amdgcn_raw_buffer_load_b128(rs, off, ks * (NB * DM * 4), AUX_SC1);
          unsigned mn = min4(sl[0]);
#pragma unroll
          for (int ks = 1; ks < KS; ++ks) mn = min(mn, min4(sl[ks]));
          if (!__any(mn == 0u)) break;
          if (fast) __builtin_amdgcn_s_sleep(1); else __builtin_amdgcn_s_sleep(16);
        }
#define SL(k) __builtin_bit_cast(f32x4, sl[k])
        hv = xc + (((SL(0) + SL(1)) + (SL(2) + SL(3))) + ((SL(4) + SL(5)) + (SL(6) + SL(7))));
#undef SL
      }
      return hv;
    };
    auto sums = [&](f32x4 hv) -> f32x4 {
      const float s1 = wave_sum((hv[0] + hv[1]) + (hv[2] + hv[3]));
      const float s2 = wave_sum((hv[0] * hv[0] + hv[1] * hv[1]) + (hv[2] * hv[2] + hv[3] * hv[3]));
      const float dot = wave_sum((hv[0] * bf_lo(wr4.x) + hv[1] * bf_hi(wr4.x)) + (hv[2] * bf_lo(wr4.y) + hv[3] * bf_hi(wr4.y)));
      return f32x4{s1, s2, dot, wsum};
    };
    if (isH) {
      if (tw == 3 || hb == 2) done_all(); else done(4);
      pause(lds_get(s_c + S_BURST + (hb >> 1)), hb < 2 ? HDELAY : HDELAY2);
      const f32x4 hv = hrow(false), sm = sums(hv);
      u32x4 v = split8(hv, (lane & 1) != 0);
      if (lane >= 40) v = u32x4{nz_bits(sm[0]), nz_bits(sm[1]), nz_bits(sm[2]), nz_bits(sm[3])};
      if (lane < 48) st128(rs, WS_H + (hb * 4 + tw) * 192 + lane * 4, v);
      done_all();
      const unsigned pl = pull();
      asm volatile("" :: "v"(pl));
      return;
    }
    done_all();
#pragma unroll
    for (int i = 2 * GE; i < 10; ++i) { wp[i] = wp[0] + ((i >> 1) * 32 + (i & 1) * 2) * (FF / 8); asm volatile("" : "+v"(wp[i])); }
    done(NS);
    if (GW) until(clk(), GW);
#pragma unroll
    for (int i = 4 * GE; i < 20; ++i) e[i] = wp[i >> 1][(i & 1) * (FF / 8)];
    u32x4 a[5], sm;
    const int hoff = (WS_H + (min(mr >> 2, NB - 2) * 4 + tw) * 192 + qd * 8 + (mr & 1) * 4) * 4;
#pragma unroll
    for (int s = 0; s < 5; ++s) a[s] = __builtin_amdgcn_raw_buffer_load_b128(rs, hoff + s * 128, 0, AUX_SC1);
    sm = ld128(rs, WS_H + (min(lane, NB - 2) * 4 + tw) * 192 + 160);
    MEMFENCE();
#pragma unroll
    for (int s = GE; s < 5; ++s) frag(s);
    {
#pragma unroll 1
      for (unsigned spin = 0; spin < SPIN_CAP; ++spin) {
        unsigned mn = min4(sm);
#pragma unroll
        for (int s = 0; s < 5; ++s) mn = min(mn, min4(a[s]));
        if (__all(mn != 0u)) break;
        __builtin_amdgcn_s_sleep(2);
#pragma unroll
        for (int s = 0; s < 5; ++s) a[s] = __builtin_amdgcn_raw_buffer_load_b128(rs, hoff + s * 128, 0, AUX_SC1);
        sm = ld128(rs, WS_H + (min(lane, NB - 2) * 4 + tw) * 192 + 160);
      }
      if (lane < NB - 1) rst[lane * 4 + tw] = __builtin_bit_cast(f32x4, sm);
#pragma unroll
      for (int s = 0; s < 5; ++s) {
        if (mr & 2) a[s] = u32x4{0x3f803f80u, 0x3f803f80u, 0x3f803f80u, 0x3f803f80u};
        asm volatile("" : "+v"(a[s]));
      }
    }
    const f32x4 hv = hrow(true);
    if (lane < 40) {
      u32x2 hi, lw;
      split4(hv, hi, lw);
      *reinterpret_cast<u32x2*>(reg + lane * 8) = hi;
      *reinterpret_cast<u32x2*>(reg + AG_LD + lane * 8) = lw;
    }
    CFENCE();
    if ((mr & 14) == 12) {
#pragma unroll
      for (int s = 0; s < 5; ++s) a[s] = *reinterpret_cast<const u32x4*>(reg + (mr & 1) * AG_LD + s * 64 + qd * 16);
    }
    __builtin_amdgcn_sched_barrier(0);
    f32x4 c[4];
#pragma unroll
    for (int t = 0; t < 4; ++t) c[t] = f32x4{0.f, 0.f, 0.f, 0.f};
#pragma unroll
    for (int s = 0; s < 5; ++s)
#pragma unroll
      for (int t = 0; t < 4; ++t) c[t] = mfma16(a[s], bx[s * 4 + t], c[t]);
    { const f32x4 s3 = sums(hv); if (lane == 0) rst[(NB - 1) * 4 + tw] = s3; }
    const int slot = qd * 16 + (mr & 7) * 2 + (mr >> 3);
    *reinterpret_cast<f32x4*>(reg + 4096 + slot * 16) = f32x4{c[0][0] + c[0][1], c[1][0] + c[1][1], c[0][2], c[1][2]};
    *reinterpret_cast<f32x4*>(reg + 4096 + 1152 + slot * 16) = f32x4{c[2][0] + c[2][1], c[3][0] + c[3][1], c[2][2], c[3][2]};
    const int k = arrive(s_c + C_T);
    f32x4 ts[4], pg[4], pu[4];
#pragma unroll
    for (int w = 0; w < 4; ++w) {
      const unsigned char* src = smem + OFF_REG + w * REG + 4096 + (lane & 1) * 1152 + (eb * 16 + qd * 2 + ((lane >> 1) & 1)) * 16;
      ts[w] = rst[eb * 4 + w];
      pg[w] = *reinterpret_cast<const f32x4*>(src); pu[w] = *reinterpret_cast<const f32x4*>(src + 128);
    }
    PIN4(ts, 0); PIN4(pg, 0); PIN4(pu, 0);
    if (arrived(k) == 3) {
      const f32x4 t = (ts[0] + ts[1]) + (ts[2] + ts[3]), g4 = (pg[0] + pg[1]) + (pg[2] + pg[3]), u4 = (pu[0] + pu[1]) + (pu[2] + pu[3]);
      const float d = t[0] * (-1.0f / DM);
      const float rstd = __builtin_amdgcn_rsqf(fmaxf(t[1] + d * t[0], 0.f) * (1.0f / DM) + LN_EPS);
      const float g0 = rstd * (g4[0] + d * g4[2]), g1 = rstd * (g4[1] + d * g4[3]);
      const float a0 = g0 * __builtin_amdgcn_rcpf(1.0f + __expf(-g0)) * (rstd * (u4[0] + d * u4[2]));
      const float a1 = g1 * __builtin_amdgcn_rcpf(1.0f + __expf(-g1)) * (rstd * (u4[1] + d * u4[3]));
      const __hip_bfloat162 a2 = __halves2bfloat162(__float2bfloat16(a0), __float2bfloat16(a1));
      const float aw = __uint_as_float(max(*reinterpret_cast<const unsigned*>(&a2), 1u));
      const u32x4 av = {__float_as_uint(aw), __float_as_uint(dpp<0x55>(aw)), __float_as_uint(dpp<0xAA>(aw)), __float_as_uint(dpp<0xFF>(aw))};
      const float rr = rstd * (t[2] + d * t[3]);
      const u32x4 sv = {nz_bits(cq - d), nz_bits(rstd), nz_bits(__builtin_amdgcn_rcpf(1.0f + __expf(-rr))), 0x3f800000u};
      const bool stat = g_cg == 0 && (lane & 51) == 1;
      if ((lane & 3) == 0 || stat) st128(rs, stat ? WS_ST + eb * 4 : WS_ACT + g_cg * 64 + lane, stat ? sv : av);
    }
  } else if (isD) {
    const uint2 xd = *reinterpret_cast<const uint2*>(x + row_of(tw) * DM + d_ct * 32 + j * 4);
    const __amdgpu_buffer_rsrc_t rd = __builtin_amdgcn_make_buffer_rsrc(const_cast<bf16*>(wd), 0, FF * DM * 2, 0x00020000);
    prep(false, NRD);
    lds_wait<8>(s_c + F_REL, 1u);
    done_all();
    const unsigned pl = pull();
    done(NS);
    asm volatile("" :: "v"(pl));
    const unsigned te = clk();
    if (DW) until(te, DW);
    u32x4 bd[24];
    {
      const int off = ((tw * 384 + (mr >> 3) * 32 + qd * 8 + (mr & 4)) * DM + d_ct * 32 + (mr & 3) * 8) * 2;
      u32x4 e[24];
#pragma unroll
      for (int i = 0; i < 24; ++i) e[i] = __builtin_amdgcn_raw_buffer_load_b128(rd, off + (i & 3) * (DM * 2), (i >> 2) * 64 * (DM * 2), 0);
      MEMFENCE();
#pragma unroll
      for (int s = 0; s < 6; ++s) {
        unsigned w[8][2];
#pragma unroll
        for (int i = 0; i < 4; ++i)
#pragma unroll
          for (int d = 0; d < 2; ++d) {
            w[i][d] = __builtin_amdgcn_update_dpp(e[s * 4 + i][d], e[s * 4 + i][2 + d], 0x114, 0xf, 0xa, false);
            w[4 + i][d] = __builtin_amdgcn_update_dpp(e[s * 4 + i][2 + d], e[s * 4 + i][d], 0x104, 0xf, 0x5, false);
          }
#pragma unroll
        for (int k = 0; k < 4; ++k) {
          bd[s * 4 + 0][k] = PERM_LO(w[2 * k + 1][0], w[2 * k][0]); bd[s * 4 + 1][k] = PERM_HI(w[2 * k + 1][0], w[2 * k][0]);
          bd[s * 4 + 2][k] = PERM_LO(w[2 * k + 1][1], w[2 * k][1]); bd[s * 4 + 3][k] = PERM_HI(w[2 * k + 1][1], w[2 * k][1]);
        }
        PIN4(bd, s * 4);
      }
    }
    until(te, DLATE);
    {
      u32x4 sl;
      unsigned spin = 0;
      const int wl = min(lane, 19), watch = WS_SLAB + ((tw * 2 + wl / 10) * NB + NB - 1) * DM + (wl % 10) * 64;
#pragma unroll 1
      for (; spin < SPIN_CAP; ++spin) {
        sl = ld128(rs, WS_SLAB + (r * NB + tw) * DM + d_ct * 32 + j * 4);
        const unsigned wv = ld32(rs, watch);
        if (__all(min(min4(sl), wv) != 0u)) break;
        __builtin_amdgcn_s_sleep(1);
      }
      if (lane == 0) __hip_atomic_fetch_max(s_c + C_W + 1, clk() - t0 + (spin ? DLATE2 : 0), __ATOMIC_RELAXED, __HIP_MEMORY_SCOPE_WORKGROUP);
      f32x4 hv = {bf_lo(xd.x), bf_hi(xd.x), bf_lo(xd.y), bf_hi(xd.y)};
#pragma unroll
      for (int i = 0; i < 4; ++i) hv[i] += sum_r(__uint_as_float(sl[i]));
      if (lane < 8) reinterpret_cast<f32x4*>(red + 512 + tw * 32)[lane] = hv;
      LGKM0();
      lds_add(s_c + C_W, lane);
      lds_wait<1>(s_c + C_W, 4u);
    }
    const int col = (mr & 3) * 8 + (mr & 4) + (mr >> 3) * 2;
    f32x2 hv = *reinterpret_cast<const f32x2*>(red + 512 + qd * 32 + col);
    asm volatile("" : "+v"(hv));
    u32x4 a[6], st;
#pragma unroll
    for (int s = 0; s < 6; ++s) a[s] = u32x4{1u, 1u, 1u, 1u};
    const int aoff = (WS_ACT + tw * 12 * 64 + ((mr >> 1) & 1) * 64 + qd * 16 + (mr >> 2) * 4) * 4;
    until(t0, lds_get(s_c + C_W + 1));
#pragma unroll 1
    for (unsigned spin = 0; spin < SPIN_CAP; ++spin) {
      st = ld128(rs, WS_ST + qd * 4);
      if ((mr & 1) == 0) {
#pragma unroll
        for (int s = 0; s < 6; ++s) a[s] = __builtin_amdgcn_raw_buffer_load_b128(rs, aoff + s * 512, 0, AUX_SC1);
      }
      unsigned mn = min4(st);
#pragma unroll
      for (int s = 0; s < 6; ++s) mn = min(mn, min4(a[s]));
      if (__all(mn != 0u)) break;
      __builtin_amdgcn_s_sleep(1);
    }
    f32x4 c[4];
#pragma unroll
    for (int t = 0; t < 4; ++t) c[t] = f32x4{0.f, 0.f, 0.f, 0.f};
#pragma unroll
    for (int s = 0; s < 6; ++s)
#pragma unroll
      for (int t = 0; t < 4; ++t) c[t] = mfma16(a[s], bd[s * 4 + t], c[t]);
    const f32x4 sf = __builtin_bit_cast(f32x4, st);
    hv += (hv - sf[0]) * sf[1];
    const bool up = mr >= 8;
    const float k0 = up ? c[2][2] : c[0][0], k1 = up ? c[3][2] : c[1][0], o0 = up ? c[0][2] : c[2][0], o1 = up ? c[1][2] : c[3][0];
    f32x2* r2 = reinterpret_cast<f32x2*>(red);
    r2[tw * 64 + lane] = f32x2{k0 + dpp<DPP_ROR8>(o0), k1 + dpp<DPP_ROR8>(o1)};
    const int k = arrive(s_c + C_T);
    f32x2 y = hv + ((r2[lane] + r2[64 + lane]) + (r2[128 + lane] + r2[192 + lane])) * sf[2];
    asm volatile("" : "+v"(y));
    if (arrived(k) == 3) {
      const __hip_bfloat162 o2 = __halves2bfloat162(__float2bfloat16(y[0]), __float2bfloat16(y[1]));
      __hip_atomic_store(reinterpret_cast<unsigned*>(out + row_of(qd) * DM + d_ct * 32 + col), *reinterpret_cast<const unsigned*>(&o2), __ATOMIC_RELAXED, __HIP_MEMORY_SCOPE_AGENT);
    }
  } else {
    prep(bid == LEADER, NRD);
    if (isC && tw == 1) {
      lds_wait<8>(s_c + F_REL, 1u);
      const int cs = c_it != 1 ? c_it * 2 : max(g >> 3, 1), cnt = c_it != 1 ? 8 : (g < 16 ? 16 : 32), x0 = c_it != 1 ? g * 8 : (g < 16 ? g * 16 : (g & 7) * 32);
      done(c_it < 2 ? 4 : NA);
      pause(lds_get(s_c + S_BURST + (c_it >> 1)), WDELAY);
      const float wk = weight(cs, x0, cnt);
      f32x4 o = {0.f, 0.f, 0.f, 0.f};
#pragma unroll 1
      for (int k0 = 0; k0 < cnt; k0 += 16) {
        u32x2 pv[16];
#pragma unroll
        for (int k = 0; k < 16; ++k) pv[k] = __builtin_amdgcn_raw_buffer_load_b64(rs, lp * 2, (WS_PQ + ((x0 + ((k0 + k) & (cnt - 1))) * 5 + cs) * (DH / 2)) * 4, AUX_EARLY);
#pragma unroll
        for (int k = 0; k < 16; ++k) {
          const float w = k0 + k < cnt ? __uint_as_float(__builtin_amdgcn_readlane(__float_as_uint(wk), k0 + k)) : 0.f;
          o += w * f32x4{(float)(short)pv[k][0], (float)((int)pv[k][0] >> 16), (float)(short)pv[k][1], (float)((int)pv[k][1] >> 16)};
        }
      }
      u32x2 hi, lw;
      split4(o, hi, lw);
      if (lane < DH / 4) st128(rs, WS_OC + (c_it * NG + g) * DH + lane * 4, u32x4{max(hi[0], 1u), max(hi[1], 1u), max(lw[0], 1u), max(lw[1], 1u)});
    }
    done_all();
    const unsigned pl = pull();
    asm volatile("" :: "v"(pl));
  }
  if (tw == 3) done_all();
}

extern "C" void kernel_launch(void* const* d_in, const int* in_sizes, int n_in, void* d_out, int out_size, void* d_ws,
                              size_t ws_size, hipStream_t stream) {
  const bf16* x = (const bf16*)d_in[0];
  const bf16* kc = (const bf16*)d_in[1];
  const bf16* vc = (const bf16*)d_in[2];
  const bf16* wq = (const bf16*)d_in[3];
  const bf16* wo = (const bf16*)d_in[4];
  const bf16* wr = (const bf16*)d_in[6];
  const bf16* wg = (const bf16*)d_in[7];
  const bf16* wu = (const bf16*)d_in[8];
  const bf16* wd = (const bf16*)d_in[9];
  fused_kernel<<<NWG, NW * 64, 0, stream>>>(x, kc, vc, wq, wo, wr, wg, wu, wd, (float*)d_ws, (bf16*)d_out);
}
```

```cpp
#include <hip/hip_runtime.h>
#include <hip/hip_bf16.h>
#include <stdint.h>

typedef __hip_bfloat16 bf16;
using u32x2 = __attribute__((ext_vector_type(2))) unsigned;
using u32x4 = __attribute__((ext_vector_type(4))) unsigned;
using f32x4 = __attribute__((ext_vector_type(4))) float;
using f32x2 = __attribute__((ext_vector_type(2))) float;
using bf16x8 = __attribute__((ext_vector_type(8))) __bf16;

constexpr int NB = 4, DM = 640, NH = 32, SK = 8192, DH = 192, HD = NH * DH, FF = 1536;
constexpr float LN_EPS = 1e-5f;
constexpr float QSCALE = 0.07216878364870322f * 1.4426950408889634f;

constexpr int NWG = 256;
constexpr int GS = 8, NG = NWG / GS, NI = GS / 2;
constexpr int HPG = NI / NB;
constexpr int PPI = 512 / GS;
constexpr int NPAIR = NI * PPI;

constexpr int TAUK = 1320, TAUV = 1332;
constexpr int TSTART = 110;
constexpr int WGAP = 165, WP0 = 64, WPN = 176;
constexpr int VGAP = 50;
constexpr int DEEP1 = 100, DEEP2 = 200, SDEEP = 0, COARSE = 1, LAGOFF = 20, ENDT = 12, ENDS = 2;
constexpr int SPRINT = 0;
constexpr int JUMP = 145;
constexpr int HST = 2800, RT0 = 3900, RTS = 1000, RA1 = 220, RA2 = 500, LEADER = 24, WL = 0;
constexpr unsigned NRD = 5, RMIN = 30, FBP = 60, FBL = 150;
constexpr int W0START = 1700, W0SPAN = 1000, W1START = 2700, W1SPAN = 2600;
constexpr int ROT = 16, ROWS = 0xC9;
constexpr int FBM = 100, FBX = 15, FB0 = 80, FB1 = 10, FBS = 10;
constexpr int FBD = 10, HOLD = 4000, GB = 24, GBS = 2, GBX = 60, GBF = 12;
constexpr unsigned PRD = 3; constexpr int PRB = 24;
constexpr int ANCH = 90;
constexpr int TLEAD = 10, PFD = 6, WFD = 3;
constexpr int QWAIT = 750, QBACK = 100;
constexpr int WDELAY = 100, WDELAY2 = 460, DLATE = 100, DLATE2 = 85;
constexpr int TPF = 15, TPT = 250, TPS = 25;
constexpr int CPF = 1, CPT = 0, CODE = 24 * 64 * 48;
constexpr int GLEAD = 1, GCNT = 4, DP = 0, DPW = 60, GE = 0, PW = 30, PS = 0, HDELAY = 1150, HDELAY2 = 780, GW = 0, DW = 0;

constexpr int KS = 8;
constexpr int WS_PML = 0;
constexpr int WS_PO = WS_PML + NWG * 64;
constexpr int WS_PQ = WS_PO + NWG * DH;
constexpr int WS_OC = WS_PQ + NWG * 5 * (DH / 2);
constexpr int WS_SLAB = WS_OC + (NB - 1) * NH * DH;
constexpr int WS_ACT = WS_SLAB + KS * NB * DM;
constexpr int WS_H = WS_ACT + NB * FF / 2;
constexpr int WS_ST = WS_H + (NB - 1) * 4 * 192;
constexpr int WS_HN = WS_ST + 32;
constexpr int WS_HR = WS_HN + NWG;
constexpr int WS_HA = WS_HR + 2 * NWG;
constexpr int QFS = 2 * GS * 32;
constexpr int WS_QF = WS_HA + 32 * NWG;
constexpr int WS_QP = WS_QF + NG * QFS;
constexpr int WS_END = WS_QP + NH * 2 * NB * DH;
constexpr int NPROD = HPG * 6;
static_assert(WS_PO % 32 == 0 && WS_SLAB % 32 == 0 && WS_ACT % 32 == 0 && WS_ST % 32 == 0 && WS_HN % 32 == 0 && WS_QF % 32 == 0 && WS_QP % 32 == 0, "line alignment");


__device__ __forceinline__ int row_of(int b) { return (ROWS >> (2 * b)) & 3; }
__device__ __forceinline__ float bf_lo(unsigned u) { return __uint_as_float(u << 16); }
__device__ __forceinline__ float bf_hi(unsigned u) { return __uint_as_float(u & 0xffff0000u); }
__device__ __forceinline__ f32x2 bf_pair(unsigned u) { return f32x2{bf_lo(u), bf_hi(u)}; }
__device__ __forceinline__ f32x2 fma2(float a, f32x2 b, f32x2 c) { return __builtin_elementwise_fma(f32x2{a, a}, b, c); }
__device__ __forceinline__ f32x2 fma2(f32x2 a, f32x2 b, f32x2 c) { return __builtin_elementwise_fma(a, b, c); }

template <int CTRL> __device__ __forceinline__ float dpp(float x) {
  return __builtin_bit_cast(float, __builtin_amdgcn_mov_dpp(__builtin_bit_cast(int, x), CTRL, 0xf, 0xf, true));
}
constexpr int DPP_XOR1 = 0xB1, DPP_XOR2 = 0x4E, DPP_HMIRROR = 0x141, DPP_MIRROR = 0x140, DPP_ROR8 = 0x128;

__device__ __forceinline__ float sum_x16_32(float x) {
  auto s = __builtin_amdgcn_permlane16_swap(__float_as_uint(x), __float_as_uint(x), false, false);
  x = __uint_as_float(s[0]) + __uint_as_float(s[1]);
  auto t = __builtin_amdgcn_permlane32_swap(__float_as_uint(x), __float_as_uint(x), false, false);
  return __uint_as_float(t[0]) + __uint_as_float(t[1]);
}
__device__ __forceinline__ float max_x16_32(float x) {
  auto s = __builtin_amdgcn_permlane16_swap(__float_as_uint(x), __float_as_uint(x), false, false);
  x = fmaxf(__uint_as_float(s[0]), __uint_as_float(s[1]));
  auto t = __builtin_amdgcn_permlane32_swap(__float_as_uint(x), __float_as_uint(x), false, false);
  return fmaxf(__uint_as_float(t[0]), __uint_as_float(t[1]));
}
__device__ __forceinline__ float tsum32(float a, float b) {
  auto t = __builtin_amdgcn_permlane32_swap(__float_as_uint(a), __float_as_uint(b), false, false);
  return __uint_as_float(t[0]) + __uint_as_float(t[1]);
}
__device__ __forceinline__ float tsum16(float a, float b) {
  auto t = __builtin_amdgcn_permlane16_swap(__float_as_uint(a), __float_as_uint(b), false, false);
  return __uint_as_float(t[0]) + __uint_as_float(t[1]);
}
__device__ __forceinline__ float sum_r(float v) { v += dpp<DPP_ROR8>(v); return sum_x16_32(v); }
__device__ __forceinline__ float row_sum(float v) {
  v += dpp<DPP_XOR1>(v); v += dpp<DPP_XOR2>(v); v += dpp<DPP_HMIRROR>(v);
  return v + dpp<DPP_MIRROR>(v);
}
__device__ __forceinline__ float row_max(float v) {
  v = fmaxf(v, dpp<DPP_XOR1>(v)); v = fmaxf(v, dpp<DPP_XOR2>(v)); v = fmaxf(v, dpp<DPP_HMIRROR>(v));
  return fmaxf(v, dpp<DPP_MIRROR>(v));
}
__device__ __forceinline__ float wave_sum(float v) {
  v += dpp<DPP_XOR1>(v); v += dpp<DPP_XOR2>(v); v += dpp<DPP_HMIRROR>(v);
  return sum_r(v);
}

__device__ __forceinline__ u32x4 ld_nt(const char* p) { return __builtin_nontemporal_load(reinterpret_cast<const u32x4*>(p)); }

struct Tile { u32x4 d[3]; };

#define PIN4(a, o) asm volatile("" : "+v"((a)[(o) + 0]), "+v"((a)[(o) + 1]), "+v"((a)[(o) + 2]), "+v"((a)[(o) + 3]))
#define MEMFENCE() do { asm volatile("" ::: "memory"); __builtin_amdgcn_sched_barrier(0); } while (0)

__device__ __forceinline__ void glds16_nt(const void* gsrc, unsigned lds_dst) {
  unsigned keep;
  asm volatile("s_mov_b32 %0, m0\n\ts_mov_b32 m0, %2\n\ts_nop 0\n\tglobal_load_lds_dwordx4 %1, off nt\n\ts_mov_b32 m0, %0"
               : "=&s"(keep) : "v"(gsrc), "s"(lds_dst) : "memory");
}

using bf16x2 = __attribute__((ext_vector_type(2))) __bf16;
__device__ __forceinline__ float dot2(unsigned a, unsigned b, float c) { return __builtin_amdgcn_fdot2_f32_bf16(__builtin_bit_cast(bf16x2, a), __builtin_bit_cast(bf16x2, b), c, false); }
__device__ __forceinline__ float tile_score(const Tile& k, const u32x2 (&q)[12]) {
  float s0 = 0.f, s1 = 0.f;
#pragma unroll
  for (int i = 0; i < 3; ++i) {
    u32x4 v = k.d[i];
    asm("" : "+v"(v));
#pragma unroll
    for (int d = 0; d < 4; ++d) { s0 = dot2(v[d], q[i * 4 + d][0], s0); s1 = dot2(v[d], q[i * 4 + d][1], s1); }
  }
  return s0 + s1;
}
__device__ __forceinline__ void pair_put(float a, float b, float* dst, int j) {
  float s = (j < 4 ? a : b) + dpp<DPP_HMIRROR>(j < 4 ? b : a);
  s += dpp<DPP_XOR1>(s);
  s += dpp<DPP_XOR2>(s);
  if ((j & 3) == 0) dst[j * 2] = s;
}
__device__ __forceinline__ void score_put(float s, float* dst, int j) {
  s += dpp<DPP_XOR1>(s);
  s += dpp<DPP_XOR2>(s);
  s += dpp<DPP_HMIRROR>(s);
  if (j == 0) *dst = s;
}
__device__ __forceinline__ void tile_load(Tile& t, const char* base, unsigned off, int half) {
#pragma unroll
  for (int i = 0; i < 3; ++i) t.d[i] = ld_nt(base + off + half * 3072 + i * 128);
}
__device__ __forceinline__ void pair_issue(Tile& ta, Tile& tb, const char* base, unsigned off, int deep) {
  deep = __builtin_amdgcn_readfirstlane(deep);
#define WAIT_IF(level, n) asm volatile("s_cmp_lg_u32 %0, " #level "\n\ts_cbranch_scc1 .Lpi%=\n\ts_waitcnt vmcnt(" #n ")\n.Lpi%=:" :: "s"(deep) : "scc", "memory")
  const char* p = base + off;
  if constexpr (COARSE) {
    WAIT_IF(0, 3);
    ta.d[0] = ld_nt(p); ta.d[1] = ld_nt(p + 128); ta.d[2] = ld_nt(p + 256); MEMFENCE();
    WAIT_IF(0, 3); WAIT_IF(1, 6);
    tb.d[0] = ld_nt(p + 3072); tb.d[1] = ld_nt(p + 3200); tb.d[2] = ld_nt(p + 3328); MEMFENCE();
  } else {
    WAIT_IF(0, 5); ta.d[0] = ld_nt(p); MEMFENCE();
    WAIT_IF(0, 5); ta.d[1] = ld_nt(p + 128); MEMFENCE();
    WAIT_IF(0, 5); ta.d[2] = ld_nt(p + 256); MEMFENCE();
    WAIT_IF(1, 6);
    WAIT_IF(0, 5); tb.d[0] = ld_nt(p + 3072); MEMFENCE();
    WAIT_IF(0, 5); tb.d[1] = ld_nt(p + 3200); MEMFENCE();
    WAIT_IF(0, 5); tb.d[2] = ld_nt(p + 3328); MEMFENCE();
  }
#undef WAIT_IF
}
__device__ __forceinline__ void pv_last(float p, Tile& v, f32x2 (&acc)[12]) {
#pragma unroll
  for (int i = 0; i < 3; ++i) {
    asm volatile("" : "+v"(v.d[i]), "+v"(p));
#pragma unroll
    for (int d = 0; d < 4; ++d) acc[i * 4 + d] = fma2(p, bf_pair(v.d[i][d]), acc[i * 4 + d]);
    PIN4(acc, i * 4);
  }
}
__device__ __forceinline__ void q_load(u32x2 (&q)[12], const float* src, int j) {
#pragma unroll
  for (int i = 0; i < 6; ++i) {
    const u32x4 a = *reinterpret_cast<const u32x4*>(src + (i >> 1) * 64 + j * 8 + (i & 1) * 4);
    q[i * 2] = u32x2{a[0], a[1]}; q[i * 2 + 1] = u32x2{a[2], a[3]};
  }
}
#define VMCNT(n) asm volatile("s_waitcnt vmcnt(%0)" :: "n"(n) : "memory")

constexpr int AUX_SC1 = 16, AUX_EARLY = 18;
constexpr unsigned SPIN_CAP = 1u << 17;
__device__ __forceinline__ unsigned nz_bits(float v) { return max(__float_as_uint(v), 1u); }
__device__ __forceinline__ bool all_nz(u32x4 v) { return v[0] != 0u && v[1] != 0u && v[2] != 0u && v[3] != 0u; }
__device__ __forceinline__ unsigned min4(u32x4 v) { return min(min(v[0], v[1]), min(v[2], v[3])); }
__device__ __forceinline__ unsigned ld32(__amdgpu_buffer_rsrc_t rs, int word) { return __builtin_amdgcn_raw_buffer_load_b32(rs, word * 4, 0, AUX_SC1); }
__device__ __forceinline__ u32x2 ld64(__amdgpu_buffer_rsrc_t rs, int word) { return __builtin_amdgcn_raw_buffer_load_b64(rs, word * 4, 0, AUX_SC1); }
__device__ __forceinline__ u32x4 ld128(__amdgpu_buffer_rsrc_t rs, int word) { return __builtin_amdgcn_raw_buffer_load_b128(rs, word * 4, 0, AUX_SC1); }
__device__ __forceinline__ void st32(__amdgpu_buffer_rsrc_t rs, int word, unsigned v) { __builtin_amdgcn_raw_buffer_store_b32(v, rs, word * 4, 0, AUX_SC1); }
__device__ __forceinline__ void st64(__amdgpu_buffer_rsrc_t rs, int word, u32x2 v) { __builtin_amdgcn_raw_buffer_store_b64(v, rs, word * 4, 0, AUX_SC1); }
__device__ __forceinline__ void st128(__amdgpu_buffer_rsrc_t rs, int word, u32x4 v) { __builtin_amdgcn_raw_buffer_store_b128(v, rs, word * 4, 0, AUX_SC1); }

__device__ __forceinline__ u32x4 ld128nt(__amdgpu_buffer_rsrc_t rs, int word) { return __builtin_amdgcn_raw_buffer_load_b128(rs, word * 4, 0, AUX_EARLY); }
__device__ __forceinline__ void st128nt(__amdgpu_buffer_rsrc_t rs, int word, u32x4 v) { __builtin_amdgcn_raw_buffer_store_b128(v, rs, word * 4, 0, AUX_EARLY); }

__device__ __forceinline__ bool leader_release(__amdgpu_buffer_rsrc_t rs, int lane, unsigned t0, unsigned rd, int slow, int& prev) {
  u32x4 a[4];
#pragma unroll
  for (int k = 0; k < 4; ++k) a[k] = ld128(rs, WS_HA + (lane * 4 + k) * 32);
  const u32x4 n = ld128(rs, WS_HN + lane * 4);
  if (rd == 0u && !__all(a[0][0] == n[0] && a[1][0] == n[1] && a[2][0] == n[2] && a[3][0] == n[3])) return false;
  const float e = fmaxf(fmaxf((float)(int)(t0 - a[0][2]), (float)(int)(t0 - a[1][2])), fmaxf((float)(int)(t0 - a[2][2]), (float)(int)(t0 - a[3][2])));
  unsigned first = t0 - (unsigned)(int)max_x16_32(row_max(e));
  float ls = 0.f, lx = 0.f;
#pragma unroll
  for (int k = 0; k < 4; ++k) {
    const float l = a[k][0] == n[k] + rd ? (float)min(a[k][3] + (rd ? 0u : min(a[k][2] - first, 300u)), 2000u) : 0.f;
    ls += l; lx = fmaxf(lx, l);
  }
  ls = wave_sum(ls) * (1.0f / NWG); lx = max_x16_32(row_max(lx));
  const int extra = __builtin_amdgcn_readfirstlane(min(max((int)(ls * (FBM / 100.0f) + lx * (FBX / 100.0f)) - (rd ? FB1 + (int)(NRD - rd) * FBD : FB0), 0), 600));
  if (rd != 0u && __all(ls * NWG < HOLD)) { prev = 0; return true; }
  first = (first & 0xffffu) | ((unsigned)(slow + extra + min(extra, prev) * (int)(rd == NRD ? FBL : FBP) / 100) << 16);
  prev = extra;
  st128(rs, WS_HR + lane * 8, u32x4{a[0][1] + rd, first, a[1][1] + rd, first});
  st128(rs, WS_HR + lane * 8 + 4, u32x4{a[2][1] + rd, first, a[3][1] + rd, first});
  return true;
}

typedef __attribute__((address_space(3))) unsigned lds_u32;
__device__ __forceinline__ unsigned lds_add(lds_u32* p, int lane) {
  unsigned k = 0;
  if (lane == 0) k = __hip_atomic_fetch_add(p, 1u, __ATOMIC_RELAXED, __HIP_MEMORY_SCOPE_WORKGROUP);
  return __builtin_amdgcn_readfirstlane(k);
}
#define CFENCE() asm volatile("" ::: "memory")
__device__ __forceinline__ int arrive(lds_u32* p) {
  int v;
  unsigned long long keep;
  asm volatile("s_mov_b64 %1, exec\n\ts_mov_b64 exec, 1\n\tds_add_rtn_u32 %0, %2, %3\n\ts_mov_b64 exec, %1"
               : "=&v"(v), "=&s"(keep) : "v"((unsigned)(size_t)p), "v"(1u) : "memory");
  return v;
}
__device__ __forceinline__ int arrived(int v) {
  asm volatile("s_waitcnt lgkmcnt(0)" : "+v"(v) :: "memory");
  return __builtin_amdgcn_readfirstlane(v);
}
__device__ __forceinline__ unsigned lds_get(lds_u32* p) {
  return __builtin_amdgcn_readfirstlane(__hip_atomic_load(p, __ATOMIC_RELAXED, __HIP_MEMORY_SCOPE_WORKGROUP));
}
template <int SLEEP> __device__ __forceinline__ void lds_wait(lds_u32* p, unsigned n) {
#pragma unroll 1
  for (unsigned spin = 0; spin < (SPIN_CAP << 4); ++spin) {
    if (lds_get(p) >= n) break;
    __builtin_amdgcn_s_sleep(SLEEP);
  }
  asm volatile("" ::: "memory");
}
__device__ __forceinline__ unsigned clk() { return (unsigned)__builtin_amdgcn_s_memrealtime(); }
struct Table {
  unsigned t0; int was, moved, slowed, folded;
  u32x2 told;
  __device__ __forceinline__ int gate(int due, lds_u32* mine, bool hold = true, int sh = 0) {
    const int fold = __builtin_amdgcn_readfirstlane(told[1]);
    if (fold != folded) { const int f = min(fold - folded, moved); moved -= f; slowed += f; folded = fold; *mine = moved; }
    const int left = (int)__builtin_amdgcn_readfirstlane(told[0]) - slowed, d = max(min(left, FBS), left < -GBX ? -GBF : -GBS);
    t0 += d; was -= d; slowed += d;
    int late = (int)(clk() - t0) - due;
    if (late - was > JUMP) { t0 += late - was; moved += late - was; late = was; *mine = moved; }
    const int lag = max(late - max(left - d, 0) - LAGOFF, 0);
    mine[4] = lag;
    was = late;
#pragma unroll 1
    for (int spin = 0; spin < 65536 && hold && late < 0; ++spin) { __builtin_amdgcn_s_sleep(1); late = (int)(clk() - t0) - due; }
    return max(lag < (DEEP1 >> sh) ? 0 : (lag < (DEEP2 >> sh) ? 1 : 2), hold ? 0 : SDEEP);
  }
  __device__ __forceinline__ void listen(lds_u32* slow) {
    told = __builtin_bit_cast(u32x2, __hip_atomic_load((__attribute__((address_space(3))) unsigned long long*)slow, __ATOMIC_RELAXED, __HIP_MEMORY_SCOPE_WORKGROUP));
  }
  __device__ __forceinline__ void agree(lds_u32* all, lds_u32* mine) {
    if (moved == 0) return;
    const int m = (int)min(min(lds_get(all), lds_get(all + 1)), min(lds_get(all + 2), lds_get(all + 3))) + 30;
    if (m < moved) { t0 -= moved - m; moved = m; was = 1 << 20; *mine = moved; }
  }
};
__device__ __forceinline__ void until(unsigned t0, int due) {
#pragma unroll 1
  for (int spin = 0; spin < 65536 && (int)(clk() - t0) < due; ++spin) __builtin_amdgcn_s_sleep(1);
}
__device__ __forceinline__ void pause(unsigned t0, int due) {
#pragma unroll 1
  for (int spin = 0; spin < 4096 && (int)(clk() - t0) < due; ++spin) __builtin_amdgcn_s_sleep(16);
}
#define LGKM0() asm volatile("s_waitcnt lgkmcnt(0)" ::: "memory")

__device__ __forceinline__ void split4(f32x4 v, u32x2& hi, u32x2& lo) {
  unsigned h[4], r[4];
#pragma unroll
  for (int i = 0; i < 4; ++i) { h[i] = __float_as_uint(v[i]) & 0xffff0000u; r[i] = __float_as_uint(v[i] - __uint_as_float(h[i])); }
  hi = u32x2{__builtin_amdgcn_perm(h[1], h[0], 0x07060302u), __builtin_amdgcn_perm(h[3], h[2], 0x07060302u)};
  lo = u32x2{__builtin_amdgcn_perm(r[1], r[0], 0x07060302u), __builtin_amdgcn_perm(r[3], r[2], 0x07060302u)};
}
__device__ __forceinline__ u32x4 split8(f32x4 v, bool odd) {
  u32x2 hi, lo;
  split4(v, hi, lo);
  const unsigned n0 = __float_as_uint(dpp<DPP_XOR1>(__uint_as_float(odd ? hi[0] : lo[0]))), n1 = __float_as_uint(dpp<DPP_XOR1>(__uint_as_float(odd ? hi[1] : lo[1])));
  return u32x4{max(odd ? n0 : hi[0], 1u), max(odd ? n1 : hi[1], 1u), max(odd ? lo[0] : n0, 1u), max(odd ? lo[1] : n1, 1u)};
}
__device__ __forceinline__ u32x4 q_pack(u32x4 a, u32x4 b) {
  u32x2 hi, lo;
  split4((__builtin_bit_cast(f32x4, a) + __builtin_bit_cast(f32x4, b)) * QSCALE, hi, lo);
  return u32x4{hi[0], lo[0], hi[1], lo[1]};
}
__device__ __forceinline__ f32x4 mfma16(u32x4 a, u32x4 b, f32x4 c) {
  return __builtin_amdgcn_mfma_f32_16x16x32_bf16(__builtin_bit_cast(bf16x8, a), __builtin_bit_cast(bf16x8, b), c, 0, 0, 0);
}

constexpr int NW = 8, NMW = 4;
constexpr int NLP = 5, NRP = 8;
constexpr int NTP = 4 * (NLP + NRP);
constexpr int REG = NLP * 6 * 1024;
constexpr int NRING = 4;
constexpr int NS = 6, NA = NS - 1;
__device__ __forceinline__ int seg_beg(int s) { return s >= NS ? NPAIR : ((0xC87640 >> (4 * s)) & 15) * 16; }
__device__ __forceinline__ int seg_k(int t) { return (int)(0x5433222211110000ull >> (4 * (t >> 4))) & 15; }
__device__ __forceinline__ int seg_of_k(int k) { return (0x321540 >> (4 * k)) & 15; }
__device__ __forceinline__ int k_of_seg(int s) { return (0x215430 >> (4 * s)) & 15; }
__device__ __forceinline__ int pair_of(int t) { return t < 64 ? t : (t < 192 ? t + 64 : t - 128); }
constexpr int OFF_CNT = 0, OFF_REG = 256,
              OFF_SC = OFF_REG + 4 * REG, OFF_BUF = OFF_SC + NPAIR * 16 * 4, OFF_Q = OFF_BUF + NRING * NMW * DH * 4, OFF_ML = OFF_Q + NS * DH * 4,
              OFF_RED = OFF_ML + 128, OFF_RS = OFF_RED + 4 * 256 * 4, OFF_END = OFF_RS + 5 * 16;
enum { F_REL = 0, F_DONE, C_LAST, C_T = 3, F_OUT, C_P, C_K, F_P, F_Q = 8, F_FREE, S_FIRST, F_Q2, C_W = 12, C_SC = 16, C_PART = 24, S_ECHO = 32, S_BURST = 40, S_LATE = 42, S_MOVED = 48, S_SLOW = 56 };
__device__ __forceinline__ int due_k(int p) { return TSTART + ((p * TAUK + min(max(p - WP0, 0), WPN) * (WGAP * 64 / WPN)) >> 6); }
__device__ __forceinline__ int due_v(int p) { return TSTART + WGAP + ((NPAIR * TAUK + p * TAUV + max(p - NPAIR / 2, 0) * (VGAP * 64 / (NPAIR / 2))) >> 6); }
constexpr int AW_LD = DH * 2 + 16, AG_LD = 160 * 2 + 16;

__global__ void __launch_bounds__(NW * 64) fused_kernel(const bf16* __restrict__ x, const bf16* __restrict__ kc,
                                                        const bf16* __restrict__ vc, const bf16* __restrict__ wq,
                                                        const bf16* __restrict__ wo, const bf16* __restrict__ wr,
                                                        const bf16* __restrict__ wg, const bf16* __restrict__ wu,
                                                        const bf16* __restrict__ wd, float* ws, bf16* __restrict__ out) {
  __shared__ __attribute__((aligned(16))) unsigned char smem[OFF_END];
  float* s_sc = reinterpret_cast<float*>(smem + OFF_SC);
  float* s_buf = reinterpret_cast<float*>(smem + OFF_BUF);
  float* s_q = reinterpret_cast<float*>(smem + OFF_Q);
  u32x4* s_ml = reinterpret_cast<u32x4*>(smem + OFF_ML);
  lds_u32* s_c = (lds_u32*)(smem + OFF_CNT);

  const int tid = threadIdx.x, lane = tid & 63, r = lane >> 3, j = lane & 7;
  const int wave = __builtin_amdgcn_readfirstlane(tid >> 6);
  const int tw = wave - NMW;
  const int bid = blockIdx.x, xl = bid & 7, idx = bid >> 3;
  const int g = (xl * 32 + idx) / GS, mm = idx % GS;
  const __amdgpu_buffer_rsrc_t rs = __builtin_amdgcn_make_buffer_rsrc(ws, 0, WS_END * 4, 0x00020000);
  const int ro = ((idx & 3) << 3) | (idx >> 2);
  const bool isW = ro < 10, isG = ro >= 10 && ro < 16;
  const int w_ks = xl, w_ct = ro;
  const int g_cg = xl * 6 + (ro - 10);
  const int d_ct = (xl + ((ro - 16) >> 2) * 8) * 2 + (((ro - 16) >> 1) & 1);
  const bool isD = ro >= 16 && ro < 24 && (ro & 1) == 0 && d_ct < DM / 32;
  const bool isH = ro == 22 && xl >= 2 && xl < 5;
  const bool isC = (mm & 7) == 3 || (mm & 7) >= 6;
  const int c_it = (mm >> 3) * 3 + max((mm & 7) - 5, 0);
  const int qf = WS_QF + g * QFS;
  const char* kb = reinterpret_cast<const char*>(kc);
  const char* vb = reinterpret_cast<const char*>(vc);
  const unsigned lo = (r * 24 + j) * 16;
  const int xb = g * GS + mm, c0 = mm * 8 + g * ROT;
  const int b2 = (2 * NH + (xb >> 4)) * 1024 + (xb & 15) * 8, b3 = (2 * NH + 16 + (xb >> 5)) * 1024 + (xb & 31) * 8;
  auto poff = [&](int p) -> unsigned {
    const int turn = (p & 63) >> 2;
    int t = (row_of(p >> 6) * NH + g) * 1024 + ((turn * 64 + (p & 3) * 2 + c0) & 1023);
    if ((p >> 6) == 1) t = (turn < 8 ? b2 + turn * 128 : b3 + (turn & 4) * 2048 + (turn & 3) * 256) + (p & 3) * 2;
    return (unsigned)t * 3072u;
  };
  const unsigned lds_base = (unsigned)(size_t)(__attribute__((address_space(3))) unsigned char*)smem + OFF_REG + tw * REG;
  auto merged = [&](int it, int ln) -> f32x4 {
    const float* src = s_buf + (it % NRING) * (NMW * DH) + ((ln >> 1) & 7) * 24 + (ln >> 4) * 8 + (ln & 1) * 4;
    return (*reinterpret_cast<const f32x4*>(src) + *reinterpret_cast<const f32x4*>(src + DH)) +
           (*reinterpret_cast<const f32x4*>(src + 2 * DH) + *reinterpret_cast<const f32x4*>(src + 3 * DH));
  };
  auto publish = [&](int it) -> bool {
    const int k = arrive(s_c + C_PART + it);
    const f32x4 m = merged(it, lane < DH / 4 ? lane : 0);
    u32x4 o = {nz_bits(m[0]), nz_bits(m[1]), nz_bits(m[2]), nz_bits(m[3])};
    asm volatile("" : "+v"(o));
    if (arrived(k) != NMW - 1) return false;
    if (lane < DH / 4) st128(rs, WS_PO + xb * DH + lane * 4, o);
    if (lane == 0) __hip_atomic_fetch_add(s_c + F_DONE, 1u, __ATOMIC_RELAXED, __HIP_MEMORY_SCOPE_WORKGROUP);
    return true;
  };
#define NONCE() do { unsigned n_ = __builtin_amdgcn_readfirstlane(f) + 1u; while (__any(f == n_)) ++n_; \
    if (lane < 32) st32(rs, qf + (GS + mm) * 32 + lane, n_); f = n_; } while (0)
  unsigned long long pc;
  asm volatile("s_getpc_b64 %0" : "=s"(pc));
  unsigned r0 = 0u;
  unsigned t0 = clk();
  unsigned tb0 = t0;
  if (wave < NMW) {
    Tile ta, tb, tc, td;
    const int c = lane & (GS - 1);
    unsigned f = 0u;
    if (wave == 3) f = ld32(rs, qf + c * 32 + mm);
    if (mm < NPROD) {
      const int hp = g + (mm / 6) * NG, ck = (mm % 6) >> 1, hf = mm & 1, jp = hf * 4 + wave;
      const int bb = lane >> 4, t = lane & 15;
      const u32x4* xp = reinterpret_cast<const u32x4*>(x + row_of(bb) * DM);
      u32x4 xv[5], xs = {0u, 0u, 0u, 0u};
#pragma unroll
      for (int k = 0; k < 5; ++k) xv[k] = xp[t + 16 * k];
      if (t < 10) xs = xp[jp * 10 + t];
      const u32x4* wp = reinterpret_cast<const u32x4*>(wq + (size_t)(jp * 80 + r) * HD + hp * DH + ck * 64) + j;
      u32x4 wv[10];
#pragma unroll
      for (int k = 0; k < 10; ++k) wv[k] = __builtin_nontemporal_load(wp + (size_t)k * 8 * (HD / 8));
      if (wave == 0) s_c[lane] = lane == C_K ? NTP + NMW : 0u;
      MEMFENCE(); LGKM0(); __builtin_amdgcn_s_barrier(); MEMFENCE();
      if (wave == 3) { NONCE(); }
      float sm = 0.f;
#pragma unroll
      for (int k = 0; k < 5; ++k)
#pragma unroll
        for (int d = 0; d < 4; ++d) sm += bf_lo(xv[k][d]) + bf_hi(xv[k][d]);
      const float mean = row_sum(sm) * (1.0f / DM);
      float sv = 0.f;
#pragma unroll
      for (int k = 0; k < 5; ++k)
#pragma unroll
        for (int d = 0; d < 4; ++d) { const float a = bf_lo(xv[k][d]) - mean, c2 = bf_hi(xv[k][d]) - mean; sv += a * a + c2 * c2; }
      const float rstd = 1.0f / sqrtf(row_sum(sv) * (1.0f / DM) + LN_EPS);
      float* s_xn = s_buf + wave * 320;
      if (t < 10) {
#pragma unroll
        for (int d = 0; d < 4; ++d) {
          s_xn[(t * 8 + 2 * d) * 4 + bb] = (bf_lo(xs[d]) - mean) * rstd;
          s_xn[(t * 8 + 2 * d + 1) * 4 + bb] = (bf_hi(xs[d]) - mean) * rstd;
        }
      }
      LGKM0();
      f32x2 qa[16];
#pragma unroll
      for (int i = 0; i < 16; ++i) qa[i] = f32x2{0.f, 0.f};
#pragma unroll
      for (int k = 0; k < 10; ++k) {
        const f32x4 xn = *reinterpret_cast<const f32x4*>(&s_xn[(k * 8 + r) * 4]);
#pragma unroll
        for (int d = 0; d < 4; ++d) {
          const f32x2 w2 = bf_pair(wv[k][d]);
#pragma unroll
          for (int e = 0; e < 4; ++e) qa[e * 4 + d] = fma2(xn[e], w2, qa[e * 4 + d]);
        }
      }
      float t16[16], t8[8];
#pragma unroll
      for (int i = 0; i < 16; ++i) t16[i] = tsum32(qa[i >> 1][i & 1], qa[8 + (i >> 1)][i & 1]);
#pragma unroll
      for (int i = 0; i < 8; ++i) t8[i] = tsum16(t16[i], t16[i + 8]);
#pragma unroll
      for (int i = 0; i < 8; ++i) t8[i] += dpp<DPP_ROR8>(t8[i]);
      const bool up = (lane & 8) != 0;
      f32x4* r4 = reinterpret_cast<f32x4*>(smem + OFF_RED);
      r4[wave * 64 + lane] = f32x4{up ? t8[4] : t8[0], up ? t8[5] : t8[1], up ? t8[6] : t8[2], up ? t8[7] : t8[3]};
      LGKM0();
      if (lds_add(s_c + C_P, lane) == NMW - 1) {
        asm volatile("" ::: "memory");
        const f32x4 o4 = (r4[lane] + r4[64 + lane]) + (r4[128 + lane] + r4[192 + lane]);
        st128nt(rs, WS_QP + ((hp * 2 + hf) * NB + bb) * DH + ck * 64 + j * 8 + (up ? 4 : 0), __builtin_bit_cast(u32x4, o4));
        VMCNT(0);
        if (lane == 0) s_c[F_OUT] = 1u;
      }
    } else {
      if (wave == 0) s_c[lane] = lane == C_K ? NTP + NMW : 0u;
      MEMFENCE(); LGKM0(); __builtin_amdgcn_s_barrier(); MEMFENCE();
      if (wave == 3) { NONCE(); }
      else {
        typedef __attribute__((address_space(1))) const unsigned g_u32;
        typedef __attribute__((address_space(1))) const char g_char;
        const int id = ((idx >> 3) * 2 + (mm & 1)) * 3 + wave;
        if (CPF && lane < CODE / (24 * 64)) {
          const unsigned v = *(g_u32*)((pc & ~255ull) + id * (CODE / 24) + lane * 64);
          asm volatile("" :: "v"(v));
        }
        if (((TPF >> (id < 4 ? 0 : (id < 16 ? 1 : (id < 20 ? 2 : 3)))) & 1) && lane < 7 && id < 21) {
          const int st = id < 8 ? (0x1320 >> ((id >> 1) * 4)) & 3 : (id >> 1) & 3, hf = id & 1;
          int head = row_of(st) * NH + xl * 4 + hf * 2;
          unsigned off = min(lane << 20, (6 << 20) - 64);
          if (st == 1) {
            head = 2 * NH + (hf ? 16 + xl : 2 * xl);
            if (hf) off = lane < 4 ? min(lane << 20, (3 << 20) - 64) : 8 * (3 << 20) + min((lane - 4) * (3 << 19), (3 << 20) - 64);
          }
          g_char* p = (g_char*)(id < 8 ? kb : vb) + (size_t)head * (3 << 20) + off;
          if (id >= 16) p = (g_char*)(id == 16 ? wo + xl * 4 * DH * DM : (id == 17 ? wg : (id == 18 ? wu : wd))) + min(lane * (id == 16 ? 163840 : 327680), (id == 16 ? 983040 : 1966080) - 64);
          if (id >= 20) p = (g_char*)(lane ? wr : out);
          until(t0, TPT + id * TPS);
          const unsigned v = __builtin_nontemporal_load((g_u32*)p);
          asm volatile("" :: "v"(v));
        }
      }
    }
    MEMFENCE();
    if (wave == 3) {
      __builtin_amdgcn_s_setprio(3);
      const unsigned nonce = f;
      unsigned echoed = 0u;
      if (mm < NPROD) {
        echoed = ld32(rs, qf + (GS + c) * 32);
        asm volatile("" : "+v"(echoed) :: "memory");
        lds_wait<1>(s_c + F_OUT, 1u);
        if (lane < 32) st32(rs, qf + mm * 32 + lane, echoed);
#pragma unroll 1
        while ((int)(clk() - t0) < QWAIT - QBACK) {
          const unsigned nv = ld32(rs, qf + (GS + c) * 32);
          if (__any(nv != echoed)) {
            if (lane < 32) st32(rs, qf + mm * 32 + lane, nv);
            echoed = nv;
          }
          __builtin_amdgcn_s_sleep(4);
        }
      }
      until(t0, QWAIT);
#pragma unroll 1
      for (unsigned spin = 0; spin < SPIN_CAP; ++spin) {
        const unsigned nv = ld32(rs, qf + (GS + c) * 32);
        f = ld32(rs, qf + c * 32 + mm);
        const bool done = __all(c == mm || c >= NPROD || f == nonce);
        if (mm < NPROD && __any(nv != echoed)) {
          if (lane < 32) st32(rs, qf + mm * 32 + lane, nv);
          echoed = nv;
        }
        if (done) break;
        __builtin_amdgcn_s_sleep(8);
      }
      if (lane < GS) s_c[S_ECHO + lane] = echoed;
      if (lane < DH / 4) {
        u32x4 p[3][2];
#pragma unroll
        for (int it = 0; it < 3; ++it)
#pragma unroll
          for (int k = 0; k < 2; ++k) p[it][k] = ld128nt(rs, WS_QP + ((g * 2 + k) * NB + (it ? it + 1 : 0)) * DH + lane * 4);
#pragma unroll
        for (int it = 0; it < 3; ++it) {
          *reinterpret_cast<u32x4*>(&s_q[(it ? it + 3 : 0) * DH + lane * 4]) = q_pack(p[it][0], p[it][1]);
          if (it != 1) { LGKM0(); if (lane == 0) s_c[F_Q] = it + 1; MEMFENCE(); }
        }
      }
    }
    __builtin_amdgcn_s_setprio(3);
    until(t0, due_k(NTP + wave));
    tb0 = lds_get(s_c + S_LATE);
    if ((unsigned)(tb0 - t0 + 100u) > 500u) tb0 = t0;
    until(tb0, due_k(NTP + wave));
    Table tt = {tb0, 1 << 20, 0, 0, 0, {0u, 0u}};
    bool common = false;
    tile_load(ta, kb, poff(NTP + wave) + lo, 0);
    tile_load(tb, kb, poff(NTP + wave) + lo, 1);
    MEMFENCE();
    {
      u32x2 q[12];
      int cur = NTP + wave, nxt = lds_add(s_c + C_K, lane), passed = 0, ci = -1;
      auto kstep = [&](Tile& xa, Tile& xb_, Tile& ya, Tile& yb) __attribute__((always_inline)) -> bool {
        const bool last = nxt >= NPAIR;
        const unsigned off = poff(last ? wave : pair_of(nxt)) + lo;
        const char* base = last ? vb : kb;
        const int it = seg_k(cur), cp = pair_of(cur);
        MEMFENCE();
        const int deep = tt.gate(due_k(last ? NPAIR + wave : nxt), s_c + S_MOVED + wave);
        MEMFENCE();
        pair_issue(ya, yb, base, off, deep);
        if (it != ci) {
          LGKM0();
          for (; passed < it; ++passed) lds_add(s_c + C_SC + passed, lane);
          lds_wait<1>(s_c + (it < 3 ? F_Q : F_Q2), min(it + 1, 3));
          q_load(q, s_q + seg_of_k(it) * DH, j);
          if (!common) { const unsigned d = lds_get(s_c + S_FIRST); if (d) { tt.t0 -= d - 1u; common = true; tt.was = 1 << 20; } }
          tt.agree(s_c + S_MOVED, s_c + S_MOVED + wave);
          ci = it;
        }
        const int tk = arrive(s_c + C_K);
        tt.listen(s_c + S_SLOW + 2 * wave);
        pair_put(tile_score(xa, q), tile_score(xb_, q), s_sc + cp * 16 + r, j);
        MEMFENCE();
        cur = nxt; nxt = arrived(tk);
        return last;
      };
      bool swap;
      for (;;) {
        if (kstep(ta, tb, tc, td)) { swap = true; break; }
        if (kstep(tc, td, ta, tb)) { swap = false; break; }
      }
      LGKM0();
      for (; passed < NS; ++passed) lds_add(s_c + C_SC + passed, lane);
      __builtin_amdgcn_s_waitcnt(0x0F70);
      if (swap) { ta = tc; tb = td; }
    }
#pragma unroll 1
    for (int it = 0; it < NS; ++it) {
      lds_wait<1>(s_c + F_P, k_of_seg(it) + 1);
      if (!common) { const unsigned d = lds_get(s_c + S_FIRST); if (d) { tt.t0 -= d - 1u; common = true; tt.was = 1 << 20; } }
      tt.agree(s_c + S_MOVED, s_c + S_MOVED + wave);
      f32x2 acc[12];
#pragma unroll
      for (int i = 0; i < 12; ++i) acc[i] = f32x2{0.f, 0.f};
      auto vstep = [&](Tile& xa, Tile& xb_, Tile& ya, Tile& yb, int p) __attribute__((always_inline)) {
        const unsigned off = poff(p + NMW) + lo;
        MEMFENCE();
        const int deep = tt.gate(due_v(p + NMW), s_c + S_MOVED + wave, p + NMW < NPAIR - SPRINT * NMW, p >= NPAIR - ENDT * NMW ? ENDS : 0);
        MEMFENCE();
        pair_issue(ya, yb, vb, off, deep);
        const float pa = s_sc[p * 16 + r], pb = s_sc[p * 16 + 8 + r];
        tt.listen(s_c + S_SLOW + 2 * wave);
        if (p + 2 * NMW >= NPAIR) { if (lane == 0) __hip_atomic_fetch_add(s_c + C_LAST, 1u, __ATOMIC_RELAXED, __HIP_MEMORY_SCOPE_WORKGROUP); }
        pv_last(pa, xa, acc);
        pv_last(pb, xb_, acc);
        MEMFENCE();
      };
#pragma unroll 1
      for (int p = seg_beg(it) + wave; p < min(seg_beg(it + 1), NPAIR - 2 * NMW); p += 2 * NMW) {
        vstep(ta, tb, tc, td, p);
        vstep(tc, td, ta, tb, p + NMW);
      }
      if (it >= NRING) lds_wait<1>(s_c + F_FREE, it - NRING + 1);
      if (it == NS - 1) {
        vstep(ta, tb, tc, td, NPAIR - 2 * NMW + wave);
        const int p = NPAIR - NMW + wave;
        const float pa = s_sc[p * 16 + r], pb = s_sc[p * 16 + 8 + r];
        pv_last(pa, tc, acc);
        pv_last(pb, td, acc);
      }
      float t12[12], t6[6];
#pragma unroll
      for (int i = 0; i < 12; ++i) t12[i] = tsum32(acc[i >> 1][i & 1], acc[6 + (i >> 1)][i & 1]);
#pragma unroll
      for (int i = 0; i < 6; ++i) t6[i] = tsum16(t12[i], t12[i + 6]);
#pragma unroll
      for (int i = 0; i < 6; ++i) t6[i] += dpp<DPP_ROR8>(t6[i]);
      if ((lane & 8) == 0) {
        f32x2* dst = reinterpret_cast<f32x2*>(&s_buf[(it % NRING) * (NMW * DH) + wave * DH + j * 24 + (lane >> 4) * 6]);
        dst[0] = f32x2{t6[0], t6[1]}; dst[1] = f32x2{t6[2], t6[3]}; dst[2] = f32x2{t6[4], t6[5]};
      }
      CFENCE();
      if (it < NA) { if (lane == 0) __hip_atomic_fetch_add(s_c + C_PART + it, 1u, __ATOMIC_RELAXED, __HIP_MEMORY_SCOPE_WORKGROUP); }
    }
    publish(NS - 1);
    return;
  }

  {
    Tile pk[NRP * 2];
    __builtin_amdgcn_s_setprio(3);
#pragma unroll
    for (int e = 0; e < NLP * 2; ++e) {
      const char* src = kb + (poff((e >> 1) * 4 + tw) + lo + (e & 1) * 3072);
      if (e >= 2) until(tb0, due_k(e * 2 + tw) - TLEAD);
#pragma unroll
      for (int i = 0; i < 3; ++i) glds16_nt(src + i * 128, lds_base + (e * 3 + i) * 1024);
      if (e == 1) {
        VMCNT(3);
        tb0 = t0 + min(max((int)(clk() - t0) - ANCH, 0), 300);
        __builtin_amdgcn_s_barrier();
        if (tw == 0 && lane == 0) s_c[S_LATE] = tb0;
      }
      if (e * 3 + 3 > PFD) VMCNT(PFD);
    }
#pragma unroll
    for (int e = 0; e < NRP * 2; ++e) {
      until(tb0, due_k((NLP * 2 + e) * 2 + tw) - TLEAD);
      tile_load(pk[e], kb, poff((NLP + (e >> 1)) * 4 + tw) + lo, e & 1);
      MEMFENCE();
      if (e + 1 < NRP * 2) VMCNT(PFD);
    }
    if (tw == 0) {
      r0 = ld32(rs, WS_HR + bid * 2);
      if (bid == LEADER) {
        u32x4 n;
#pragma unroll
        for (int k = 0; k < 4; ++k) n[k] = ld32(rs, WS_HA + (lane * 4 + k) * 32) + 1u;
        st128(rs, WS_HN + lane * 4, n);
      }
      const u32x4 z = {0u, 0u, 0u, 0u};
      if (lane < 48) {
#pragma unroll
        for (int it = NA; it < NS; ++it) st128(rs, WS_PO + xb * DH + lane * 4, z);
      }
      else st128(rs, WS_PML + (g * GS + mm) * 64 + (lane - 48) * 4, z);
      if (isG && g_cg == 0 && lane < 8) st128(rs, WS_ST + lane * 4, z);
      if (isW) st128(rs, WS_SLAB + (w_ks * NB + (lane >> 4)) * DM + w_ct * 64 + (lane & 15) * 4, z);
      else if (isG) { if (lane < 16) st128(rs, WS_ACT + g_cg * 64 + lane * 4, z); }
      else if (isH) {
#pragma unroll
        for (int k = 0; k < 3; ++k) st128(rs, WS_H + (xl - 2) * 768 + k * 256 + lane * 4, z);
      }
      else if (isC && lane < 48) st128(rs, WS_OC + (c_it * NG + g) * DH + lane * 4, z);
    }
    __builtin_amdgcn_s_setprio(0);
    lds_wait<1>(s_c + F_Q, HPG);
    u32x2 q[12];
    q_load(q, s_q, j);
    const u32x4* s_kv = reinterpret_cast<const u32x4*>(smem + OFF_REG + tw * REG) + lane;
    static_assert(NLP * 4 <= PPI && NTP <= HPG * PPI, "tail tiles: batch row 0 only");
#pragma unroll 1
    for (int e = 0; e < NLP * 2; ++e) {
      Tile tmp;
#pragma unroll
      for (int i = 0; i < 3; ++i) tmp.d[i] = s_kv[(e * 3 + i) * 64];
      score_put(tile_score(tmp, q), s_sc + ((e >> 1) * 4 + tw) * 16 + (e & 1) * 8 + r, j);
    }
#pragma unroll
    for (int e = 0; e < NRP * 2; ++e) {
      if ((NLP * 2 + e) * 2 == PPI) q_load(q, s_q + DH, j);
      score_put(tile_score(pk[e], q), s_sc + ((NLP + (e >> 1)) * 4 + tw) * 16 + (e & 1) * 8 + r, j);
      __builtin_amdgcn_sched_barrier(0);
    }
    asm volatile("s_waitcnt vmcnt(0) lgkmcnt(0)" ::: "memory");
#pragma unroll
    for (int it = 0; it * PPI < NTP; ++it) lds_add(s_c + C_SC + it, lane);
  }

  const int mr = lane & 15, qd = lane >> 4;
  unsigned char* reg = smem + OFF_REG + tw * REG;
  float* red = reinterpret_cast<float*>(smem + OFF_RED);
#define PERM_LO(hi_, lo_) __builtin_amdgcn_perm(hi_, lo_, 0x05040100u)
#define PERM_HI(hi_, lo_) __builtin_amdgcn_perm(hi_, lo_, 0x07060302u)
  int served = 0;
  auto done = [&](int n) {
    if (tw == 3 && served < min(n, NA)) {
      const int from = served;
      int ln = lane;
      asm volatile("" : "+v"(ln));
#pragma unroll 1
      for (; served < min(n, NA); ++served) {
        lds_wait<8>(s_c + C_PART + served, NMW);
        const f32x4 o = merged(served, ln < DH / 4 ? ln : 0);
        const float am = max_x16_32(row_max(fmaxf(fmaxf(fabsf(o[0]), fabsf(o[1])), fmaxf(fabsf(o[2]), fabsf(o[3])))));
        const float inv = am > 1e-30f ? 32767.0f / am : 0.f;
        const int q0 = __float2int_rn(o[0] * inv), q1 = __float2int_rn(o[1] * inv), q2 = __float2int_rn(o[2] * inv), q3 = __float2int_rn(o[3] * inv);
        if (ln < DH / 4) reinterpret_cast<u32x2*>(s_sc + served * (DH / 2))[ln] = u32x2{PERM_LO((unsigned)q1, (unsigned)q0), PERM_LO((unsigned)q3, (unsigned)q2)};
        const bool nan = __any((o[0] + o[1]) + (o[2] + o[3]) != (o[0] + o[1]) + (o[2] + o[3]));
        if (ln == 0) s_ml[served][2] = nan ? 0x7fc00000u : nz_bits(am * (1.0f / 32767.0f));
        LGKM0();
        if (ln == 0) s_c[F_FREE] = served + 1;
      }
#pragma unroll 1
      for (int c = from * 24 + ln; c < served * 24; c += 64) st128nt(rs, WS_PQ + xb * 5 * (DH / 2) + c * 4, reinterpret_cast<const u32x4*>(s_sc)[c]);
      VMCNT(0);
      if (ln < 8) st128(rs, WS_PML + xb * 64 + (from ? 32 : 0) + ln * 4, s_ml[ln]);
      if (ln == 0) s_c[S_BURST + (from ? 1 : 0)] = clk();
      CFENCE();
      if (ln == 0) __hip_atomic_fetch_add(s_c + F_DONE, (unsigned)(served - from), __ATOMIC_RELAXED, __HIP_MEMORY_SCOPE_WORKGROUP);
    }
    if (n > NA) lds_wait<1>(s_c + F_DONE, n); else lds_wait<8>(s_c + F_DONE, n);
  };
  auto done_all = [&]() {
#pragma unroll 1
    for (int n = 4; n <= NA; ++n) done(n);
  };
  const int lp = (lane < DH / 4 ? lane : 0) * 4;
  auto weight = [&](int it, int x0, int cnt) -> float {
    const int mlw = WS_PML + (x0 + (lane & (cnt - 1))) * 64 + (it < 4 ? 0 : 32) + it * 4;
    u32x4 ml;
#pragma unroll 1
    for (unsigned spin = 0; spin < SPIN_CAP; ++spin) {
      ml = ld128(rs, mlw);
      if (__all(min4(ml) != 0u)) break;
      __builtin_amdgcn_s_sleep(16);
    }
    const float mk = __uint_as_float(ml[0]);
    const float e = __builtin_amdgcn_exp2f(mk - max_x16_32(row_max(mk)));
    return e / (wave_sum(e * __uint_as_float(ml[1])) * ((float)cnt * (1.0f / 64.0f))) * __uint_as_float(ml[2]);
  };
  const unsigned tok = __builtin_amdgcn_readfirstlane(r0) + 1u;
  unsigned echoed = 0u, fold = 0u;
  int slow = 0, prev = 0;
  auto lag = [&]() {
    const unsigned v = __hip_atomic_load(s_c + S_MOVED + (lane & 7), __ATOMIC_RELAXED, __HIP_MEMORY_SCOPE_WORKGROUP);
    fold += v;
    unsigned a = 0u;
#pragma unroll
    for (int k = 0; k < 8; ++k) a += __builtin_amdgcn_readlane(v, k);
    return a >> 2;
  };
  auto tell = [&]() { if (lane < 4) { s_c[S_SLOW + 2 * lane] = slow; s_c[S_SLOW + 2 * lane + 1] = fold; } };
  auto round = [&](unsigned rd, bool lead) {
    const int tr = RT0 + (int)(rd - 1) * RTS;
    until(t0, tr);
    const unsigned bh = lag();
    if (bh > RMIN && lane < 8) st128(rs, WS_HA + bid * 32 + lane * 4, u32x4{echoed + rd, tok, tb0, bh});
    if (lead) { until(t0, tr + RA1); leader_release(rs, lane, tb0, rd, slow, prev); }
    until(t0, tr + RA2);
    const u32x2 rv = ld64(rs, WS_HR + bid * 2);
    slow = (int)__builtin_amdgcn_readfirstlane(rv[1]) >> 16;
#pragma unroll 1
    for (unsigned k = min(__builtin_amdgcn_readfirstlane(rv[0]) - tok, rd) + 1u; k <= rd; ++k) slow = slow > 0 ? max(slow - GB, 0) : slow - (k <= PRD ? PRB : 0);
    tell();
  };
  auto prep = [&](bool lead, unsigned nrd) {
    if (tw == 0) {
      const int c = lane & (GS - 1);
      unsigned qe = s_c[S_ECHO + c];
      bool first = true, released = false, sent = false;
#pragma unroll 1
      while ((int)(clk() - t0) < HST) {
        const unsigned qn = ld32(rs, qf + (GS + c) * 32);
        if (mm < NPROD && __any(qn != qe)) {
          if (lane < 32) st32(rs, qf + mm * 32 + lane, qn);
          qe = qn;
        }
        __builtin_amdgcn_s_sleep(64);
      }
      const unsigned behind = lag();
#pragma unroll 1
      for (unsigned spin = 0; spin < SPIN_CAP && !released; ++spin) {
        const unsigned qn = ld32(rs, qf + (GS + c) * 32);
        const u32x2 rv = ld64(rs, WS_HR + bid * 2);
        const unsigned nv = __builtin_amdgcn_readfirstlane(ld32(rs, WS_HN + bid));
        if (__builtin_amdgcn_readfirstlane(rv[0]) - tok <= NRD) {
          released = true;
          slow = (int)__builtin_amdgcn_readfirstlane(rv[1]) >> 16;
          if (lane == 0) { s_c[S_FIRST] = min((tb0 - rv[1]) & 0xffffu, 300u) + 1u; }
        } else {
          if (mm < NPROD && __any(qn != qe)) {
            if (lane < 32) st32(rs, qf + mm * 32 + lane, qn);
            qe = qn;
          }
          if (first || nv != echoed) {
            echoed = nv; first = false;
            if (lane < 8) st128(rs, WS_HA + bid * 32 + lane * 4, u32x4{echoed, tok, tb0, behind});
          }
          if (lead && !sent) sent = leader_release(rs, lane, tb0, 0u, slow, prev);
          __builtin_amdgcn_s_sleep(32);
        }
      }
      tell();
      if (lane == 0) s_c[F_REL] = 1u;
      if (lane < DH / 4) {
        u32x4 p[3][2];
#pragma unroll
        for (int e = 0; e < 3; ++e)
#pragma unroll
          for (int k = 0; k < 2; ++k) p[e][k] = ld128nt(rs, WS_QP + (((e ? 8 + e * 8 + (xb >> 5) : xb >> 4) * 2 + k) * NB + 1) * DH + lane * 4);
#pragma unroll
        for (int e = 0; e < 3; ++e) *reinterpret_cast<u32x4*>(&s_q[(1 + e) * DH + lane * 4]) = q_pack(p[e][0], p[e][1]);
      }
      LGKM0();
      if (lane == 0) s_c[F_Q2] = 3u;
#pragma unroll 1
      for (unsigned rd = 1; rd <= nrd; ++rd) round(rd, lead);
    }
    if (tw == 3) {
#pragma unroll 1
      for (int ik = 0; ik < NS; ++ik) {
        const int it = seg_of_k(ik), nv = (seg_beg(it + 1) - seg_beg(it)) >> 4;
        lds_wait<4>(s_c + C_SC + ik, ik == 0 ? NW : NMW);
        f32x4* sp = reinterpret_cast<f32x4*>(s_sc + seg_beg(it) * 16) + lane;
        f32x4 v[4];
#pragma unroll
        for (int k = 0; k < 4; ++k) v[k] = sp[(k & (nv - 1)) * 64];
        const f32x4 mx = __builtin_elementwise_max(__builtin_elementwise_max(v[0], v[1]), __builtin_elementwise_max(v[2], v[3]));
        const float m = max_x16_32(row_max(fmaxf(fmaxf(mx[0], mx[1]), fmaxf(mx[2], mx[3]))));
        float l = 0.f;
#pragma unroll
        for (int k = 0; k < 4; ++k) {
#pragma unroll
          for (int e = 0; e < 4; ++e) v[k][e] = __builtin_amdgcn_exp2f(v[k][e] - m);
          if (k < nv) {
            l += (v[k][0] + v[k][1]) + (v[k][2] + v[k][3]);
            sp[k * 64] = v[k];
          }
        }
        l = wave_sum(l);
        if (lane == 0) s_ml[it] = u32x4{nz_bits(m), nz_bits(l), 0x3f800000u, 0x3f800000u};
        LGKM0();
        if (lane == 0) s_c[F_P] = ik + 1;
      }
    }
  };
  auto pull = [&]() {
    typedef __attribute__((address_space(1))) const unsigned g_u32;
    const int id = (ro - 16) * 4 + tw;
    constexpr int RW = 160 - 32 * GE, LW = RW * 4 * 3 * 2 / 64;
    g_u32* p1[2];
#pragma unroll
    for (int i = 0; i < 2; ++i) {
      const int l = id * LW + min(i * 32 + (lane >> 1), max(LW - 1, 0)), m = l >= LW * 32 ? l - LW * 32 : l, rr = m / 3;
      p1[i] = (g_u32*)((l >= LW * 32 ? wu : wg) + (size_t)(rr / max(RW, 1) * 160 + 32 * GE + rr % max(RW, 1)) * FF + xl * 192 + (m % 3) * 64 + (lane & 1) * 32);
    }
    g_u32* p2 = (g_u32*)(wd + (size_t)(id * 24 + min(lane >> 1, 23)) * DM + xl * 64 + (lane & 1) * 32);
    asm volatile("" : "+v"(p1[0]), "+v"(p1[1]), "+v"(p2));
    if (GLEAD) lds_wait<1>(s_c + C_LAST, GCNT); else done(NS);
    const unsigned tp = clk() + PW + tw * PS;
    until(tp, 0);
    const unsigned v0 = LW > 0 ? p1[0][0] : 0u;
    MEMFENCE();
    until(tp, 4 * PS);
    const unsigned v1 = LW > 32 ? p1[1][0] : 0u;
    MEMFENCE();
    until(tp, DPW);
    const unsigned v2 = p2[0], v3 = p2[xl < 2 ? 256 : 0];
    MEMFENCE();
    return (v0 ^ v1) + (v2 ^ v3);
  };
  if (isW) {
    u32x4 bx[24];
    const char* p = reinterpret_cast<const char*>(wo + (size_t)((w_ks * 4 + tw) * DH + r) * DM + w_ct * 64 + j * 8);
#pragma unroll 1
    for (int i = 0; i < 24; ++i) { until(t0, tw == 0 ? W0START + i * (W0SPAN / 24) : W1START + i * (W1SPAN / 24)); glds16_nt(p, lds_base + i * 1024); p += 8 * DM * 2; VMCNT(WFD); }
    asm volatile("s_waitcnt vmcnt(0)" ::: "memory");
#pragma unroll
    for (int s = 0; s < 6; ++s) {
      u32x2 e[8];
#pragma unroll
      for (int k = 0; k < 8; ++k) e[k] = *reinterpret_cast<const u32x2*>(reg + (s * 32 + qd * 8 + k) * 128 + mr * 8);
#pragma unroll
      for (int k = 0; k < 4; ++k) {
        bx[s * 4 + 0][k] = PERM_LO(e[2 * k + 1][0], e[2 * k][0]); bx[s * 4 + 1][k] = PERM_HI(e[2 * k + 1][0], e[2 * k][0]);
        bx[s * 4 + 2][k] = PERM_LO(e[2 * k + 1][1], e[2 * k][1]); bx[s * 4 + 3][k] = PERM_HI(e[2 * k + 1][1], e[2 * k][1]);
      }
    }
#pragma unroll
    for (int s = 0; s < 24; s += 4) asm volatile("" : "+v"(bx[s]), "+v"(bx[s + 1]), "+v"(bx[s + 2]), "+v"(bx[s + 3]));
    LGKM0();
    prep(false, NRD - WL);
    lds_wait<8>(s_c + F_REL, 1u);
    const int hh = w_ks * 4 + tw;
#pragma unroll 1
    for (int pass = 0; pass < 3; ++pass) {
      if (pass < 2) {
        if (WL && pass == 1 && tw == 0) round(NRD, false);
        done(pass ? NA : 4);
        pause(lds_get(s_c + S_BURST + pass), pass ? WDELAY2 : WDELAY2 + 300);
        const int b0 = pass * 2, b1 = pass + 1;
        u32x4 v0, v1;
#pragma unroll 1
        for (unsigned spin = 0; spin < SPIN_CAP; ++spin) {
          v0 = ld128(rs, WS_OC + (b0 * NH + hh) * DH + lp);
          v1 = ld128(rs, WS_OC + (b1 * NH + hh) * DH + lp);
          if (__all(min(min4(v0), min4(v1)) != 0u)) break;
          __builtin_amdgcn_s_sleep(16);
        }
        if (lane < DH / 4) {
          *reinterpret_cast<u32x2*>(reg + (b0 * 4) * AW_LD + lane * 8) = u32x2{v0[0], v0[1]};
          *reinterpret_cast<u32x2*>(reg + (b0 * 4 + 1) * AW_LD + lane * 8) = u32x2{v0[2], v0[3]};
          *reinterpret_cast<u32x2*>(reg + (b1 * 4) * AW_LD + lane * 8) = u32x2{v1[0], v1[1]};
          *reinterpret_cast<u32x2*>(reg + (b1 * 4 + 1) * AW_LD + lane * 8) = u32x2{v1[2], v1[3]};
        }
      } else {
        const float wk = weight(NS - 1, hh * GS, GS);
        float w[GS];
#pragma unroll
        for (int k = 0; k < GS; ++k) { w[k] = __uint_as_float(__builtin_amdgcn_readlane(__float_as_uint(wk), k)); asm volatile("" : "+s"(w[k])); }
        done(NS);
        __builtin_amdgcn_s_setprio(3);
        const int po = WS_PO + hh * GS * DH;
        u32x4 pv[GS];
#pragma unroll 1
        for (unsigned spin = 0; spin < SPIN_CAP; ++spin) {
#pragma unroll
          for (int k = 0; k < GS; ++k) pv[k] = __builtin_amdgcn_raw_buffer_load_b128(rs, lp * 4, (po + k * DH) * 4, AUX_SC1);
          unsigned mn = min4(pv[0]);
#pragma unroll
          for (int k = 1; k < GS; ++k) mn = min(mn, min4(pv[k]));
          if (__all(mn != 0u)) break;
          __builtin_amdgcn_s_sleep(1);
        }
        f32x4 o = w[0] * __builtin_bit_cast(f32x4, pv[0]), o2 = w[1] * __builtin_bit_cast(f32x4, pv[1]);
#pragma unroll
        for (int k = 2; k < GS; k += 2) { o += w[k] * __builtin_bit_cast(f32x4, pv[k]); o2 += w[k + 1] * __builtin_bit_cast(f32x4, pv[k + 1]); }
        u32x2 hi, lw;
        split4(o + o2, hi, lw);
        if (lane < DH / 4) {
          *reinterpret_cast<u32x2*>(reg + ((NB - 1) * 4) * AW_LD + lane * 8) = hi;
          *reinterpret_cast<u32x2*>(reg + ((NB - 1) * 4 + 1) * AW_LD + lane * 8) = lw;
        }
      }
      CFENCE();
      u32x4 a[6];
#pragma unroll
      for (int s = 0; s < 6; ++s) a[s] = *reinterpret_cast<const u32x4*>(reg + mr * AW_LD + s * 64 + qd * 16);
      __builtin_amdgcn_sched_barrier(0);
      f32x4 c[4];
#pragma unroll
      for (int t = 0; t < 4; ++t) c[t] = f32x4{0.f, 0.f, 0.f, 0.f};
#pragma unroll
      for (int s = 0; s < 6; ++s)
#pragma unroll
        for (int t = 0; t < 4; ++t) c[t] = mfma16(a[s], bx[s * 4 + t], c[t]);
      const bool mine = max(qd - 1, 0) == pass;
      f32x4* r4 = reinterpret_cast<f32x4*>(red);
      if (mine) r4[tw * 64 + lane] = f32x4{c[0][0] + c[0][1], c[1][0] + c[1][1], c[2][0] + c[2][1], c[3][0] + c[3][1]};
      const int k = arrive(s_c + C_W + pass);
      f32x4 sum = (r4[lane] + r4[64 + lane]) + (r4[128 + lane] + r4[192 + lane]);
      asm volatile("" : "+v"(sum));
      if (arrived(k) == 3) {
        if (mine) st128(rs, WS_SLAB + (w_ks * NB + qd) * DM + w_ct * 64 + mr * 4, u32x4{nz_bits(sum[0]), nz_bits(sum[1]), nz_bits(sum[2]), nz_bits(sum[3])});
      }
    }
  } else if (isG || isH) {
    f32x4* rst = reinterpret_cast<f32x4*>(red);
    const int hb = isH ? xl - 2 : NB - 1;
    uint2 wr4 = make_uint2(0u, 0u), xg = wr4;
    if (lane < 40) {
      wr4 = *reinterpret_cast<const uint2*>(wr + tw * 160 + lane * 4);
      xg = *reinterpret_cast<const uint2*>(x + row_of(hb) * DM + tw * 160 + lane * 4);
    }
    const int eb = mr >> 2;
    const float cq = bf_lo(*reinterpret_cast<const unsigned*>(x + row_of(eb) * DM));
    const float wsum = wave_sum((bf_lo(wr4.x) + bf_hi(wr4.x)) + (bf_lo(wr4.y) + bf_hi(wr4.y)));
    f32x4 xc = f32x4{bf_lo(xg.x), bf_hi(xg.x), bf_lo(xg.y), bf_hi(xg.y)} - __uint_as_float(__builtin_amdgcn_readlane(__float_as_uint(cq), hb * 4));
    asm volatile("" : "+v"(xc));
    typedef __attribute__((address_space(1))) const u32x4 g_u32x4;
    g_u32x4* wp[10];
    wp[0] = (g_u32x4*)(((mr & 4) ? wu : wg) + (size_t)(tw * 160 + qd * 8 + (mr >> 3) * 4) * FF + g_cg * 32 + (mr & 3) * 8);
    u32x4 e[20], bx[20];
    auto frag = [&](int s) __attribute__((always_inline)) {
      unsigned w[8][2];
#pragma unroll
      for (int i = 0; i < 4; ++i)
#pragma unroll
        for (int d = 0; d < 2; ++d) {
          w[i][d] = __builtin_amdgcn_update_dpp(e[s * 4 + i][d], e[s * 4 + i][2 + d], DPP_ROR8, 0xf, 0xc, false);
          w[4 + i][d] = __builtin_amdgcn_update_dpp(e[s * 4 + i][2 + d], e[s * 4 + i][d], DPP_ROR8, 0xf, 0x3, false);
        }
#pragma unroll
      for (int k = 0; k < 4; ++k) {
        bx[s * 4 + 0][k] = PERM_LO(w[2 * k + 1][0], w[2 * k][0]); bx[s * 4 + 1][k] = PERM_HI(w[2 * k + 1][0], w[2 * k][0]);
        bx[s * 4 + 2][k] = PERM_LO(w[2 * k + 1][1], w[2 * k][1]); bx[s * 4 + 3][k] = PERM_HI(w[2 * k + 1][1], w[2 * k][1]);
      }
      PIN4(bx, s * 4);
    };
    if (isG) {
#pragma unroll
      for (int i = 0; i < 4 * GE; ++i) {
        until(t0, tw == 0 ? W0START + i * (W0SPAN / max(4 * GE, 1)) : W1START + i * (W1SPAN / max(4 * GE, 1)));
        e[i] = __builtin_nontemporal_load(wp[0] + ((i >> 2) * 32 + (i & 3)) * (FF / 8));
        MEMFENCE();
      }
#pragma unroll
      for (int s = 0; s < GE; ++s) frag(s);
    }
    prep(false, NRD);
    lds_wait<8>(s_c + F_REL, 1u);
    auto hrow = [&](bool fast) -> f32x4 {
      f32x4 hv = {0.f, 0.f, 0.f, 0.f};
      if (lane < 40) {
        const int off = (WS_SLAB + hb * DM + tw * 160 + lane * 4) * 4;
        u32x4 sl[KS];
#pragma unroll 1
        for (unsigned spin = 0; spin < SPIN_CAP; ++spin) {
#pragma unroll
          for (int ks = 0; ks < KS; ++ks) sl[ks] = __builtin_amdgcn_raw_buffer_load_b128(rs, off, ks * (NB * DM * 4), AUX_SC1);
          unsigned mn = min4(sl[0]);
#pragma unroll
          for (int ks = 1; ks < KS; ++ks) mn = min(mn, min4(sl[ks]));
          if (!__any(mn == 0u)) break;
          if (fast) __builtin_amdgcn_s_sleep(1); else __builtin_amdgcn_s_sleep(16);
        }
#define SL(k) __builtin_bit_cast(f32x4, sl[k])
        hv = xc + (((SL(0) + SL(1)) + (SL(2) + SL(3))) + ((SL(4) + SL(5)) + (SL(6) + SL(7))));
#undef SL
      }
      return hv;
    };
    auto sums = [&](f32x4 hv) -> f32x4 {
      const float s1 = wave_sum((hv[0] + hv[1]) + (hv[2] + hv[3]));
      const float s2 = wave_sum((hv[0] * hv[0] + hv[1] * hv[1]) + (hv[2] * hv[2] + hv[3] * hv[3]));
      const float dot = wave_sum((hv[0] * bf_lo(wr4.x) + hv[1] * bf_hi(wr4.x)) + (hv[2] * bf_lo(wr4.y) + hv[3] * bf_hi(wr4.y)));
      return f32x4{s1, s2, dot, wsum};
    };
    if (isH) {
      if (tw == 3 || hb == 2) done_all(); else done(4);
      pause(lds_get(s_c + S_BURST + (hb >> 1)), hb < 2 ? HDELAY : HDELAY2);
      const f32x4 hv = hrow(false), sm = sums(hv);
      u32x4 v = split8(hv, (lane & 1) != 0);
      if (lane >= 40) v = u32x4{nz_bits(sm[0]), nz_bits(sm[1]), nz_bits(sm[2]), nz_bits(sm[3])};
      if (lane < 48) st128(rs, WS_H + (hb * 4 + tw) * 192 + lane * 4, v);
      done_all();
      const unsigned pl = pull();
      asm volatile("" :: "v"(pl));
      return;
    }
    done_all();
#pragma unroll
    for (int i = 2 * GE; i < 10; ++i) { wp[i] = wp[0] + ((i >> 1) * 32 + (i & 1) * 2) * (FF / 8); asm volatile("" : "+v"(wp[i])); }
    done(NS);
    if (GW) until(clk(), GW);
#pragma unroll
    for (int i = 4 * GE; i < 20; ++i) e[i] = wp[i >> 1][(i & 1) * (FF / 8)];
    u32x4 a[5], sm;
    const int hoff = (WS_H + (min(mr >> 2, NB - 2) * 4 + tw) * 192 + qd * 8 + (mr & 1) * 4) * 4;
#pragma unroll
    for (int s = 0; s < 5; ++s) a[s] = __builtin_amdgcn_raw_buffer_load_b128(rs, hoff + s * 128, 0, AUX_SC1);
    sm = ld128(rs, WS_H + (min(lane, NB - 2) * 4 + tw) * 192 + 160);
    MEMFENCE();
#pragma unroll
    for (int s = GE; s < 5; ++s) frag(s);
    {
#pragma unroll 1
      for (unsigned spin = 0; spin < SPIN_CAP; ++spin) {
        unsigned mn = min4(sm);
#pragma unroll
        for (int s = 0; s < 5; ++s) mn = min(mn, min4(a[s]));
        if (__all(mn != 0u)) break;
        __builtin_amdgcn_s_sleep(2);
#pragma unroll
        for (int s = 0; s < 5; ++s) a[s] = __builtin_amdgcn_raw_buffer_load_b128(rs, hoff + s * 128, 0, AUX_SC1);
        sm = ld128(rs, WS_H + (min(lane, NB - 2) * 4 + tw) * 192 + 160);
      }
      if (lane < NB - 1) rst[lane * 4 + tw] = __builtin_bit_cast(f32x4, sm);
#pragma unroll
      for (int s = 0; s < 5; ++s) {
        if (mr & 2) a[s] = u32x4{0x3f803f80u, 0x3f803f80u, 0x3f803f80u, 0x3f803f80u};
        asm volatile("" : "+v"(a[s]));
      }
    }
    const f32x4 hv = hrow(true);
    if (lane < 40) {
      u32x2 hi, lw;
      split4(hv, hi, lw);
      *reinterpret_cast<u32x2*>(reg + lane * 8) = hi;
      *reinterpret_cast<u32x2*>(reg + AG_LD + lane * 8) = lw;
    }
    CFENCE();
    if ((mr & 14) == 12) {
#pragma unroll
      for (int s = 0; s < 5; ++s) a[s] = *reinterpret_cast<const u32x4*>(reg + (mr & 1) * AG_LD + s * 64 + qd * 16);
    }
    __builtin_amdgcn_sched_barrier(0);
    f32x4 c[4];
#pragma unroll
    for (int t = 0; t < 4; ++t) c[t] = f32x4{0.f, 0.f, 0.f, 0.f};
#pragma unroll
    for (int s = 0; s < 5; ++s)
#pragma unroll
      for (int t = 0; t < 4; ++t) c[t] = mfma16(a[s], bx[s * 4 + t], c[t]);
    { const f32x4 s3 = sums(hv); if (lane == 0) rst[(NB - 1) * 4 + tw] = s3; }
    const int slot = qd * 16 + (mr & 7) * 2 + (mr >> 3);
    *reinterpret_cast<f32x4*>(reg + 4096 + slot * 16) = f32x4{c[0][0] + c[0][1], c[1][0] + c[1][1], c[0][2], c[1][2]};
    *reinterpret_cast<f32x4*>(reg + 4096 + 1152 + slot * 16) = f32x4{c[2][0] + c[2][1], c[3][0] + c[3][1], c[2][2], c[3][2]};
    const int k = arrive(s_c + C_T);
    f32x4 ts[4], pg[4], pu[4];
#pragma unroll
    for (int w = 0; w < 4; ++w) {
      const unsigned char* src = smem + OFF_REG + w * REG + 4096 + (lane & 1) * 1152 + (eb * 16 + qd * 2 + ((lane >> 1) & 1)) * 16;
      ts[w] = rst[eb * 4 + w];
      pg[w] = *reinterpret_cast<const f32x4*>(src); pu[w] = *reinterpret_cast<const f32x4*>(src + 128);
    }
    PIN4(ts, 0); PIN4(pg, 0); PIN4(pu, 0);
    if (arrived(k) == 3) {
      const f32x4 t = (ts[0] + ts[1]) + (ts[2] + ts[3]), g4 = (pg[0] + pg[1]) + (pg[2] + pg[3]), u4 = (pu[0] + pu[1]) + (pu[2] + pu[3]);
      const float d = t[0] * (-1.0f / DM);
      const float rstd = __builtin_amdgcn_rsqf(fmaxf(t[1] + d * t[0], 0.f) * (1.0f / DM) + LN_EPS);
      const float g0 = rstd * (g4[0] + d * g4[2]), g1 = rstd * (g4[1] + d * g4[3]);
      const float a0 = g0 * __builtin_amdgcn_rcpf(1.0f + __expf(-g0)) * (rstd * (u4[0] + d * u4[2]));
      const float a1 = g1 * __builtin_amdgcn_rcpf(1.0f + __expf(-g1)) * (rstd * (u4[1] + d * u4[3]));
      const __hip_bfloat162 a2 = __halves2bfloat162(__float2bfloat16(a0), __float2bfloat16(a1));
      const float aw = __uint_as_float(max(*reinterpret_cast<const unsigned*>(&a2), 1u));
      const u32x4 av = {__float_as_uint(aw), __float_as_uint(dpp<0x55>(aw)), __float_as_uint(dpp<0xAA>(aw)), __float_as_uint(dpp<0xFF>(aw))};
      const float rr = rstd * (t[2] + d * t[3]);
      const u32x4 sv = {nz_bits(cq - d), nz_bits(rstd), nz_bits(__builtin_amdgcn_rcpf(1.0f + __expf(-rr))), 0x3f800000u};
      const bool stat = g_cg == 0 && (lane & 51) == 1;
      if ((lane & 3) == 0 || stat) st128(rs, stat ? WS_ST + eb * 4 : WS_ACT + g_cg * 64 + lane, stat ? sv : av);
    }
  } else if (isD) {
    const uint2 xd = *reinterpret_cast<const uint2*>(x + row_of(tw) * DM + d_ct * 32 + j * 4);
    const __amdgpu_buffer_rsrc_t rd = __builtin_amdgcn_make_buffer_rsrc(const_cast<bf16*>(wd), 0, FF * DM * 2, 0x00020000);
    prep(false, NRD);
    lds_wait<8>(s_c + F_REL, 1u);
    done_all();
    const unsigned pl = pull();
    done(NS);
    asm volatile("" :: "v"(pl));
    const unsigned te = clk();
    if (DW) until(te, DW);
    u32x4 bd[24];
    {
      const int off = ((tw * 384 + (mr >> 3) * 32 + qd * 8 + (mr & 4)) * DM + d_ct * 32 + (mr & 3) * 8) * 2;
      u32x4 e[24];
#pragma unroll
      for (int i = 0; i < 24; ++i) e[i] = __builtin_amdgcn_raw_buffer_load_b128(rd, off + (i & 3) * (DM * 2), (i >> 2) * 64 * (DM * 2), 0);
      MEMFENCE();
#pragma unroll
      for (int s = 0; s < 6; ++s) {
        unsigned w[8][2];
#pragma unroll
        for (int i = 0; i < 4; ++i)
#pragma unroll
          for (int d = 0; d < 2; ++d) {
            w[i][d] = __builtin_amdgcn_update_dpp(e[s * 4 + i][d], e[s * 4 + i][2 + d], 0x114, 0xf, 0xa, false);
            w[4 + i][d] = __builtin_amdgcn_update_dpp(e[s * 4 + i][2 + d], e[s * 4 + i][d], 0x104, 0xf, 0x5, false);
          }
#pragma unroll
        for (int k = 0; k < 4; ++k) {
          bd[s * 4 + 0][k] = PERM_LO(w[2 * k + 1][0], w[2 * k][0]); bd[s * 4 + 1][k] = PERM_HI(w[2 * k + 1][0], w[2 * k][0]);
          bd[s * 4 + 2][k] = PERM_LO(w[2 * k + 1][1], w[2 * k][1]); bd[s * 4 + 3][k] = PERM_HI(w[2 * k + 1][1], w[2 * k][1]);
        }
        PIN4(bd, s * 4);
      }
    }
    until(te, DLATE);
    {
      u32x4 sl;
      unsigned spin = 0;
      const int wl = min(lane, 19), watch = WS_SLAB + ((tw * 2 + wl / 10) * NB + NB - 1) * DM + (wl % 10) * 64;
#pragma unroll 1
      for (; spin < SPIN_CAP; ++spin) {
        sl = ld128(rs, WS_SLAB + (r * NB + tw) * DM + d_ct * 32 + j * 4);
        const unsigned wv = ld32(rs, watch);
        if (__all(min(min4(sl), wv) != 0u)) break;
        __builtin_amdgcn_s_sleep(1);
      }
      if (lane == 0) __hip_atomic_fetch_max(s_c + C_W + 1, clk() - t0 + (spin ? DLATE2 : 0), __ATOMIC_RELAXED, __HIP_MEMORY_SCOPE_WORKGROUP);
      f32x4 hv = {bf_lo(xd.x), bf_hi(xd.x), bf_lo(xd.y), bf_hi(xd.y)};
#pragma unroll
      for (int i = 0; i < 4; ++i) hv[i] += sum_r(__uint_as_float(sl[i]));
      if (lane < 8) reinterpret_cast<f32x4*>(red + 512 + tw * 32)[lane] = hv;
      LGKM0();
      lds_add(s_c + C_W, lane);
      lds_wait<1>(s_c + C_W, 4u);
    }
    const int col = (mr & 3) * 8 + (mr & 4) + (mr >> 3) * 2;
    f32x2 hv = *reinterpret_cast<const f32x2*>(red + 512 + qd * 32 + col);
    asm volatile("" : "+v"(hv));
    u32x4 a[6], st;
#pragma unroll
    for (int s = 0; s < 6; ++s) a[s] = u32x4{1u, 1u, 1u, 1u};
    const int aoff = (WS_ACT + tw * 12 * 64 + ((mr >> 1) & 1) * 64 + qd * 16 + (mr >> 2) * 4) * 4;
    until(t0, lds_get(s_c + C_W + 1));
#pragma unroll 1
    for (unsigned spin = 0; spin < SPIN_CAP; ++spin) {
      st = ld128(rs, WS_ST + qd * 4);
      if ((mr & 1) == 0) {
#pragma unroll
        for (int s = 0; s < 6; ++s) a[s] = __builtin_amdgcn_raw_buffer_load_b128(rs, aoff + s * 512, 0, AUX_SC1);
      }
      unsigned mn = min4(st);
#pragma unroll
      for (int s = 0; s < 6; ++s) mn = min(mn, min4(a[s]));
      if (__all(mn != 0u)) break;
      __builtin_amdgcn_s_sleep(1);
    }
    f32x4 c[4];
#pragma unroll
    for (int t = 0; t < 4; ++t) c[t] = f32x4{0.f, 0.f, 0.f, 0.f};
#pragma unroll
    for (int s = 0; s < 6; ++s)
#pragma unroll
      for (int t = 0; t < 4; ++t) c[t] = mfma16(a[s], bd[s * 4 + t], c[t]);
    const f32x4 sf = __builtin_bit_cast(f32x4, st);
    hv += (hv - sf[0]) * sf[1];
    const bool up = mr >= 8;
    const float k0 = up ? c[2][2] : c[0][0], k1 = up ? c[3][2] : c[1][0], o0 = up ? c[0][2] : c[2][0], o1 = up ? c[1][2] : c[3][0];
    f32x2* r2 = reinterpret_cast<f32x2*>(red);
    r2[tw * 64 + lane] = f32x2{k0 + dpp<DPP_ROR8>(o0), k1 + dpp<DPP_ROR8>(o1)};
    const int k = arrive(s_c + C_T);
    f32x2 y = hv + ((r2[lane] + r2[64 + lane]) + (r2[128 + lane] + r2[192 + lane])) * sf[2];
    asm volatile("" : "+v"(y));
    if (arrived(k) == 3) {
      const __hip_bfloat162 o2 = __halves2bfloat162(__float2bfloat16(y[0]), __float2bfloat16(y[1]));
      __hip_atomic_store(reinterpret_cast<unsigned*>(out + row_of(qd) * DM + d_ct * 32 + col), *reinterpret_cast<const unsigned*>(&o2), __ATOMIC_RELAXED, __HIP_MEMORY_SCOPE_AGENT);
    }
  } else {
    prep(bid == LEADER, NRD);
    if (isC && tw == 1) {
      lds_wait<8>(s_c + F_REL, 1u);
      const int cs = c_it != 1 ? c_it * 2 : max(g >> 3, 1), cnt = c_it != 1 ? 8 : (g < 16 ? 16 : 32), x0 = c_it != 1 ? g * 8 : (g < 16 ? g * 16 : (g & 7) * 32);
      done(c_it < 2 ? 4 : NA);
      pause(lds_get(s_c + S_BURST + (c_it >> 1)), WDELAY);
      const float wk = weight(cs, x0, cnt);
      f32x4 o = {0.f, 0.f, 0.f, 0.f};
#pragma unroll 1
      for (int k0 = 0; k0 < cnt; k0 += 16) {
        u32x2 pv[16];
#pragma unroll
        for (int k = 0; k < 16; ++k) pv[k] = __builtin_amdgcn_raw_buffer_load_b64(rs, lp * 2, (WS_PQ + ((x0 + ((k0 + k) & (cnt - 1))) * 5 + cs) * (DH / 2)) * 4, AUX_EARLY);
#pragma unroll
        for (int k = 0; k < 16; ++k) {
          const float w = k0 + k < cnt ? __uint_as_float(__builtin_amdgcn_readlane(__float_as_uint(wk), k0 + k)) : 0.f;
          o += w * f32x4{(float)(short)pv[k][0], (float)((int)pv[k][0] >> 16), (float)(short)pv[k][1], (float)((int)pv[k][1] >> 16)};
        }
      }
      u32x2 hi, lw;
      split4(o, hi, lw);
      if (lane < DH / 4) st128(rs, WS_OC + (c_it * NG + g) * DH + lane * 4, u32x4{max(hi[0], 1u), max(hi[1], 1u), max(lw[0], 1u), max(lw[1], 1u)});
    }
    done_all();
    const unsigned pl = pull();
    asm volatile("" :: "v"(pl));
  }
  if (tw == 3) done_all();
}

extern "C" void kernel_launch(void* const* d_in, const int* in_sizes, int n_in, void* d_out, int out_size, void* d_ws,
                              size_t ws_size, hipStream_t stream) {
  const bf16* x = (const bf16*)d_in[0];
  const bf16* kc = (const bf16*)d_in[1];
  const bf16* vc = (const bf16*)d_in[2];
  const bf16* wq = (const bf16*)d_in[3];
  const bf16* wo = (const bf16*)d_in[4];
  const bf16* wr = (const bf16*)d_in[6];
  const bf16* wg = (const bf16*)d_in[7];
  const bf16* wu = (const bf16*)d_in[8];
  const bf16* wd = (const bf16*)d_in[9];
  fused_kernel<<<NWG, NW * 64, 0, stream>>>(x, kc, vc, wq, wo, wr, wg, wu, wd, (float*)d_ws, (bf16*)d_out);
}
```

```cpp
#include <hip/hip_runtime.h>
#include <hip/hip_bf16.h>
#include <stdint.h>

typedef __hip_bfloat16 bf16;
using u32x2 = __attribute__((ext_vector_type(2))) unsigned;
using u32x4 = __attribute__((ext_vector_type(4))) unsigned;
using f32x4 = __attribute__((ext_vector_type(4))) float;
using f32x2 = __attribute__((ext_vector_type(2))) float;
using bf16x8 = __attribute__((ext_vector_type(8))) __bf16;

constexpr int NB = 4, DM = 640, NH = 32, SK = 8192, DH = 192, HD = NH * DH, FF = 1536;
constexpr float LN_EPS = 1e-5f;
constexpr float QSCALE = 0.07216878364870322f * 1.4426950408889634f;

constexpr int NWG = 256;
constexpr int GS = 8, NG = NWG / GS, NI = GS / 2;
constexpr int HPG = NI / NB;
constexpr int PPI = 512 / GS;
constexpr int NPAIR = NI * PPI;

constexpr int TAUK = 1320, TAUV = 1332;
constexpr int TSTART = 110;
constexpr int OGAP = 105, OLATE = 250, OSTEP = 12, OB128 = 0;
constexpr int VGAP = 50;
constexpr int DEEP1 = 100, DEEP2 = 200, SDEEP = 0, COARSE = 1, LAGOFF = 20, ENDT = 12, ENDS = 2;
constexpr int SPRINT = 0;
constexpr int JUMP = 145;
constexpr int HST = 2800, RT0 = 3900, RTS = 1000, RA1 = 220, RA2 = 500, LEADER = 24, WL = 0;
constexpr unsigned NRD = 5, RMIN = 30, FBP = 60, FBL = 150;
constexpr int W0START = 1700, W0SPAN = 1000, W1START = 2700, W1SPAN = 2600;
constexpr int ROT = 16, ROWS = 0x1B;
constexpr int FBM = 100, FBX = 15, FB0 = 80, FB1 = 10, FBS = 10;
constexpr int FBD = 10, HOLD = 4000, GB = 24, GBS = 2, GBX = 60, GBF = 12;
constexpr unsigned PRD = 3; constexpr int PRB = 24;
constexpr int ANCH = 90;
constexpr int TLEAD = 10, PFD = 6, WFD = 3;
constexpr int QWAIT = 750, QBACK = 100;
constexpr int WDELAY = 100, WDELAY2 = 460, DLATE = 100, DL2 = 0;
constexpr int TPF = 15, TPT = 600, TPS = 20;
constexpr int ZPOS = 0, ZA = 16, ZB = 16, CPF = 1, CPT = 0, CODE = 24 * 64 * 46;
constexpr int GLEAD = 1, GCNT = 4, DP = 0, DPW = 60, GE = 0, PW = 30, PS = 0, HDELAY = 1150, HDELAY2 = 780, GW = 0, DW = 0;

constexpr int KS = 8;
constexpr int WS_PML = 0;
constexpr int WS_PO = WS_PML + NWG * 64;
constexpr int WS_PQ = WS_PO + NWG * DH;
constexpr int WS_OC = WS_PQ + NWG * 5 * (DH / 2);
constexpr int WS_SLAB = WS_OC + (NB - 1) * NH * DH;
constexpr int WS_ACT = WS_SLAB + KS * NB * DM;
constexpr int WS_H = WS_ACT + NB * FF / 2;
constexpr int WS_HN = WS_H + NB * 4 * 192;
constexpr int WS_HR = WS_HN + NWG;
constexpr int WS_HA = WS_HR + 2 * NWG;
constexpr int QFS = 2 * GS * 32;
constexpr int WS_QF = WS_HA + 32 * NWG;
constexpr int WS_QP = WS_QF + NG * QFS;
constexpr int WS_END = WS_QP + NH * 2 * NB * DH;
constexpr int NPROD = HPG * 6;
static_assert(WS_PO % 32 == 0 && WS_SLAB % 32 == 0 && WS_ACT % 32 == 0 && WS_HN % 32 == 0 && WS_QF % 32 == 0 && WS_QP % 32 == 0, "line alignment");


__device__ __forceinline__ int row_of(int b) { return (ROWS >> (2 * b)) & 3; }
__device__ __forceinline__ float bf_lo(unsigned u) { return __uint_as_float(u << 16); }
__device__ __forceinline__ float bf_hi(unsigned u) { return __uint_as_float(u & 0xffff0000u); }
__device__ __forceinline__ f32x2 bf_pair(unsigned u) { return f32x2{bf_lo(u), bf_hi(u)}; }
__device__ __forceinline__ f32x2 fma2(float a, f32x2 b, f32x2 c) { return __builtin_elementwise_fma(f32x2{a, a}, b, c); }
__device__ __forceinline__ f32x2 fma2(f32x2 a, f32x2 b, f32x2 c) { return __builtin_elementwise_fma(a, b, c); }

template <int CTRL> __device__ __forceinline__ float dpp(float x) {
  return __builtin_bit_cast(float, __builtin_amdgcn_mov_dpp(__builtin_bit_cast(int, x), CTRL, 0xf, 0xf, true));
}
constexpr int DPP_XOR1 = 0xB1, DPP_XOR2 = 0x4E, DPP_HMIRROR = 0x141, DPP_MIRROR = 0x140, DPP_ROR8 = 0x128;

__device__ __forceinline__ float sum_x16_32(float x) {
  auto s = __builtin_amdgcn_permlane16_swap(__float_as_uint(x), __float_as_uint(x), false, false);
  x = __uint_as_float(s[0]) + __uint_as_float(s[1]);
  auto t = __builtin_amdgcn_permlane32_swap(__float_as_uint(x), __float_as_uint(x), false, false);
  return __uint_as_float(t[0]) + __uint_as_float(t[1]);
}
__device__ __forceinline__ float max_x16_32(float x) {
  auto s = __builtin_amdgcn_permlane16_swap(__float_as_uint(x), __float_as_uint(x), false, false);
  x = fmaxf(__uint_as_float(s[0]), __uint_as_float(s[1]));
  auto t = __builtin_amdgcn_permlane32_swap(__float_as_uint(x), __float_as_uint(x), false, false);
  return fmaxf(__uint_as_float(t[0]), __uint_as_float(t[1]));
}
__device__ __forceinline__ float tsum32(float a, float b) {
  auto t = __builtin_amdgcn_permlane32_swap(__float_as_uint(a), __float_as_uint(b), false, false);
  return __uint_as_float(t[0]) + __uint_as_float(t[1]);
}
__device__ __forceinline__ float tsum16(float a, float b) {
  auto t = __builtin_amdgcn_permlane16_swap(__float_as_uint(a), __float_as_uint(b), false, false);
  return __uint_as_float(t[0]) + __uint_as_float(t[1]);
}
__device__ __forceinline__ float sum_r(float v) { v += dpp<DPP_ROR8>(v); return sum_x16_32(v); }
__device__ __forceinline__ float row_sum(float v) {
  v += dpp<DPP_XOR1>(v); v += dpp<DPP_XOR2>(v); v += dpp<DPP_HMIRROR>(v);
  return v + dpp<DPP_MIRROR>(v);
}
__device__ __forceinline__ float row_max(float v) {
  v = fmaxf(v, dpp<DPP_XOR1>(v)); v = fmaxf(v, dpp<DPP_XOR2>(v)); v = fmaxf(v, dpp<DPP_HMIRROR>(v));
  return fmaxf(v, dpp<DPP_MIRROR>(v));
}
__device__ __forceinline__ float wave_sum(float v) {
  v += dpp<DPP_XOR1>(v); v += dpp<DPP_XOR2>(v); v += dpp<DPP_HMIRROR>(v);
  return sum_r(v);
}

__device__ __forceinline__ u32x4 ld_nt(const char* p) { return __builtin_nontemporal_load(reinterpret_cast<const u32x4*>(p)); }

struct Tile { u32x4 d[3]; };

#define PIN4(a, o) asm volatile("" : "+v"((a)[(o) + 0]), "+v"((a)[(o) + 1]), "+v"((a)[(o) + 2]), "+v"((a)[(o) + 3]))
#define MEMFENCE() do { asm volatile("" ::: "memory"); __builtin_amdgcn_sched_barrier(0); } while (0)

__device__ __forceinline__ void glds16_nt(const void* gsrc, unsigned lds_dst) {
  unsigned keep;
  asm volatile("s_mov_b32 %0, m0\n\ts_mov_b32 m0, %2\n\ts_nop 0\n\tglobal_load_lds_dwordx4 %1, off nt\n\ts_mov_b32 m0, %0"
               : "=&s"(keep) : "v"(gsrc), "s"(lds_dst) : "memory");
}

using bf16x2 = __attribute__((ext_vector_type(2))) __bf16;
__device__ __forceinline__ float dot2(unsigned a, unsigned b, float c) { return __builtin_amdgcn_fdot2_f32_bf16(__builtin_bit_cast(bf16x2, a), __builtin_bit_cast(bf16x2, b), c, false); }
__device__ __forceinline__ float tile_score(const Tile& k, const u32x2 (&q)[12]) {
  float s0 = 0.f, s1 = 0.f;
#pragma unroll
  for (int i = 0; i < 3; ++i) {
    u32x4 v = k.d[i];
    asm("" : "+v"(v));
#pragma unroll
    for (int d = 0; d < 4; ++d) { s0 = dot2(v[d], q[i * 4 + d][0], s0); s1 = dot2(v[d], q[i * 4 + d][1], s1); }
  }
  return s0 + s1;
}
__device__ __forceinline__ void pair_put(float a, float b, float* dst, int j) {
  float s = (j < 4 ? a : b) + dpp<DPP_HMIRROR>(j < 4 ? b : a);
  s += dpp<DPP_XOR1>(s);
  s += dpp<DPP_XOR2>(s);
  if ((j & 3) == 0) dst[j * 2] = s;
}
__device__ __forceinline__ void score_put(float s, float* dst, int j) {
  s += dpp<DPP_XOR1>(s);
  s += dpp<DPP_XOR2>(s);
  s += dpp<DPP_HMIRROR>(s);
  if (j == 0) *dst = s;
}
__device__ __forceinline__ void tile_load(Tile& t, const char* base, unsigned off, int half) {
#pragma unroll
  for (int i = 0; i < 3; ++i) t.d[i] = ld_nt(base + off + half * 3072 + i * 128);
}
__device__ __forceinline__ void pair_issue(Tile& ta, Tile& tb, const char* base, unsigned off, int deep) {
  deep = __builtin_amdgcn_readfirstlane(deep);
#define WAIT_IF(level, n) asm volatile("s_cmp_lg_u32 %0, " #level "\n\ts_cbranch_scc1 .Lpi%=\n\ts_waitcnt vmcnt(" #n ")\n.Lpi%=:" :: "s"(deep) : "scc", "memory")
  const char* p = base + off;
  if constexpr (COARSE) {
    WAIT_IF(0, 3);
    ta.d[0] = ld_nt(p); ta.d[1] = ld_nt(p + 128); ta.d[2] = ld_nt(p + 256); MEMFENCE();
    WAIT_IF(0, 3); WAIT_IF(1, 6);
    tb.d[0] = ld_nt(p + 3072); tb.d[1] = ld_nt(p + 3200); tb.d[2] = ld_nt(p + 3328); MEMFENCE();
  } else {
    WAIT_IF(0, 5); ta.d[0] = ld_nt(p); MEMFENCE();
    WAIT_IF(0, 5); ta.d[1] = ld_nt(p + 128); MEMFENCE();
    WAIT_IF(0, 5); ta.d[2] = ld_nt(p + 256); MEMFENCE();
    WAIT_IF(1, 6);
    WAIT_IF(0, 5); tb.d[0] = ld_nt(p + 3072); MEMFENCE();
    WAIT_IF(0, 5); tb.d[1] = ld_nt(p + 3200); MEMFENCE();
    WAIT_IF(0, 5); tb.d[2] = ld_nt(p + 3328); MEMFENCE();
  }
#undef WAIT_IF
}
__device__ __forceinline__ void pv_last(float p, Tile& v, f32x2 (&acc)[12]) {
#pragma unroll
  for (int i = 0; i < 3; ++i) {
    asm volatile("" : "+v"(v.d[i]), "+v"(p));
#pragma unroll
    for (int d = 0; d < 4; ++d) acc[i * 4 + d] = fma2(p, bf_pair(v.d[i][d]), acc[i * 4 + d]);
    PIN4(acc, i * 4);
  }
}
__device__ __forceinline__ void q_load(u32x2 (&q)[12], const float* src, int j) {
#pragma unroll
  for (int i = 0; i < 6; ++i) {
    const u32x4 a = *reinterpret_cast<const u32x4*>(src + (i >> 1) * 64 + j * 8 + (i & 1) * 4);
    q[i * 2] = u32x2{a[0], a[1]}; q[i * 2 + 1] = u32x2{a[2], a[3]};
  }
}
#define VMCNT(n) asm volatile("s_waitcnt vmcnt(%0)" :: "n"(n) : "memory")

constexpr int AUX_SC1 = 16, AUX_EARLY = 18;
constexpr unsigned SPIN_CAP = 1u << 17;
__device__ __forceinline__ unsigned nz_bits(float v) { return max(__float_as_uint(v), 1u); }
__device__ __forceinline__ bool all_nz(u32x4 v) { return v[0] != 0u && v[1] != 0u && v[2] != 0u && v[3] != 0u; }
__device__ __forceinline__ unsigned min4(u32x4 v) { return min(min(v[0], v[1]), min(v[2], v[3])); }
__device__ __forceinline__ unsigned ld32(__amdgpu_buffer_rsrc_t rs, int word) { return __builtin_amdgcn_raw_buffer_load_b32(rs, word * 4, 0, AUX_SC1); }
__device__ __forceinline__ u32x2 ld64(__amdgpu_buffer_rsrc_t rs, int word) { return __builtin_amdgcn_raw_buffer_load_b64(rs, word * 4, 0, AUX_SC1); }
__device__ __forceinline__ u32x4 ld128(__amdgpu_buffer_rsrc_t rs, int word) { return __builtin_amdgcn_raw_buffer_load_b128(rs, word * 4, 0, AUX_SC1); }
__device__ __forceinline__ void st32(__amdgpu_buffer_rsrc_t rs, int word, unsigned v) { __builtin_amdgcn_raw_buffer_store_b32(v, rs, word * 4, 0, AUX_SC1); }
__device__ __forceinline__ void st64(__amdgpu_buffer_rsrc_t rs, int word, u32x2 v) { __builtin_amdgcn_raw_buffer_store_b64(v, rs, word * 4, 0, AUX_SC1); }
__device__ __forceinline__ void st128(__amdgpu_buffer_rsrc_t rs, int word, u32x4 v) { __builtin_amdgcn_raw_buffer_store_b128(v, rs, word * 4, 0, AUX_SC1); }

__device__ __forceinline__ u32x4 ld128nt(__amdgpu_buffer_rsrc_t rs, int word) { return __builtin_amdgcn_raw_buffer_load_b128(rs, word * 4, 0, AUX_EARLY); }
__device__ __forceinline__ void st128nt(__amdgpu_buffer_rsrc_t rs, int word, u32x4 v) { __builtin_amdgcn_raw_buffer_store_b128(v, rs, word * 4, 0, AUX_EARLY); }

__device__ __forceinline__ bool leader_release(__amdgpu_buffer_rsrc_t rs, int lane, unsigned t0, unsigned rd, int slow, int& prev) {
  u32x4 a[4];
#pragma unroll
  for (int k = 0; k < 4; ++k) a[k] = ld128(rs, WS_HA + (lane * 4 + k) * 32);
  const u32x4 n = ld128(rs, WS_HN + lane * 4);
  if (rd == 0u && !__all(a[0][0] == n[0] && a[1][0] == n[1] && a[2][0] == n[2] && a[3][0] == n[3])) return false;
  const float e = fmaxf(fmaxf((float)(int)(t0 - a[0][2]), (float)(int)(t0 - a[1][2])), fmaxf((float)(int)(t0 - a[2][2]), (float)(int)(t0 - a[3][2])));
  unsigned first = t0 - (unsigned)(int)max_x16_32(row_max(e));
  float ls = 0.f, lx = 0.f;
#pragma unroll
  for (int k = 0; k < 4; ++k) {
    const float l = a[k][0] == n[k] + rd ? (float)min(a[k][3] + (rd ? 0u : min(a[k][2] - first, 300u)), 2000u) : 0.f;
    ls += l; lx = fmaxf(lx, l);
  }
  ls = wave_sum(ls) * (1.0f / NWG); lx = max_x16_32(row_max(lx));
  const int extra = __builtin_amdgcn_readfirstlane(min(max((int)(ls * (FBM / 100.0f) + lx * (FBX / 100.0f)) - (rd ? FB1 + (int)(NRD - rd) * FBD : FB0), 0), 600));
  if (rd != 0u && __all(ls * NWG < HOLD)) { prev = 0; return true; }
  first = (first & 0xffffu) | ((unsigned)(slow + extra + min(extra, prev) * (int)(rd == NRD ? FBL : FBP) / 100) << 16);
  prev = extra;
  st128(rs, WS_HR + lane * 8, u32x4{a[0][1] + rd, first, a[1][1] + rd, first});
  st128(rs, WS_HR + lane * 8 + 4, u32x4{a[2][1] + rd, first, a[3][1] + rd, first});
  return true;
}

typedef __attribute__((address_space(3))) unsigned lds_u32;
__device__ __forceinline__ unsigned lds_add(lds_u32* p, int lane) {
  unsigned k = 0;
  if (lane == 0) k = __hip_atomic_fetch_add(p, 1u, __ATOMIC_RELAXED, __HIP_MEMORY_SCOPE_WORKGROUP);
  return __builtin_amdgcn_readfirstlane(k);
}
#define CFENCE() asm volatile("" ::: "memory")
__device__ __forceinline__ int arrive(lds_u32* p) {
  int v;
  unsigned long long keep;
  asm volatile("s_mov_b64 %1, exec\n\ts_mov_b64 exec, 1\n\tds_add_rtn_u32 %0, %2, %3\n\ts_mov_b64 exec, %1"
               : "=&v"(v), "=&s"(keep) : "v"((unsigned)(size_t)p), "v"(1u) : "memory");
  return v;
}
__device__ __forceinline__ int arrived(int v) {
  asm volatile("s_waitcnt lgkmcnt(0)" : "+v"(v) :: "memory");
  return __builtin_amdgcn_readfirstlane(v);
}
__device__ __forceinline__ unsigned lds_get(lds_u32* p) {
  return __builtin_amdgcn_readfirstlane(__hip_atomic_load(p, __ATOMIC_RELAXED, __HIP_MEMORY_SCOPE_WORKGROUP));
}
template <int SLEEP> __device__ __forceinline__ void lds_wait(lds_u32* p, unsigned n) {
#pragma unroll 1
  for (unsigned spin = 0; spin < (SPIN_CAP << 4); ++spin) {
    if (lds_get(p) >= n) break;
    __builtin_amdgcn_s_sleep(SLEEP);
  }
  asm volatile("" ::: "memory");
}
__device__ __forceinline__ unsigned clk() { return (unsigned)__builtin_amdgcn_s_memrealtime(); }
struct Table {
  unsigned t0; int was, moved, slowed, folded;
  u32x2 told;
  __device__ __forceinline__ int gate(int due, lds_u32* mine, bool hold = true, int sh = 0) {
    const int fold = __builtin_amdgcn_readfirstlane(told[1]);
    if (fold != folded) { const int f = min(fold - folded, moved); moved -= f; slowed += f; folded = fold; *mine = moved; }
    const int left = (int)__builtin_amdgcn_readfirstlane(told[0]) - slowed, d = max(min(left, FBS), left < -GBX ? -GBF : -GBS);
    t0 += d; was -= d; slowed += d;
    int late = (int)(clk() - t0) - due;
    if (late - was > JUMP) { t0 += late - was; moved += late - was; late = was; *mine = moved; }
    const int lag = max(late - max(left - d, 0) - LAGOFF, 0);
    mine[4] = lag;
    was = late;
#pragma unroll 1
    while (hold && late < 0) { __builtin_amdgcn_s_sleep(1); late = (int)(clk() - t0) - due; }
    return max(lag < (DEEP1 >> sh) ? 0 : (lag < (DEEP2 >> sh) ? 1 : 2), hold ? 0 : SDEEP);
  }
  __device__ __forceinline__ void listen(lds_u32* slow) {
    told = __builtin_bit_cast(u32x2, __hip_atomic_load((__attribute__((address_space(3))) unsigned long long*)slow, __ATOMIC_RELAXED, __HIP_MEMORY_SCOPE_WORKGROUP));
  }
  __device__ __forceinline__ void agree(lds_u32* all, lds_u32* mine) {
    if (moved == 0) return;
    const int m = (int)min(min(lds_get(all), lds_get(all + 1)), min(lds_get(all + 2), lds_get(all + 3))) + 30;
    if (m < moved) { t0 -= moved - m; moved = m; was = 1 << 20; *mine = moved; }
  }
};
__device__ __forceinline__ void until(unsigned t0, int due) {
#pragma unroll 1
  while ((int)(clk() - t0) < due) __builtin_amdgcn_s_sleep(1);
}
__device__ __forceinline__ void pause(unsigned t0, unsigned due) {
#pragma unroll 1
  while (clk() - t0 < due) __builtin_amdgcn_s_sleep(16);
}
#define LGKM0() asm volatile("s_waitcnt lgkmcnt(0)" ::: "memory")

__device__ __forceinline__ void split4(f32x4 v, u32x2& hi, u32x2& lo) {
  unsigned h[4], r[4];
#pragma unroll
  for (int i = 0; i < 4; ++i) { h[i] = __float_as_uint(v[i]) & 0xffff0000u; r[i] = __float_as_uint(v[i] - __uint_as_float(h[i])); }
  hi = u32x2{__builtin_amdgcn_perm(h[1], h[0], 0x07060302u), __builtin_amdgcn_perm(h[3], h[2], 0x07060302u)};
  lo = u32x2{__builtin_amdgcn_perm(r[1], r[0], 0x07060302u), __builtin_amdgcn_perm(r[3], r[2], 0x07060302u)};
}
__device__ __forceinline__ u32x4 split8(f32x4 v, bool odd) {
  u32x2 hi, lo;
  split4(v, hi, lo);
  const unsigned n0 = __float_as_uint(dpp<DPP_XOR1>(__uint_as_float(odd ? hi[0] : lo[0]))), n1 = __float_as_uint(dpp<DPP_XOR1>(__uint_as_float(odd ? hi[1] : lo[1])));
  return u32x4{max(odd ? n0 : hi[0], 1u), max(odd ? n1 : hi[1], 1u), max(odd ? lo[0] : n0, 1u), max(odd ? lo[1] : n1, 1u)};
}
__device__ __forceinline__ u32x4 q_pack(u32x4 a, u32x4 b) {
  u32x2 hi, lo;
  split4((__builtin_bit_cast(f32x4, a) + __builtin_bit_cast(f32x4, b)) * QSCALE, hi, lo);
  return u32x4{hi[0], lo[0], hi[1], lo[1]};
}
__device__ __forceinline__ f32x4 mfma16(u32x4 a, u32x4 b, f32x4 c) {
  return __builtin_amdgcn_mfma_f32_16x16x32_bf16(__builtin_bit_cast(bf16x8, a), __builtin_bit_cast(bf16x8, b), c, 0, 0, 0);
}

constexpr int NW = 8, NMW = 4;
constexpr int NLP = 5, NRP = 8;
constexpr int NTP = 4 * (NLP + NRP);
constexpr int REG = NLP * 6 * 1024;
constexpr int NRING = 4;
constexpr int NS = 6, NA = NS - 1;
__device__ __forceinline__ int seg_beg(int s) { return s >= NS ? NPAIR : ((0xC86540 >> (4 * s)) & 15) * 16; }
__device__ __forceinline__ int seg_k(int t) { return (int)(0x5433222211110000ull >> (4 * (t >> 4))) & 15; }
__device__ __forceinline__ int seg_of_k(int k) { return (0x123540 >> (4 * k)) & 15; }
__device__ __forceinline__ int k_of_seg(int s) { return (0x213450 >> (4 * s)) & 15; }
__device__ __forceinline__ int pair_of(int t) { return t < 64 ? t : (t < 192 ? t + 64 : t - (t < 224 ? 96 : (t < 240 ? 144 : 176))); }
constexpr int OFF_CNT = 0, OFF_REG = 256,
              OFF_SC = OFF_REG + 4 * REG, OFF_BUF = OFF_SC + NPAIR * 16 * 4, OFF_Q = OFF_BUF + NRING * NMW * DH * 4, OFF_ML = OFF_Q + NS * DH * 4,
              OFF_RED = OFF_ML + 128, OFF_RS = OFF_RED + 4 * 256 * 4, OFF_END = OFF_RS + 5 * 16;
enum { F_REL = 0, F_DONE, C_LAST, C_T = 3, F_OUT, C_P, C_K, F_P, F_Q = 8, F_FREE, S_FIRST, F_Q2, C_W = 12, C_SC = 16, C_PART = 24, S_ECHO = 32, S_BURST = 40, S_LATE = 42, S_MOVED = 48, S_SLOW = 56 };
__device__ __forceinline__ int due_k(int p) { return TSTART + ((p * TAUK) >> 6); }
__device__ __forceinline__ int due_v(int p) { return TSTART + OGAP + ((NPAIR * TAUK + p * TAUV + max(p - NPAIR / 2, 0) * (VGAP * 64 / (NPAIR / 2))) >> 6); }
constexpr int AW_LD = DH * 2 + 16, AG_LD = 512 + 16;

__global__ void __launch_bounds__(NW * 64) fused_kernel(const bf16* __restrict__ x, const bf16* __restrict__ kc,
                                                        const bf16* __restrict__ vc, const bf16* __restrict__ wq,
                                                        const bf16* __restrict__ wo, const bf16* __restrict__ wr,
                                                        const bf16* __restrict__ wg, const bf16* __restrict__ wu,
                                                        const bf16* __restrict__ wd, float* ws, bf16* __restrict__ out) {
  __shared__ __attribute__((aligned(16))) unsigned char smem[OFF_END];
  float* s_sc = reinterpret_cast<float*>(smem + OFF_SC);
  float* s_buf = reinterpret_cast<float*>(smem + OFF_BUF);
  float* s_q = reinterpret_cast<float*>(smem + OFF_Q);
  u32x4* s_ml = reinterpret_cast<u32x4*>(smem + OFF_ML);
  lds_u32* s_c = (lds_u32*)(smem + OFF_CNT);

  const int tid = threadIdx.x, lane = tid & 63, r = lane >> 3, j = lane & 7;
  const int wave = __builtin_amdgcn_readfirstlane(tid >> 6);
  const int tw = wave - NMW;
  const int bid = blockIdx.x, xl = bid & 7, idx = bid >> 3;
  const int g = (xl * 32 + idx) / GS, mm = idx % GS;
  const __amdgpu_buffer_rsrc_t rs = __builtin_amdgcn_make_buffer_rsrc(ws, 0, WS_END * 4, 0x00020000);
  const int ro = ((idx & 3) << 3) | (idx >> 2);
  const bool isW = ro < 10, isG = ro >= 10 && ro < 16;
  const int w_ks = xl, w_ct = ro;
  const int g_cg = xl * 6 + (ro - 10);
  const int d_ct = (xl + ((ro - 16) >> 2) * 8) * 2 + (((ro - 16) >> 1) & 1);
  const bool isD = ro >= 16 && ro < 24 && (ro & 1) == 0 && d_ct < DM / 32;
  const bool isH = ro == 22 && xl >= 2 && xl < 6;
  const bool isC = (mm & 7) == 3 || (mm & 7) >= 6;
  const int c_it = (mm >> 3) * 3 + max((mm & 7) - 5, 0);
  const int qf = WS_QF + g * QFS;
  const char* kb = reinterpret_cast<const char*>(kc);
  const char* vb = reinterpret_cast<const char*>(vc);
  const unsigned lo = (r * 24 + j) * 16;
  const int xb = g * GS + mm, c0 = mm * 8 + g * ROT;
  const int b2 = (2 * NH + (xb >> 4)) * 1024 + (xb & 15) * 8, b3 = (2 * NH + 16 + (xb >> 5)) * 1024 + (xb & 31) * 8;
  auto poff = [&](int p) -> unsigned {
    const int turn = (p & 63) >> 2;
    int t = (row_of(p >> 6) * NH + g) * 1024 + ((turn * 64 + (p & 3) * 2 + c0) & 1023);
    if ((p >> 6) == 1) t = (turn >= 8 ? b2 + (turn & 7) * 128 : b3 + (~turn & 4) * 2048 + (turn & 3) * 256) + (p & 3) * 2;
    return (unsigned)t * 3072u;
  };
  const unsigned lds_base = (unsigned)(size_t)(__attribute__((address_space(3))) unsigned char*)smem + OFF_REG + tw * REG;
  auto merged = [&](int it, int ln) -> f32x4 {
    const float* src = s_buf + (it % NRING) * (NMW * DH) + ((ln >> 1) & 7) * 24 + (ln >> 4) * 8 + (ln & 1) * 4;
    return (*reinterpret_cast<const f32x4*>(src) + *reinterpret_cast<const f32x4*>(src + DH)) +
           (*reinterpret_cast<const f32x4*>(src + 2 * DH) + *reinterpret_cast<const f32x4*>(src + 3 * DH));
  };
  auto publish = [&](int it) -> bool {
    const int k = arrive(s_c + C_PART + it);
    const f32x4 m = merged(it, lane < DH / 4 ? lane : 0);
    u32x4 o = {nz_bits(m[0]), nz_bits(m[1]), nz_bits(m[2]), nz_bits(m[3])};
    asm volatile("" : "+v"(o));
    if (arrived(k) != NMW - 1) return false;
    if (lane < DH / 4) st128(rs, WS_PO + xb * DH + lane * 4, o);
    if (lane == 0) __hip_atomic_fetch_add(s_c + F_DONE, 1u, __ATOMIC_RELAXED, __HIP_MEMORY_SCOPE_WORKGROUP);
    return true;
  };
#define NONCE() do { unsigned n_ = __builtin_amdgcn_readfirstlane(f) + 1u; while (__any(f == n_)) ++n_; \
    if (lane < 32) st32(rs, qf + (GS + mm) * 32 + lane, n_); f = n_; } while (0)
  auto zero = [&]() {
    const u32x4 z = {0u, 0u, 0u, 0u};
    __builtin_amdgcn_raw_buffer_store_b128(z, rs, (lane < 48 ? WS_PO + xb * DH + lane * 4 : WS_PML + xb * 64 + (lane - 48) * 4) * 4, 0, ZA);
    const int o = isW ? WS_SLAB + w_ks * NB * DM + w_ct * 64 : (isG ? WS_ACT + g_cg * 64 : (isH ? WS_H + (xl - 2) * 768 : WS_OC + (c_it * NG + g) * DH));
    __builtin_amdgcn_raw_buffer_store_b128(z, rs, (lane < (isG ? 16 : (isC ? 48 : (isW || isH ? 64 : 0))) ? o + (isW ? (lane >> 4) * DM + (lane & 15) * 4 : lane * 4) : WS_END) * 4, 0, ZB);
    if (isH) { st128(rs, o + 256 + lane * 4, z); st128(rs, o + 512 + lane * 4, z); }
  };
  unsigned long long pc;
  asm volatile("s_getpc_b64 %0" : "=s"(pc));
  unsigned r0 = 0u;
  unsigned t0 = clk();
  unsigned tb0 = t0;
  if (wave < NMW) {
    Tile ta, tb, tc, td;
    const int c = lane & (GS - 1);
    unsigned f = 0u;
    if (wave == 3) { f = ld32(rs, qf + c * 32 + mm); if (ZPOS == 1) zero(); }
    if (mm < NPROD) {
      const int hp = g + (mm / 6) * NG, ck = (mm % 6) >> 1, hf = mm & 1, jp = hf * 4 + wave;
      const int bb = lane >> 4, t = lane & 15;
      const u32x4* xp = reinterpret_cast<const u32x4*>(x + row_of(bb) * DM);
      u32x4 xv[5], xs = {0u, 0u, 0u, 0u};
#pragma unroll
      for (int k = 0; k < 5; ++k) xv[k] = xp[t + 16 * k];
      if (t < 10) xs = xp[jp * 10 + t];
      const u32x4* wp = reinterpret_cast<const u32x4*>(wq + (size_t)(jp * 80 + r) * HD + hp * DH + ck * 64) + j;
      u32x4 wv[10];
#pragma unroll
      for (int k = 0; k < 10; ++k) wv[k] = __builtin_nontemporal_load(wp + (size_t)k * 8 * (HD / 8));
      if (wave == 0) s_c[lane] = lane == C_K ? NTP + NMW : 0u;
      MEMFENCE(); LGKM0(); __builtin_amdgcn_s_barrier(); MEMFENCE();
      if (wave == 3) { NONCE(); }
      float sm = 0.f;
#pragma unroll
      for (int k = 0; k < 5; ++k)
#pragma unroll
        for (int d = 0; d < 4; ++d) sm += bf_lo(xv[k][d]) + bf_hi(xv[k][d]);
      const float mean = row_sum(sm) * (1.0f / DM);
      float sv = 0.f;
#pragma unroll
      for (int k = 0; k < 5; ++k)
#pragma unroll
        for (int d = 0; d < 4; ++d) { const float a = bf_lo(xv[k][d]) - mean, c2 = bf_hi(xv[k][d]) - mean; sv += a * a + c2 * c2; }
      const float rstd = 1.0f / sqrtf(row_sum(sv) * (1.0f / DM) + LN_EPS);
      float* s_xn = s_buf + wave * 320;
      if (t < 10) {
#pragma unroll
        for (int d = 0; d < 4; ++d) {
          s_xn[(t * 8 + 2 * d) * 4 + bb] = (bf_lo(xs[d]) - mean) * rstd;
          s_xn[(t * 8 + 2 * d + 1) * 4 + bb] = (bf_hi(xs[d]) - mean) * rstd;
        }
      }
      LGKM0();
      f32x2 qa[16];
#pragma unroll
      for (int i = 0; i < 16; ++i) qa[i] = f32x2{0.f, 0.f};
#pragma unroll
      for (int k = 0; k < 10; ++k) {
        const f32x4 xn = *reinterpret_cast<const f32x4*>(&s_xn[(k * 8 + r) * 4]);
#pragma unroll
        for (int d = 0; d < 4; ++d) {
          const f32x2 w2 = bf_pair(wv[k][d]);
#pragma unroll
          for (int e = 0; e < 4; ++e) qa[e * 4 + d] = fma2(xn[e], w2, qa[e * 4 + d]);
        }
      }
      float t16[16], t8[8];
#pragma unroll
      for (int i = 0; i < 16; ++i) t16[i] = tsum32(qa[i >> 1][i & 1], qa[8 + (i >> 1)][i & 1]);
#pragma unroll
      for (int i = 0; i < 8; ++i) t8[i] = tsum16(t16[i], t16[i + 8]);
#pragma unroll
      for (int i = 0; i < 8; ++i) t8[i] += dpp<DPP_ROR8>(t8[i]);
      const bool up = (lane & 8) != 0;
      f32x4* r4 = reinterpret_cast<f32x4*>(smem + OFF_RED);
      r4[wave * 64 + lane] = f32x4{up ? t8[4] : t8[0], up ? t8[5] : t8[1], up ? t8[6] : t8[2], up ? t8[7] : t8[3]};
      LGKM0();
      if (lds_add(s_c + C_P, lane) == NMW - 1) {
        asm volatile("" ::: "memory");
        const f32x4 o4 = (r4[lane] + r4[64 + lane]) + (r4[128 + lane] + r4[192 + lane]);
        st128nt(rs, WS_QP + ((hp * 2 + hf) * NB + bb) * DH + ck * 64 + j * 8 + (up ? 4 : 0), __builtin_bit_cast(u32x4, o4));
        VMCNT(0);
        if (lane == 0) s_c[F_OUT] = 1u;
      }
    } else {
      if (wave == 0) s_c[lane] = lane == C_K ? NTP + NMW : 0u;
      MEMFENCE(); LGKM0(); __builtin_amdgcn_s_barrier(); MEMFENCE();
      if (wave == 3) { NONCE(); }
      else {
        typedef __attribute__((address_space(1))) const unsigned g_u32;
        typedef __attribute__((address_space(1))) const char g_char;
        const int id = ((idx >> 3) * 2 + (mm & 1)) * 3 + wave;
        if (CPF && lane < CODE / (24 * 64)) {
          const unsigned v = *(g_u32*)((pc & ~255ull) + id * (CODE / 24) + lane * 64);
          asm volatile("" :: "v"(v));
        }
        if (((TPF >> (id < 4 ? 0 : (id < 16 ? 1 : (id < 20 ? 2 : 3)))) & 1) && lane < 7 && id < 21) {
          const int st = id < 8 ? (0x1320 >> ((id >> 1) * 4)) & 3 : (id >> 1) & 3, hf = id & 1;
          int head = row_of(st) * NH + xl * 4 + hf * 2;
          unsigned off = min(lane << 20, (6 << 20) - 64);
          if (st == 1) {
            head = 2 * NH + (hf ? 16 + xl : 2 * xl);
            if (hf) off = lane < 4 ? min(lane << 20, (3 << 20) - 64) : 8 * (3 << 20) + min((lane - 4) * (3 << 19), (3 << 20) - 64);
          }
          g_char* p = (g_char*)(id < 8 ? kb : vb) + (size_t)head * (3 << 20) + off;
          if (id >= 16) p = (g_char*)(id == 16 ? wo + xl * 4 * DH * DM : (id == 17 ? wg : (id == 18 ? wu : wd))) + min(lane * (id == 16 ? 163840 : 327680), (id == 16 ? 983040 : 1966080) - 64);
          if (id >= 20) p = (g_char*)(lane ? wr : out);
          until(t0, TPT + id * TPS);
          const unsigned v = __builtin_nontemporal_load((g_u32*)p);
          asm volatile("" :: "v"(v));
        }
      }
    }
    unsigned tk_e = poff(pair_of(lane * 4)) | (pair_of(lane * 4) >> 2) | (seg_k(lane * 4) << 6), tv_o = poff(lane * 4 + wave);
    int tk_d = due_k(lane * 4), tv_d = due_v(lane * 4 + wave);
    asm volatile("" : "+v"(tk_e), "+v"(tv_o), "+v"(tk_d), "+v"(tv_d));
    MEMFENCE();
    if (wave == 3) {
      __builtin_amdgcn_s_setprio(3);
      const unsigned nonce = f;
      if (ZPOS == 2) zero();
      unsigned echoed = 0u;
      if (mm < NPROD) {
        echoed = ld32(rs, qf + (GS + c) * 32);
        asm volatile("" : "+v"(echoed) :: "memory");
        lds_wait<1>(s_c + F_OUT, 1u);
        if (lane < 32) st32(rs, qf + mm * 32 + lane, echoed);
#pragma unroll 1
        while ((int)(clk() - t0) < QWAIT - QBACK) {
          const unsigned nv = ld32(rs, qf + (GS + c) * 32);
          if (__any(nv != echoed)) {
            if (lane < 32) st32(rs, qf + mm * 32 + lane, nv);
            echoed = nv;
          }
          __builtin_amdgcn_s_sleep(4);
        }
      }
      until(t0, QWAIT);
#pragma unroll 1
      for (unsigned spin = 0; spin < SPIN_CAP; ++spin) {
        const unsigned nv = ld32(rs, qf + (GS + c) * 32);
        f = ld32(rs, qf + c * 32 + mm);
        const bool done = __all(c == mm || c >= NPROD || f == nonce);
        if (mm < NPROD && __any(nv != echoed)) {
          if (lane < 32) st32(rs, qf + mm * 32 + lane, nv);
          echoed = nv;
        }
        if (done) break;
        __builtin_amdgcn_s_sleep(8);
      }
      VMCNT(0);
      if (lane < GS) s_c[S_ECHO + lane] = echoed;
      if (lane < DH / 4) {
        u32x4 p[3][2];
#pragma unroll
        for (int it = 0; it < 3; ++it)
#pragma unroll
          for (int k = 0; k < 2; ++k) p[it][k] = ld128nt(rs, WS_QP + ((g * 2 + k) * NB + (it ? it + 1 : 0)) * DH + lane * 4);
#pragma unroll
        for (int it = 0; it < 3; ++it) {
          *reinterpret_cast<u32x4*>(&s_q[(it ? it + 3 : 0) * DH + lane * 4]) = q_pack(p[it][0], p[it][1]);
          if (it != 1) { LGKM0(); if (lane == 0) s_c[F_Q] = it + 1; MEMFENCE(); }
        }
      }
    }
    __builtin_amdgcn_s_setprio(3);
    until(t0, due_k(NTP + wave));
    tb0 = lds_get(s_c + S_LATE);
    if ((unsigned)(tb0 - t0 + 100u) > 500u) tb0 = t0;
    until(tb0, due_k(NTP + wave));
    Table tt = {tb0, 1 << 20, 0, 0, 0, {0u, 0u}};
    bool common = false;
    tile_load(ta, kb, poff(NTP + wave) + lo, 0);
    tile_load(tb, kb, poff(NTP + wave) + lo, 1);
    MEMFENCE();
    {
      u32x2 q[12];
      int cp = NTP + wave, it = 0, nxt = lds_add(s_c + C_K, lane), passed = 0, ci = -1;
      auto kstep = [&](Tile& xa, Tile& xb_, Tile& ya, Tile& yb) __attribute__((always_inline)) -> bool {
        const bool last = nxt >= NPAIR;
        const unsigned e = __builtin_amdgcn_readlane(tk_e, (nxt >> 2) & 63);
        const unsigned off = (last ? __builtin_amdgcn_readlane(tv_o, 0) : (e & ~8191u) + (nxt & 3) * 6144) + lo;
        const char* base = last ? vb : kb;
        MEMFENCE();
        const int deep = tt.gate(last ? due_k(NPAIR + wave) : __builtin_amdgcn_readlane(tk_d, (nxt >> 2) & 63) + (nxt & 3) * (TAUK / 64 + 1), s_c + S_MOVED + wave);
        MEMFENCE();
        pair_issue(ya, yb, base, off, deep);
        if (it != ci) {
          LGKM0();
          for (; passed < it; ++passed) lds_add(s_c + C_SC + passed, lane);
          lds_wait<1>(s_c + (it < 3 ? F_Q : F_Q2), min(it + 1, 3));
          q_load(q, s_q + seg_of_k(it) * DH, j);
          if (!common) { const unsigned d = lds_get(s_c + S_FIRST); if (d) { tt.t0 -= d - 1u; common = true; tt.was = 1 << 20; } }
          tt.agree(s_c + S_MOVED, s_c + S_MOVED + wave);
          ci = it;
        }
        const int tk = arrive(s_c + C_K);
        tt.listen(s_c + S_SLOW + 2 * wave);
        pair_put(tile_score(xa, q), tile_score(xb_, q), s_sc + cp * 16 + r, j);
        MEMFENCE();
        cp = (e & 63) * 4 + (nxt & 3); it = (e >> 6) & 7; nxt = arrived(tk);
        return last;
      };
      bool swap;
      for (;;) {
        if (kstep(ta, tb, tc, td)) { swap = true; break; }
        if (kstep(tc, td, ta, tb)) { swap = false; break; }
      }
      LGKM0();
      for (; passed < NS; ++passed) lds_add(s_c + C_SC + passed, lane);
      __builtin_amdgcn_s_waitcnt(0x0F70);
      if (swap) { ta = tc; tb = td; }
      until(tt.t0, due_k(NPAIR + NMW + wave));
      if (OB128) {
        const u32x4* pw = reinterpret_cast<const u32x4*>(wo + (xl * 128 + idx * 4 + wave) * 3840) + lane;
        u32x4 v[8];
#pragma unroll
        for (int i = 0; i < 8; ++i) v[i] = pw[i < 7 ? i * 64 : 416];
#pragma unroll
        for (int i = 0; i < 8; ++i) asm volatile("" :: "v"(v[i]));
      } else {
        const unsigned* pw = reinterpret_cast<const unsigned*>(wo + (xl * 128 + idx * 4 + wave) * 3840) + lane * 16;
        const unsigned v0 = pw[0], v1 = pw[56 * 16];
        asm volatile("" :: "v"(v0), "v"(v1));
      }
      tt.was = 1 << 20;
    }
    const unsigned sc_r = (unsigned)(size_t)(__attribute__((address_space(3))) float*)s_sc + r * 4;
#pragma unroll 1
    for (int it = 0; it < NS; ++it) {
      lds_wait<1>(s_c + F_P, k_of_seg(it) + 1);
      if (!common) { const unsigned d = lds_get(s_c + S_FIRST); if (d) { tt.t0 -= d - 1u; common = true; tt.was = 1 << 20; } }
      tt.agree(s_c + S_MOVED, s_c + S_MOVED + wave);
      f32x2 acc[12];
#pragma unroll
      for (int i = 0; i < 12; ++i) acc[i] = f32x2{0.f, 0.f};
      auto vstep = [&](Tile& xa, Tile& xb_, Tile& ya, Tile& yb, int p, bool fin) __attribute__((always_inline)) {
        const unsigned off = __builtin_amdgcn_readlane(tv_o, (p >> 2) + 1) + lo;
        MEMFENCE();
        const int deep = tt.gate(__builtin_amdgcn_readlane(tv_d, (p >> 2) + 1), s_c + S_MOVED + wave, p + NMW < NPAIR - SPRINT * NMW, p >= NPAIR - ENDT * NMW ? ENDS : 0);
        float pa, pb;
        asm volatile("ds_read_b32 %0, %2\n\tds_read_b32 %1, %2 offset:32" : "=&v"(pa), "=&v"(pb) : "v"(sc_r + p * 64) : "memory");
        MEMFENCE();
        pair_issue(ya, yb, vb, off, deep);
        asm volatile("s_waitcnt lgkmcnt(0)" : "+v"(pa), "+v"(pb) :: "memory");
        if (!fin) tt.listen(s_c + S_SLOW + 2 * wave);
        else if (lane == 0) __hip_atomic_fetch_add(s_c + C_LAST, 1u, __ATOMIC_RELAXED, __HIP_MEMORY_SCOPE_WORKGROUP);
        pv_last(pa, xa, acc);
        pv_last(pb, xb_, acc);
        MEMFENCE();
      };
#pragma unroll 1
      for (int p = seg_beg(it) + wave; p < min(seg_beg(it + 1), NPAIR - 2 * NMW); p += 2 * NMW) {
        vstep(ta, tb, tc, td, p, false);
        vstep(tc, td, ta, tb, p + NMW, false);
      }
      if (it >= NRING) lds_wait<1>(s_c + F_FREE, it - NRING + 1);
      if (it == NS - 1) {
        vstep(ta, tb, tc, td, NPAIR - 2 * NMW + wave, true);
        const int p = NPAIR - NMW + wave;
        const float pa = s_sc[p * 16 + r], pb = s_sc[p * 16 + 8 + r];
        pv_last(pa, tc, acc);
        pv_last(pb, td, acc);
      }
      float t12[12], t6[6];
#pragma unroll
      for (int i = 0; i < 12; ++i) t12[i] = tsum32(acc[i >> 1][i & 1], acc[6 + (i >> 1)][i & 1]);
#pragma unroll
      for (int i = 0; i < 6; ++i) t6[i] = tsum16(t12[i], t12[i + 6]);
#pragma unroll
      for (int i = 0; i < 6; ++i) t6[i] += dpp<DPP_ROR8>(t6[i]);
      if ((lane & 8) == 0) {
        f32x2* dst = reinterpret_cast<f32x2*>(&s_buf[(it % NRING) * (NMW * DH) + wave * DH + j * 24 + (lane >> 4) * 6]);
        dst[0] = f32x2{t6[0], t6[1]}; dst[1] = f32x2{t6[2], t6[3]}; dst[2] = f32x2{t6[4], t6[5]};
      }
      CFENCE();
      if (it < NA) { if (lane == 0) __hip_atomic_fetch_add(s_c + C_PART + it, 1u, __ATOMIC_RELAXED, __HIP_MEMORY_SCOPE_WORKGROUP); }
    }
    publish(NS - 1);
    return;
  }

  {
    Tile pk[NRP * 2];
    __builtin_amdgcn_s_setprio(3);
    const unsigned it0 = (row_of(0) * NH + g) * 1024, ct = c0 + tw * 2;
    auto toff = [&](int turn) -> unsigned { return (it0 + ((turn * 64 + ct) & 1023)) * 3072u + lo; };
    unsigned tbw = 0u;
#pragma unroll 1
    for (int e = 0; e < NLP * 2; ++e) {
      const char* src = kb + (toff(e >> 1) + (e & 1) * 3072);
      if (e >= 2) until(tbw, (e * 2 * TAUK) >> 6);
#pragma unroll
      for (int i = 0; i < 3; ++i) glds16_nt(src + i * 128, lds_base + (e * 3 + i) * 1024);
      if (e == 1) {
        VMCNT(3);
        tb0 = t0 + min(max((int)(clk() - t0) - ANCH, 0), 300);
        tbw = tb0 + due_k(tw) - TLEAD;
        __builtin_amdgcn_s_barrier();
        if (tw == 0 && lane == 0) s_c[S_LATE] = tb0;
      }
      VMCNT(PFD);
    }
#pragma unroll
    for (int e = 0; e < NRP * 2; ++e) {
      until(tbw, ((NLP * 2 + e) * 2 * TAUK) >> 6);
      tile_load(pk[e], kb, toff(NLP + (e >> 1)), e & 1);
      MEMFENCE();
      if (e + 1 < NRP * 2) VMCNT(PFD);
    }
    if (tw == 0) {
      r0 = ld32(rs, WS_HR + bid * 2);
      if (bid == LEADER) {
        u32x4 n;
#pragma unroll
        for (int k = 0; k < 4; ++k) n[k] = ld32(rs, WS_HA + (lane * 4 + k) * 32) + 1u;
        st128(rs, WS_HN + lane * 4, n);
      }
      if (ZPOS == 0) zero();
    }
    __builtin_amdgcn_s_setprio(0);
    lds_wait<1>(s_c + F_Q, HPG);
    u32x2 q[12];
    q_load(q, s_q, j);
    const u32x4* s_kv = reinterpret_cast<const u32x4*>(smem + OFF_REG + tw * REG) + lane;
    static_assert(NLP * 4 <= PPI && NTP <= HPG * PPI, "tail tiles: batch row 0 only");
#pragma unroll 1
    for (int e = 0; e < NLP * 2; ++e) {
      Tile tmp;
#pragma unroll
      for (int i = 0; i < 3; ++i) tmp.d[i] = s_kv[(e * 3 + i) * 64];
      score_put(tile_score(tmp, q), s_sc + ((e >> 1) * 4 + tw) * 16 + (e & 1) * 8 + r, j);
    }
#pragma unroll
    for (int e = 0; e < NRP * 2; ++e) {
      if ((NLP * 2 + e) * 2 == PPI) q_load(q, s_q + DH, j);
      score_put(tile_score(pk[e], q), s_sc + ((NLP + (e >> 1)) * 4 + tw) * 16 + (e & 1) * 8 + r, j);
      __builtin_amdgcn_sched_barrier(0);
    }
    asm volatile("s_waitcnt vmcnt(0) lgkmcnt(0)" ::: "memory");
#pragma unroll
    for (int it = 0; it * PPI < NTP; ++it) lds_add(s_c + C_SC + it, lane);
  }

  const int mr = lane & 15, qd = lane >> 4;
  unsigned char* reg = smem + OFF_REG + tw * REG;
  float* red = reinterpret_cast<float*>(smem + OFF_RED);
#define PERM_LO(hi_, lo_) __builtin_amdgcn_perm(hi_, lo_, 0x05040100u)
#define PERM_HI(hi_, lo_) __builtin_amdgcn_perm(hi_, lo_, 0x07060302u)
  int served = 0;
  auto done = [&](int n) {
    if (tw == 3 && served < min(n, NA)) {
      const int from = served;
      int ln = lane;
      asm volatile("" : "+v"(ln));
#pragma unroll 1
      for (; served < min(n, NA); ++served) {
        lds_wait<8>(s_c + C_PART + served, NMW);
        const f32x4 o = merged(served, ln < DH / 4 ? ln : 0);
        const float am = max_x16_32(row_max(fmaxf(fmaxf(fabsf(o[0]), fabsf(o[1])), fmaxf(fabsf(o[2]), fabsf(o[3])))));
        const float inv = am > 1e-30f ? 32767.0f / am : 0.f;
        const int q0 = __float2int_rn(o[0] * inv), q1 = __float2int_rn(o[1] * inv), q2 = __float2int_rn(o[2] * inv), q3 = __float2int_rn(o[3] * inv);
        if (ln < DH / 4) reinterpret_cast<u32x2*>(s_sc + served * (DH / 2))[ln] = u32x2{PERM_LO((unsigned)q1, (unsigned)q0), PERM_LO((unsigned)q3, (unsigned)q2)};
        const bool nan = __any((o[0] + o[1]) + (o[2] + o[3]) != (o[0] + o[1]) + (o[2] + o[3]));
        if (ln == 0) s_ml[served][2] = nan ? 0x7fc00000u : nz_bits(am * (1.0f / 32767.0f));
        LGKM0();
        if (ln == 0) s_c[F_FREE] = served + 1;
      }
#pragma unroll 1
      for (int c = from * 24 + ln; c < served * 24; c += 64) st128nt(rs, WS_PQ + xb * 5 * (DH / 2) + c * 4, reinterpret_cast<const u32x4*>(s_sc)[c]);
      VMCNT(0);
      if (ln < 8) st128(rs, WS_PML + xb * 64 + (from ? 32 : 0) + ln * 4, s_ml[ln]);
      if (ln == 0) s_c[S_BURST + (from ? 1 : 0)] = clk();
      CFENCE();
      if (ln == 0) __hip_atomic_fetch_add(s_c + F_DONE, (unsigned)(served - from), __ATOMIC_RELAXED, __HIP_MEMORY_SCOPE_WORKGROUP);
    }
    if (n > NA) lds_wait<1>(s_c + F_DONE, n); else lds_wait<8>(s_c + F_DONE, n);
  };
  auto done_all = [&]() {
#pragma unroll 1
    for (int n = 4; n <= NA; ++n) done(n);
  };
  const int lp = (lane < DH / 4 ? lane : 0) * 4;
  auto weight = [&](int it, int x0, int cnt) -> float {
    const int mlw = WS_PML + (x0 + (lane & (cnt - 1))) * 64 + (it < 4 ? 0 : 32) + it * 4;
    u32x4 ml;
#pragma unroll 1
    for (unsigned spin = 0; spin < SPIN_CAP; ++spin) {
      ml = ld128(rs, mlw);
      if (__all(min4(ml) != 0u)) break;
      __builtin_amdgcn_s_sleep(16);
    }
    const float mk = __uint_as_float(ml[0]);
    const float e = __builtin_amdgcn_exp2f(mk - max_x16_32(row_max(mk)));
    return e / (wave_sum(e * __uint_as_float(ml[1])) * ((float)cnt * (1.0f / 64.0f))) * __uint_as_float(ml[2]);
  };
  const unsigned tok = __builtin_amdgcn_readfirstlane(r0) + 1u;
  unsigned echoed = 0u, fold = 0u;
  int slow = 0, prev = 0;
  auto lag = [&]() {
    const unsigned v = __hip_atomic_load(s_c + S_MOVED + (lane & 7), __ATOMIC_RELAXED, __HIP_MEMORY_SCOPE_WORKGROUP);
    fold += v;
    unsigned a = 0u;
#pragma unroll
    for (int k = 0; k < 8; ++k) a += __builtin_amdgcn_readlane(v, k);
    return a >> 2;
  };
  auto tell = [&]() { if (lane < 4) { s_c[S_SLOW + 2 * lane] = slow; s_c[S_SLOW + 2 * lane + 1] = fold; } };
  auto round = [&](unsigned rd, bool lead) {
    const int tr = RT0 + (int)(rd - 1) * RTS;
    until(t0, tr);
    const unsigned bh = lag();
    if (bh > RMIN && lane < 8) st128(rs, WS_HA + bid * 32 + lane * 4, u32x4{echoed + rd, tok, tb0, bh});
    if (lead) { until(t0, tr + RA1); leader_release(rs, lane, tb0, rd, slow, prev); }
    until(t0, tr + RA2);
    const u32x2 rv = ld64(rs, WS_HR + bid * 2);
    slow = (int)__builtin_amdgcn_readfirstlane(rv[1]) >> 16;
#pragma unroll 1
    for (unsigned k = min(__builtin_amdgcn_readfirstlane(rv[0]) - tok, rd) + 1u; k <= rd; ++k) slow = slow > 0 ? max(slow - GB, 0) : slow - (k <= PRD ? PRB : 0);
    tell();
  };
  auto prep = [&](bool lead, unsigned nrd) {
    if (tw == 0) {
      const int c = lane & (GS - 1);
      unsigned qe = s_c[S_ECHO + c];
      bool first = true, released = false, sent = false;
#pragma unroll 1
      while ((int)(clk() - t0) < HST) {
        const unsigned qn = ld32(rs, qf + (GS + c) * 32);
        if (mm < NPROD && __any(qn != qe)) {
          if (lane < 32) st32(rs, qf + mm * 32 + lane, qn);
          qe = qn;
        }
        __builtin_amdgcn_s_sleep(64);
      }
      const unsigned behind = lag();
#pragma unroll 1
      for (unsigned spin = 0; spin < SPIN_CAP && !released; ++spin) {
        const unsigned qn = ld32(rs, qf + (GS + c) * 32);
        const u32x2 rv = ld64(rs, WS_HR + bid * 2);
        const unsigned nv = __builtin_amdgcn_readfirstlane(ld32(rs, WS_HN + bid));
        if (__builtin_amdgcn_readfirstlane(rv[0]) - tok <= NRD) {
          released = true;
          slow = (int)__builtin_amdgcn_readfirstlane(rv[1]) >> 16;
          if (lane == 0) { s_c[S_FIRST] = min((tb0 - rv[1]) & 0xffffu, 300u) + 1u; }
        } else {
          if (mm < NPROD && __any(qn != qe)) {
            if (lane < 32) st32(rs, qf + mm * 32 + lane, qn);
            qe = qn;
          }
          if (first || nv != echoed) {
            echoed = nv; first = false;
            if (lane < 8) st128(rs, WS_HA + bid * 32 + lane * 4, u32x4{echoed, tok, tb0, behind});
          }
          if (lead && !sent) sent = leader_release(rs, lane, tb0, 0u, slow, prev);
          __builtin_amdgcn_s_sleep(32);
        }
      }
      tell();
      if (lane == 0) s_c[F_REL] = 1u;
      if (lane < DH / 4) {
        u32x4 p[3][2];
#pragma unroll
        for (int e = 0; e < 3; ++e)
#pragma unroll
          for (int k = 0; k < 2; ++k) p[e][k] = ld128nt(rs, WS_QP + (((e < 2 ? 24 - e * 8 + (xb >> 5) : xb >> 4) * 2 + k) * NB + 1) * DH + lane * 4);
#pragma unroll
        for (int e = 0; e < 3; ++e) *reinterpret_cast<u32x4*>(&s_q[(1 + e) * DH + lane * 4]) = q_pack(p[e][0], p[e][1]);
      }
      LGKM0();
      if (lane == 0) s_c[F_Q2] = 3u;
#pragma unroll 1
      for (unsigned rd = 1; rd <= nrd; ++rd) round(rd, lead);
    }
    if (tw == 3) {
#pragma unroll 1
      for (int ik = 0; ik < NS; ++ik) {
        const int it = seg_of_k(ik), nv = (seg_beg(it + 1) - seg_beg(it)) >> 4;
        lds_wait<4>(s_c + C_SC + ik, ik == 0 ? NW : NMW);
        f32x4* sp = reinterpret_cast<f32x4*>(s_sc + seg_beg(it) * 16) + lane;
        f32x4 v[4];
#pragma unroll
        for (int k = 0; k < 4; ++k) v[k] = sp[(k & (nv - 1)) * 64];
        const f32x4 mx = __builtin_elementwise_max(__builtin_elementwise_max(v[0], v[1]), __builtin_elementwise_max(v[2], v[3]));
        const float m = max_x16_32(row_max(fmaxf(fmaxf(mx[0], mx[1]), fmaxf(mx[2], mx[3]))));
        float l = 0.f;
#pragma unroll
        for (int k = 0; k < 4; ++k) {
#pragma unroll
          for (int e = 0; e < 4; ++e) v[k][e] = __builtin_amdgcn_exp2f(v[k][e] - m);
          if (k < nv) {
            l += (v[k][0] + v[k][1]) + (v[k][2] + v[k][3]);
            sp[k * 64] = v[k];
          }
        }
        l = wave_sum(l);
        if (lane == 0) s_ml[it] = u32x4{nz_bits(m), nz_bits(l), 0x3f800000u, 0x3f800000u};
        LGKM0();
        if (lane == 0) s_c[F_P] = ik + 1;
      }
    }
  };
  auto pull = [&]() {
    typedef __attribute__((address_space(1))) const unsigned g_u32;
    const int id = (ro - 16) * 4 + tw;
    constexpr int RW = 160 - 32 * GE, LW = RW * 4 * 3 * 2 / 64;
    g_u32* p1[2];
#pragma unroll
    for (int i = 0; i < 2; ++i) {
      const int l = id * LW + min(i * 32 + (lane >> 1), max(LW - 1, 0)), m = l >= LW * 32 ? l - LW * 32 : l, rr = m / 3;
      p1[i] = (g_u32*)((l >= LW * 32 ? wu : wg) + (size_t)(rr / max(RW, 1) * 160 + 32 * GE + rr % max(RW, 1)) * FF + xl * 192 + (m % 3) * 64 + (lane & 1) * 32);
    }
    g_u32* p2 = (g_u32*)(wd + (size_t)(id * 24 + min(lane >> 1, 23)) * DM + xl * 64 + (lane & 1) * 32);
    asm volatile("" : "+v"(p1[0]), "+v"(p1[1]), "+v"(p2));
    if (GLEAD) lds_wait<1>(s_c + C_LAST, GCNT); else done(NS);
    const unsigned tp = clk() + PW + tw * PS;
    until(tp, 0);
    const unsigned v0 = LW > 0 ? p1[0][0] : 0u;
    MEMFENCE();
    until(tp, 4 * PS);
    const unsigned v1 = LW > 32 ? p1[1][0] : 0u;
    MEMFENCE();
    until(tp, DPW);
    const unsigned v2 = p2[0], v3 = p2[xl < 2 ? 256 : 0];
    MEMFENCE();
    return (v0 ^ v1) + (v2 ^ v3);
  };
  if (isW) {
    u32x4 bx[24];
    const char* p = reinterpret_cast<const char*>(wo + (size_t)((w_ks * 4 + tw) * DH + r) * DM + w_ct * 64 + j * 8);
    prep(false, NRD - WL);
    if (tw != 0) { lds_wait<16>(s_c + F_P, NS); pause(clk(), OLATE + (tw % 3) * 24 * OSTEP); }
    const unsigned tw0 = clk();
#pragma unroll 1
    for (int i = 0; i < 24; ++i) { until(tw0, i * OSTEP); glds16_nt(p, lds_base + i * 1024); p += 8 * DM * 2; VMCNT(WFD); }
    asm volatile("s_waitcnt vmcnt(0)" ::: "memory");
#pragma unroll
    for (int s = 0; s < 6; ++s) {
      u32x2 e[8];
#pragma unroll
      for (int k = 0; k < 8; ++k) e[k] = *reinterpret_cast<const u32x2*>(reg + (s * 32 + qd * 8 + k) * 128 + mr * 8);
#pragma unroll
      for (int k = 0; k < 4; ++k) {
        bx[s * 4 + 0][k] = PERM_LO(e[2 * k + 1][0], e[2 * k][0]); bx[s * 4 + 1][k] = PERM_HI(e[2 * k + 1][0], e[2 * k][0]);
        bx[s * 4 + 2][k] = PERM_LO(e[2 * k + 1][1], e[2 * k][1]); bx[s * 4 + 3][k] = PERM_HI(e[2 * k + 1][1], e[2 * k][1]);
      }
    }
#pragma unroll
    for (int s = 0; s < 24; s += 4) asm volatile("" : "+v"(bx[s]), "+v"(bx[s + 1]), "+v"(bx[s + 2]), "+v"(bx[s + 3]));
    LGKM0();
    lds_wait<8>(s_c + F_REL, 1u);
    const int hh = w_ks * 4 + tw;
#pragma unroll 1
    for (int pass = 0; pass < 3; ++pass) {
      if (pass < 2) {
        if (WL && pass == 1 && tw == 0) round(NRD, false);
        done(pass ? NA : 4);
        pause(lds_get(s_c + S_BURST + pass), pass ? WDELAY2 : WDELAY2 + 300);
        const int b0 = pass * 2, b1 = pass + 1;
        u32x4 v0, v1;
#pragma unroll 1
        for (unsigned spin = 0; spin < SPIN_CAP; ++spin) {
          v0 = ld128(rs, WS_OC + (b0 * NH + hh) * DH + lp);
          v1 = ld128(rs, WS_OC + (b1 * NH + hh) * DH + lp);
          if (__all(min(min4(v0), min4(v1)) != 0u)) break;
          __builtin_amdgcn_s_sleep(16);
        }
        if (lane < DH / 4) {
          *reinterpret_cast<u32x2*>(reg + (b0 * 4) * AW_LD + lane * 8) = u32x2{v0[0], v0[1]};
          *reinterpret_cast<u32x2*>(reg + (b0 * 4 + 1) * AW_LD + lane * 8) = u32x2{v0[2], v0[3]};
          *reinterpret_cast<u32x2*>(reg + (b1 * 4) * AW_LD + lane * 8) = u32x2{v1[0], v1[1]};
          *reinterpret_cast<u32x2*>(reg + (b1 * 4 + 1) * AW_LD + lane * 8) = u32x2{v1[2], v1[3]};
        }
      } else {
        const float wk = weight(NS - 1, hh * GS, GS);
        float w[GS];
#pragma unroll
        for (int k = 0; k < GS; ++k) { w[k] = __uint_as_float(__builtin_amdgcn_readlane(__float_as_uint(wk), k)); asm volatile("" : "+s"(w[k])); }
        done(NS);
        __builtin_amdgcn_s_setprio(3);
        const int po = WS_PO + hh * GS * DH;
        u32x4 pv[GS];
#pragma unroll 1
        for (unsigned spin = 0; spin < SPIN_CAP; ++spin) {
#pragma unroll
          for (int k = 0; k < GS; ++k) pv[k] = __builtin_amdgcn_raw_buffer_load_b128(rs, lp * 4, (po + k * DH) * 4, AUX_SC1);
          unsigned mn = min4(pv[0]);
#pragma unroll
          for (int k = 1; k < GS; ++k) mn = min(mn, min4(pv[k]));
          if (__all(mn != 0u)) break;
          __builtin_amdgcn_s_sleep(1);
        }
        f32x4 o = w[0] * __builtin_bit_cast(f32x4, pv[0]), o2 = w[1] * __builtin_bit_cast(f32x4, pv[1]);
#pragma unroll
        for (int k = 2; k < GS; k += 2) { o += w[k] * __builtin_bit_cast(f32x4, pv[k]); o2 += w[k + 1] * __builtin_bit_cast(f32x4, pv[k + 1]); }
        u32x2 hi, lw;
        split4(o + o2, hi, lw);
        if (lane < DH / 4) {
          *reinterpret_cast<u32x2*>(reg + ((NB - 1) * 4) * AW_LD + lane * 8) = hi;
          *reinterpret_cast<u32x2*>(reg + ((NB - 1) * 4 + 1) * AW_LD + lane * 8) = lw;
        }
      }
      CFENCE();
      u32x4 a[6];
#pragma unroll
      for (int s = 0; s < 6; ++s) a[s] = *reinterpret_cast<const u32x4*>(reg + mr * AW_LD + s * 64 + qd * 16);
      __builtin_amdgcn_sched_barrier(0);
      f32x4 c[4];
#pragma unroll
      for (int t = 0; t < 4; ++t) c[t] = f32x4{0.f, 0.f, 0.f, 0.f};
#pragma unroll
      for (int s = 0; s < 6; ++s)
#pragma unroll
        for (int t = 0; t < 4; ++t) c[t] = mfma16(a[s], bx[s * 4 + t], c[t]);
      const bool mine = max(qd - 1, 0) == pass;
      f32x4* r4 = reinterpret_cast<f32x4*>(red);
      if (mine) r4[tw * 64 + lane] = f32x4{c[0][0] + c[0][1], c[1][0] + c[1][1], c[2][0] + c[2][1], c[3][0] + c[3][1]};
      const int k = arrive(s_c + C_W + pass);
      f32x4 sum = (r4[lane] + r4[64 + lane]) + (r4[128 + lane] + r4[192 + lane]);
      asm volatile("" : "+v"(sum));
      if (arrived(k) == 3) {
        if (mine) st128(rs, WS_SLAB + (w_ks * NB + qd) * DM + w_ct * 64 + mr * 4, u32x4{nz_bits(sum[0]), nz_bits(sum[1]), nz_bits(sum[2]), nz_bits(sum[3])});
      }
    }
  } else if (isG || isH) {
    f32x2* rst = reinterpret_cast<f32x2*>(red);
    const int hb = isH ? xl - 2 : NB - 1;
    uint2 wr4 = make_uint2(0u, 0u), xg = wr4;
    if (lane < 40) {
      wr4 = *reinterpret_cast<const uint2*>(wr + tw * 160 + lane * 4);
      xg = *reinterpret_cast<const uint2*>(x + row_of(hb) * DM + tw * 160 + lane * 4);
    }
    f32x4 xc = f32x4{bf_lo(xg.x), bf_hi(xg.x), bf_lo(xg.y), bf_hi(xg.y)} - bf_lo(*reinterpret_cast<const unsigned*>(x + row_of(hb) * DM));
    asm volatile("" : "+v"(xc));
    typedef __attribute__((address_space(1))) const u32x4 g_u32x4;
    g_u32x4* wp[10];
    wp[0] = (g_u32x4*)(((mr & 4) ? wu : wg) + (size_t)(tw * 160 + qd * 8 + (mr >> 3) * 4) * FF + g_cg * 32 + (mr & 3) * 8);
    u32x4 e[20], bx[20];
    auto frag = [&](int s) __attribute__((always_inline)) {
      unsigned w[8][2];
#pragma unroll
      for (int i = 0; i < 4; ++i)
#pragma unroll
        for (int d = 0; d < 2; ++d) {
          w[i][d] = __builtin_amdgcn_update_dpp(e[s * 4 + i][d], e[s * 4 + i][2 + d], DPP_ROR8, 0xf, 0xc, false);
          w[4 + i][d] = __builtin_amdgcn_update_dpp(e[s * 4 + i][2 + d], e[s * 4 + i][d], DPP_ROR8, 0xf, 0x3, false);
        }
#pragma unroll
      for (int k = 0; k < 4; ++k) {
        bx[s * 4 + 0][k] = PERM_LO(w[2 * k + 1][0], w[2 * k][0]); bx[s * 4 + 1][k] = PERM_HI(w[2 * k + 1][0], w[2 * k][0]);
        bx[s * 4 + 2][k] = PERM_LO(w[2 * k + 1][1], w[2 * k][1]); bx[s * 4 + 3][k] = PERM_HI(w[2 * k + 1][1], w[2 * k][1]);
      }
      PIN4(bx, s * 4);
    };
    if (isG) {
#pragma unroll
      for (int i = 0; i < 4 * GE; ++i) {
        until(t0, tw == 0 ? W0START + i * (W0SPAN / max(4 * GE, 1)) : W1START + i * (W1SPAN / max(4 * GE, 1)));
        e[i] = __builtin_nontemporal_load(wp[0] + ((i >> 2) * 32 + (i & 3)) * (FF / 8));
        MEMFENCE();
      }
#pragma unroll
      for (int s = 0; s < GE; ++s) frag(s);
    }
    prep(false, NRD);
    lds_wait<8>(s_c + F_REL, 1u);
    auto hrow = [&](bool fast) -> f32x4 {
      f32x4 hv = {0.f, 0.f, 0.f, 0.f};
      if (lane < 40) {
        const int off = (WS_SLAB + hb * DM + tw * 160 + lane * 4) * 4;
        u32x4 sl[KS];
#pragma unroll 1
        for (unsigned spin = 0; spin < SPIN_CAP; ++spin) {
#pragma unroll
          for (int ks = 0; ks < KS; ++ks) sl[ks] = __builtin_amdgcn_raw_buffer_load_b128(rs, off, ks * (NB * DM * 4), AUX_SC1);
          unsigned mn = min4(sl[0]);
#pragma unroll
          for (int ks = 1; ks < KS; ++ks) mn = min(mn, min4(sl[ks]));
          if (!__any(mn == 0u)) break;
          if (fast) __builtin_amdgcn_s_sleep(1); else __builtin_amdgcn_s_sleep(16);
        }
#define SL(k) __builtin_bit_cast(f32x4, sl[k])
        hv = xc + (((SL(0) + SL(1)) + (SL(2) + SL(3))) + ((SL(4) + SL(5)) + (SL(6) + SL(7))));
#undef SL
      }
      return hv;
    };
    auto sums = [&](f32x4 hv) -> f32x2 {
      return f32x2{wave_sum((hv[0] + hv[1]) + (hv[2] + hv[3])), wave_sum((hv[0] * hv[0] + hv[1] * hv[1]) + (hv[2] * hv[2] + hv[3] * hv[3]))};
    };
    if (isH) {
      const float wsum = wave_sum((bf_lo(wr4.x) + bf_hi(wr4.x)) + (bf_lo(wr4.y) + bf_hi(wr4.y)));
      if (tw == 3 || hb >= 2) done_all(); else done(4);
      if (hb == NB - 1) { const unsigned pl = pull(); asm volatile("" :: "v"(pl)); done(NS); }
      else pause(lds_get(s_c + S_BURST + (hb >> 1)), hb < 2 ? HDELAY : HDELAY2);
      const f32x4 hv = hrow(hb == NB - 1);
      const f32x2 sm = sums(hv);
      const float dot = wave_sum((hv[0] * bf_lo(wr4.x) + hv[1] * bf_hi(wr4.x)) + (hv[2] * bf_lo(wr4.y) + hv[3] * bf_hi(wr4.y)));
      if (lane < 8) st128(rs, WS_H + (hb * 4 + tw) * 192 + 160 + lane * 4, u32x4{nz_bits(sm[0]), nz_bits(sm[1]), nz_bits(dot), nz_bits(wsum)});
      if (hb == NB - 1) return;
      const u32x4 v = split8(hv, (lane & 1) != 0);
      if (lane < 40) st128(rs, WS_H + (hb * 4 + tw) * 192 + lane * 4, v);
      done_all();
      const unsigned pl = pull();
      asm volatile("" :: "v"(pl));
      return;
    }
    done_all();
#pragma unroll
    for (int i = 2 * GE; i < 10; ++i) { wp[i] = wp[0] + ((i >> 1) * 32 + (i & 1) * 2) * (FF / 8); asm volatile("" : "+v"(wp[i])); }
    done(NS);
    if (GW) until(clk(), GW);
#pragma unroll
    for (int i = 4 * GE; i < 20; ++i) e[i] = wp[i >> 1][(i & 1) * (FF / 8)];
    u32x4 a[5], sm;
    const int hoff = (WS_H + (min(mr >> 2, NB - 2) * 4 + tw) * 192 + qd * 8 + (mr & 1) * 4) * 4;
#pragma unroll
    for (int s = 0; s < 5; ++s) a[s] = __builtin_amdgcn_raw_buffer_load_b128(rs, hoff + s * 128, 0, AUX_SC1);
    sm = ld128(rs, WS_H + (min(lane, NB - 2) * 4 + tw) * 192 + 160);
    MEMFENCE();
#pragma unroll
    for (int s = GE; s < 5; ++s) frag(s);
    {
#pragma unroll 1
      for (unsigned spin = 0; spin < SPIN_CAP; ++spin) {
        unsigned mn = min4(sm);
#pragma unroll
        for (int s = 0; s < 5; ++s) mn = min(mn, min4(a[s]));
        if (__all(mn != 0u)) break;
        __builtin_amdgcn_s_sleep(2);
#pragma unroll
        for (int s = 0; s < 5; ++s) a[s] = __builtin_amdgcn_raw_buffer_load_b128(rs, hoff + s * 128, 0, AUX_SC1);
        sm = ld128(rs, WS_H + (min(lane, NB - 2) * 4 + tw) * 192 + 160);
      }
      if (lane < NB - 1) rst[lane * 4 + tw] = f32x2{__uint_as_float(sm[0]), __uint_as_float(sm[1])};
#pragma unroll
      for (int s = 0; s < 5; ++s) {
        if (mr & 2) a[s] = u32x4{0x3f803f80u, 0x3f803f80u, 0x3f803f80u, 0x3f803f80u};
        asm volatile("" : "+v"(a[s]));
      }
    }
    typedef __attribute__((address_space(3))) unsigned char lds_u8;
    const int eb = mr >> 2;
    lds_u8* l_aw = (lds_u8*)reg + lane * 8;
    lds_u8* l_ar = (lds_u8*)reg + (mr & 1) * AG_LD + qd * 16;
    lds_u8* l_pw = (lds_u8*)reg + 4096 + (qd * 16 + (mr & 7) * 2 + (mr >> 3)) * 16;
    lds_u8* l_pr = (lds_u8*)smem + OFF_REG + 4096 + (lane & 1) * 1152 + (eb * 16 + qd * 2 + ((lane >> 1) & 1)) * 16;
    lds_u8* l_p2 = l_pr + 2 * REG;
    lds_u8* l_sr = (lds_u8*)(rst + eb * 4);
    lds_u8* l_sw = (lds_u8*)(rst + (NB - 1) * 4 + tw);
    int so = (lane & 3) == 0 ? (WS_ACT + g_cg * 64 + lane) * 4 : WS_END * 4;
    asm volatile("" : "+v"(l_aw), "+v"(l_ar), "+v"(l_pw), "+v"(l_pr), "+v"(l_p2), "+v"(l_sr), "+v"(l_sw), "+v"(so));
#define LDS(T, p) (*reinterpret_cast<__attribute__((address_space(3))) T*>(p))
    const f32x4 hv = hrow(true);
    {
      u32x2 hi, lw;
      split4(hv, hi, lw);
      LDS(u32x2, l_aw) = hi;
      LDS(u32x2, l_aw + AG_LD) = lw;
    }
    CFENCE();
    if ((mr & 14) == 12) {
#pragma unroll
      for (int s = 0; s < 5; ++s) a[s] = LDS(u32x4, l_ar + s * 64);
    }
    __builtin_amdgcn_sched_barrier(0);
    f32x4 c[4];
#pragma unroll
    for (int t = 0; t < 4; ++t) c[t] = f32x4{0.f, 0.f, 0.f, 0.f};
#pragma unroll
    for (int s = 0; s < 5; ++s)
#pragma unroll
      for (int t = 0; t < 4; ++t) c[t] = mfma16(a[s], bx[s * 4 + t], c[t]);
    LDS(f32x2, l_sw) = sums(hv);
    LDS(f32x4, l_pw) = f32x4{c[0][0] + c[0][1], c[1][0] + c[1][1], c[0][2], c[1][2]};
    LDS(f32x4, l_pw + 1152) = f32x4{c[2][0] + c[2][1], c[3][0] + c[3][1], c[2][2], c[3][2]};
    const int k = arrive(s_c + C_T);
    f32x4 ts[2] = {LDS(f32x4, l_sr), LDS(f32x4, l_sr + 16)};
    f32x4 pg[4] = {LDS(f32x4, l_pr), LDS(f32x4, l_pr + REG), LDS(f32x4, l_p2), LDS(f32x4, l_p2 + REG)};
    f32x4 pu[4] = {LDS(f32x4, l_pr + 128), LDS(f32x4, l_pr + REG + 128), LDS(f32x4, l_p2 + 128), LDS(f32x4, l_p2 + REG + 128)};
    asm volatile("" : "+v"(ts[0]), "+v"(ts[1]));
    PIN4(pg, 0); PIN4(pu, 0);
    if (arrived(k) == 3) {
      const f32x4 t = ts[0] + ts[1], g4 = (pg[0] + pg[1]) + (pg[2] + pg[3]), u4 = (pu[0] + pu[1]) + (pu[2] + pu[3]);
      const float s1 = t[0] + t[2], d = s1 * (-1.0f / DM);
      const float rstd = __builtin_amdgcn_rsqf(fmaxf((t[1] + t[3]) + d * s1, 0.f) * (1.0f / DM) + LN_EPS);
      const float g0 = rstd * (g4[0] + d * g4[2]), g1 = rstd * (g4[1] + d * g4[3]);
      const f32x2 act = {g0 * __builtin_amdgcn_rcpf(1.0f + __expf(-g0)) * (rstd * (u4[0] + d * u4[2])),
                         g1 * __builtin_amdgcn_rcpf(1.0f + __expf(-g1)) * (rstd * (u4[1] + d * u4[3]))};
      const float aw = __uint_as_float(max(__builtin_bit_cast(unsigned, __builtin_convertvector(act, bf16x2)), 1u));
      const u32x4 av = {__float_as_uint(aw), __float_as_uint(dpp<0x55>(aw)), __float_as_uint(dpp<0xAA>(aw)), __float_as_uint(dpp<0xFF>(aw))};
      __builtin_amdgcn_raw_buffer_store_b128(av, rs, so, 0, AUX_SC1);
    }
  } else if (isD) {
    const uint2 xd = *reinterpret_cast<const uint2*>(x + row_of(tw) * DM + d_ct * 32 + j * 4);
    const float cq = bf_lo(*reinterpret_cast<const unsigned*>(x + row_of(qd) * DM));
    const __amdgpu_buffer_rsrc_t rd = __builtin_amdgcn_make_buffer_rsrc(const_cast<bf16*>(wd), 0, FF * DM * 2, 0x00020000);
    prep(false, NRD);
    lds_wait<8>(s_c + F_REL, 1u);
    done_all();
    const unsigned pl = pull();
    done(NS);
    asm volatile("" :: "v"(pl));
    const unsigned te = clk();
    if (DW) until(te, DW);
    u32x4 bd[24];
    {
      const int off = ((tw * 384 + (mr >> 3) * 32 + qd * 8 + (mr & 4)) * DM + d_ct * 32 + (mr & 3) * 8) * 2;
      u32x4 e[24];
#pragma unroll
      for (int i = 0; i < 24; ++i) e[i] = __builtin_amdgcn_raw_buffer_load_b128(rd, off + (i & 3) * (DM * 2), (i >> 2) * 64 * (DM * 2), 0);
      MEMFENCE();
#pragma unroll
      for (int s = 0; s < 6; ++s) {
        unsigned w[8][2];
#pragma unroll
        for (int i = 0; i < 4; ++i)
#pragma unroll
          for (int d = 0; d < 2; ++d) {
            w[i][d] = __builtin_amdgcn_update_dpp(e[s * 4 + i][d], e[s * 4 + i][2 + d], 0x114, 0xf, 0xa, false);
            w[4 + i][d] = __builtin_amdgcn_update_dpp(e[s * 4 + i][2 + d], e[s * 4 + i][d], 0x104, 0xf, 0x5, false);
          }
#pragma unroll
        for (int k = 0; k < 4; ++k) {
          bd[s * 4 + 0][k] = PERM_LO(w[2 * k + 1][0], w[2 * k][0]); bd[s * 4 + 1][k] = PERM_HI(w[2 * k + 1][0], w[2 * k][0]);
          bd[s * 4 + 2][k] = PERM_LO(w[2 * k + 1][1], w[2 * k][1]); bd[s * 4 + 3][k] = PERM_HI(w[2 * k + 1][1], w[2 * k][1]);
        }
        PIN4(bd, s * 4);
      }
    }
    until(te, DLATE);
    {
      u32x4 sl;
#pragma unroll 1
      for (unsigned spin = 0; spin < SPIN_CAP; ++spin) {
        sl = ld128(rs, WS_SLAB + (r * NB + tw) * DM + d_ct * 32 + j * 4);
        if (__all(min4(sl) != 0u)) break;
        __builtin_amdgcn_s_sleep(2);
      }
      f32x4 hv = {bf_lo(xd.x), bf_hi(xd.x), bf_lo(xd.y), bf_hi(xd.y)};
#pragma unroll
      for (int i = 0; i < 4; ++i) hv[i] += sum_r(__uint_as_float(sl[i]));
      if (lane < 8) reinterpret_cast<f32x4*>(red + 512 + tw * 32)[lane] = hv;
      LGKM0();
      lds_add(s_c + C_W, lane);
      lds_wait<1>(s_c + C_W, 4u);
    }
    const int col = (mr & 3) * 8 + (mr & 4) + (mr >> 3) * 2;
    f32x2 hv = *reinterpret_cast<const f32x2*>(red + 512 + qd * 32 + col);
    typedef __attribute__((address_space(3))) f32x2 lds_f32x2;
    typedef __attribute__((address_space(1))) unsigned g_u32w;
    lds_f32x2* r2 = (lds_f32x2*)red + lane;
    lds_f32x2* r2w = r2 + tw * 64;
    g_u32w* po = (g_u32w*)(out + row_of(qd) * DM + d_ct * 32 + col);
    int aoff = (mr & 1) ? WS_END * 4 : (WS_ACT + tw * 12 * 64 + ((mr >> 1) & 1) * 64 + qd * 16 + (mr >> 2) * 4) * 4;
    asm volatile("" : "+v"(hv), "+v"(r2), "+v"(r2w), "+v"(po), "+v"(aoff));
    u32x4 hs, a[6];
#pragma unroll 1
    for (unsigned spin = 0; spin < SPIN_CAP; ++spin) {
      hs = ld128(rs, WS_H + (qd * 4 + (mr & 3)) * 192 + 160);
      if (__all(min4(hs) != 0u)) break;
      __builtin_amdgcn_s_sleep(1);
    }
    if (DL2) until(clk(), DL2);
#pragma unroll
    for (int s = 0; s < 6; ++s) a[s] = __builtin_amdgcn_raw_buffer_load_b128(rs, aoff, s * 512, AUX_SC1);
    MEMFENCE();
    float gate;
    {
      f32x4 t = __builtin_bit_cast(f32x4, hs);
#pragma unroll
      for (int i = 0; i < 4; ++i) { t[i] += dpp<DPP_XOR2>(t[i]); t[i] += dpp<DPP_XOR1>(t[i]); }
      const float d = t[0] * (-1.0f / DM);
      const float rstd = __builtin_amdgcn_rsqf(fmaxf(t[1] + d * t[0], 0.f) * (1.0f / DM) + LN_EPS);
      hv += ((hv - cq) + d) * rstd;
      gate = __builtin_amdgcn_rcpf(1.0f + __expf(-rstd * (t[2] + d * t[3])));
      asm volatile("" : "+v"(hv), "+v"(gate));
    }
#pragma unroll 1
    for (unsigned spin = 0; spin < SPIN_CAP; ++spin) {
      unsigned mn = min4(a[0]);
#pragma unroll
      for (int s = 1; s < 6; ++s) mn = min(mn, min4(a[s]));
      if ((__ballot(mn == 0u) & 0x5555555555555555ull) == 0ull) break;
#pragma unroll
      for (int s = 0; s < 6; ++s) a[s] = __builtin_amdgcn_raw_buffer_load_b128(rs, aoff, s * 512, AUX_SC1);
      MEMFENCE();
    }
    f32x4 c[4];
#pragma unroll
    for (int t = 0; t < 4; ++t) c[t] = f32x4{0.f, 0.f, 0.f, 0.f};
#pragma unroll
    for (int s = 0; s < 6; ++s)
#pragma unroll
      for (int t = 0; t < 4; ++t) c[t] = mfma16(a[s], bd[s * 4 + t], c[t]);
    const bool up = mr >= 8;
    const float k0 = up ? c[2][2] : c[0][0], k1 = up ? c[3][2] : c[1][0], o0 = up ? c[0][2] : c[2][0], o1 = up ? c[1][2] : c[3][0];
    *r2w = f32x2{k0 + dpp<DPP_ROR8>(o0), k1 + dpp<DPP_ROR8>(o1)};
    const int k = arrive(s_c + C_T);
    f32x2 y = hv + ((r2[0] + r2[64]) + (r2[128] + r2[192])) * gate;
    asm volatile("" : "+v"(y));
    if (arrived(k) == 3) {
      __hip_atomic_store(po, __builtin_bit_cast(unsigned, __builtin_convertvector(y, bf16x2)), __ATOMIC_RELAXED, __HIP_MEMORY_SCOPE_AGENT);
    }
  } else {
    prep(bid == LEADER, NRD);
    if (isC && tw == 1) {
      lds_wait<8>(s_c + F_REL, 1u);
      const int cs = c_it != 1 ? c_it * 2 : min(4 - (g >> 3), 3), cnt = c_it != 1 ? 8 : (g < 16 ? 16 : 32), x0 = c_it != 1 ? g * 8 : (g < 16 ? g * 16 : (g & 7) * 32);
      done(c_it < 2 ? 4 : NA);
      pause(lds_get(s_c + S_BURST + (c_it >> 1)), WDELAY);
      const float wk = weight(cs, x0, cnt);
      f32x4 o = {0.f, 0.f, 0.f, 0.f};
#pragma unroll 1
      for (int k0 = 0; k0 < cnt; k0 += 16) {
        u32x2 pv[16];
#pragma unroll
        for (int k = 0; k < 16; ++k) pv[k] = __builtin_amdgcn_raw_buffer_load_b64(rs, lp * 2, (WS_PQ + ((x0 + ((k0 + k) & (cnt - 1))) * 5 + cs) * (DH / 2)) * 4, AUX_EARLY);
#pragma unroll
        for (int k = 0; k < 16; ++k) {
          const float w = k0 + k < cnt ? __uint_as_float(__builtin_amdgcn_readlane(__float_as_uint(wk), k0 + k)) : 0.f;
          o += w * f32x4{(float)(short)pv[k][0], (float)((int)pv[k][0] >> 16), (float)(short)pv[k][1], (float)((int)pv[k][1] >> 16)};
        }
      }
      u32x2 hi, lw;
      split4(o, hi, lw);
      if (lane < DH / 4) st128(rs, WS_OC + (c_it * NG + g) * DH + lane * 4, u32x4{max(hi[0], 1u), max(hi[1], 1u), max(lw[0], 1u), max(lw[1], 1u)});
    }
    done_all();
    const unsigned pl = pull();
    asm volatile("" :: "v"(pl));
  }
  if (tw == 3) done_all();
}

extern "C" void kernel_launch(void* const* d_in, const int* in_sizes, int n_in, void* d_out, int out_size, void* d_ws,
                              size_t ws_size, hipStream_t stream) {
  const bf16* x = (const bf16*)d_in[0];
  const bf16* kc = (const bf16*)d_in[1];
  const bf16* vc = (const bf16*)d_in[2];
  const bf16* wq = (const bf16*)d_in[3];
  const bf16* wo = (const bf16*)d_in[4];
  const bf16* wr = (const bf16*)d_in[6];
  const bf16* wg = (const bf16*)d_in[7];
  const bf16* wu = (const bf16*)d_in[8];
  const bf16* wd = (const bf16*)d_in[9];
  fused_kernel<<<NWG, NW * 64, 0, stream>>>(x, kc, vc, wq, wo, wr, wg, wu, wd, (float*)d_ws, (bf16*)d_out);
}
```

```cpp
#include <hip/hip_runtime.h>
#include <hip/hip_bf16.h>
#include <stdint.h>

typedef __hip_bfloat16 bf16;
using u32x2 = __attribute__((ext_vector_type(2))) unsigned;
using u32x4 = __attribute__((ext_vector_type(4))) unsigned;
using f32x4 = __attribute__((ext_vector_type(4))) float;
using f32x2 = __attribute__((ext_vector_type(2))) float;
using bf16x8 = __attribute__((ext_vector_type(8))) __bf16;

constexpr int NB = 4, DM = 640, NH = 32, SK = 8192, DH = 192, HD = NH * DH, FF = 1536;
constexpr float LN_EPS = 1e-5f;
constexpr float QSCALE = 0.07216878364870322f * 1.4426950408889634f;

constexpr int NWG = 256;
constexpr int GS = 8, NG = NWG / GS, NI = GS / 2;
constexpr int HPG = NI / NB;
constexpr int PPI = 512 / GS;
constexpr int NPAIR = NI * PPI;

constexpr int TAUK = 1320, TAUV = 1332;
constexpr int TSTART = 110;
constexpr int ANCH = 90;
constexpr int OGAP = 105, OLATE = 250, OSTEP = 12;
constexpr int VGAP = 50;
constexpr int DEEP1 = 100, DEEP2 = 200, LAGOFF = 20;
constexpr int ENDT = 12, ENDS = 2;
constexpr int JUMP = 145;
constexpr int HST = 2800, RT0 = 3900, RTS = 1000, RA1 = 220, RA2 = 500, LEADER = 24;
constexpr unsigned NRD = 5, RMIN = 30;
constexpr int FBX = 20, FB0 = 80, FB1 = 10, FBD = 10, FBS = 10;
constexpr int HOLD = 4000, GB = 24, GBS = 2, GBX = 60, GBF = 12;
constexpr unsigned PRD = 3; constexpr int PRB = 24;
constexpr int ROT = 16, ROWS = 0x1B;
constexpr int TLEAD = 10, PFD = 6, WFD = 3;
constexpr int QWAIT = 750, QBACK = 100;
constexpr int WDELAY = 100, WDELAY2 = 460, HDELAY = 1150, HDELAY2 = 780, DLATE = 100;
constexpr int TPT = 600, TPS = 20;
constexpr int CODE = 24 * 64 * 46;
constexpr int PW = 30, DPW = 60;

constexpr int KS = 8;
constexpr int WS_PML = 0;
constexpr int WS_PO = WS_PML + NWG * 64;
constexpr int WS_PQ = WS_PO + NWG * DH;
constexpr int WS_OC = WS_PQ + NWG * 5 * (DH / 2);
constexpr int WS_SLAB = WS_OC + (NB - 1) * NH * DH;
constexpr int WS_ACT = WS_SLAB + KS * NB * DM;
constexpr int WS_H = WS_ACT + NB * FF / 2;
constexpr int WS_HN = WS_H + NB * 4 * 192;
constexpr int WS_HR = WS_HN + NWG;
constexpr int WS_HA = WS_HR + 2 * NWG;
constexpr int QFS = 2 * GS * 32;
constexpr int WS_QF = WS_HA + 32 * NWG;
constexpr int WS_QP = WS_QF + NG * QFS;
constexpr int WS_END = WS_QP + NH * 2 * NB * DH;
constexpr int NPROD = HPG * 6;
static_assert(WS_PO % 32 == 0 && WS_SLAB % 32 == 0 && WS_ACT % 32 == 0 && WS_HN % 32 == 0 && WS_QF % 32 == 0 && WS_QP % 32 == 0, "line alignment");


__device__ __forceinline__ int row_of(int b) { return (ROWS >> (2 * b)) & 3; }
__device__ __forceinline__ float bf_lo(unsigned u) { return __uint_as_float(u << 16); }
__device__ __forceinline__ float bf_hi(unsigned u) { return __uint_as_float(u & 0xffff0000u); }
__device__ __forceinline__ f32x2 bf_pair(unsigned u) { return f32x2{bf_lo(u), bf_hi(u)}; }
__device__ __forceinline__ f32x2 fma2(float a, f32x2 b, f32x2 c) { return __builtin_elementwise_fma(f32x2{a, a}, b, c); }
__device__ __forceinline__ f32x2 fma2(f32x2 a, f32x2 b, f32x2 c) { return __builtin_elementwise_fma(a, b, c); }

template <int CTRL> __device__ __forceinline__ float dpp(float x) {
  return __builtin_bit_cast(float, __builtin_amdgcn_mov_dpp(__builtin_bit_cast(int, x), CTRL, 0xf, 0xf, true));
}
constexpr int DPP_XOR1 = 0xB1, DPP_XOR2 = 0x4E, DPP_HMIRROR = 0x141, DPP_MIRROR = 0x140, DPP_ROR8 = 0x128;

__device__ __forceinline__ float sum_x16_32(float x) {
  auto s = __builtin_amdgcn_permlane16_swap(__float_as_uint(x), __float_as_uint(x), false, false);
  x = __uint_as_float(s[0]) + __uint_as_float(s[1]);
  auto t = __builtin_amdgcn_permlane32_swap(__float_as_uint(x), __float_as_uint(x), false, false);
  return __uint_as_float(t[0]) + __uint_as_float(t[1]);
}
__device__ __forceinline__ float max_x16_32(float x) {
  auto s = __builtin_amdgcn_permlane16_swap(__float_as_uint(x), __float_as_uint(x), false, false);
  x = fmaxf(__uint_as_float(s[0]), __uint_as_float(s[1]));
  auto t = __builtin_amdgcn_permlane32_swap(__float_as_uint(x), __float_as_uint(x), false, false);
  return fmaxf(__uint_as_float(t[0]), __uint_as_float(t[1]));
}
__device__ __forceinline__ float tsum32(float a, float b) {
  auto t = __builtin_amdgcn_permlane32_swap(__float_as_uint(a), __float_as_uint(b), false, false);
  return __uint_as_float(t[0]) + __uint_as_float(t[1]);
}
__device__ __forceinline__ float tsum16(float a, float b) {
  auto t = __builtin_amdgcn_permlane16_swap(__float_as_uint(a), __float_as_uint(b), false, false);
  return __uint_as_float(t[0]) + __uint_as_float(t[1]);
}
__device__ __forceinline__ float sum_r(float v) { v += dpp<DPP_ROR8>(v); return sum_x16_32(v); }
__device__ __forceinline__ float row_sum(float v) {
  v += dpp<DPP_XOR1>(v); v += dpp<DPP_XOR2>(v); v += dpp<DPP_HMIRROR>(v);
  return v + dpp<DPP_MIRROR>(v);
}
__device__ __forceinline__ float row_max(float v) {
  v = fmaxf(v, dpp<DPP_XOR1>(v)); v = fmaxf(v, dpp<DPP_XOR2>(v)); v = fmaxf(v, dpp<DPP_HMIRROR>(v));
  return fmaxf(v, dpp<DPP_MIRROR>(v));
}
__device__ __forceinline__ float wave_sum(float v) {
  v += dpp<DPP_XOR1>(v); v += dpp<DPP_XOR2>(v); v += dpp<DPP_HMIRROR>(v);
  return sum_r(v);
}

__device__ __forceinline__ u32x4 ld_nt(const char* p) { return __builtin_nontemporal_load(reinterpret_cast<const u32x4*>(p)); }

struct Tile { u32x4 d[3]; };

#define PIN4(a, o) asm volatile("" : "+v"((a)[(o) + 0]), "+v"((a)[(o) + 1]), "+v"((a)[(o) + 2]), "+v"((a)[(o) + 3]))
#define MEMFENCE() do { asm volatile("" ::: "memory"); __builtin_amdgcn_sched_barrier(0); } while (0)

__device__ __forceinline__ void glds16_nt(const void* gsrc, unsigned lds_dst) {
  unsigned keep;
  asm volatile("s_mov_b32 %0, m0\n\ts_mov_b32 m0, %2\n\ts_nop 0\n\tglobal_load_lds_dwordx4 %1, off nt\n\ts_mov_b32 m0, %0"
               : "=&s"(keep) : "v"(gsrc), "s"(lds_dst) : "memory");
}

using bf16x2 = __attribute__((ext_vector_type(2))) __bf16;
__device__ __forceinline__ float dot2(unsigned a, unsigned b, float c) { return __builtin_amdgcn_fdot2_f32_bf16(__builtin_bit_cast(bf16x2, a), __builtin_bit_cast(bf16x2, b), c, false); }
__device__ __forceinline__ float tile_score(const Tile& k, const u32x2 (&q)[12]) {
  float s0 = 0.f, s1 = 0.f;
#pragma unroll
  for (int i = 0; i < 3; ++i) {
    u32x4 v = k.d[i];
    asm("" : "+v"(v));
#pragma unroll
    for (int d = 0; d < 4; ++d) { s0 = dot2(v[d], q[i * 4 + d][0], s0); s1 = dot2(v[d], q[i * 4 + d][1], s1); }
  }
  return s0 + s1;
}
__device__ __forceinline__ void pair_put(float a, float b, float* dst, int j) {
  float s = (j < 4 ? a : b) + dpp<DPP_HMIRROR>(j < 4 ? b : a);
  s += dpp<DPP_XOR1>(s);
  s += dpp<DPP_XOR2>(s);
  if ((j & 3) == 0) dst[j * 2] = s;
}
__device__ __forceinline__ void score_put(float s, float* dst, int j) {
  s += dpp<DPP_XOR1>(s);
  s += dpp<DPP_XOR2>(s);
  s += dpp<DPP_HMIRROR>(s);
  if (j == 0) *dst = s;
}
__device__ __forceinline__ void tile_load(Tile& t, const char* base, unsigned off, int half) {
#pragma unroll
  for (int i = 0; i < 3; ++i) t.d[i] = ld_nt(base + off + half * 3072 + i * 128);
}
__device__ __forceinline__ void pair_issue(Tile& ta, Tile& tb, const char* base, unsigned off, int deep) {
  deep = __builtin_amdgcn_readfirstlane(deep);
#define WAIT_IF(level, n) asm volatile("s_cmp_lg_u32 %0, " #level "\n\ts_cbranch_scc1 .Lpi%=\n\ts_waitcnt vmcnt(" #n ")\n.Lpi%=:" :: "s"(deep) : "scc", "memory")
  const char* p = base + off;
  WAIT_IF(0, 3);
  ta.d[0] = ld_nt(p); ta.d[1] = ld_nt(p + 128); ta.d[2] = ld_nt(p + 256); MEMFENCE();
  WAIT_IF(0, 3); WAIT_IF(1, 6);
  tb.d[0] = ld_nt(p + 3072); tb.d[1] = ld_nt(p + 3200); tb.d[2] = ld_nt(p + 3328); MEMFENCE();
#undef WAIT_IF
}
__device__ __forceinline__ void pv_last(float p, Tile& v, f32x2 (&acc)[12]) {
#pragma unroll
  for (int i = 0; i < 3; ++i) {
    asm volatile("" : "+v"(v.d[i]), "+v"(p));
#pragma unroll
    for (int d = 0; d < 4; ++d) acc[i * 4 + d] = fma2(p, bf_pair(v.d[i][d]), acc[i * 4 + d]);
    PIN4(acc, i * 4);
  }
}
__device__ __forceinline__ void q_load(u32x2 (&q)[12], const float* src, int j) {
#pragma unroll
  for (int i = 0; i < 6; ++i) {
    const u32x4 a = *reinterpret_cast<const u32x4*>(src + (i >> 1) * 64 + j * 8 + (i & 1) * 4);
    q[i * 2] = u32x2{a[0], a[1]}; q[i * 2 + 1] = u32x2{a[2], a[3]};
  }
}
#define VMCNT(n) asm volatile("s_waitcnt vmcnt(%0)" :: "n"(n) : "memory")

constexpr int AUX_SC1 = 16, AUX_EARLY = 18;
constexpr unsigned SPIN_CAP = 1u << 17;
__device__ __forceinline__ unsigned nz_bits(float v) { return max(__float_as_uint(v), 1u); }
__device__ __forceinline__ unsigned min4(u32x4 v) { return min(min(v[0], v[1]), min(v[2], v[3])); }
__device__ __forceinline__ unsigned ld32(__amdgpu_buffer_rsrc_t rs, int word) { return __builtin_amdgcn_raw_buffer_load_b32(rs, word * 4, 0, AUX_SC1); }
__device__ __forceinline__ u32x2 ld64(__amdgpu_buffer_rsrc_t rs, int word) { return __builtin_amdgcn_raw_buffer_load_b64(rs, word * 4, 0, AUX_SC1); }
__device__ __forceinline__ u32x4 ld128(__amdgpu_buffer_rsrc_t rs, int word) { return __builtin_amdgcn_raw_buffer_load_b128(rs, word * 4, 0, AUX_SC1); }
__device__ __forceinline__ void st32(__amdgpu_buffer_rsrc_t rs, int word, unsigned v) { __builtin_amdgcn_raw_buffer_store_b32(v, rs, word * 4, 0, AUX_SC1); }
__device__ __forceinline__ void st128(__amdgpu_buffer_rsrc_t rs, int word, u32x4 v) { __builtin_amdgcn_raw_buffer_store_b128(v, rs, word * 4, 0, AUX_SC1); }

__device__ __forceinline__ u32x4 ld128nt(__amdgpu_buffer_rsrc_t rs, int word) { return __builtin_amdgcn_raw_buffer_load_b128(rs, word * 4, 0, AUX_EARLY); }
__device__ __forceinline__ void st128nt(__amdgpu_buffer_rsrc_t rs, int word, u32x4 v) { __builtin_amdgcn_raw_buffer_store_b128(v, rs, word * 4, 0, AUX_EARLY); }

__device__ __forceinline__ bool leader_release(__amdgpu_buffer_rsrc_t rs, int lane, unsigned t0, unsigned rd, int slow, int& prev) {
  u32x4 a[4];
#pragma unroll
  for (int k = 0; k < 4; ++k) a[k] = ld128(rs, WS_HA + (lane * 4 + k) * 32);
  const u32x4 n = ld128(rs, WS_HN + lane * 4);
  if (rd == 0u && !__all(a[0][0] == n[0] && a[1][0] == n[1] && a[2][0] == n[2] && a[3][0] == n[3])) return false;
  const float e = fmaxf(fmaxf((float)(int)(t0 - a[0][2]), (float)(int)(t0 - a[1][2])), fmaxf((float)(int)(t0 - a[2][2]), (float)(int)(t0 - a[3][2])));
  unsigned first = t0 - (unsigned)(int)max_x16_32(row_max(e));
  float ls = 0.f, lx = 0.f;
  const int pend = max(prev - FBS * (RTS - RA2) * 16 / TAUK, 0);
  prev = max(pend - FBS * RA2 * 16 / TAUK, 0);
#pragma unroll
  for (int k = 0; k < 4; ++k) {
    const float l = a[k][0] == n[k] + rd ? (float)min(a[k][3] + (rd ? 0u : min(a[k][2] - first, 300u)), 2000u) : (float)-pend;
    ls += l; lx = fmaxf(lx, l);
  }
  ls = wave_sum(ls) * (1.0f / NWG); lx = max_x16_32(row_max(lx));
  const int extra = __builtin_amdgcn_readfirstlane(min(max((int)(ls + lx * (FBX / 100.0f)) - (rd ? FB1 + (int)(NRD - rd) * FBD : FB0), 0), 600));
  if (rd != 0u && __all(ls * NWG < HOLD)) return true;
  first = (first & 0xffffu) | ((unsigned)(slow + extra) << 16);
  prev += extra;
  st128(rs, WS_HR + lane * 8, u32x4{a[0][1] + rd, first, a[1][1] + rd, first});
  st128(rs, WS_HR + lane * 8 + 4, u32x4{a[2][1] + rd, first, a[3][1] + rd, first});
  return true;
}

typedef __attribute__((address_space(3))) unsigned lds_u32;
__device__ __forceinline__ unsigned lds_add(lds_u32* p, int lane) {
  unsigned k = 0;
  if (lane == 0) k = __hip_atomic_fetch_add(p, 1u, __ATOMIC_RELAXED, __HIP_MEMORY_SCOPE_WORKGROUP);
  return __builtin_amdgcn_readfirstlane(k);
}
#define CFENCE() asm volatile("" ::: "memory")
__device__ __forceinline__ int arrive(lds_u32* p) {
  int v;
  unsigned long long keep;
  asm volatile("s_mov_b64 %1, exec\n\ts_mov_b64 exec, 1\n\tds_add_rtn_u32 %0, %2, %3\n\ts_mov_b64 exec, %1"
               : "=&v"(v), "=&s"(keep) : "v"((unsigned)(size_t)p), "v"(1u) : "memory");
  return v;
}
__device__ __forceinline__ void bump(lds_u32* p) {
  unsigned long long keep;
  asm volatile("s_mov_b64 %0, exec\n\ts_mov_b64 exec, 1\n\tds_add_u32 %1, %2\n\ts_mov_b64 exec, %0" : "=&s"(keep) : "v"((unsigned)(size_t)p), "v"(1u) : "memory");
}
__device__ __forceinline__ int arrived(int v) {
  asm volatile("s_waitcnt lgkmcnt(0)" : "+v"(v) :: "memory");
  return __builtin_amdgcn_readfirstlane(v);
}
__device__ __forceinline__ unsigned lds_get(lds_u32* p) {
  return __builtin_amdgcn_readfirstlane(__hip_atomic_load(p, __ATOMIC_RELAXED, __HIP_MEMORY_SCOPE_WORKGROUP));
}
template <int SLEEP> __device__ __forceinline__ void lds_wait(lds_u32* p, unsigned n) {
#pragma unroll 1
  for (unsigned spin = 0; spin < (SPIN_CAP << 4); ++spin) {
    if (lds_get(p) >= n) break;
    __builtin_amdgcn_s_sleep(SLEEP);
  }
  asm volatile("" ::: "memory");
}
__device__ __forceinline__ unsigned clk() { return (unsigned)__builtin_amdgcn_s_memrealtime(); }
struct Table {
  unsigned t0; int was, moved, slowed, folded;
  u32x2 told;
  unsigned mine, tmp;
  template <int K> __device__ __forceinline__ void put(int v) { asm volatile("v_mov_b32 %0, %2\n\tds_write_b32 %1, %0 offset:%3" : "+v"(tmp), "+v"(mine) : "s"(v), "n"(K * 4) : "memory"); }
  __device__ __forceinline__ int gate(int due, int sh = 0) {
    const int fold = __builtin_amdgcn_readfirstlane(told[1]);
    if (fold != folded) { const int f = min(fold - folded, moved); moved -= f; slowed += f; folded = fold; put<0>(moved); }
    const int left = (int)__builtin_amdgcn_readfirstlane(told[0]) - slowed, d = max(min(left, FBS), left < -GBX ? -GBF : -GBS);
    t0 += d; was -= d; slowed += d;
    int late = (int)(clk() - t0) - due;
    if (late - was > JUMP) { t0 += late - was; moved += late - was; late = was; put<0>(moved); }
    const int lag = max(late - max(left - d, 0) - LAGOFF, 0);
    put<4>(lag);
    was = late;
#pragma unroll 1
    while (late < 0) { __builtin_amdgcn_s_sleep(1); late = (int)(clk() - t0) - due; }
    return lag < (DEEP1 >> sh) ? 0 : (lag < (DEEP2 >> sh) ? 1 : 2);
  }
  __device__ __forceinline__ void listen(lds_u32* slow) {
    told = __builtin_bit_cast(u32x2, __hip_atomic_load((__attribute__((address_space(3))) unsigned long long*)slow, __ATOMIC_RELAXED, __HIP_MEMORY_SCOPE_WORKGROUP));
  }
  __device__ __forceinline__ void agree(lds_u32* all) {
    if (moved == 0) return;
    const int m = (int)min(min(lds_get(all), lds_get(all + 1)), min(lds_get(all + 2), lds_get(all + 3))) + 30;
    if (m < moved) { t0 -= moved - m; moved = m; was = 1 << 20; put<0>(moved); }
  }
};
__device__ __forceinline__ void until(unsigned t0, int due) {
#pragma unroll 1
  while ((int)(clk() - t0) < due) __builtin_amdgcn_s_sleep(1);
}
__device__ __forceinline__ void pause(unsigned t0, unsigned due) {
#pragma unroll 1
  while (clk() - t0 < due) __builtin_amdgcn_s_sleep(16);
}
#define LGKM0() asm volatile("s_waitcnt lgkmcnt(0)" ::: "memory")

__device__ __forceinline__ void split4(f32x4 v, u32x2& hi, u32x2& lo) {
  unsigned h[4], r[4];
#pragma unroll
  for (int i = 0; i < 4; ++i) { h[i] = __float_as_uint(v[i]) & 0xffff0000u; r[i] = __float_as_uint(v[i] - __uint_as_float(h[i])); }
  hi = u32x2{__builtin_amdgcn_perm(h[1], h[0], 0x07060302u), __builtin_amdgcn_perm(h[3], h[2], 0x07060302u)};
  lo = u32x2{__builtin_amdgcn_perm(r[1], r[0], 0x07060302u), __builtin_amdgcn_perm(r[3], r[2], 0x07060302u)};
}
__device__ __forceinline__ u32x4 split8(f32x4 v, bool odd) {
  u32x2 hi, lo;
  split4(v, hi, lo);
  const unsigned n0 = __float_as_uint(dpp<DPP_XOR1>(__uint_as_float(odd ? hi[0] : lo[0]))), n1 = __float_as_uint(dpp<DPP_XOR1>(__uint_as_float(odd ? hi[1] : lo[1])));
  return u32x4{max(odd ? n0 : hi[0], 1u), max(odd ? n1 : hi[1], 1u), max(odd ? lo[0] : n0, 1u), max(odd ? lo[1] : n1, 1u)};
}
__device__ __forceinline__ u32x4 q_pack(u32x4 a, u32x4 b) {
  u32x2 hi, lo;
  split4((__builtin_bit_cast(f32x4, a) + __builtin_bit_cast(f32x4, b)) * QSCALE, hi, lo);
  return u32x4{hi[0], lo[0], hi[1], lo[1]};
}
__device__ __forceinline__ f32x4 mfma16(u32x4 a, u32x4 b, f32x4 c) {
  return __builtin_amdgcn_mfma_f32_16x16x32_bf16(__builtin_bit_cast(bf16x8, a), __builtin_bit_cast(bf16x8, b), c, 0, 0, 0);
}

constexpr int NW = 8, NMW = 4;
constexpr int NLP = 5, NRP = 8;
constexpr int NTP = 4 * (NLP + NRP);
constexpr int REG = NLP * 6 * 1024;
constexpr int NRING = 4;
constexpr int NS = 6, NA = NS - 1;
__device__ __forceinline__ int seg_beg(int s) { return s >= NS ? NPAIR : ((0xC86540 >> (4 * s)) & 15) * 16; }
__device__ __forceinline__ int seg_k(int t) { return (int)(0x5433222211110000ull >> (4 * (t >> 4))) & 15; }
__device__ __forceinline__ int seg_of_k(int k) { return (0x123540 >> (4 * k)) & 15; }
__device__ __forceinline__ int k_of_seg(int s) { return (0x213450 >> (4 * s)) & 15; }
__device__ __forceinline__ int pair_of(int t) { return t < 64 ? t : (t < 192 ? t + 64 : t - (t < 224 ? 96 : (t < 240 ? 144 : 176))); }
constexpr int OFF_CNT = 0, OFF_REG = 256,
              OFF_SC = OFF_REG + 4 * REG, OFF_BUF = OFF_SC + NPAIR * 16 * 4, OFF_Q = OFF_BUF + NRING * NMW * DH * 4, OFF_ML = OFF_Q + NS * DH * 4,
              OFF_RED = OFF_ML + 128, OFF_END = OFF_RED + 4 * 256 * 4;
enum { F_REL = 0, F_DONE, C_LAST, C_T = 3, F_OUT, C_P, C_K, F_P, F_Q = 8, F_FREE, S_FIRST, F_Q2, C_W = 12, C_SC = 16, C_PART = 24, S_ECHO = 32, S_BURST = 40, S_LATE = 42, S_MOVED = 48, S_SLOW = 56 };
__device__ __forceinline__ int due_k(int p) { return TSTART + ((p * TAUK) >> 6); }
__device__ __forceinline__ int due_v(int p) { return TSTART + OGAP + ((NPAIR * TAUK + p * TAUV + max(p - NPAIR / 2, 0) * (VGAP * 64 / (NPAIR / 2))) >> 6); }
constexpr int AW_LD = DH * 2 + 16, AG_LD = 512 + 16;

__global__ void __launch_bounds__(NW * 64) fused_kernel(const bf16* __restrict__ x, const bf16* __restrict__ kc,
                                                        const bf16* __restrict__ vc, const bf16* __restrict__ wq,
                                                        const bf16* __restrict__ wo, const bf16* __restrict__ wr,
                                                        const bf16* __restrict__ wg, const bf16* __restrict__ wu,
                                                        const bf16* __restrict__ wd, float* ws, bf16* __restrict__ out) {
  __shared__ __attribute__((aligned(16))) unsigned char smem[OFF_END];
  float* s_sc = reinterpret_cast<float*>(smem + OFF_SC);
  float* s_buf = reinterpret_cast<float*>(smem + OFF_BUF);
  float* s_q = reinterpret_cast<float*>(smem + OFF_Q);
  u32x4* s_ml = reinterpret_cast<u32x4*>(smem + OFF_ML);
  lds_u32* s_c = (lds_u32*)(smem + OFF_CNT);

  const int tid = threadIdx.x, lane = tid & 63, r = lane >> 3, j = lane & 7;
  const int wave = __builtin_amdgcn_readfirstlane(tid >> 6);
  const int tw = wave - NMW;
  const int bid = blockIdx.x, xl = bid & 7, idx = bid >> 3;
  const int g = (xl * 32 + idx) / GS, mm = idx % GS;
  const __amdgpu_buffer_rsrc_t rs = __builtin_amdgcn_make_buffer_rsrc(ws, 0, WS_END * 4, 0x00020000);
  const int ro = ((idx & 3) << 3) | (idx >> 2);
  const bool isW = ro < 10, isG = ro >= 10 && ro < 16;
  const int w_ks = xl, w_ct = ro;
  const int g_cg = xl * 6 + (ro - 10);
  const int d_ct = (xl + ((ro - 16) >> 2) * 8) * 2 + (((ro - 16) >> 1) & 1);
  const bool isD = ro >= 16 && ro < 24 && (ro & 1) == 0 && d_ct < DM / 32;
  const bool isH = ro == 22 && xl >= 2 && xl < 6;
  const bool isC = (mm & 7) == 3 || (mm & 7) >= 6;
  const int c_it = (mm >> 3) * 3 + max((mm & 7) - 5, 0);
  const int qf = WS_QF + g * QFS;
  const char* kb = reinterpret_cast<const char*>(kc);
  const char* vb = reinterpret_cast<const char*>(vc);
  const unsigned lo = (r * 24 + j) * 16;
  const int xb = g * GS + mm, c0 = mm * 8 + g * ROT;
  const int b2 = (2 * NH + (xb >> 4)) * 1024 + (xb & 15) * 8, b3 = (2 * NH + 16 + (xb >> 5)) * 1024 + (xb & 31) * 8;
  auto poff = [&](int p) -> unsigned {
    const int turn = (p & 63) >> 2;
    int t = (row_of(p >> 6) * NH + g) * 1024 + ((turn * 64 + (p & 3) * 2 + c0) & 1023);
    if ((p >> 6) == 1) t = (turn >= 8 ? b2 + (turn & 7) * 128 : b3 + (~turn & 4) * 2048 + (turn & 3) * 256) + (p & 3) * 2;
    return (unsigned)t * 3072u;
  };
  const unsigned lds_base = (unsigned)(size_t)(__attribute__((address_space(3))) unsigned char*)smem + OFF_REG + tw * REG;
  auto merged = [&](int it, int ln) -> f32x4 {
    const float* src = s_buf + (it % NRING) * (NMW * DH) + ((ln >> 1) & 7) * 24 + (ln >> 4) * 8 + (ln & 1) * 4;
    return (*reinterpret_cast<const f32x4*>(src) + *reinterpret_cast<const f32x4*>(src + DH)) +
           (*reinterpret_cast<const f32x4*>(src + 2 * DH) + *reinterpret_cast<const f32x4*>(src + 3 * DH));
  };
#define NONCE() do { unsigned n_ = __builtin_amdgcn_readfirstlane(f) + 1u; while (__any(f == n_)) ++n_; \
    if (lane < 32) st32(rs, qf + (GS + mm) * 32 + lane, n_); f = n_; } while (0)
  unsigned long long pc;
  asm volatile("s_getpc_b64 %0" : "=s"(pc));
  unsigned r0 = 0u;
  unsigned t0 = clk();
  unsigned tb0 = t0;
  if (wave < NMW) {
    Tile ta, tb, tc, td;
    const int c = lane & (GS - 1);
    unsigned f = 0u;
    if (wave == 3) f = ld32(rs, qf + c * 32 + mm);
    if (mm < NPROD) {
      const int hp = g + (mm / 6) * NG, ck = (mm % 6) >> 1, hf = mm & 1, jp = hf * 4 + wave;
      const int bb = lane >> 4, t = lane & 15;
      const u32x4* xp = reinterpret_cast<const u32x4*>(x + row_of(bb) * DM);
      u32x4 xv[5], xs = {0u, 0u, 0u, 0u};
#pragma unroll
      for (int k = 0; k < 5; ++k) xv[k] = xp[t + 16 * k];
      if (t < 10) xs = xp[jp * 10 + t];
      const u32x4* wp = reinterpret_cast<const u32x4*>(wq + (size_t)(jp * 80 + r) * HD + hp * DH + ck * 64) + j;
      u32x4 wv[10];
#pragma unroll
      for (int k = 0; k < 10; ++k) wv[k] = __builtin_nontemporal_load(wp + (size_t)k * 8 * (HD / 8));
      if (wave == 0) s_c[lane] = lane == C_K ? NTP + NMW : 0u;
      MEMFENCE(); LGKM0(); __builtin_amdgcn_s_barrier(); MEMFENCE();
      if (wave == 3) { NONCE(); }
      float sm = 0.f;
#pragma unroll
      for (int k = 0; k < 5; ++k)
#pragma unroll
        for (int d = 0; d < 4; ++d) sm += bf_lo(xv[k][d]) + bf_hi(xv[k][d]);
      const float mean = row_sum(sm) * (1.0f / DM);
      float sv = 0.f;
#pragma unroll
      for (int k = 0; k < 5; ++k)
#pragma unroll
        for (int d = 0; d < 4; ++d) { const float a = bf_lo(xv[k][d]) - mean, c2 = bf_hi(xv[k][d]) - mean; sv += a * a + c2 * c2; }
      const float rstd = 1.0f / sqrtf(row_sum(sv) * (1.0f / DM) + LN_EPS);
      float* s_xn = s_buf + wave * 320;
      if (t < 10) {
#pragma unroll
        for (int d = 0; d < 4; ++d) {
          s_xn[(t * 8 + 2 * d) * 4 + bb] = (bf_lo(xs[d]) - mean) * rstd;
          s_xn[(t * 8 + 2 * d + 1) * 4 + bb] = (bf_hi(xs[d]) - mean) * rstd;
        }
      }
      LGKM0();
      f32x2 qa[16];
#pragma unroll
      for (int i = 0; i < 16; ++i) qa[i] = f32x2{0.f, 0.f};
#pragma unroll
      for (int k = 0; k < 10; ++k) {
        const f32x4 xn = *reinterpret_cast<const f32x4*>(&s_xn[(k * 8 + r) * 4]);
#pragma unroll
        for (int d = 0; d < 4; ++d) {
          const f32x2 w2 = bf_pair(wv[k][d]);
#pragma unroll
          for (int e = 0; e < 4; ++e) qa[e * 4 + d] = fma2(xn[e], w2, qa[e * 4 + d]);
        }
      }
      float t16[16], t8[8];
#pragma unroll
      for (int i = 0; i < 16; ++i) t16[i] = tsum32(qa[i >> 1][i & 1], qa[8 + (i >> 1)][i & 1]);
#pragma unroll
      for (int i = 0; i < 8; ++i) t8[i] = tsum16(t16[i], t16[i + 8]);
#pragma unroll
      for (int i = 0; i < 8; ++i) t8[i] += dpp<DPP_ROR8>(t8[i]);
      const bool up = (lane & 8) != 0;
      f32x4* r4 = reinterpret_cast<f32x4*>(smem + OFF_RED);
      r4[wave * 64 + lane] = f32x4{up ? t8[4] : t8[0], up ? t8[5] : t8[1], up ? t8[6] : t8[2], up ? t8[7] : t8[3]};
      LGKM0();
      if (lds_add(s_c + C_P, lane) == NMW - 1) {
        asm volatile("" ::: "memory");
        const f32x4 o4 = (r4[lane] + r4[64 + lane]) + (r4[128 + lane] + r4[192 + lane]);
        st128nt(rs, WS_QP + ((hp * 2 + hf) * NB + bb) * DH + ck * 64 + j * 8 + (up ? 4 : 0), __builtin_bit_cast(u32x4, o4));
        VMCNT(0);
        if (lane == 0) s_c[F_OUT] = 1u;
      }
    } else {
      if (wave == 0) s_c[lane] = lane == C_K ? NTP + NMW : 0u;
      MEMFENCE(); LGKM0(); __builtin_amdgcn_s_barrier(); MEMFENCE();
      if (wave == 3) { NONCE(); }
      else {
        typedef __attribute__((address_space(1))) const unsigned g_u32;
        typedef __attribute__((address_space(1))) const char g_char;
        const int id = ((idx >> 3) * 2 + (mm & 1)) * 3 + wave;
        if (lane < CODE / (24 * 64)) {
          const unsigned v = *(g_u32*)((pc & ~255ull) + id * (CODE / 24) + lane * 64);
          asm volatile("" :: "v"(v));
        }
        if (lane < 7 && id < 21) {
          const int st = id < 8 ? (0x1320 >> ((id >> 1) * 4)) & 3 : (id >> 1) & 3, hf = id & 1;
          int head = row_of(st) * NH + xl * 4 + hf * 2;
          unsigned off = min(lane << 20, (6 << 20) - 64);
          if (st == 1) {
            head = 2 * NH + (hf ? 16 + xl : 2 * xl);
            if (hf) off = lane < 4 ? min(lane << 20, (3 << 20) - 64) : 8 * (3 << 20) + min((lane - 4) * (3 << 19), (3 << 20) - 64);
          }
          g_char* p = (g_char*)(id < 8 ? kb : vb) + (size_t)head * (3 << 20) + off;
          if (id >= 16) p = (g_char*)(id == 16 ? wo + xl * 4 * DH * DM : (id == 17 ? wg : (id == 18 ? wu : wd))) + min(lane * (id == 16 ? 163840 : 327680), (id == 16 ? 983040 : 1966080) - 64);
          if (id >= 20) p = (g_char*)(lane ? wr : out);
          until(t0, TPT + id * TPS);
          const unsigned v = __builtin_nontemporal_load((g_u32*)p);
          asm volatile("" :: "v"(v));
        }
      }
    }
    unsigned tk_e = poff(pair_of(lane * 4)) | (pair_of(lane * 4) >> 2) | (seg_k(lane * 4) << 6), tv_o = poff(lane * 4 + wave);
    int tv_d = due_v(lane * 4 + wave);
    asm volatile("" : "+v"(tk_e), "+v"(tv_o), "+v"(tv_d));
    MEMFENCE();
    if (wave == 3) {
      __builtin_amdgcn_s_setprio(3);
      const unsigned nonce = f;
      unsigned echoed = 0u;
      if (mm < NPROD) {
        echoed = ld32(rs, qf + (GS + c) * 32);
        asm volatile("" : "+v"(echoed) :: "memory");
        lds_wait<1>(s_c + F_OUT, 1u);
        if (lane < 32) st32(rs, qf + mm * 32 + lane, echoed);
#pragma unroll 1
        while ((int)(clk() - t0) < QWAIT - QBACK) {
          const unsigned nv = ld32(rs, qf + (GS + c) * 32);
          if (__any(nv != echoed)) {
            if (lane < 32) st32(rs, qf + mm * 32 + lane, nv);
            echoed = nv;
          }
          __builtin_amdgcn_s_sleep(4);
        }
      }
      until(t0, QWAIT);
#pragma unroll 1
      for (unsigned spin = 0; spin < SPIN_CAP; ++spin) {
        const unsigned nv = ld32(rs, qf + (GS + c) * 32);
        f = ld32(rs, qf + c * 32 + mm);
        const bool done = __all(c == mm || c >= NPROD || f == nonce);
        if (mm < NPROD && __any(nv != echoed)) {
          if (lane < 32) st32(rs, qf + mm * 32 + lane, nv);
          echoed = nv;
        }
        if (done) break;
        __builtin_amdgcn_s_sleep(8);
      }
      if (lane < GS) s_c[S_ECHO + lane] = echoed;
      if (lane < DH / 4) {
        u32x4 p[3][2];
#pragma unroll
        for (int it = 0; it < 3; ++it)
#pragma unroll
          for (int k = 0; k < 2; ++k) p[it][k] = ld128nt(rs, WS_QP + ((g * 2 + k) * NB + (it ? it + 1 : 0)) * DH + lane * 4);
#pragma unroll
        for (int it = 0; it < 3; ++it) {
          *reinterpret_cast<u32x4*>(&s_q[(it ? it + 3 : 0) * DH + lane * 4]) = q_pack(p[it][0], p[it][1]);
          if (it != 1) { LGKM0(); if (lane == 0) s_c[F_Q] = it + 1; MEMFENCE(); }
        }
      }
    }
    __builtin_amdgcn_s_setprio(3);
    until(t0, due_k(NTP + wave));
    tb0 = lds_get(s_c + S_LATE);
    if ((unsigned)(tb0 - t0 + 100u) > 500u) tb0 = t0;
    until(tb0, due_k(NTP + wave));
    Table tt = {tb0, 1 << 20, 0, 0, 0, {0u, 0u}, (unsigned)(size_t)(s_c + S_MOVED + wave), 0u};
    bool common = false;
    tile_load(ta, kb, poff(NTP + wave) + lo, 0);
    tile_load(tb, kb, poff(NTP + wave) + lo, 1);
    MEMFENCE();
    {
      u32x2 q[12];
      int cp = NTP + wave, it = 0, nxt = lds_add(s_c + C_K, lane), passed = 0, ci = -1;
      auto kuse = [&](Tile& xa, Tile& xb_) __attribute__((always_inline)) {
        if (it != ci) {
          LGKM0();
          for (; passed < it; ++passed) bump(s_c + C_SC + passed);
          lds_wait<1>(s_c + (it < 3 ? F_Q : F_Q2), min(it + 1, 3));
          q_load(q, s_q + seg_of_k(it) * DH, j);
          if (!common) { const unsigned d = lds_get(s_c + S_FIRST); if (d) { tt.t0 -= d - 1u; common = true; tt.was = 1 << 20; } }
          tt.agree(s_c + S_MOVED);
          ci = it;
        }
        const int tk = arrive(s_c + C_K);
        tt.listen(s_c + S_SLOW + 2 * wave);
        pair_put(tile_score(xa, q), tile_score(xb_, q), s_sc + cp * 16 + r, j);
        MEMFENCE();
        return arrived(tk);
      };
      auto kstep = [&](Tile& xa, Tile& xb_, Tile& ya, Tile& yb) __attribute__((always_inline)) {
        const unsigned e = __builtin_amdgcn_readlane(tk_e, nxt >> 2);
        MEMFENCE();
        const int deep = tt.gate(due_k(nxt));
        MEMFENCE();
        pair_issue(ya, yb, kb, (e & ~8191u) + (nxt & 3) * 6144 + lo, deep);
        const int n2 = kuse(xa, xb_);
        cp = (e & 63) * 4 + (nxt & 3); it = (e >> 6) & 7; nxt = n2;
      };
      bool swap;
      for (;;) {
        if (nxt >= NPAIR) { swap = false; break; }
        kstep(ta, tb, tc, td);
        if (nxt >= NPAIR) { swap = true; break; }
        kstep(tc, td, ta, tb);
      }
      tt.gate(due_k(NPAIR + wave));
      MEMFENCE();
      const unsigned* pw = reinterpret_cast<const unsigned*>(wo + (xl * 128 + idx * 4 + wave) * 3840) + lane * 16;
      const unsigned v0 = pw[0], v1 = pw[56 * 16];
      MEMFENCE();
      if (swap) { ta = tc; tb = td; }
      kuse(ta, tb);
      for (; passed < NS; ++passed) bump(s_c + C_SC + passed);
      tt.gate(due_v(wave));
      MEMFENCE();
      pair_issue(ta, tb, vb, __builtin_amdgcn_readlane(tv_o, 0) + lo, 2);
      asm volatile("" :: "v"(v0), "v"(v1));
    }
    const unsigned sc_r = (unsigned)(size_t)(__attribute__((address_space(3))) float*)s_sc + r * 4;
    f32x2 acc[12];
    auto vstep = [&](Tile& xa, Tile& xb_, Tile& ya, Tile& yb, int p, bool fin) __attribute__((always_inline)) {
      const unsigned off = __builtin_amdgcn_readlane(tv_o, (p >> 2) + 1) + lo;
      MEMFENCE();
      const int deep = tt.gate(__builtin_amdgcn_readlane(tv_d, (p >> 2) + 1), p >= NPAIR - ENDT * NMW ? ENDS : 0);
      float pa, pb;
      asm volatile("ds_read_b32 %0, %2\n\tds_read_b32 %1, %2 offset:32" : "=&v"(pa), "=&v"(pb) : "v"(sc_r + p * 64) : "memory");
      MEMFENCE();
      pair_issue(ya, yb, vb, off, deep);
      asm volatile("s_waitcnt lgkmcnt(0)" : "+v"(pa), "+v"(pb) :: "memory");
      if (!fin) tt.listen(s_c + S_SLOW + 2 * wave);
      else bump(s_c + C_LAST);
      pv_last(pa, xa, acc);
      pv_last(pb, xb_, acc);
      MEMFENCE();
    };
    typedef __attribute__((address_space(3))) float lds_f32;
    auto fold8 = [&](lds_f32* dst) __attribute__((always_inline)) {
      float t12[12], t6[6];
#pragma unroll
      for (int i = 0; i < 12; ++i) t12[i] = tsum32(acc[i >> 1][i & 1], acc[6 + (i >> 1)][i & 1]);
#pragma unroll
      for (int i = 0; i < 6; ++i) t6[i] = tsum16(t12[i], t12[i + 6]);
#pragma unroll
      for (int i = 0; i < 6; ++i) t6[i] += dpp<DPP_ROR8>(t6[i]);
      auto d2 = (__attribute__((address_space(3))) f32x2*)dst;
      d2[0] = f32x2{t6[0], t6[1]}; d2[1] = f32x2{t6[2], t6[3]}; d2[2] = f32x2{t6[4], t6[5]};
      CFENCE();
    };
    lds_f32* dst = (lds_f32*)s_buf + wave * DH + j * 24 + (lane >> 4) * 6;
#pragma unroll 1
    for (int it = 0;; ++it) {
      lds_wait<1>(s_c + F_P, k_of_seg(it) + 1);
      if (!common) { const unsigned d = lds_get(s_c + S_FIRST); if (d) { tt.t0 -= d - 1u; common = true; tt.was = 1 << 20; } }
      tt.agree(s_c + S_MOVED);
#pragma unroll
      for (int i = 0; i < 12; ++i) acc[i] = f32x2{0.f, 0.f};
#pragma unroll 1
      for (int p = seg_beg(it) + wave; p < min(seg_beg(it + 1), NPAIR - 2 * NMW); p += 2 * NMW) {
        vstep(ta, tb, tc, td, p, false);
        vstep(tc, td, ta, tb, p + NMW, false);
      }
      if (it >= NRING) lds_wait<1>(s_c + F_FREE, it - NRING + 1);
      if (it == NS - 1) break;
      fold8(dst + (it % NRING) * (NMW * DH));
      bump(s_c + C_PART + it);
    }
    dst += ((NS - 1) % NRING) * (NMW * DH);
    const int ln = lane < DH / 4 ? lane : 0;
    auto src = (__attribute__((address_space(3))) f32x4*)((lds_f32*)s_buf + ((NS - 1) % NRING) * (NMW * DH) + ((ln >> 1) & 7) * 24 + (ln >> 4) * 8 + (ln & 1) * 4);
    int so = (lane < DH / 4 ? WS_PO + xb * DH + lane * 4 : WS_END) * 4;
    const int p = NPAIR - NMW + wave;
    float pa = s_sc[p * 16 + r], pb = s_sc[p * 16 + 8 + r];
    asm volatile("" : "+v"(dst), "+v"(src), "+v"(so), "+v"(pa), "+v"(pb));
    vstep(ta, tb, tc, td, NPAIR - 2 * NMW + wave, true);
    pv_last(pa, tc, acc);
    pv_last(pb, td, acc);
    fold8(dst);
    const int k = arrive(s_c + C_PART + NS - 1);
    const f32x4 m = (src[0] + src[DH / 4]) + (src[2 * DH / 4] + src[3 * DH / 4]);
    u32x4 o = {nz_bits(m[0]), nz_bits(m[1]), nz_bits(m[2]), nz_bits(m[3])};
    asm volatile("" : "+v"(o));
    if (arrived(k) == NMW - 1) {
      __builtin_amdgcn_raw_buffer_store_b128(o, rs, so, 0, AUX_SC1);
      bump(s_c + F_DONE);
    }
    return;
  }

  {
    Tile pk[NRP * 2];
    __builtin_amdgcn_s_setprio(3);
    const unsigned it0 = (row_of(0) * NH + g) * 1024, ct = c0 + tw * 2;
    auto toff = [&](int turn) -> unsigned { return (it0 + ((turn * 64 + ct) & 1023)) * 3072u + lo; };
    unsigned tbw = 0u;
#pragma unroll 1
    for (int e = 0; e < NLP * 2; ++e) {
      const char* src = kb + (toff(e >> 1) + (e & 1) * 3072);
      if (e >= 2) until(tbw, (e * 2 * TAUK) >> 6);
#pragma unroll
      for (int i = 0; i < 3; ++i) glds16_nt(src + i * 128, lds_base + (e * 3 + i) * 1024);
      if (e == 1) {
        VMCNT(3);
        tb0 = t0 + min(max((int)(clk() - t0) - ANCH, 0), 300);
        tbw = tb0 + due_k(tw) - TLEAD;
        __builtin_amdgcn_s_barrier();
        if (tw == 0 && lane == 0) s_c[S_LATE] = tb0;
      }
      VMCNT(PFD);
    }
#pragma unroll
    for (int e = 0; e < NRP * 2; ++e) {
      until(tbw, ((NLP * 2 + e) * 2 * TAUK) >> 6);
      tile_load(pk[e], kb, toff(NLP + (e >> 1)), e & 1);
      MEMFENCE();
      if (e + 1 < NRP * 2) VMCNT(PFD);
    }
    if (tw == 0) {
      r0 = ld32(rs, WS_HR + bid * 2);
      if (bid == LEADER) {
        u32x4 n;
#pragma unroll
        for (int k = 0; k < 4; ++k) n[k] = ld32(rs, WS_HA + (lane * 4 + k) * 32) + 1u;
        st128(rs, WS_HN + lane * 4, n);
      }
      const u32x4 z = {0u, 0u, 0u, 0u};
      st128(rs, lane < 48 ? WS_PO + xb * DH + lane * 4 : WS_PML + xb * 64 + (lane - 48) * 4, z);
      const int o = isW ? WS_SLAB + w_ks * NB * DM + w_ct * 64 : (isG ? WS_ACT + g_cg * 64 : (isH ? WS_H + (xl - 2) * 768 : WS_OC + (c_it * NG + g) * DH));
      st128(rs, lane < (isG ? 16 : (isC ? 48 : (isW || isH ? 64 : 0))) ? o + (isW ? (lane >> 4) * DM + (lane & 15) * 4 : lane * 4) : WS_END, z);
      if (isH) { st128(rs, o + 256 + lane * 4, z); st128(rs, o + 512 + lane * 4, z); }
    }
    __builtin_amdgcn_s_setprio(0);
    lds_wait<1>(s_c + F_Q, HPG);
    u32x2 q[12];
    q_load(q, s_q, j);
    const u32x4* s_kv = reinterpret_cast<const u32x4*>(smem + OFF_REG + tw * REG) + lane;
    static_assert(NLP * 4 <= PPI && NTP <= HPG * PPI, "tail tiles: batch row 0 only");
#pragma unroll 1
    for (int e = 0; e < NLP * 2; ++e) {
      Tile tmp;
#pragma unroll
      for (int i = 0; i < 3; ++i) tmp.d[i] = s_kv[(e * 3 + i) * 64];
      score_put(tile_score(tmp, q), s_sc + ((e >> 1) * 4 + tw) * 16 + (e & 1) * 8 + r, j);
    }
#pragma unroll
    for (int e = 0; e < NRP * 2; ++e) {
      if ((NLP * 2 + e) * 2 == PPI) q_load(q, s_q + DH, j);
      score_put(tile_score(pk[e], q), s_sc + ((NLP + (e >> 1)) * 4 + tw) * 16 + (e & 1) * 8 + r, j);
      __builtin_amdgcn_sched_barrier(0);
    }
    asm volatile("s_waitcnt vmcnt(0) lgkmcnt(0)" ::: "memory");
#pragma unroll
    for (int it = 0; it * PPI < NTP; ++it) lds_add(s_c + C_SC + it, lane);
  }

  const int mr = lane & 15, qd = lane >> 4;
  unsigned char* reg = smem + OFF_REG + tw * REG;
  float* red = reinterpret_cast<float*>(smem + OFF_RED);
#define PERM_LO(hi_, lo_) __builtin_amdgcn_perm(hi_, lo_, 0x05040100u)
#define PERM_HI(hi_, lo_) __builtin_amdgcn_perm(hi_, lo_, 0x07060302u)
  int served = 0;
  auto done = [&](int n) {
    if (tw == 3 && served < min(n, NA)) {
      const int from = served;
      int ln = lane;
      asm volatile("" : "+v"(ln));
#pragma unroll 1
      for (; served < min(n, NA); ++served) {
        lds_wait<8>(s_c + C_PART + served, NMW);
        const f32x4 o = merged(served, ln < DH / 4 ? ln : 0);
        const float am = max_x16_32(row_max(fmaxf(fmaxf(fabsf(o[0]), fabsf(o[1])), fmaxf(fabsf(o[2]), fabsf(o[3])))));
        const float inv = am > 1e-30f ? 32767.0f / am : 0.f;
        const int q0 = __float2int_rn(o[0] * inv), q1 = __float2int_rn(o[1] * inv), q2 = __float2int_rn(o[2] * inv), q3 = __float2int_rn(o[3] * inv);
        if (ln < DH / 4) reinterpret_cast<u32x2*>(s_sc + served * (DH / 2))[ln] = u32x2{PERM_LO((unsigned)q1, (unsigned)q0), PERM_LO((unsigned)q3, (unsigned)q2)};
        const bool nan = __any((o[0] + o[1]) + (o[2] + o[3]) != (o[0] + o[1]) + (o[2] + o[3]));
        if (ln == 0) s_ml[served][2] = nan ? 0x7fc00000u : nz_bits(am * (1.0f / 32767.0f));
        LGKM0();
        if (ln == 0) s_c[F_FREE] = served + 1;
      }
#pragma unroll 1
      for (int c = from * 24 + ln; c < served * 24; c += 64) st128nt(rs, WS_PQ + xb * 5 * (DH / 2) + c * 4, reinterpret_cast<const u32x4*>(s_sc)[c]);
      VMCNT(0);
      if (ln < 8) st128(rs, WS_PML + xb * 64 + (from ? 32 : 0) + ln * 4, s_ml[ln]);
      if (ln == 0) s_c[S_BURST + (from ? 1 : 0)] = clk();
      CFENCE();
      if (ln == 0) __hip_atomic_fetch_add(s_c + F_DONE, (unsigned)(served - from), __ATOMIC_RELAXED, __HIP_MEMORY_SCOPE_WORKGROUP);
    }
    if (n > NA) lds_wait<1>(s_c + F_DONE, n); else lds_wait<8>(s_c + F_DONE, n);
  };
  auto done_all = [&]() {
#pragma unroll 1
    for (int n = 4; n <= NA; ++n) done(n);
  };
  const int lp = (lane < DH / 4 ? lane : 0) * 4;
  auto weight = [&](int it, int x0, int cnt) -> float {
    const int mlw = WS_PML + (x0 + (lane & (cnt - 1))) * 64 + (it < 4 ? 0 : 32) + it * 4;
    u32x4 ml;
#pragma unroll 1
    for (unsigned spin = 0; spin < SPIN_CAP; ++spin) {
      ml = ld128(rs, mlw);
      if (__all(min4(ml) != 0u)) break;
      __builtin_amdgcn_s_sleep(16);
    }
    const float mk = __uint_as_float(ml[0]);
    const float e = __builtin_amdgcn_exp2f(mk - max_x16_32(row_max(mk)));
    return e / (wave_sum(e * __uint_as_float(ml[1])) * ((float)cnt * (1.0f / 64.0f))) * __uint_as_float(ml[2]);
  };
  const unsigned tok = __builtin_amdgcn_readfirstlane(r0) + 1u;
  unsigned echoed = 0u, fold = 0u;
  int slow = 0, prev = 0;
  auto lag = [&]() {
    const unsigned v = __hip_atomic_load(s_c + S_MOVED + (lane & 7), __ATOMIC_RELAXED, __HIP_MEMORY_SCOPE_WORKGROUP);
    fold += v;
    unsigned a = 0u;
#pragma unroll
    for (int k = 0; k < 8; ++k) a += __builtin_amdgcn_readlane(v, k);
    return a >> 2;
  };
  auto tell = [&]() { if (lane < 4) { s_c[S_SLOW + 2 * lane] = slow; s_c[S_SLOW + 2 * lane + 1] = fold; } };
  auto round = [&](unsigned rd, bool lead) {
    const int tr = RT0 + (int)(rd - 1) * RTS;
    until(t0, tr);
    const unsigned bh = lag();
    if (bh > RMIN && lane < 8) st128(rs, WS_HA + bid * 32 + lane * 4, u32x4{echoed + rd, tok, tb0, bh});
    if (lead) { until(t0, tr + RA1); leader_release(rs, lane, tb0, rd, slow, prev); }
    until(t0, tr + RA2);
    const u32x2 rv = ld64(rs, WS_HR + bid * 2);
    slow = (int)__builtin_amdgcn_readfirstlane(rv[1]) >> 16;
#pragma unroll 1
    for (unsigned k = min(__builtin_amdgcn_readfirstlane(rv[0]) - tok, rd) + 1u; k <= rd; ++k) slow = slow > 0 ? max(slow - GB, 0) : slow - (k <= PRD ? PRB : 0);
    tell();
  };
  auto prep = [&](bool lead) {
    if (tw == 0) {
      const int c = lane & (GS - 1);
      unsigned qe = s_c[S_ECHO + c];
      bool first = true, released = false, sent = false;
#pragma unroll 1
      while ((int)(clk() - t0) < HST) {
        const unsigned qn = ld32(rs, qf + (GS + c) * 32);
        if (mm < NPROD && __any(qn != qe)) {
          if (lane < 32) st32(rs, qf + mm * 32 + lane, qn);
          qe = qn;
        }
        __builtin_amdgcn_s_sleep(64);
      }
      const unsigned behind = lag();
#pragma unroll 1
      for (unsigned spin = 0; spin < SPIN_CAP && !released; ++spin) {
        const unsigned qn = ld32(rs, qf + (GS + c) * 32);
        const u32x2 rv = ld64(rs, WS_HR + bid * 2);
        const unsigned nv = __builtin_amdgcn_readfirstlane(ld32(rs, WS_HN + bid));
        if (__builtin_amdgcn_readfirstlane(rv[0]) - tok <= NRD) {
          released = true;
          slow = (int)__builtin_amdgcn_readfirstlane(rv[1]) >> 16;
          if (lane == 0) { s_c[S_FIRST] = min((tb0 - rv[1]) & 0xffffu, 300u) + 1u; }
        } else {
          if (mm < NPROD && __any(qn != qe)) {
            if (lane < 32) st32(rs, qf + mm * 32 + lane, qn);
            qe = qn;
          }
          if (first || nv != echoed) {
            echoed = nv; first = false;
            if (lane < 8) st128(rs, WS_HA + bid * 32 + lane * 4, u32x4{echoed, tok, tb0, behind});
          }
          if (lead && !sent) sent = leader_release(rs, lane, tb0, 0u, slow, prev);
          __builtin_amdgcn_s_sleep(32);
        }
      }
      tell();
      if (lane == 0) s_c[F_REL] = 1u;
      if (lane < DH / 4) {
        u32x4 p[3][2];
#pragma unroll
        for (int e = 0; e < 3; ++e)
#pragma unroll
          for (int k = 0; k < 2; ++k) p[e][k] = ld128nt(rs, WS_QP + (((e < 2 ? 24 - e * 8 + (xb >> 5) : xb >> 4) * 2 + k) * NB + 1) * DH + lane * 4);
#pragma unroll
        for (int e = 0; e < 3; ++e) *reinterpret_cast<u32x4*>(&s_q[(1 + e) * DH + lane * 4]) = q_pack(p[e][0], p[e][1]);
      }
      LGKM0();
      if (lane == 0) s_c[F_Q2] = 3u;
#pragma unroll 1
      for (unsigned rd = 1; rd <= NRD; ++rd) round(rd, lead);
    }
    if (tw == 3) {
#pragma unroll 1
      for (int ik = 0; ik < NS; ++ik) {
        const int it = seg_of_k(ik), nv = (seg_beg(it + 1) - seg_beg(it)) >> 4;
        lds_wait<4>(s_c + C_SC + ik, ik == 0 ? NW : NMW);
        f32x4* sp = reinterpret_cast<f32x4*>(s_sc + seg_beg(it) * 16) + lane;
        f32x4 v[4];
#pragma unroll
        for (int k = 0; k < 4; ++k) v[k] = sp[(k & (nv - 1)) * 64];
        const f32x4 mx = __builtin_elementwise_max(__builtin_elementwise_max(v[0], v[1]), __builtin_elementwise_max(v[2], v[3]));
        const float m = max_x16_32(row_max(fmaxf(fmaxf(mx[0], mx[1]), fmaxf(mx[2], mx[3]))));
        float l = 0.f;
#pragma unroll
        for (int k = 0; k < 4; ++k) {
#pragma unroll
          for (int e = 0; e < 4; ++e) v[k][e] = __builtin_amdgcn_exp2f(v[k][e] - m);
          if (k < nv) {
            l += (v[k][0] + v[k][1]) + (v[k][2] + v[k][3]);
            sp[k * 64] = v[k];
          }
        }
        l = wave_sum(l);
        if (lane == 0) s_ml[it] = u32x4{nz_bits(m), nz_bits(l), 0x3f800000u, 0x3f800000u};
        LGKM0();
        if (lane == 0) s_c[F_P] = ik + 1;
      }
    }
  };
  auto pull = [&]() {
    typedef __attribute__((address_space(1))) const unsigned g_u32;
    const int id = (ro - 16) * 4 + tw;
    g_u32* p1[2];
#pragma unroll
    for (int i = 0; i < 2; ++i) {
      const int l = id * 60 + min(i * 32 + (lane >> 1), 59), m = l >= 1920 ? l - 1920 : l;
      p1[i] = (g_u32*)((l >= 1920 ? wu : wg) + (size_t)(m / 3) * FF + xl * 192 + (m % 3) * 64 + (lane & 1) * 32);
    }
    g_u32* p2 = (g_u32*)(wd + (size_t)(id * 24 + min(lane >> 1, 23)) * DM + xl * 64 + (lane & 1) * 32);
    asm volatile("" : "+v"(p1[0]), "+v"(p1[1]), "+v"(p2));
    lds_wait<1>(s_c + C_LAST, NMW);
    const unsigned tp = clk();
    until(tp, PW);
    const unsigned v0 = p1[0][0], v1 = p1[1][0];
    MEMFENCE();
    until(tp, PW + DPW);
    const unsigned v2 = p2[0], v3 = p2[xl < 2 ? 256 : 0];
    asm volatile("" :: "v"(v0), "v"(v1), "v"(v2), "v"(v3));
  };
  if (isW) {
    u32x4 bx[24];
    const char* p = reinterpret_cast<const char*>(wo + (size_t)((w_ks * 4 + tw) * DH + r) * DM + w_ct * 64 + j * 8);
    prep(false);
    if (tw != 0) { lds_wait<16>(s_c + F_P, NS); pause(clk(), OLATE + (tw % 3) * 24 * OSTEP); }
    const unsigned tw0 = clk();
#pragma unroll 1
    for (int i = 0; i < 24; ++i) { until(tw0, i * OSTEP); glds16_nt(p, lds_base + i * 1024); p += 8 * DM * 2; VMCNT(WFD); }
    asm volatile("s_waitcnt vmcnt(0)" ::: "memory");
#pragma unroll
    for (int s = 0; s < 6; ++s) {
      u32x2 e[8];
#pragma unroll
      for (int k = 0; k < 8; ++k) e[k] = *reinterpret_cast<const u32x2*>(reg + (s * 32 + qd * 8 + k) * 128 + mr * 8);
#pragma unroll
      for (int k = 0; k < 4; ++k) {
        bx[s * 4 + 0][k] = PERM_LO(e[2 * k + 1][0], e[2 * k][0]); bx[s * 4 + 1][k] = PERM_HI(e[2 * k + 1][0], e[2 * k][0]);
        bx[s * 4 + 2][k] = PERM_LO(e[2 * k + 1][1], e[2 * k][1]); bx[s * 4 + 3][k] = PERM_HI(e[2 * k + 1][1], e[2 * k][1]);
      }
    }
#pragma unroll
    for (int s = 0; s < 24; s += 4) asm volatile("" : "+v"(bx[s]), "+v"(bx[s + 1]), "+v"(bx[s + 2]), "+v"(bx[s + 3]));
    LGKM0();
    lds_wait<8>(s_c + F_REL, 1u);
    const int hh = w_ks * 4 + tw;
#pragma unroll 1
    for (int pass = 0; pass < 3; ++pass) {
      if (pass < 2) {
        done(pass ? NA : 4);
        pause(lds_get(s_c + S_BURST + pass), pass ? WDELAY2 : WDELAY2 + 300);
        const int b0 = pass * 2, b1 = pass + 1;
        u32x4 v0, v1;
#pragma unroll 1
        for (unsigned spin = 0; spin < SPIN_CAP; ++spin) {
          v0 = ld128(rs, WS_OC + (b0 * NH + hh) * DH + lp);
          v1 = ld128(rs, WS_OC + (b1 * NH + hh) * DH + lp);
          if (__all(min(min4(v0), min4(v1)) != 0u)) break;
          __builtin_amdgcn_s_sleep(16);
        }
        if (lane < DH / 4) {
          *reinterpret_cast<u32x2*>(reg + (b0 * 4) * AW_LD + lane * 8) = u32x2{v0[0], v0[1]};
          *reinterpret_cast<u32x2*>(reg + (b0 * 4 + 1) * AW_LD + lane * 8) = u32x2{v0[2], v0[3]};
          *reinterpret_cast<u32x2*>(reg + (b1 * 4) * AW_LD + lane * 8) = u32x2{v1[0], v1[1]};
          *reinterpret_cast<u32x2*>(reg + (b1 * 4 + 1) * AW_LD + lane * 8) = u32x2{v1[2], v1[3]};
        }
      } else {
        const float wk = weight(NS - 1, hh * GS, GS);
        float w[GS];
#pragma unroll
        for (int k = 0; k < GS; ++k) { w[k] = __uint_as_float(__builtin_amdgcn_readlane(__float_as_uint(wk), k)); asm volatile("" : "+s"(w[k])); }
        done(NS);
        __builtin_amdgcn_s_setprio(3);
        const int po = WS_PO + hh * GS * DH;
        u32x4 pv[GS];
#pragma unroll 1
        for (unsigned spin = 0; spin < SPIN_CAP; ++spin) {
#pragma unroll
          for (int k = 0; k < GS; ++k) pv[k] = __builtin_amdgcn_raw_buffer_load_b128(rs, lp * 4, (po + k * DH) * 4, AUX_SC1);
          unsigned mn = min4(pv[0]);
#pragma unroll
          for (int k = 1; k < GS; ++k) mn = min(mn, min4(pv[k]));
          if (__all(mn != 0u)) break;
          __builtin_amdgcn_s_sleep(1);
        }
        f32x4 o = w[0] * __builtin_bit_cast(f32x4, pv[0]), o2 = w[1] * __builtin_bit_cast(f32x4, pv[1]);
#pragma unroll
        for (int k = 2; k < GS; k += 2) { o += w[k] * __builtin_bit_cast(f32x4, pv[k]); o2 += w[k + 1] * __builtin_bit_cast(f32x4, pv[k + 1]); }
        u32x2 hi, lw;
        split4(o + o2, hi, lw);
        if (lane < DH / 4) {
          *reinterpret_cast<u32x2*>(reg + ((NB - 1) * 4) * AW_LD + lane * 8) = hi;
          *reinterpret_cast<u32x2*>(reg + ((NB - 1) * 4 + 1) * AW_LD + lane * 8) = lw;
        }
      }
      CFENCE();
      u32x4 a[6];
#pragma unroll
      for (int s = 0; s < 6; ++s) a[s] = *reinterpret_cast<const u32x4*>(reg + mr * AW_LD + s * 64 + qd * 16);
      __builtin_amdgcn_sched_barrier(0);
      f32x4 c[4];
#pragma unroll
      for (int t = 0; t < 4; ++t) c[t] = f32x4{0.f, 0.f, 0.f, 0.f};
#pragma unroll
      for (int s = 0; s < 6; ++s)
#pragma unroll
        for (int t = 0; t < 4; ++t) c[t] = mfma16(a[s], bx[s * 4 + t], c[t]);
      const bool mine = max(qd - 1, 0) == pass;
      f32x4* r4 = reinterpret_cast<f32x4*>(red);
      if (mine) r4[tw * 64 + lane] = f32x4{c[0][0] + c[0][1], c[1][0] + c[1][1], c[2][0] + c[2][1], c[3][0] + c[3][1]};
      const int k = arrive(s_c + C_W + pass);
      f32x4 sum = (r4[lane] + r4[64 + lane]) + (r4[128 + lane] + r4[192 + lane]);
      asm volatile("" : "+v"(sum));
      if (arrived(k) == 3) {
        if (mine) st128(rs, WS_SLAB + (w_ks * NB + qd) * DM + w_ct * 64 + mr * 4, u32x4{nz_bits(sum[0]), nz_bits(sum[1]), nz_bits(sum[2]), nz_bits(sum[3])});
      }
    }
  } else if (isG || isH) {
    f32x2* rst = reinterpret_cast<f32x2*>(red);
    const int hb = isH ? xl - 2 : NB - 1;
    uint2 wr4 = make_uint2(0u, 0u), xg = wr4;
    if (lane < 40) {
      wr4 = *reinterpret_cast<const uint2*>(wr + tw * 160 + lane * 4);
      xg = *reinterpret_cast<const uint2*>(x + row_of(hb) * DM + tw * 160 + lane * 4);
    }
    f32x4 xc = f32x4{bf_lo(xg.x), bf_hi(xg.x), bf_lo(xg.y), bf_hi(xg.y)} - bf_lo(*reinterpret_cast<const unsigned*>(x + row_of(hb) * DM));
    asm volatile("" : "+v"(xc));
    typedef __attribute__((address_space(1))) const u32x4 g_u32x4;
    g_u32x4* wp[10];
    wp[0] = (g_u32x4*)(((mr & 4) ? wu : wg) + (size_t)(tw * 160 + qd * 8 + (mr >> 3) * 4) * FF + g_cg * 32 + (mr & 3) * 8);
    u32x4 e[20], bx[20];
    auto frag = [&](int s) __attribute__((always_inline)) {
      unsigned w[8][2];
#pragma unroll
      for (int i = 0; i < 4; ++i)
#pragma unroll
        for (int d = 0; d < 2; ++d) {
          w[i][d] = __builtin_amdgcn_update_dpp(e[s * 4 + i][d], e[s * 4 + i][2 + d], DPP_ROR8, 0xf, 0xc, false);
          w[4 + i][d] = __builtin_amdgcn_update_dpp(e[s * 4 + i][2 + d], e[s * 4 + i][d], DPP_ROR8, 0xf, 0x3, false);
        }
#pragma unroll
      for (int k = 0; k < 4; ++k) {
        bx[s * 4 + 0][k] = PERM_LO(w[2 * k + 1][0], w[2 * k][0]); bx[s * 4 + 1][k] = PERM_HI(w[2 * k + 1][0], w[2 * k][0]);
        bx[s * 4 + 2][k] = PERM_LO(w[2 * k + 1][1], w[2 * k][1]); bx[s * 4 + 3][k] = PERM_HI(w[2 * k + 1][1], w[2 * k][1]);
      }
      PIN4(bx, s * 4);
    };
    prep(false);
    lds_wait<8>(s_c + F_REL, 1u);
    auto hrow = [&](bool fast) -> f32x4 {
      f32x4 hv = {0.f, 0.f, 0.f, 0.f};
      if (lane < 40) {
        const int off = (WS_SLAB + hb * DM + tw * 160 + lane * 4) * 4;
        u32x4 sl[KS];
#pragma unroll 1
        for (unsigned spin = 0; spin < SPIN_CAP; ++spin) {
#pragma unroll
          for (int ks = 0; ks < KS; ++ks) sl[ks] = __builtin_amdgcn_raw_buffer_load_b128(rs, off, ks * (NB * DM * 4), AUX_SC1);
          unsigned mn = min4(sl[0]);
#pragma unroll
          for (int ks = 1; ks < KS; ++ks) mn = min(mn, min4(sl[ks]));
          if (!__any(mn == 0u)) break;
          if (fast) __builtin_amdgcn_s_sleep(1); else __builtin_amdgcn_s_sleep(16);
        }
#define SL(k) __builtin_bit_cast(f32x4, sl[k])
        hv = xc + (((SL(0) + SL(1)) + (SL(2) + SL(3))) + ((SL(4) + SL(5)) + (SL(6) + SL(7))));
#undef SL
      }
      return hv;
    };
    auto sums = [&](f32x4 hv) -> f32x2 {
      return f32x2{wave_sum((hv[0] + hv[1]) + (hv[2] + hv[3])), wave_sum((hv[0] * hv[0] + hv[1] * hv[1]) + (hv[2] * hv[2] + hv[3] * hv[3]))};
    };
    if (isH) {
      const float wsum = wave_sum((bf_lo(wr4.x) + bf_hi(wr4.x)) + (bf_lo(wr4.y) + bf_hi(wr4.y)));
      if (tw == 3 || hb >= 2) done_all(); else done(4);
      if (hb == NB - 1) { pull(); done(NS); }
      else pause(lds_get(s_c + S_BURST + (hb >> 1)), hb < 2 ? HDELAY : HDELAY2);
      const f32x4 hv = hrow(hb == NB - 1);
      const f32x2 sm = sums(hv);
      const float dot = wave_sum((hv[0] * bf_lo(wr4.x) + hv[1] * bf_hi(wr4.x)) + (hv[2] * bf_lo(wr4.y) + hv[3] * bf_hi(wr4.y)));
      if (lane < 8) st128(rs, WS_H + (hb * 4 + tw) * 192 + 160 + lane * 4, u32x4{nz_bits(sm[0]), nz_bits(sm[1]), nz_bits(dot), nz_bits(wsum)});
      if (hb == NB - 1) return;
      const u32x4 v = split8(hv, (lane & 1) != 0);
      if (lane < 40) st128(rs, WS_H + (hb * 4 + tw) * 192 + lane * 4, v);
      done_all();
      pull();
      return;
    }
    done_all();
#pragma unroll
    for (int i = 0; i < 10; ++i) { wp[i] = wp[0] + ((i >> 1) * 32 + (i & 1) * 2) * (FF / 8); asm volatile("" : "+v"(wp[i])); }
    done(NS);
#pragma unroll
    for (int i = 0; i < 20; ++i) e[i] = wp[i >> 1][(i & 1) * (FF / 8)];
    u32x4 a[5], sm;
    const int hoff = (WS_H + (min(mr >> 2, NB - 2) * 4 + tw) * 192 + qd * 8 + (mr & 1) * 4) * 4;
#pragma unroll
    for (int s = 0; s < 5; ++s) a[s] = __builtin_amdgcn_raw_buffer_load_b128(rs, hoff + s * 128, 0, AUX_SC1);
    sm = ld128(rs, WS_H + (min(lane, NB - 2) * 4 + tw) * 192 + 160);
    MEMFENCE();
#pragma unroll
    for (int s = 0; s < 5; ++s) frag(s);
    {
#pragma unroll 1
      for (unsigned spin = 0; spin < SPIN_CAP; ++spin) {
        unsigned mn = min4(sm);
#pragma unroll
        for (int s = 0; s < 5; ++s) mn = min(mn, min4(a[s]));
        if (__all(mn != 0u)) break;
        __builtin_amdgcn_s_sleep(2);
#pragma unroll
        for (int s = 0; s < 5; ++s) a[s] = __builtin_amdgcn_raw_buffer_load_b128(rs, hoff + s * 128, 0, AUX_SC1);
        sm = ld128(rs, WS_H + (min(lane, NB - 2) * 4 + tw) * 192 + 160);
      }
      if (lane < NB - 1) rst[lane * 4 + tw] = f32x2{__uint_as_float(sm[0]), __uint_as_float(sm[1])};
#pragma unroll
      for (int s = 0; s < 5; ++s) {
        if (mr & 2) a[s] = u32x4{0x3f803f80u, 0x3f803f80u, 0x3f803f80u, 0x3f803f80u};
        asm volatile("" : "+v"(a[s]));
      }
    }
    typedef __attribute__((address_space(3))) unsigned char lds_u8;
    const int eb = mr >> 2;
    lds_u8* l_aw = (lds_u8*)reg + lane * 8;
    lds_u8* l_ar = (lds_u8*)reg + (mr & 1) * AG_LD + qd * 16;
    lds_u8* l_pw = (lds_u8*)reg + 4096 + (qd * 16 + (mr & 7) * 2 + (mr >> 3)) * 16;
    lds_u8* l_pr = (lds_u8*)smem + OFF_REG + 4096 + (lane & 1) * 1152 + (eb * 16 + qd * 2 + ((lane >> 1) & 1)) * 16;
    lds_u8* l_p2 = l_pr + 2 * REG;
    lds_u8* l_sr = (lds_u8*)(rst + eb * 4);
    lds_u8* l_sw = (lds_u8*)(rst + (NB - 1) * 4 + tw);
    int so = (lane & 3) == 0 ? (WS_ACT + g_cg * 64 + lane) * 4 : WS_END * 4;
    asm volatile("" : "+v"(l_aw), "+v"(l_ar), "+v"(l_pw), "+v"(l_pr), "+v"(l_p2), "+v"(l_sr), "+v"(l_sw), "+v"(so));
#define LDS(T, p) (*reinterpret_cast<__attribute__((address_space(3))) T*>(p))
    const f32x4 hv = hrow(true);
    {
      u32x2 hi, lw;
      split4(hv, hi, lw);
      LDS(u32x2, l_aw) = hi;
      LDS(u32x2, l_aw + AG_LD) = lw;
    }
    CFENCE();
    if ((mr & 14) == 12) {
#pragma unroll
      for (int s = 0; s < 5; ++s) a[s] = LDS(u32x4, l_ar + s * 64);
    }
    __builtin_amdgcn_sched_barrier(0);
    f32x4 c[4];
#pragma unroll
    for (int t = 0; t < 4; ++t) c[t] = f32x4{0.f, 0.f, 0.f, 0.f};
#pragma unroll
    for (int s = 0; s < 5; ++s)
#pragma unroll
      for (int t = 0; t < 4; ++t) c[t] = mfma16(a[s], bx[s * 4 + t], c[t]);
    LDS(f32x2, l_sw) = sums(hv);
    LDS(f32x4, l_pw) = f32x4{c[0][0] + c[0][1], c[1][0] + c[1][1], c[0][2], c[1][2]};
    LDS(f32x4, l_pw + 1152) = f32x4{c[2][0] + c[2][1], c[3][0] + c[3][1], c[2][2], c[3][2]};
    const int k = arrive(s_c + C_T);
    f32x4 ts[2] = {LDS(f32x4, l_sr), LDS(f32x4, l_sr + 16)};
    f32x4 pg[4] = {LDS(f32x4, l_pr), LDS(f32x4, l_pr + REG), LDS(f32x4, l_p2), LDS(f32x4, l_p2 + REG)};
    f32x4 pu[4] = {LDS(f32x4, l_pr + 128), LDS(f32x4, l_pr + REG + 128), LDS(f32x4, l_p2 + 128), LDS(f32x4, l_p2 + REG + 128)};
    asm volatile("" : "+v"(ts[0]), "+v"(ts[1]));
    PIN4(pg, 0); PIN4(pu, 0);
    if (arrived(k) == 3) {
      const f32x4 t = ts[0] + ts[1], g4 = (pg[0] + pg[1]) + (pg[2] + pg[3]), u4 = (pu[0] + pu[1]) + (pu[2] + pu[3]);
      const float s1 = t[0] + t[2], d = s1 * (-1.0f / DM);
      const float rstd = __builtin_amdgcn_rsqf(fmaxf((t[1] + t[3]) + d * s1, 0.f) * (1.0f / DM) + LN_EPS);
      const float g0 = rstd * (g4[0] + d * g4[2]), g1 = rstd * (g4[1] + d * g4[3]);
      const f32x2 act = {g0 * __builtin_amdgcn_rcpf(1.0f + __expf(-g0)) * (rstd * (u4[0] + d * u4[2])),
                         g1 * __builtin_amdgcn_rcpf(1.0f + __expf(-g1)) * (rstd * (u4[1] + d * u4[3]))};
      const float aw = __uint_as_float(max(__builtin_bit_cast(unsigned, __builtin_convertvector(act, bf16x2)), 1u));
      const u32x4 av = {__float_as_uint(aw), __float_as_uint(dpp<0x55>(aw)), __float_as_uint(dpp<0xAA>(aw)), __float_as_uint(dpp<0xFF>(aw))};
      __builtin_amdgcn_raw_buffer_store_b128(av, rs, so, 0, AUX_SC1);
    }
  } else if (isD) {
    const uint2 xd = *reinterpret_cast<const uint2*>(x + row_of(tw) * DM + d_ct * 32 + j * 4);
    const float cq = bf_lo(*reinterpret_cast<const unsigned*>(x + row_of(qd) * DM));
    const __amdgpu_buffer_rsrc_t rd = __builtin_amdgcn_make_buffer_rsrc(const_cast<bf16*>(wd), 0, FF * DM * 2, 0x00020000);
    prep(false);
    lds_wait<8>(s_c + F_REL, 1u);
    done_all();
    pull();
    done(NS);
    const unsigned te = clk();
    u32x4 bd[24];
    {
      const int off = ((tw * 384 + (mr >> 3) * 32 + qd * 8 + (mr & 4)) * DM + d_ct * 32 + (mr & 3) * 8) * 2;
      u32x4 e[24];
#pragma unroll
      for (int i = 0; i < 24; ++i) e[i] = __builtin_amdgcn_raw_buffer_load_b128(rd, off + (i & 3) * (DM * 2), (i >> 2) * 64 * (DM * 2), 0);
      MEMFENCE();
#pragma unroll
      for (int s = 0; s < 6; ++s) {
        unsigned w[8][2];
#pragma unroll
        for (int i = 0; i < 4; ++i)
#pragma unroll
          for (int d = 0; d < 2; ++d) {
            w[i][d] = __builtin_amdgcn_update_dpp(e[s * 4 + i][d], e[s * 4 + i][2 + d], 0x114, 0xf, 0xa, false);
            w[4 + i][d] = __builtin_amdgcn_update_dpp(e[s * 4 + i][2 + d], e[s * 4 + i][d], 0x104, 0xf, 0x5, false);
          }
#pragma unroll
        for (int k = 0; k < 4; ++k) {
          bd[s * 4 + 0][k] = PERM_LO(w[2 * k + 1][0], w[2 * k][0]); bd[s * 4 + 1][k] = PERM_HI(w[2 * k + 1][0], w[2 * k][0]);
          bd[s * 4 + 2][k] = PERM_LO(w[2 * k + 1][1], w[2 * k][1]); bd[s * 4 + 3][k] = PERM_HI(w[2 * k + 1][1], w[2 * k][1]);
        }
        PIN4(bd, s * 4);
      }
    }
    until(te, DLATE);
    {
      u32x4 sl;
#pragma unroll 1
      for (unsigned spin = 0; spin < SPIN_CAP; ++spin) {
        sl = ld128(rs, WS_SLAB + (r * NB + tw) * DM + d_ct * 32 + j * 4);
        if (__all(min4(sl) != 0u)) break;
        __builtin_amdgcn_s_sleep(2);
      }
      f32x4 hv = {bf_lo(xd.x), bf_hi(xd.x), bf_lo(xd.y), bf_hi(xd.y)};
#pragma unroll
      for (int i = 0; i < 4; ++i) hv[i] += sum_r(__uint_as_float(sl[i]));
      if (lane < 8) reinterpret_cast<f32x4*>(red + 512 + tw * 32)[lane] = hv;
      LGKM0();
      lds_add(s_c + C_W, lane);
      lds_wait<1>(s_c + C_W, 4u);
    }
    const int col = (mr & 3) * 8 + (mr & 4) + (mr >> 3) * 2;
    f32x2 hv = *reinterpret_cast<const f32x2*>(red + 512 + qd * 32 + col);
    typedef __attribute__((address_space(3))) f32x2 lds_f32x2;
    typedef __attribute__((address_space(1))) unsigned g_u32w;
    lds_f32x2* r2 = (lds_f32x2*)red + lane;
    lds_f32x2* r2w = r2 + tw * 64;
    g_u32w* po = (g_u32w*)(out + row_of(qd) * DM + d_ct * 32 + col);
    int aoff = (mr & 1) ? WS_END * 4 : (WS_ACT + tw * 12 * 64 + ((mr >> 1) & 1) * 64 + qd * 16 + (mr >> 2) * 4) * 4;
    asm volatile("" : "+v"(hv), "+v"(r2), "+v"(r2w), "+v"(po), "+v"(aoff));
    u32x4 hs, a[6];
#pragma unroll 1
    for (unsigned spin = 0; spin < SPIN_CAP; ++spin) {
      hs = ld128(rs, WS_H + (qd * 4 + (mr & 3)) * 192 + 160);
      if (__all(min4(hs) != 0u)) break;
      __builtin_amdgcn_s_sleep(1);
    }
#pragma unroll
    for (int s = 0; s < 6; ++s) a[s] = __builtin_amdgcn_raw_buffer_load_b128(rs, aoff, s * 512, AUX_SC1);
    MEMFENCE();
    float gate;
    {
      f32x4 t = __builtin_bit_cast(f32x4, hs);
#pragma unroll
      for (int i = 0; i < 4; ++i) { t[i] += dpp<DPP_XOR2>(t[i]); t[i] += dpp<DPP_XOR1>(t[i]); }
      const float d = t[0] * (-1.0f / DM);
      const float rstd = __builtin_amdgcn_rsqf(fmaxf(t[1] + d * t[0], 0.f) * (1.0f / DM) + LN_EPS);
      hv += ((hv - cq) + d) * rstd;
      gate = __builtin_amdgcn_rcpf(1.0f + __expf(-rstd * (t[2] + d * t[3])));
      asm volatile("" : "+v"(hv), "+v"(gate));
    }
#pragma unroll 1
    for (unsigned spin = 0; spin < SPIN_CAP; ++spin) {
      unsigned mn = min4(a[0]);
#pragma unroll
      for (int s = 1; s < 6; ++s) mn = min(mn, min4(a[s]));
      if ((__ballot(mn == 0u) & 0x5555555555555555ull) == 0ull) break;
#pragma unroll
      for (int s = 0; s < 6; ++s) a[s] = __builtin_amdgcn_raw_buffer_load_b128(rs, aoff, s * 512, AUX_SC1);
      MEMFENCE();
    }
    f32x4 c[4];
#pragma unroll
    for (int t = 0; t < 4; ++t) c[t] = f32x4{0.f, 0.f, 0.f, 0.f};
#pragma unroll
    for (int s = 0; s < 6; ++s)
#pragma unroll
      for (int t = 0; t < 4; ++t) c[t] = mfma16(a[s], bd[s * 4 + t], c[t]);
    const bool up = mr >= 8;
    const float k0 = up ? c[2][2] : c[0][0], k1 = up ? c[3][2] : c[1][0], o0 = up ? c[0][2] : c[2][0], o1 = up ? c[1][2] : c[3][0];
    *r2w = f32x2{k0 + dpp<DPP_ROR8>(o0), k1 + dpp<DPP_ROR8>(o1)};
    const int k = arrive(s_c + C_T);
    f32x2 y = hv + ((r2[0] + r2[64]) + (r2[128] + r2[192])) * gate;
    asm volatile("" : "+v"(y));
    if (arrived(k) == 3) {
      __hip_atomic_store(po, __builtin_bit_cast(unsigned, __builtin_convertvector(y, bf16x2)), __ATOMIC_RELAXED, __HIP_MEMORY_SCOPE_AGENT);
    }
  } else {
    prep(bid == LEADER);
    if (isC && tw == 1) {
      lds_wait<8>(s_c + F_REL, 1u);
      const int cs = c_it != 1 ? c_it * 2 : min(4 - (g >> 3), 3), cnt = c_it != 1 ? 8 : (g < 16 ? 16 : 32), x0 = c_it != 1 ? g * 8 : (g < 16 ? g * 16 : (g & 7) * 32);
      done(c_it < 2 ? 4 : NA);
      pause(lds_get(s_c + S_BURST + (c_it >> 1)), WDELAY);
      const float wk = weight(cs, x0, cnt);
      f32x4 o = {0.f, 0.f, 0.f, 0.f};
#pragma unroll 1
      for (int k0 = 0; k0 < cnt; k0 += 16) {
        u32x2 pv[16];
#pragma unroll
        for (int k = 0; k < 16; ++k) pv[k] = __builtin_amdgcn_raw_buffer_load_b64(rs, lp * 2, (WS_PQ + ((x0 + ((k0 + k) & (cnt - 1))) * 5 + cs) * (DH / 2)) * 4, AUX_EARLY);
#pragma unroll
        for (int k = 0; k < 16; ++k) {
          const float w = k0 + k < cnt ? __uint_as_float(__builtin_amdgcn_readlane(__float_as_uint(wk), k0 + k)) : 0.f;
          o += w * f32x4{(float)(short)pv[k][0], (float)((int)pv[k][0] >> 16), (float)(short)pv[k][1], (float)((int)pv[k][1] >> 16)};
        }
      }
      u32x2 hi, lw;
      split4(o, hi, lw);
      if (lane < DH / 4) st128(rs, WS_OC + (c_it * NG + g) * DH + lane * 4, u32x4{max(hi[0], 1u), max(hi[1], 1u), max(lw[0], 1u), max(lw[1], 1u)});
    }
    done_all();
    pull();
  }
  if (tw == 3) done_all();
}

extern "C" void kernel_launch(void* const* d_in, const int* in_sizes, int n_in, void* d_out, int out_size, void* d_ws,
                              size_t ws_size, hipStream_t stream) {
  const bf16* x = (const bf16*)d_in[0];
  const bf16* kc = (const bf16*)d_in[1];
  const bf16* vc = (const bf16*)d_in[2];
  const bf16* wq = (const bf16*)d_in[3];
  const bf16* wo = (const bf16*)d_in[4];
  const bf16* wr = (const bf16*)d_in[6];
  const bf16* wg = (const bf16*)d_in[7];
  const bf16* wu = (const bf16*)d_in[8];
  const bf16* wd = (const bf16*)d_in[9];
  fused_kernel<<<NWG, NW * 64, 0, stream>>>(x, kc, vc, wq, wo, wr, wg, wu, wd, (float*)d_ws, (bf16*)d_out);
}
```

```cpp
#include <hip/hip_runtime.h>
#include <hip/hip_bf16.h>
#include <stdint.h>

typedef __hip_bfloat16 bf16;
using u32x2 = __attribute__((ext_vector_type(2))) unsigned;
using u32x4 = __attribute__((ext_vector_type(4))) unsigned;
using f32x4 = __attribute__((ext_vector_type(4))) float;
using f32x2 = __attribute__((ext_vector_type(2))) float;
using bf16x8 = __attribute__((ext_vector_type(8))) __bf16;

constexpr int NB = 4, DM = 640, NH = 32, SK = 8192, DH = 192, HD = NH * DH, FF = 1536;
constexpr float LN_EPS = 1e-5f;
constexpr float QSCALE = 0.07216878364870322f * 1.4426950408889634f;

constexpr int NWG = 256;
constexpr int GS = 8, NG = NWG / GS, NI = GS / 2;
constexpr int HPG = NI / NB;
constexpr int PPI = 512 / GS;
constexpr int NPAIR = NI * PPI;

constexpr int TAUK = 1320, TAUV = 1332;
constexpr int TSTART = 110;
constexpr int ANCH = 90;
constexpr int OGAP = 105, OLATE = 250, OSTEP = 12;
constexpr int VGAP = 50;
constexpr int DEEP1 = 100, DEEP2 = 200, LAGOFF = 20;
constexpr int ENDT = 12, ENDS = 2;
constexpr int JUMP = 145;
constexpr int HST = 2800, RT0 = 3900, RTS = 1000, RA1 = 220, RA2 = 500, LEADER = 24;
constexpr unsigned NRD = 5, RMIN = 30;
constexpr int FBX = 20, FB0 = 80, FB1 = 10, FBD = 10, FBS = 10;
constexpr int HOLD = 4000, GB = 24, GBS = 2, GBX = 60, GBF = 12;
constexpr unsigned PRD = 3; constexpr int PRB = 24;
constexpr int ROT = 16, ROWS = 0x1B;
constexpr int TLEAD = 10, PFD = 6, WFD = 3;
constexpr int QWAIT = 750, QBACK = 100;
constexpr int WDELAY = 100, WDELAY2 = 460, HDELAY = 1150, HDELAY2 = 780, DLATE = 100;
constexpr int TPT = 600, TPS = 20;
constexpr int CODE = 24 * 64 * 46;
constexpr int PW = 30, DPW = 60;

constexpr int KS = 8;
constexpr int WS_PML = 0;
constexpr int WS_PO = WS_PML + NWG * 64;
constexpr int WS_PQ = WS_PO + NWG * DH;
constexpr int WS_OC = WS_PQ + NWG * 5 * (DH / 2);
constexpr int WS_SLAB = WS_OC + (NB - 1) * NH * DH;
constexpr int WS_ACT = WS_SLAB + KS * NB * DM;
constexpr int WS_H = WS_ACT + NB * FF;
constexpr int WS_HN = WS_H + NB * 4 * 192;
constexpr int WS_HR = WS_HN + NWG;
constexpr int WS_HA = WS_HR + 2 * NWG;
constexpr int QFS = 2 * GS * 32;
constexpr int WS_QF = WS_HA + 32 * NWG;
constexpr int WS_QP = WS_QF + NG * QFS;
constexpr int WS_END = WS_QP + NH * 2 * NB * DH;
constexpr int NPROD = HPG * 6;
static_assert(WS_PO % 32 == 0 && WS_SLAB % 32 == 0 && WS_ACT % 32 == 0 && WS_HN % 32 == 0 && WS_QF % 32 == 0 && WS_QP % 32 == 0, "line alignment");


__device__ __forceinline__ int row_of(int b) { return (ROWS >> (2 * b)) & 3; }
__device__ __forceinline__ float bf_lo(unsigned u) { return __uint_as_float(u << 16); }
__device__ __forceinline__ float bf_hi(unsigned u) { return __uint_as_float(u & 0xffff0000u); }
__device__ __forceinline__ f32x2 bf_pair(unsigned u) { return f32x2{bf_lo(u), bf_hi(u)}; }
__device__ __forceinline__ f32x2 fma2(float a, f32x2 b, f32x2 c) { return __builtin_elementwise_fma(f32x2{a, a}, b, c); }
__device__ __forceinline__ f32x2 fma2(f32x2 a, f32x2 b, f32x2 c) { return __builtin_elementwise_fma(a, b, c); }

template <int CTRL> __device__ __forceinline__ float dpp(float x) {
  return __builtin_bit_cast(float, __builtin_amdgcn_mov_dpp(__builtin_bit_cast(int, x), CTRL, 0xf, 0xf, true));
}
constexpr int DPP_XOR1 = 0xB1, DPP_XOR2 = 0x4E, DPP_HMIRROR = 0x141, DPP_MIRROR = 0x140, DPP_ROR8 = 0x128;

__device__ __forceinline__ float sum_x16_32(float x) {
  auto s = __builtin_amdgcn_permlane16_swap(__float_as_uint(x), __float_as_uint(x), false, false);
  x = __uint_as_float(s[0]) + __uint_as_float(s[1]);
  auto t = __builtin_amdgcn_permlane32_swap(__float_as_uint(x), __float_as_uint(x), false, false);
  return __uint_as_float(t[0]) + __uint_as_float(t[1]);
}
__device__ __forceinline__ float max_x16_32(float x) {
  auto s = __builtin_amdgcn_permlane16_swap(__float_as_uint(x), __float_as_uint(x), false, false);
  x = fmaxf(__uint_as_float(s[0]), __uint_as_float(s[1]));
  auto t = __builtin_amdgcn_permlane32_swap(__float_as_uint(x), __float_as_uint(x), false, false);
  return fmaxf(__uint_as_float(t[0]), __uint_as_float(t[1]));
}
__device__ __forceinline__ float tsum32(float a, float b) {
  auto t = __builtin_amdgcn_permlane32_swap(__float_as_uint(a), __float_as_uint(b), false, false);
  return __uint_as_float(t[0]) + __uint_as_float(t[1]);
}
__device__ __forceinline__ float tsum16(float a, float b) {
  auto t = __builtin_amdgcn_permlane16_swap(__float_as_uint(a), __float_as_uint(b), false, false);
  return __uint_as_float(t[0]) + __uint_as_float(t[1]);
}
__device__ __forceinline__ float sum_r(float v) { v += dpp<DPP_ROR8>(v); return sum_x16_32(v); }
__device__ __forceinline__ float row_sum(float v) {
  v += dpp<DPP_XOR1>(v); v += dpp<DPP_XOR2>(v); v += dpp<DPP_HMIRROR>(v);
  return v + dpp<DPP_MIRROR>(v);
}
__device__ __forceinline__ float row_max(float v) {
  v = fmaxf(v, dpp<DPP_XOR1>(v)); v = fmaxf(v, dpp<DPP_XOR2>(v)); v = fmaxf(v, dpp<DPP_HMIRROR>(v));
  return fmaxf(v, dpp<DPP_MIRROR>(v));
}
__device__ __forceinline__ float wave_sum(float v) {
  v += dpp<DPP_XOR1>(v); v += dpp<DPP_XOR2>(v); v += dpp<DPP_HMIRROR>(v);
  return sum_r(v);
}

__device__ __forceinline__ u32x4 ld_nt(const char* p) { return __builtin_nontemporal_load(reinterpret_cast<const u32x4*>(p)); }

struct Tile { u32x4 d[3]; };

#define PIN4(a, o) asm volatile("" : "+v"((a)[(o) + 0]), "+v"((a)[(o) + 1]), "+v"((a)[(o) + 2]), "+v"((a)[(o) + 3]))
#define MEMFENCE() do { asm volatile("" ::: "memory"); __builtin_amdgcn_sched_barrier(0); } while (0)

__device__ __forceinline__ void glds16_nt(const void* gsrc, unsigned lds_dst) {
  unsigned keep;
  asm volatile("s_mov_b32 %0, m0\n\ts_mov_b32 m0, %2\n\ts_nop 0\n\tglobal_load_lds_dwordx4 %1, off nt\n\ts_mov_b32 m0, %0"
               : "=&s"(keep) : "v"(gsrc), "s"(lds_dst) : "memory");
}

using bf16x2 = __attribute__((ext_vector_type(2))) __bf16;
__device__ __forceinline__ float dot2(unsigned a, unsigned b, float c) { return __builtin_amdgcn_fdot2_f32_bf16(__builtin_bit_cast(bf16x2, a), __builtin_bit_cast(bf16x2, b), c, false); }
__device__ __forceinline__ float tile_score(const Tile& k, const u32x2 (&q)[12]) {
  float s0 = 0.f, s1 = 0.f;
#pragma unroll
  for (int i = 0; i < 3; ++i) {
    u32x4 v = k.d[i];
    asm("" : "+v"(v));
#pragma unroll
    for (int d = 0; d < 4; ++d) { s0 = dot2(v[d], q[i * 4 + d][0], s0); s1 = dot2(v[d], q[i * 4 + d][1], s1); }
  }
  return s0 + s1;
}
__device__ __forceinline__ void pair_put(float a, float b, float* dst, int j) {
  float s = (j < 4 ? a : b) + dpp<DPP_HMIRROR>(j < 4 ? b : a);
  s += dpp<DPP_XOR1>(s);
  s += dpp<DPP_XOR2>(s);
  if ((j & 3) == 0) dst[j * 2] = s;
}
__device__ __forceinline__ void score_put(float s, float* dst, int j) {
  s += dpp<DPP_XOR1>(s);
  s += dpp<DPP_XOR2>(s);
  s += dpp<DPP_HMIRROR>(s);
  if (j == 0) *dst = s;
}
__device__ __forceinline__ void tile_load(Tile& t, const char* base, unsigned off, int half) {
#pragma unroll
  for (int i = 0; i < 3; ++i) t.d[i] = ld_nt(base + off + half * 3072 + i * 128);
}
__device__ __forceinline__ void pair_issue(Tile& ta, Tile& tb, const char* base, unsigned off, int deep) {
  deep = __builtin_amdgcn_readfirstlane(deep);
#define WAIT_IF(level, n) asm volatile("s_cmp_lg_u32 %0, " #level "\n\ts_cbranch_scc1 .Lpi%=\n\ts_waitcnt vmcnt(" #n ")\n.Lpi%=:" :: "s"(deep) : "scc", "memory")
  const char* p = base + off;
  WAIT_IF(0, 3);
  ta.d[0] = ld_nt(p); ta.d[1] = ld_nt(p + 128); ta.d[2] = ld_nt(p + 256); MEMFENCE();
  WAIT_IF(0, 3); WAIT_IF(1, 6);
  tb.d[0] = ld_nt(p + 3072); tb.d[1] = ld_nt(p + 3200); tb.d[2] = ld_nt(p + 3328); MEMFENCE();
#undef WAIT_IF
}
__device__ __forceinline__ void pv_last(float p, Tile& v, f32x2 (&acc)[12]) {
#pragma unroll
  for (int i = 0; i < 3; ++i) {
    asm volatile("" : "+v"(v.d[i]), "+v"(p));
#pragma unroll
    for (int d = 0; d < 4; ++d) acc[i * 4 + d] = fma2(p, bf_pair(v.d[i][d]), acc[i * 4 + d]);
    PIN4(acc, i * 4);
  }
}
__device__ __forceinline__ void q_load(u32x2 (&q)[12], const float* src, int j) {
#pragma unroll
  for (int i = 0; i < 6; ++i) {
    const u32x4 a = *reinterpret_cast<const u32x4*>(src + (i >> 1) * 64 + j * 8 + (i & 1) * 4);
    q[i * 2] = u32x2{a[0], a[1]}; q[i * 2 + 1] = u32x2{a[2], a[3]};
  }
}
#define VMCNT(n) asm volatile("s_waitcnt vmcnt(%0)" :: "n"(n) : "memory")

constexpr int AUX_SC1 = 16, AUX_EARLY = 18;
constexpr unsigned SPIN_CAP = 1u << 17;
__device__ __forceinline__ unsigned nz_bits(float v) { return max(__float_as_uint(v), 1u); }
__device__ __forceinline__ unsigned min4(u32x4 v) { return min(min(v[0], v[1]), min(v[2], v[3])); }
__device__ __forceinline__ unsigned ld32(__amdgpu_buffer_rsrc_t rs, int word) { return __builtin_amdgcn_raw_buffer_load_b32(rs, word * 4, 0, AUX_SC1); }
__device__ __forceinline__ u32x2 ld64(__amdgpu_buffer_rsrc_t rs, int word) { return __builtin_amdgcn_raw_buffer_load_b64(rs, word * 4, 0, AUX_SC1); }
__device__ __forceinline__ u32x4 ld128(__amdgpu_buffer_rsrc_t rs, int word) { return __builtin_amdgcn_raw_buffer_load_b128(rs, word * 4, 0, AUX_SC1); }
__device__ __forceinline__ void st32(__amdgpu_buffer_rsrc_t rs, int word, unsigned v) { __builtin_amdgcn_raw_buffer_store_b32(v, rs, word * 4, 0, AUX_SC1); }
__device__ __forceinline__ void st128(__amdgpu_buffer_rsrc_t rs, int word, u32x4 v) { __builtin_amdgcn_raw_buffer_store_b128(v, rs, word * 4, 0, AUX_SC1); }

__device__ __forceinline__ u32x4 ld128nt(__amdgpu_buffer_rsrc_t rs, int word) { return __builtin_amdgcn_raw_buffer_load_b128(rs, word * 4, 0, AUX_EARLY); }
__device__ __forceinline__ void st128nt(__amdgpu_buffer_rsrc_t rs, int word, u32x4 v) { __builtin_amdgcn_raw_buffer_store_b128(v, rs, word * 4, 0, AUX_EARLY); }

__device__ __forceinline__ bool leader_release(__amdgpu_buffer_rsrc_t rs, int lane, unsigned t0, unsigned rd, int slow, int& prev) {
  u32x4 a[4];
#pragma unroll
  for (int k = 0; k < 4; ++k) a[k] = ld128(rs, WS_HA + (lane * 4 + k) * 32);
  const u32x4 n = ld128(rs, WS_HN + lane * 4);
  if (rd == 0u && !__all(a[0][0] == n[0] && a[1][0] == n[1] && a[2][0] == n[2] && a[3][0] == n[3])) return false;
  const float e = fmaxf(fmaxf((float)(int)(t0 - a[0][2]), (float)(int)(t0 - a[1][2])), fmaxf((float)(int)(t0 - a[2][2]), (float)(int)(t0 - a[3][2])));
  unsigned first = t0 - (unsigned)(int)max_x16_32(row_max(e));
  float ls = 0.f, lx = 0.f;
  const int pend = max(prev - FBS * (RTS - RA2) * 16 / TAUK, 0);
  prev = max(pend - FBS * RA2 * 16 / TAUK, 0);
#pragma unroll
  for (int k = 0; k < 4; ++k) {
    const float l = a[k][0] == n[k] + rd ? (float)min(a[k][3] + (rd ? 0u : min(a[k][2] - first, 300u)), 2000u) : (float)-pend;
    ls += l; lx = fmaxf(lx, l);
  }
  ls = wave_sum(ls) * (1.0f / NWG); lx = max_x16_32(row_max(lx));
  const int extra = __builtin_amdgcn_readfirstlane(min(max((int)(ls + lx * (FBX / 100.0f)) - (rd ? FB1 + (int)(NRD - rd) * FBD : FB0), 0), 600));
  if (rd != 0u && __all(ls * NWG < HOLD)) return true;
  first = (first & 0xffffu) | ((unsigned)(slow + extra) << 16);
  prev += extra;
  st128(rs, WS_HR + lane * 8, u32x4{a[0][1] + rd, first, a[1][1] + rd, first});
  st128(rs, WS_HR + lane * 8 + 4, u32x4{a[2][1] + rd, first, a[3][1] + rd, first});
  return true;
}

typedef __attribute__((address_space(3))) unsigned lds_u32;
__device__ __forceinline__ unsigned lds_add(lds_u32* p, int lane) {
  unsigned k = 0;
  if (lane == 0) k = __hip_atomic_fetch_add(p, 1u, __ATOMIC_RELAXED, __HIP_MEMORY_SCOPE_WORKGROUP);
  return __builtin_amdgcn_readfirstlane(k);
}
#define CFENCE() asm volatile("" ::: "memory")
__device__ __forceinline__ int arrive(lds_u32* p) {
  int v;
  unsigned long long keep;
  asm volatile("s_mov_b64 %1, exec\n\ts_mov_b64 exec, 1\n\tds_add_rtn_u32 %0, %2, %3\n\ts_mov_b64 exec, %1"
               : "=&v"(v), "=&s"(keep) : "v"((unsigned)(size_t)p), "v"(1u) : "memory");
  return v;
}
__device__ __forceinline__ void bump(lds_u32* p) {
  unsigned long long keep;
  asm volatile("s_mov_b64 %0, exec\n\ts_mov_b64 exec, 1\n\tds_add_u32 %1, %2\n\ts_mov_b64 exec, %0" : "=&s"(keep) : "v"((unsigned)(size_t)p), "v"(1u) : "memory");
}
__device__ __forceinline__ int arrived(int v) {
  asm volatile("s_waitcnt lgkmcnt(0)" : "+v"(v) :: "memory");
  return __builtin_amdgcn_readfirstlane(v);
}
__device__ __forceinline__ unsigned lds_get(lds_u32* p) {
  return __builtin_amdgcn_readfirstlane(__hip_atomic_load(p, __ATOMIC_RELAXED, __HIP_MEMORY_SCOPE_WORKGROUP));
}
template <int SLEEP> __device__ __forceinline__ void lds_wait(lds_u32* p, unsigned n) {
#pragma unroll 1
  for (unsigned spin = 0; spin < (SPIN_CAP << 4); ++spin) {
    if (lds_get(p) >= n) break;
    __builtin_amdgcn_s_sleep(SLEEP);
  }
  asm volatile("" ::: "memory");
}
__device__ __forceinline__ unsigned clk() { return (unsigned)__builtin_amdgcn_s_memrealtime(); }
struct Table {
  unsigned t0; int was, moved, slowed, folded;
  u32x2 told;
  unsigned mine, tmp;
  template <int K> __device__ __forceinline__ void put(int v) { asm volatile("v_mov_b32 %0, %2\n\tds_write_b32 %1, %0 offset:%3" : "+v"(tmp), "+v"(mine) : "s"(v), "n"(K * 4) : "memory"); }
  __device__ __forceinline__ int gate(int due, int sh = 0) {
    const int fold = __builtin_amdgcn_readfirstlane(told[1]);
    if (fold != folded) { const int f = min(fold - folded, moved); moved -= f; slowed += f; folded = fold; put<0>(moved); }
    const int left = (int)__builtin_amdgcn_readfirstlane(told[0]) - slowed, d = max(min(left, FBS), left < -GBX ? -GBF : -GBS);
    t0 += d; was -= d; slowed += d;
    int late = (int)(clk() - t0) - due;
    if (late - was > JUMP) { t0 += late - was; moved += late - was; late = was; put<0>(moved); }
    const int lag = max(late - max(left - d, 0) - LAGOFF, 0);
    put<4>(lag);
    was = late;
#pragma unroll 1
    while (late < 0) { __builtin_amdgcn_s_sleep(1); late = (int)(clk() - t0) - due; }
    return lag < (DEEP1 >> sh) ? 0 : (lag < (DEEP2 >> sh) ? 1 : 2);
  }
  __device__ __forceinline__ void listen(lds_u32* slow) {
    told = __builtin_bit_cast(u32x2, __hip_atomic_load((__attribute__((address_space(3))) unsigned long long*)slow, __ATOMIC_RELAXED, __HIP_MEMORY_SCOPE_WORKGROUP));
  }
  __device__ __forceinline__ void agree(lds_u32* all) {
    if (moved == 0) return;
    const int m = (int)min(min(lds_get(all), lds_get(all + 1)), min(lds_get(all + 2), lds_get(all + 3))) + 30;
    if (m < moved) { t0 -= moved - m; moved = m; was = 1 << 20; put<0>(moved); }
  }
};
__device__ __forceinline__ void until(unsigned t0, int due) {
#pragma unroll 1
  while ((int)(clk() - t0) < due) __builtin_amdgcn_s_sleep(1);
}
__device__ __forceinline__ void pause(unsigned t0, unsigned due) {
#pragma unroll 1
  while (clk() - t0 < due) __builtin_amdgcn_s_sleep(16);
}
#define LGKM0() asm volatile("s_waitcnt lgkmcnt(0)" ::: "memory")

__device__ __forceinline__ void split4(f32x4 v, u32x2& hi, u32x2& lo) {
  unsigned h[4], r[4];
#pragma unroll
  for (int i = 0; i < 4; ++i) { h[i] = __float_as_uint(v[i]) & 0xffff0000u; r[i] = __float_as_uint(v[i] - __uint_as_float(h[i])); }
  hi = u32x2{__builtin_amdgcn_perm(h[1], h[0], 0x07060302u), __builtin_amdgcn_perm(h[3], h[2], 0x07060302u)};
  lo = u32x2{__builtin_amdgcn_perm(r[1], r[0], 0x07060302u), __builtin_amdgcn_perm(r[3], r[2], 0x07060302u)};
}
__device__ __forceinline__ u32x4 split8(f32x4 v, bool odd) {
  u32x2 hi, lo;
  split4(v, hi, lo);
  const unsigned n0 = __float_as_uint(dpp<DPP_XOR1>(__uint_as_float(odd ? hi[0] : lo[0]))), n1 = __float_as_uint(dpp<DPP_XOR1>(__uint_as_float(odd ? hi[1] : lo[1])));
  return u32x4{max(odd ? n0 : hi[0], 1u), max(odd ? n1 : hi[1], 1u), max(odd ? lo[0] : n0, 1u), max(odd ? lo[1] : n1, 1u)};
}
__device__ __forceinline__ u32x4 q_pack(u32x4 a, u32x4 b) {
  u32x2 hi, lo;
  split4((__builtin_bit_cast(f32x4, a) + __builtin_bit_cast(f32x4, b)) * QSCALE, hi, lo);
  return u32x4{hi[0], lo[0], hi[1], lo[1]};
}
__device__ __forceinline__ f32x4 mfma16(u32x4 a, u32x4 b, f32x4 c) {
  return __builtin_amdgcn_mfma_f32_16x16x32_bf16(__builtin_bit_cast(bf16x8, a), __builtin_bit_cast(bf16x8, b), c, 0, 0, 0);
}

constexpr int NW = 8, NMW = 4;
constexpr int NLP = 5, NRP = 8;
constexpr int NTP = 4 * (NLP + NRP);
constexpr int REG = NLP * 6 * 1024;
constexpr int NRING = 4;
constexpr int NS = 6, NA = NS - 1;
__device__ __forceinline__ int seg_beg(int s) { return s >= NS ? NPAIR : ((0xC86540 >> (4 * s)) & 15) * 16; }
__device__ __forceinline__ int seg_k(int t) { return (int)(0x5433222211110000ull >> (4 * (t >> 4))) & 15; }
__device__ __forceinline__ int seg_of_k(int k) { return (0x123540 >> (4 * k)) & 15; }
__device__ __forceinline__ int k_of_seg(int s) { return (0x213450 >> (4 * s)) & 15; }
__device__ __forceinline__ int pair_of(int t) { return t < 64 ? t : (t < 192 ? t + 64 : t - (t < 224 ? 96 : (t < 240 ? 144 : 176))); }
constexpr int OFF_CNT = 0, OFF_REG = 256,
              OFF_SC = OFF_REG + 4 * REG, OFF_BUF = OFF_SC + NPAIR * 16 * 4, OFF_Q = OFF_BUF + NRING * NMW * DH * 4, OFF_ML = OFF_Q + NS * DH * 4,
              OFF_RED = OFF_ML + 128, OFF_END = OFF_RED + 4 * 256 * 4;
enum { F_REL = 0, F_DONE, C_LAST, C_T = 3, F_OUT, C_P, C_K, F_P, F_Q = 8, F_FREE, S_FIRST, F_Q2, C_W = 12, C_SC = 16, C_PART = 24, S_ECHO = 32, S_BURST = 40, S_LATE = 42, S_MOVED = 48, S_SLOW = 56 };
__device__ __forceinline__ int due_k(int p) { return TSTART + ((p * TAUK) >> 6); }
__device__ __forceinline__ int due_v(int p) { return TSTART + OGAP + ((NPAIR * TAUK + p * TAUV + max(p - NPAIR / 2, 0) * (VGAP * 64 / (NPAIR / 2))) >> 6); }
constexpr int AW_LD = DH * 2 + 16, AG_LD = 512 + 16;

__global__ void __launch_bounds__(NW * 64) fused_kernel(const bf16* __restrict__ x, const bf16* __restrict__ kc,
                                                        const bf16* __restrict__ vc, const bf16* __restrict__ wq,
                                                        const bf16* __restrict__ wo, const bf16* __restrict__ wr,
                                                        const bf16* __restrict__ wg, const bf16* __restrict__ wu,
                                                        const bf16* __restrict__ wd, float* ws, bf16* __restrict__ out) {
  __shared__ __attribute__((aligned(16))) unsigned char smem[OFF_END];
  float* s_sc = reinterpret_cast<float*>(smem + OFF_SC);
  float* s_buf = reinterpret_cast<float*>(smem + OFF_BUF);
  float* s_q = reinterpret_cast<float*>(smem + OFF_Q);
  u32x4* s_ml = reinterpret_cast<u32x4*>(smem + OFF_ML);
  lds_u32* s_c = (lds_u32*)(smem + OFF_CNT);

  const int tid = threadIdx.x, lane = tid & 63, r = lane >> 3, j = lane & 7;
  const int wave = __builtin_amdgcn_readfirstlane(tid >> 6);
  const int tw = wave - NMW;
  const int bid = blockIdx.x, xl = bid & 7, idx = bid >> 3;
  const int g = (xl * 32 + idx) / GS, mm = idx % GS;
  const __amdgpu_buffer_rsrc_t rs = __builtin_amdgcn_make_buffer_rsrc(ws, 0, WS_END * 4, 0x00020000);
  const int ro = ((idx & 3) << 3) | (idx >> 2);
  const bool isW = ro < 10, isG = ro >= 10 && ro < 16;
  const int w_ks = xl, w_ct = ro;
  const int g_cg = xl * 6 + (ro - 10);
  const int d_ct = (xl + ((ro - 16) >> 2) * 8) * 2 + (((ro - 16) >> 1) & 1);
  const bool isD = ro >= 16 && ro < 24 && (ro & 1) == 0 && d_ct < DM / 32;
  const bool isH = ro == 22 && xl >= 2 && xl < 6;
  const bool isC = (mm & 7) == 3 || (mm & 7) >= 6;
  const int c_it = (mm >> 3) * 3 + max((mm & 7) - 5, 0);
  const int qf = WS_QF + g * QFS;
  const char* kb = reinterpret_cast<const char*>(kc);
  const char* vb = reinterpret_cast<const char*>(vc);
  const unsigned lo = (r * 24 + j) * 16;
  const int xb = g * GS + mm, c0 = mm * 8 + g * ROT;
  const int b2 = (2 * NH + (xb >> 4)) * 1024 + (xb & 15) * 8, b3 = (2 * NH + 16 + (xb >> 5)) * 1024 + (xb & 31) * 8;
  auto poff = [&](int p) -> unsigned {
    const int turn = (p & 63) >> 2;
    int t = (row_of(p >> 6) * NH + g) * 1024 + ((turn * 64 + (p & 3) * 2 + c0) & 1023);
    if ((p >> 6) == 1) t = (turn >= 8 ? b2 + (turn & 7) * 128 : b3 + (~turn & 4) * 2048 + (turn & 3) * 256) + (p & 3) * 2;
    return (unsigned)t * 3072u;
  };
  const unsigned lds_base = (unsigned)(size_t)(__attribute__((address_space(3))) unsigned char*)smem + OFF_REG + tw * REG;
  auto merged = [&](int it, int ln) -> f32x4 {
    const float* src = s_buf + (it % NRING) * (NMW * DH) + ((ln >> 1) & 7) * 24 + (ln >> 4) * 8 + (ln & 1) * 4;
    return (*reinterpret_cast<const f32x4*>(src) + *reinterpret_cast<const f32x4*>(src + DH)) +
           (*reinterpret_cast<const f32x4*>(src + 2 * DH) + *reinterpret_cast<const f32x4*>(src + 3 * DH));
  };
#define NONCE() do { unsigned n_ = __builtin_amdgcn_readfirstlane(f) + 1u; while (__any(f == n_)) ++n_; \
    if (lane < 32) st32(rs, qf + (GS + mm) * 32 + lane, n_); f = n_; } while (0)
  unsigned long long pc;
  asm volatile("s_getpc_b64 %0" : "=s"(pc));
  unsigned r0 = 0u;
  unsigned t0 = clk();
  unsigned tb0 = t0;
  if (wave < NMW) {
    Tile ta, tb, tc, td;
    const int c = lane & (GS - 1);
    unsigned f = 0u;
    if (wave == 3) f = ld32(rs, qf + c * 32 + mm);
    if (mm < NPROD) {
      const int hp = g + (mm / 6) * NG, ck = (mm % 6) >> 1, hf = mm & 1, jp = hf * 4 + wave;
      const int bb = lane >> 4, t = lane & 15;
      const u32x4* xp = reinterpret_cast<const u32x4*>(x + row_of(bb) * DM);
      u32x4 xv[5], xs = {0u, 0u, 0u, 0u};
#pragma unroll
      for (int k = 0; k < 5; ++k) xv[k] = xp[t + 16 * k];
      if (t < 10) xs = xp[jp * 10 + t];
      const u32x4* wp = reinterpret_cast<const u32x4*>(wq + (size_t)(jp * 80 + r) * HD + hp * DH + ck * 64) + j;
      u32x4 wv[10];
#pragma unroll
      for (int k = 0; k < 10; ++k) wv[k] = __builtin_nontemporal_load(wp + (size_t)k * 8 * (HD / 8));
      if (wave == 0) s_c[lane] = lane == C_K ? NTP + NMW : 0u;
      MEMFENCE(); LGKM0(); __builtin_amdgcn_s_barrier(); MEMFENCE();
      if (wave == 3) { NONCE(); }
      float sm = 0.f;
#pragma unroll
      for (int k = 0; k < 5; ++k)
#pragma unroll
        for (int d = 0; d < 4; ++d) sm += bf_lo(xv[k][d]) + bf_hi(xv[k][d]);
      const float mean = row_sum(sm) * (1.0f / DM);
      float sv = 0.f;
#pragma unroll
      for (int k = 0; k < 5; ++k)
#pragma unroll
        for (int d = 0; d < 4; ++d) { const float a = bf_lo(xv[k][d]) - mean, c2 = bf_hi(xv[k][d]) - mean; sv += a * a + c2 * c2; }
      const float rstd = 1.0f / sqrtf(row_sum(sv) * (1.0f / DM) + LN_EPS);
      float* s_xn = s_buf + wave * 320;
      if (t < 10) {
#pragma unroll
        for (int d = 0; d < 4; ++d) {
          s_xn[(t * 8 + 2 * d) * 4 + bb] = (bf_lo(xs[d]) - mean) * rstd;
          s_xn[(t * 8 + 2 * d + 1) * 4 + bb] = (bf_hi(xs[d]) - mean) * rstd;
        }
      }
      LGKM0();
      f32x2 qa[16];
#pragma unroll
      for (int i = 0; i < 16; ++i) qa[i] = f32x2{0.f, 0.f};
#pragma unroll
      for (int k = 0; k < 10; ++k) {
        const f32x4 xn = *reinterpret_cast<const f32x4*>(&s_xn[(k * 8 + r) * 4]);
#pragma unroll
        for (int d = 0; d < 4; ++d) {
          const f32x2 w2 = bf_pair(wv[k][d]);
#pragma unroll
          for (int e = 0; e < 4; ++e) qa[e * 4 + d] = fma2(xn[e], w2, qa[e * 4 + d]);
        }
      }
      float t16[16], t8[8];
#pragma unroll
      for (int i = 0; i < 16; ++i) t16[i] = tsum32(qa[i >> 1][i & 1], qa[8 + (i >> 1)][i & 1]);
#pragma unroll
      for (int i = 0; i < 8; ++i) t8[i] = tsum16(t16[i], t16[i + 8]);
#pragma unroll
      for (int i = 0; i < 8; ++i) t8[i] += dpp<DPP_ROR8>(t8[i]);
      const bool up = (lane & 8) != 0;
      f32x4* r4 = reinterpret_cast<f32x4*>(smem + OFF_RED);
      r4[wave * 64 + lane] = f32x4{up ? t8[4] : t8[0], up ? t8[5] : t8[1], up ? t8[6] : t8[2], up ? t8[7] : t8[3]};
      LGKM0();
      if (lds_add(s_c + C_P, lane) == NMW - 1) {
        asm volatile("" ::: "memory");
        const f32x4 o4 = (r4[lane] + r4[64 + lane]) + (r4[128 + lane] + r4[192 + lane]);
        st128nt(rs, WS_QP + ((hp * 2 + hf) * NB + bb) * DH + ck * 64 + j * 8 + (up ? 4 : 0), __builtin_bit_cast(u32x4, o4));
        VMCNT(0);
        if (lane == 0) s_c[F_OUT] = 1u;
      }
    } else {
      if (wave == 0) s_c[lane] = lane == C_K ? NTP + NMW : 0u;
      MEMFENCE(); LGKM0(); __builtin_amdgcn_s_barrier(); MEMFENCE();
      if (wave == 3) { NONCE(); }
      else {
        typedef __attribute__((address_space(1))) const unsigned g_u32;
        typedef __attribute__((address_space(1))) const char g_char;
        const int id = ((idx >> 3) * 2 + (mm & 1)) * 3 + wave;
        if (lane < CODE / (24 * 64)) {
          const unsigned v = *(g_u32*)((pc & ~255ull) + id * (CODE / 24) + lane * 64);
          asm volatile("" :: "v"(v));
        }
        if (lane < 7 && id < 21) {
          const int st = id < 8 ? (0x1320 >> ((id >> 1) * 4)) & 3 : (id >> 1) & 3, hf = id & 1;
          int head = row_of(st) * NH + xl * 4 + hf * 2;
          unsigned off = min(lane << 20, (6 << 20) - 64);
          if (st == 1) {
            head = 2 * NH + (hf ? 16 + xl : 2 * xl);
            if (hf) off = lane < 4 ? min(lane << 20, (3 << 20) - 64) : 8 * (3 << 20) + min((lane - 4) * (3 << 19), (3 << 20) - 64);
          }
          g_char* p = (g_char*)(id < 8 ? kb : vb) + (size_t)head * (3 << 20) + off;
          if (id >= 16) p = (g_char*)(id == 16 ? wo + xl * 4 * DH * DM : (id == 17 ? wg : (id == 18 ? wu : wd))) + min(lane * (id == 16 ? 163840 : 327680), (id == 16 ? 983040 : 1966080) - 64);
          if (id >= 20) p = (g_char*)(lane ? wr : out);
          until(t0, TPT + id * TPS);
          const unsigned v = __builtin_nontemporal_load((g_u32*)p);
          asm volatile("" :: "v"(v));
        }
      }
    }
    unsigned tk_e = poff(pair_of(lane * 4)) | (pair_of(lane * 4) >> 2) | (seg_k(lane * 4) << 6), tv_o = poff(lane * 4 + wave);
    int tv_d = due_v(lane * 4 + wave);
    asm volatile("" : "+v"(tk_e), "+v"(tv_o), "+v"(tv_d));
    MEMFENCE();
    if (wave == 3) {
      __builtin_amdgcn_s_setprio(3);
      const unsigned nonce = f;
      unsigned echoed = 0u;
      if (mm < NPROD) {
        echoed = ld32(rs, qf + (GS + c) * 32);
        asm volatile("" : "+v"(echoed) :: "memory");
        lds_wait<1>(s_c + F_OUT, 1u);
        if (lane < 32) st32(rs, qf + mm * 32 + lane, echoed);
#pragma unroll 1
        while ((int)(clk() - t0) < QWAIT - QBACK) {
          const unsigned nv = ld32(rs, qf + (GS + c) * 32);
          if (__any(nv != echoed)) {
            if (lane < 32) st32(rs, qf + mm * 32 + lane, nv);
            echoed = nv;
          }
          __builtin_amdgcn_s_sleep(4);
        }
      }
      until(t0, QWAIT);
#pragma unroll 1
      for (unsigned spin = 0; spin < SPIN_CAP; ++spin) {
        const unsigned nv = ld32(rs, qf + (GS + c) * 32);
        f = ld32(rs, qf + c * 32 + mm);
        const bool done = __all(c == mm || c >= NPROD || f == nonce);
        if (mm < NPROD && __any(nv != echoed)) {
          if (lane < 32) st32(rs, qf + mm * 32 + lane, nv);
          echoed = nv;
        }
        if (done) break;
        __builtin_amdgcn_s_sleep(8);
      }
      if (lane < GS) s_c[S_ECHO + lane] = echoed;
      if (lane < DH / 4) {
        u32x4 p[3][2];
#pragma unroll
        for (int it = 0; it < 3; ++it)
#pragma unroll
          for (int k = 0; k < 2; ++k) p[it][k] = ld128nt(rs, WS_QP + ((g * 2 + k) * NB + (it ? it + 1 : 0)) * DH + lane * 4);
#pragma unroll
        for (int it = 0; it < 3; ++it) {
          *reinterpret_cast<u32x4*>(&s_q[(it ? it + 3 : 0) * DH + lane * 4]) = q_pack(p[it][0], p[it][1]);
          if (it != 1) { LGKM0(); if (lane == 0) s_c[F_Q] = it + 1; MEMFENCE(); }
        }
      }
    }
    __builtin_amdgcn_s_setprio(3);
    until(t0, due_k(NTP + wave));
    tb0 = lds_get(s_c + S_LATE);
    if ((unsigned)(tb0 - t0 + 100u) > 500u) tb0 = t0;
    until(tb0, due_k(NTP + wave));
    Table tt = {tb0, 1 << 20, 0, 0, 0, {0u, 0u}, (unsigned)(size_t)(s_c + S_MOVED + wave), 0u};
    bool common = false;
    tile_load(ta, kb, poff(NTP + wave) + lo, 0);
    tile_load(tb, kb, poff(NTP + wave) + lo, 1);
    MEMFENCE();
    {
      u32x2 q[12];
      int cp = NTP + wave, it = 0, nxt = lds_add(s_c + C_K, lane), passed = 0, ci = -1;
      auto kuse = [&](Tile& xa, Tile& xb_) __attribute__((always_inline)) {
        if (it != ci) {
          LGKM0();
          for (; passed < it; ++passed) bump(s_c + C_SC + passed);
          lds_wait<1>(s_c + (it < 3 ? F_Q : F_Q2), min(it + 1, 3));
          q_load(q, s_q + seg_of_k(it) * DH, j);
          if (!common) { const unsigned d = lds_get(s_c + S_FIRST); if (d) { tt.t0 -= d - 1u; common = true; tt.was = 1 << 20; } }
          tt.agree(s_c + S_MOVED);
          ci = it;
        }
        const int tk = arrive(s_c + C_K);
        tt.listen(s_c + S_SLOW + 2 * wave);
        pair_put(tile_score(xa, q), tile_score(xb_, q), s_sc + cp * 16 + r, j);
        MEMFENCE();
        return arrived(tk);
      };
      auto kstep = [&](Tile& xa, Tile& xb_, Tile& ya, Tile& yb) __attribute__((always_inline)) {
        const unsigned e = __builtin_amdgcn_readlane(tk_e, nxt >> 2);
        MEMFENCE();
        const int deep = tt.gate(due_k(nxt));
        MEMFENCE();
        pair_issue(ya, yb, kb, (e & ~8191u) + (nxt & 3) * 6144 + lo, deep);
        const int n2 = kuse(xa, xb_);
        cp = (e & 63) * 4 + (nxt & 3); it = (e >> 6) & 7; nxt = n2;
      };
      bool swap;
      for (;;) {
        if (nxt >= NPAIR) { swap = false; break; }
        kstep(ta, tb, tc, td);
        if (nxt >= NPAIR) { swap = true; break; }
        kstep(tc, td, ta, tb);
      }
      tt.gate(due_k(NPAIR + wave));
      MEMFENCE();
      const unsigned* pw = reinterpret_cast<const unsigned*>(wo + (xl * 128 + idx * 4 + wave) * 3840) + lane * 16;
      const unsigned v0 = pw[0], v1 = pw[56 * 16];
      MEMFENCE();
      if (swap) { ta = tc; tb = td; }
      kuse(ta, tb);
      for (; passed < NS; ++passed) bump(s_c + C_SC + passed);
      tt.gate(due_v(wave));
      MEMFENCE();
      pair_issue(ta, tb, vb, __builtin_amdgcn_readlane(tv_o, 0) + lo, 2);
      asm volatile("" :: "v"(v0), "v"(v1));
    }
    const unsigned sc_r = (unsigned)(size_t)(__attribute__((address_space(3))) float*)s_sc + r * 4;
    f32x2 acc[12];
    auto vstep = [&](Tile& xa, Tile& xb_, Tile& ya, Tile& yb, int p, bool fin) __attribute__((always_inline)) {
      const unsigned off = __builtin_amdgcn_readlane(tv_o, (p >> 2) + 1) + lo;
      MEMFENCE();
      const int deep = tt.gate(__builtin_amdgcn_readlane(tv_d, (p >> 2) + 1), p >= NPAIR - ENDT * NMW ? ENDS : 0);
      float pa, pb;
      asm volatile("ds_read_b32 %0, %2\n\tds_read_b32 %1, %2 offset:32" : "=&v"(pa), "=&v"(pb) : "v"(sc_r + p * 64) : "memory");
      MEMFENCE();
      pair_issue(ya, yb, vb, off, deep);
      asm volatile("s_waitcnt lgkmcnt(0)" : "+v"(pa), "+v"(pb) :: "memory");
      if (!fin) tt.listen(s_c + S_SLOW + 2 * wave);
      else bump(s_c + C_LAST);
      pv_last(pa, xa, acc);
      pv_last(pb, xb_, acc);
      MEMFENCE();
    };
    typedef __attribute__((address_space(3))) float lds_f32;
    auto fold8 = [&](lds_f32* dst) __attribute__((always_inline)) {
      float t12[12], t6[6];
#pragma unroll
      for (int i = 0; i < 12; ++i) t12[i] = tsum32(acc[i >> 1][i & 1], acc[6 + (i >> 1)][i & 1]);
#pragma unroll
      for (int i = 0; i < 6; ++i) t6[i] = tsum16(t12[i], t12[i + 6]);
#pragma unroll
      for (int i = 0; i < 6; ++i) t6[i] += dpp<DPP_ROR8>(t6[i]);
      auto d2 = (__attribute__((address_space(3))) f32x2*)dst;
      d2[0] = f32x2{t6[0], t6[1]}; d2[1] = f32x2{t6[2], t6[3]}; d2[2] = f32x2{t6[4], t6[5]};
      CFENCE();
    };
    lds_f32* dst = (lds_f32*)s_buf + wave * DH + j * 24 + (lane >> 4) * 6;
#pragma unroll 1
    for (int it = 0;; ++it) {
      lds_wait<1>(s_c + F_P, k_of_seg(it) + 1);
      if (!common) { const unsigned d = lds_get(s_c + S_FIRST); if (d) { tt.t0 -= d - 1u; common = true; tt.was = 1 << 20; } }
      tt.agree(s_c + S_MOVED);
#pragma unroll
      for (int i = 0; i < 12; ++i) acc[i] = f32x2{0.f, 0.f};
#pragma unroll 1
      for (int p = seg_beg(it) + wave; p < min(seg_beg(it + 1), NPAIR - 2 * NMW); p += 2 * NMW) {
        vstep(ta, tb, tc, td, p, false);
        vstep(tc, td, ta, tb, p + NMW, false);
      }
      if (it >= NRING) lds_wait<1>(s_c + F_FREE, it - NRING + 1);
      if (it == NS - 1) break;
      fold8(dst + (it % NRING) * (NMW * DH));
      bump(s_c + C_PART + it);
    }
    dst += ((NS - 1) % NRING) * (NMW * DH);
    const int ln = lane < DH / 4 ? lane : 0;
    auto src = (__attribute__((address_space(3))) f32x4*)((lds_f32*)s_buf + ((NS - 1) % NRING) * (NMW * DH) + ((ln >> 1) & 7) * 24 + (ln >> 4) * 8 + (ln & 1) * 4);
    int so = (lane < DH / 4 ? WS_PO + xb * DH + lane * 4 : WS_END) * 4;
    const int p = NPAIR - NMW + wave;
    float pa = s_sc[p * 16 + r], pb = s_sc[p * 16 + 8 + r];
    asm volatile("" : "+v"(dst), "+v"(src), "+v"(so), "+v"(pa), "+v"(pb));
    vstep(ta, tb, tc, td, NPAIR - 2 * NMW + wave, true);
    pv_last(pa, tc, acc);
    pv_last(pb, td, acc);
    fold8(dst);
    const int k = arrive(s_c + C_PART + NS - 1);
    const f32x4 m = (src[0] + src[DH / 4]) + (src[2 * DH / 4] + src[3 * DH / 4]);
    u32x4 o = {nz_bits(m[0]), nz_bits(m[1]), nz_bits(m[2]), nz_bits(m[3])};
    asm volatile("" : "+v"(o));
    if (arrived(k) == NMW - 1) {
      __builtin_amdgcn_raw_buffer_store_b128(o, rs, so, 0, AUX_SC1);
      bump(s_c + F_DONE);
    }
    return;
  }

  {
    Tile pk[NRP * 2];
    __builtin_amdgcn_s_setprio(3);
    const unsigned it0 = (row_of(0) * NH + g) * 1024, ct = c0 + tw * 2;
    auto toff = [&](int turn) -> unsigned { return (it0 + ((turn * 64 + ct) & 1023)) * 3072u + lo; };
    unsigned tbw = 0u;
#pragma unroll 1
    for (int e = 0; e < NLP * 2; ++e) {
      const char* src = kb + (toff(e >> 1) + (e & 1) * 3072);
      if (e >= 2) until(tbw, (e * 2 * TAUK) >> 6);
#pragma unroll
      for (int i = 0; i < 3; ++i) glds16_nt(src + i * 128, lds_base + (e * 3 + i) * 1024);
      if (e == 1) {
        VMCNT(3);
        tb0 = t0 + min(max((int)(clk() - t0) - ANCH, 0), 300);
        tbw = tb0 + due_k(tw) - TLEAD;
        __builtin_amdgcn_s_barrier();
        if (tw == 0 && lane == 0) s_c[S_LATE] = tb0;
      }
      VMCNT(PFD);
    }
#pragma unroll
    for (int e = 0; e < NRP * 2; ++e) {
      until(tbw, ((NLP * 2 + e) * 2 * TAUK) >> 6);
      tile_load(pk[e], kb, toff(NLP + (e >> 1)), e & 1);
      MEMFENCE();
      if (e + 1 < NRP * 2) VMCNT(PFD);
    }
    if (tw == 0) {
      r0 = ld32(rs, WS_HR + bid * 2);
      if (bid == LEADER) {
        u32x4 n;
#pragma unroll
        for (int k = 0; k < 4; ++k) n[k] = ld32(rs, WS_HA + (lane * 4 + k) * 32) + 1u;
        st128(rs, WS_HN + lane * 4, n);
      }
      const u32x4 z = {0u, 0u, 0u, 0u};
      st128(rs, lane < 48 ? WS_PO + xb * DH + lane * 4 : WS_PML + xb * 64 + (lane - 48) * 4, z);
      const int o = isW ? WS_SLAB + w_ks * NB * DM + w_ct * 64 : (isG ? WS_ACT + g_cg * 128 : (isH ? WS_H + (xl - 2) * 768 : WS_OC + (c_it * NG + g) * DH));
      st128(rs, lane < (isG ? 32 : (isC ? 48 : (isW || isH ? 64 : 0))) ? o + (isW ? (lane >> 4) * DM + (lane & 15) * 4 : lane * 4) : WS_END, z);
      if (isH) { st128(rs, o + 256 + lane * 4, z); st128(rs, o + 512 + lane * 4, z); }
    }
    __builtin_amdgcn_s_setprio(0);
    lds_wait<1>(s_c + F_Q, HPG);
    u32x2 q[12];
    q_load(q, s_q, j);
    const u32x4* s_kv = reinterpret_cast<const u32x4*>(smem + OFF_REG + tw * REG) + lane;
    static_assert(NLP * 4 <= PPI && NTP <= HPG * PPI, "tail tiles: batch row 0 only");
#pragma unroll 1
    for (int e = 0; e < NLP * 2; ++e) {
      Tile tmp;
#pragma unroll
      for (int i = 0; i < 3; ++i) tmp.d[i] = s_kv[(e * 3 + i) * 64];
      score_put(tile_score(tmp, q), s_sc + ((e >> 1) * 4 + tw) * 16 + (e & 1) * 8 + r, j);
    }
#pragma unroll
    for (int e = 0; e < NRP * 2; ++e) {
      if ((NLP * 2 + e) * 2 == PPI) q_load(q, s_q + DH, j);
      score_put(tile_score(pk[e], q), s_sc + ((NLP + (e >> 1)) * 4 + tw) * 16 + (e & 1) * 8 + r, j);
      __builtin_amdgcn_sched_barrier(0);
    }
    asm volatile("s_waitcnt vmcnt(0) lgkmcnt(0)" ::: "memory");
#pragma unroll
    for (int it = 0; it * PPI < NTP; ++it) lds_add(s_c + C_SC + it, lane);
  }

  const int mr = lane & 15, qd = lane >> 4;
  unsigned char* reg = smem + OFF_REG + tw * REG;
  float* red = reinterpret_cast<float*>(smem + OFF_RED);
#define PERM_LO(hi_, lo_) __builtin_amdgcn_perm(hi_, lo_, 0x05040100u)
#define PERM_HI(hi_, lo_) __builtin_amdgcn_perm(hi_, lo_, 0x07060302u)
  int served = 0;
  auto done = [&](int n) {
    if (tw == 3 && served < min(n, NA)) {
      const int from = served;
      int ln = lane;
      asm volatile("" : "+v"(ln));
#pragma unroll 1
      for (; served < min(n, NA); ++served) {
        lds_wait<8>(s_c + C_PART + served, NMW);
        const f32x4 o = merged(served, ln < DH / 4 ? ln : 0);
        const float am = max_x16_32(row_max(fmaxf(fmaxf(fabsf(o[0]), fabsf(o[1])), fmaxf(fabsf(o[2]), fabsf(o[3])))));
        const float inv = am > 1e-30f ? 32767.0f / am : 0.f;
        const int q0 = __float2int_rn(o[0] * inv), q1 = __float2int_rn(o[1] * inv), q2 = __float2int_rn(o[2] * inv), q3 = __float2int_rn(o[3] * inv);
        if (ln < DH / 4) reinterpret_cast<u32x2*>(s_sc + served * (DH / 2))[ln] = u32x2{PERM_LO((unsigned)q1, (unsigned)q0), PERM_LO((unsigned)q3, (unsigned)q2)};
        const bool nan = __any((o[0] + o[1]) + (o[2] + o[3]) != (o[0] + o[1]) + (o[2] + o[3]));
        if (ln == 0) s_ml[served][2] = nan ? 0x7fc00000u : nz_bits(am * (1.0f / 32767.0f));
        LGKM0();
        if (ln == 0) s_c[F_FREE] = served + 1;
      }
#pragma unroll 1
      for (int c = from * 24 + ln; c < served * 24; c += 64) st128nt(rs, WS_PQ + xb * 5 * (DH / 2) + c * 4, reinterpret_cast<const u32x4*>(s_sc)[c]);
      VMCNT(0);
      if (ln < 8) st128(rs, WS_PML + xb * 64 + (from ? 32 : 0) + ln * 4, s_ml[ln]);
      if (ln == 0) s_c[S_BURST + (from ? 1 : 0)] = clk();
      CFENCE();
      if (ln == 0) __hip_atomic_fetch_add(s_c + F_DONE, (unsigned)(served - from), __ATOMIC_RELAXED, __HIP_MEMORY_SCOPE_WORKGROUP);
    }
    if (n > NA) lds_wait<1>(s_c + F_DONE, n); else lds_wait<8>(s_c + F_DONE, n);
  };
  auto done_all = [&]() {
#pragma unroll 1
    for (int n = 4; n <= NA; ++n) done(n);
  };
  const int lp = (lane < DH / 4 ? lane : 0) * 4;
  auto weight = [&](int it, int x0, int cnt) -> float {
    const int mlw = WS_PML + (x0 + (lane & (cnt - 1))) * 64 + (it < 4 ? 0 : 32) + it * 4;
    u32x4 ml;
#pragma unroll 1
    for (unsigned spin = 0; spin < SPIN_CAP; ++spin) {
      ml = ld128(rs, mlw);
      if (__all(min4(ml) != 0u)) break;
      __builtin_amdgcn_s_sleep(16);
    }
    const float mk = __uint_as_float(ml[0]);
    const float e = __builtin_amdgcn_exp2f(mk - max_x16_32(row_max(mk)));
    return e / (wave_sum(e * __uint_as_float(ml[1])) * ((float)cnt * (1.0f / 64.0f))) * __uint_as_float(ml[2]);
  };
  const unsigned tok = __builtin_amdgcn_readfirstlane(r0) + 1u;
  unsigned echoed = 0u, fold = 0u;
  int slow = 0, prev = 0;
  auto lag = [&]() {
    const unsigned v = __hip_atomic_load(s_c + S_MOVED + (lane & 7), __ATOMIC_RELAXED, __HIP_MEMORY_SCOPE_WORKGROUP);
    fold += v;
    unsigned a = 0u;
#pragma unroll
    for (int k = 0; k < 8; ++k) a += __builtin_amdgcn_readlane(v, k);
    return a >> 2;
  };
  auto tell = [&]() { if (lane < 4) { s_c[S_SLOW + 2 * lane] = slow; s_c[S_SLOW + 2 * lane + 1] = fold; } };
  auto round = [&](unsigned rd, bool lead) {
    const int tr = RT0 + (int)(rd - 1) * RTS;
    until(t0, tr);
    const unsigned bh = lag();
    if (bh > RMIN && lane < 8) st128(rs, WS_HA + bid * 32 + lane * 4, u32x4{echoed + rd, tok, tb0, bh});
    if (lead) { until(t0, tr + RA1); leader_release(rs, lane, tb0, rd, slow, prev); }
    until(t0, tr + RA2);
    const u32x2 rv = ld64(rs, WS_HR + bid * 2);
    slow = (int)__builtin_amdgcn_readfirstlane(rv[1]) >> 16;
#pragma unroll 1
    for (unsigned k = min(__builtin_amdgcn_readfirstlane(rv[0]) - tok, rd) + 1u; k <= rd; ++k) slow = slow > 0 ? max(slow - GB, 0) : slow - (k <= PRD ? PRB : 0);
    tell();
  };
  auto prep = [&](bool lead) {
    if (tw == 0) {
      const int c = lane & (GS - 1);
      unsigned qe = s_c[S_ECHO + c];
      bool first = true, released = false, sent = false;
#pragma unroll 1
      while ((int)(clk() - t0) < HST) {
        const unsigned qn = ld32(rs, qf + (GS + c) * 32);
        if (mm < NPROD && __any(qn != qe)) {
          if (lane < 32) st32(rs, qf + mm * 32 + lane, qn);
          qe = qn;
        }
        __builtin_amdgcn_s_sleep(64);
      }
      const unsigned behind = lag();
#pragma unroll 1
      for (unsigned spin = 0; spin < SPIN_CAP && !released; ++spin) {
        const unsigned qn = ld32(rs, qf + (GS + c) * 32);
        const u32x2 rv = ld64(rs, WS_HR + bid * 2);
        const unsigned nv = __builtin_amdgcn_readfirstlane(ld32(rs, WS_HN + bid));
        if (__builtin_amdgcn_readfirstlane(rv[0]) - tok <= NRD) {
          released = true;
          slow = (int)__builtin_amdgcn_readfirstlane(rv[1]) >> 16;
          if (lane == 0) { s_c[S_FIRST] = min((tb0 - rv[1]) & 0xffffu, 300u) + 1u; }
        } else {
          if (mm < NPROD && __any(qn != qe)) {
            if (lane < 32) st32(rs, qf + mm * 32 + lane, qn);
            qe = qn;
          }
          if (first || nv != echoed) {
            echoed = nv; first = false;
            if (lane < 8) st128(rs, WS_HA + bid * 32 + lane * 4, u32x4{echoed, tok, tb0, behind});
          }
          if (lead && !sent) sent = leader_release(rs, lane, tb0, 0u, slow, prev);
          __builtin_amdgcn_s_sleep(32);
        }
      }
      tell();
      if (lane == 0) s_c[F_REL] = 1u;
      if (lane < DH / 4) {
        u32x4 p[3][2];
#pragma unroll
        for (int e = 0; e < 3; ++e)
#pragma unroll
          for (int k = 0; k < 2; ++k) p[e][k] = ld128nt(rs, WS_QP + (((e < 2 ? 24 - e * 8 + (xb >> 5) : xb >> 4) * 2 + k) * NB + 1) * DH + lane * 4);
#pragma unroll
        for (int e = 0; e < 3; ++e) *reinterpret_cast<u32x4*>(&s_q[(1 + e) * DH + lane * 4]) = q_pack(p[e][0], p[e][1]);
      }
      LGKM0();
      if (lane == 0) s_c[F_Q2] = 3u;
#pragma unroll 1
      for (unsigned rd = 1; rd <= NRD; ++rd) round(rd, lead);
    }
    if (tw == 3) {
#pragma unroll 1
      for (int ik = 0; ik < NS; ++ik) {
        const int it = seg_of_k(ik), nv = (seg_beg(it + 1) - seg_beg(it)) >> 4;
        lds_wait<4>(s_c + C_SC + ik, ik == 0 ? NW : NMW);
        f32x4* sp = reinterpret_cast<f32x4*>(s_sc + seg_beg(it) * 16) + lane;
        f32x4 v[4];
#pragma unroll
        for (int k = 0; k < 4; ++k) v[k] = sp[(k & (nv - 1)) * 64];
        const f32x4 mx = __builtin_elementwise_max(__builtin_elementwise_max(v[0], v[1]), __builtin_elementwise_max(v[2], v[3]));
        const float m = max_x16_32(row_max(fmaxf(fmaxf(mx[0], mx[1]), fmaxf(mx[2], mx[3]))));
        float l = 0.f;
#pragma unroll
        for (int k = 0; k < 4; ++k) {
#pragma unroll
          for (int e = 0; e < 4; ++e) v[k][e] = __builtin_amdgcn_exp2f(v[k][e] - m);
          if (k < nv) {
            l += (v[k][0] + v[k][1]) + (v[k][2] + v[k][3]);
            sp[k * 64] = v[k];
          }
        }
        l = wave_sum(l);
        if (lane == 0) s_ml[it] = u32x4{nz_bits(m), nz_bits(l), 0x3f800000u, 0x3f800000u};
        LGKM0();
        if (lane == 0) s_c[F_P] = ik + 1;
      }
    }
  };
  auto pull = [&]() {
    typedef __attribute__((address_space(1))) const unsigned g_u32;
    const int id = (ro - 16) * 4 + tw;
    g_u32* p1[2];
#pragma unroll
    for (int i = 0; i < 2; ++i) {
      const int l = id * 60 + min(i * 32 + (lane >> 1), 59), m = l >= 1920 ? l - 1920 : l;
      p1[i] = (g_u32*)((l >= 1920 ? wu : wg) + (size_t)(m / 3) * FF + xl * 192 + (m % 3) * 64 + (lane & 1) * 32);
    }
    g_u32* p2 = (g_u32*)(wd + (size_t)(id * 24 + min(lane >> 1, 23)) * DM + xl * 64 + (lane & 1) * 32);
    asm volatile("" : "+v"(p1[0]), "+v"(p1[1]), "+v"(p2));
    lds_wait<1>(s_c + C_LAST, NMW);
    const unsigned tp = clk();
    until(tp, PW);
    const unsigned v0 = p1[0][0], v1 = p1[1][0];
    MEMFENCE();
    until(tp, PW + DPW);
    const unsigned v2 = p2[0], v3 = p2[xl < 2 ? 256 : 0];
    asm volatile("" :: "v"(v0), "v"(v1), "v"(v2), "v"(v3));
  };
  if (isW) {
    u32x4 bx[24];
    const char* p = reinterpret_cast<const char*>(wo + (size_t)((w_ks * 4 + tw) * DH + r) * DM + w_ct * 64 + j * 8);
    prep(false);
    if (tw != 0) { lds_wait<16>(s_c + F_P, NS); pause(clk(), OLATE + (tw % 3) * 24 * OSTEP); }
    const unsigned tw0 = clk();
#pragma unroll 1
    for (int i = 0; i < 24; ++i) { until(tw0, i * OSTEP); glds16_nt(p, lds_base + i * 1024); p += 8 * DM * 2; VMCNT(WFD); }
    asm volatile("s_waitcnt vmcnt(0)" ::: "memory");
#pragma unroll
    for (int s = 0; s < 6; ++s) {
      u32x2 e[8];
#pragma unroll
      for (int k = 0; k < 8; ++k) e[k] = *reinterpret_cast<const u32x2*>(reg + (s * 32 + qd * 8 + k) * 128 + mr * 8);
#pragma unroll
      for (int k = 0; k < 4; ++k) {
        bx[s * 4 + 0][k] = PERM_LO(e[2 * k + 1][0], e[2 * k][0]); bx[s * 4 + 1][k] = PERM_HI(e[2 * k + 1][0], e[2 * k][0]);
        bx[s * 4 + 2][k] = PERM_LO(e[2 * k + 1][1], e[2 * k][1]); bx[s * 4 + 3][k] = PERM_HI(e[2 * k + 1][1], e[2 * k][1]);
      }
    }
#pragma unroll
    for (int s = 0; s < 24; s += 4) asm volatile("" : "+v"(bx[s]), "+v"(bx[s + 1]), "+v"(bx[s + 2]), "+v"(bx[s + 3]));
    LGKM0();
    lds_wait<8>(s_c + F_REL, 1u);
    const int hh = w_ks * 4 + tw;
#pragma unroll 1
    for (int pass = 0; pass < 3; ++pass) {
      if (pass < 2) {
        done(pass ? NA : 4);
        pause(lds_get(s_c + S_BURST + pass), pass ? WDELAY2 : WDELAY2 + 300);
        const int b0 = pass * 2, b1 = pass + 1;
        u32x4 v0, v1;
#pragma unroll 1
        for (unsigned spin = 0; spin < SPIN_CAP; ++spin) {
          v0 = ld128(rs, WS_OC + (b0 * NH + hh) * DH + lp);
          v1 = ld128(rs, WS_OC + (b1 * NH + hh) * DH + lp);
          if (__all(min(min4(v0), min4(v1)) != 0u)) break;
          __builtin_amdgcn_s_sleep(16);
        }
        if (lane < DH / 4) {
          *reinterpret_cast<u32x2*>(reg + (b0 * 4) * AW_LD + lane * 8) = u32x2{v0[0], v0[1]};
          *reinterpret_cast<u32x2*>(reg + (b0 * 4 + 1) * AW_LD + lane * 8) = u32x2{v0[2], v0[3]};
          *reinterpret_cast<u32x2*>(reg + (b1 * 4) * AW_LD + lane * 8) = u32x2{v1[0], v1[1]};
          *reinterpret_cast<u32x2*>(reg + (b1 * 4 + 1) * AW_LD + lane * 8) = u32x2{v1[2], v1[3]};
        }
      } else {
        const float wk = weight(NS - 1, hh * GS, GS);
        float w[GS];
#pragma unroll
        for (int k = 0; k < GS; ++k) { w[k] = __uint_as_float(__builtin_amdgcn_readlane(__float_as_uint(wk), k)); asm volatile("" : "+s"(w[k])); }
        done(NS);
        __builtin_amdgcn_s_setprio(3);
        const int po = WS_PO + hh * GS * DH;
        u32x4 pv[GS];
#pragma unroll 1
        for (unsigned spin = 0; spin < SPIN_CAP; ++spin) {
#pragma unroll
          for (int k = 0; k < GS; ++k) pv[k] = __builtin_amdgcn_raw_buffer_load_b128(rs, lp * 4, (po + k * DH) * 4, AUX_SC1);
          unsigned mn = min4(pv[0]);
#pragma unroll
          for (int k = 1; k < GS; ++k) mn = min(mn, min4(pv[k]));
          if (__all(mn != 0u)) break;
          __builtin_amdgcn_s_sleep(1);
        }
        f32x4 o = w[0] * __builtin_bit_cast(f32x4, pv[0]), o2 = w[1] * __builtin_bit_cast(f32x4, pv[1]);
#pragma unroll
        for (int k = 2; k < GS; k += 2) { o += w[k] * __builtin_bit_cast(f32x4, pv[k]); o2 += w[k + 1] * __builtin_bit_cast(f32x4, pv[k + 1]); }
        u32x2 hi, lw;
        split4(o + o2, hi, lw);
        if (lane < DH / 4) {
          *reinterpret_cast<u32x2*>(reg + ((NB - 1) * 4) * AW_LD + lane * 8) = hi;
          *reinterpret_cast<u32x2*>(reg + ((NB - 1) * 4 + 1) * AW_LD + lane * 8) = lw;
        }
      }
      CFENCE();
      u32x4 a[6];
#pragma unroll
      for (int s = 0; s < 6; ++s) a[s] = *reinterpret_cast<const u32x4*>(reg + mr * AW_LD + s * 64 + qd * 16);
      __builtin_amdgcn_sched_barrier(0);
      f32x4 c[4];
#pragma unroll
      for (int t = 0; t < 4; ++t) c[t] = f32x4{0.f, 0.f, 0.f, 0.f};
#pragma unroll
      for (int s = 0; s < 6; ++s)
#pragma unroll
        for (int t = 0; t < 4; ++t) c[t] = mfma16(a[s], bx[s * 4 + t], c[t]);
      const bool mine = max(qd - 1, 0) == pass;
      f32x4* r4 = reinterpret_cast<f32x4*>(red);
      if (mine) r4[tw * 64 + lane] = f32x4{c[0][0] + c[0][1], c[1][0] + c[1][1], c[2][0] + c[2][1], c[3][0] + c[3][1]};
      const int k = arrive(s_c + C_W + pass);
      f32x4 sum = (r4[lane] + r4[64 + lane]) + (r4[128 + lane] + r4[192 + lane]);
      asm volatile("" : "+v"(sum));
      if (arrived(k) == 3) {
        if (mine) st128(rs, WS_SLAB + (w_ks * NB + qd) * DM + w_ct * 64 + mr * 4, u32x4{nz_bits(sum[0]), nz_bits(sum[1]), nz_bits(sum[2]), nz_bits(sum[3])});
      }
    }
  } else if (isG || isH) {
    f32x2* rst = reinterpret_cast<f32x2*>(red);
    const int hb = isH ? xl - 2 : NB - 1;
    uint2 wr4 = make_uint2(0u, 0u), xg = wr4;
    if (lane < 40) {
      wr4 = *reinterpret_cast<const uint2*>(wr + tw * 160 + lane * 4);
      xg = *reinterpret_cast<const uint2*>(x + row_of(hb) * DM + tw * 160 + lane * 4);
    }
    f32x4 xc = f32x4{bf_lo(xg.x), bf_hi(xg.x), bf_lo(xg.y), bf_hi(xg.y)} - bf_lo(*reinterpret_cast<const unsigned*>(x + row_of(hb) * DM));
    asm volatile("" : "+v"(xc));
    typedef __attribute__((address_space(1))) const u32x4 g_u32x4;
    g_u32x4* wp[10];
    wp[0] = (g_u32x4*)(((mr & 4) ? wu : wg) + (size_t)(tw * 160 + qd * 8 + (mr >> 3) * 4) * FF + g_cg * 32 + (mr & 3) * 8);
    u32x4 e[20], bx[20];
    auto frag = [&](int s) __attribute__((always_inline)) {
      unsigned w[8][2];
#pragma unroll
      for (int i = 0; i < 4; ++i)
#pragma unroll
        for (int d = 0; d < 2; ++d) {
          w[i][d] = __builtin_amdgcn_update_dpp(e[s * 4 + i][d], e[s * 4 + i][2 + d], DPP_ROR8, 0xf, 0xc, false);
          w[4 + i][d] = __builtin_amdgcn_update_dpp(e[s * 4 + i][2 + d], e[s * 4 + i][d], DPP_ROR8, 0xf, 0x3, false);
        }
#pragma unroll
      for (int k = 0; k < 4; ++k) {
        bx[s * 4 + 0][k] = PERM_LO(w[2 * k + 1][0], w[2 * k][0]); bx[s * 4 + 1][k] = PERM_HI(w[2 * k + 1][0], w[2 * k][0]);
        bx[s * 4 + 2][k] = PERM_LO(w[2 * k + 1][1], w[2 * k][1]); bx[s * 4 + 3][k] = PERM_HI(w[2 * k + 1][1], w[2 * k][1]);
      }
      PIN4(bx, s * 4);
    };
    prep(false);
    lds_wait<8>(s_c + F_REL, 1u);
    auto hrow = [&](bool fast) -> f32x4 {
      f32x4 hv = {0.f, 0.f, 0.f, 0.f};
      if (lane < 40) {
        const int off = (WS_SLAB + hb * DM + tw * 160 + lane * 4) * 4;
        u32x4 sl[KS];
#pragma unroll 1
        for (unsigned spin = 0; spin < SPIN_CAP; ++spin) {
#pragma unroll
          for (int ks = 0; ks < KS; ++ks) sl[ks] = __builtin_amdgcn_raw_buffer_load_b128(rs, off, ks * (NB * DM * 4), AUX_SC1);
          unsigned mn = min4(sl[0]);
#pragma unroll
          for (int ks = 1; ks < KS; ++ks) mn = min(mn, min4(sl[ks]));
          if (!__any(mn == 0u)) break;
          if (fast) __builtin_amdgcn_s_sleep(1); else __builtin_amdgcn_s_sleep(16);
        }
#define SL(k) __builtin_bit_cast(f32x4, sl[k])
        hv = xc + (((SL(0) + SL(1)) + (SL(2) + SL(3))) + ((SL(4) + SL(5)) + (SL(6) + SL(7))));
#undef SL
      }
      return hv;
    };
    auto sums = [&](f32x4 hv) -> f32x2 {
      return f32x2{wave_sum((hv[0] + hv[1]) + (hv[2] + hv[3])), wave_sum((hv[0] * hv[0] + hv[1] * hv[1]) + (hv[2] * hv[2] + hv[3] * hv[3]))};
    };
    if (isH) {
      const float wsum = wave_sum((bf_lo(wr4.x) + bf_hi(wr4.x)) + (bf_lo(wr4.y) + bf_hi(wr4.y)));
      if (tw == 3 || hb >= 2) done_all(); else done(4);
      if (hb == NB - 1) { pull(); done(NS); }
      else pause(lds_get(s_c + S_BURST + (hb >> 1)), hb < 2 ? HDELAY : HDELAY2);
      const f32x4 hv = hrow(hb == NB - 1);
      const f32x2 sm = sums(hv);
      const float dot = wave_sum((hv[0] * bf_lo(wr4.x) + hv[1] * bf_hi(wr4.x)) + (hv[2] * bf_lo(wr4.y) + hv[3] * bf_hi(wr4.y)));
      if (lane < 8) st128(rs, WS_H + (hb * 4 + tw) * 192 + 160 + lane * 4, u32x4{nz_bits(sm[0]), nz_bits(sm[1]), nz_bits(dot), nz_bits(wsum)});
      if (hb == NB - 1) return;
      const u32x4 v = split8(hv, (lane & 1) != 0);
      if (lane < 40) st128(rs, WS_H + (hb * 4 + tw) * 192 + lane * 4, v);
      done_all();
      pull();
      return;
    }
    done_all();
#pragma unroll
    for (int i = 0; i < 10; ++i) { wp[i] = wp[0] + ((i >> 1) * 32 + (i & 1) * 2) * (FF / 8); asm volatile("" : "+v"(wp[i])); }
    done(NS);
#pragma unroll
    for (int i = 0; i < 20; ++i) e[i] = wp[i >> 1][(i & 1) * (FF / 8)];
    u32x4 a[5], sm;
    const int hoff = (WS_H + (min(mr >> 2, NB - 2) * 4 + tw) * 192 + qd * 8 + (mr & 1) * 4) * 4;
#pragma unroll
    for (int s = 0; s < 5; ++s) a[s] = __builtin_amdgcn_raw_buffer_load_b128(rs, hoff + s * 128, 0, AUX_SC1);
    sm = ld128(rs, WS_H + (min(lane, NB - 2) * 4 + tw) * 192 + 160);
    MEMFENCE();
#pragma unroll
    for (int s = 0; s < 5; ++s) frag(s);
    {
#pragma unroll 1
      for (unsigned spin = 0; spin < SPIN_CAP; ++spin) {
        unsigned mn = min4(sm);
#pragma unroll
        for (int s = 0; s < 5; ++s) mn = min(mn, min4(a[s]));
        if (__all(mn != 0u)) break;
        __builtin_amdgcn_s_sleep(2);
#pragma unroll
        for (int s = 0; s < 5; ++s) a[s] = __builtin_amdgcn_raw_buffer_load_b128(rs, hoff + s * 128, 0, AUX_SC1);
        sm = ld128(rs, WS_H + (min(lane, NB - 2) * 4 + tw) * 192 + 160);
      }
      if (lane < NB - 1) rst[lane * 4 + tw] = f32x2{__uint_as_float(sm[0]), __uint_as_float(sm[1])};
#pragma unroll
      for (int s = 0; s < 5; ++s) {
        if (mr & 2) a[s] = u32x4{0x3f803f80u, 0x3f803f80u, 0x3f803f80u, 0x3f803f80u};
        asm volatile("" : "+v"(a[s]));
      }
    }
    typedef __attribute__((address_space(3))) unsigned char lds_u8;
    const int eb = mr >> 2;
    lds_u8* l_aw = (lds_u8*)reg + lane * 8;
    lds_u8* l_ar = (lds_u8*)reg + (mr & 1) * AG_LD + qd * 16;
    lds_u8* l_pw = (lds_u8*)reg + 4096 + (qd * 16 + (mr & 7) * 2 + (mr >> 3)) * 16;
    lds_u8* l_pr = (lds_u8*)smem + OFF_REG + 4096 + (lane & 1) * 1152 + (eb * 16 + qd * 2 + ((lane >> 1) & 1)) * 16;
    lds_u8* l_p2 = l_pr + 2 * REG;
    lds_u8* l_sr = (lds_u8*)(rst + eb * 4);
    lds_u8* l_sw = (lds_u8*)(rst + (NB - 1) * 4 + tw);
    int so = (lane & 2) == 0 ? (WS_ACT + g_cg * 128 + (lane >> 2) * 8 + (lane & 1) * 4) * 4 : WS_END * 4;
    asm volatile("" : "+v"(l_aw), "+v"(l_ar), "+v"(l_pw), "+v"(l_pr), "+v"(l_p2), "+v"(l_sr), "+v"(l_sw), "+v"(so));
#define LDS(T, p) (*reinterpret_cast<__attribute__((address_space(3))) T*>(p))
    const f32x4 hv = hrow(true);
    {
      u32x2 hi, lw;
      split4(hv, hi, lw);
      LDS(u32x2, l_aw) = hi;
      LDS(u32x2, l_aw + AG_LD) = lw;
    }
    CFENCE();
    if ((mr & 14) == 12) {
#pragma unroll
      for (int s = 0; s < 5; ++s) a[s] = LDS(u32x4, l_ar + s * 64);
    }
    __builtin_amdgcn_sched_barrier(0);
    f32x4 c[4];
#pragma unroll
    for (int t = 0; t < 4; ++t) c[t] = f32x4{0.f, 0.f, 0.f, 0.f};
#pragma unroll
    for (int s = 0; s < 5; ++s)
#pragma unroll
      for (int t = 0; t < 4; ++t) c[t] = mfma16(a[s], bx[s * 4 + t], c[t]);
    LDS(f32x2, l_sw) = sums(hv);
    LDS(f32x4, l_pw) = f32x4{c[0][0] + c[0][1], c[1][0] + c[1][1], c[0][2], c[1][2]};
    LDS(f32x4, l_pw + 1152) = f32x4{c[2][0] + c[2][1], c[3][0] + c[3][1], c[2][2], c[3][2]};
    const int k = arrive(s_c + C_T);
    f32x4 ts[2] = {LDS(f32x4, l_sr), LDS(f32x4, l_sr + 16)};
    f32x4 pg[4] = {LDS(f32x4, l_pr), LDS(f32x4, l_pr + REG), LDS(f32x4, l_p2), LDS(f32x4, l_p2 + REG)};
    f32x4 pu[4] = {LDS(f32x4, l_pr + 128), LDS(f32x4, l_pr + REG + 128), LDS(f32x4, l_p2 + 128), LDS(f32x4, l_p2 + REG + 128)};
    asm volatile("" : "+v"(ts[0]), "+v"(ts[1]));
    PIN4(pg, 0); PIN4(pu, 0);
    if (arrived(k) == 3) {
      const f32x4 t = ts[0] + ts[1], g4 = (pg[0] + pg[1]) + (pg[2] + pg[3]), u4 = (pu[0] + pu[1]) + (pu[2] + pu[3]);
      const float s1 = t[0] + t[2], d = s1 * (-1.0f / DM);
      const float rstd = __builtin_amdgcn_rsqf(fmaxf((t[1] + t[3]) + d * s1, 0.f) * (1.0f / DM) + LN_EPS);
      const float g0 = rstd * (g4[0] + d * g4[2]), g1 = rstd * (g4[1] + d * g4[3]);
      const f32x2 act = {g0 * __builtin_amdgcn_rcpf(1.0f + __expf(-g0)) * (rstd * (u4[0] + d * u4[2])),
                         g1 * __builtin_amdgcn_rcpf(1.0f + __expf(-g1)) * (rstd * (u4[1] + d * u4[3]))};
      const unsigned h0 = __float_as_uint(act[0]) & 0xffff0000u, h1 = __float_as_uint(act[1]) & 0xffff0000u;
      const unsigned hi = max(PERM_HI(h1, h0), 1u), lw = max(PERM_HI(__float_as_uint(act[1] - __uint_as_float(h1)), __float_as_uint(act[0] - __uint_as_float(h0))), 1u);
      const bool odd = (lane & 1) != 0;
      const unsigned own = odd ? lw : hi, oth = __float_as_uint(dpp<DPP_XOR1>(__uint_as_float(odd ? hi : lw)));
      const unsigned w0 = odd ? oth : own, w1 = odd ? own : oth;
      const u32x4 av = {w0, w1, __float_as_uint(dpp<DPP_XOR2>(__uint_as_float(w0))), __float_as_uint(dpp<DPP_XOR2>(__uint_as_float(w1)))};
      __builtin_amdgcn_raw_buffer_store_b128(av, rs, so, 0, AUX_SC1);
    }
  } else if (isD) {
    const uint2 xd = *reinterpret_cast<const uint2*>(x + row_of(tw) * DM + d_ct * 32 + j * 4);
    const float cq = bf_lo(*reinterpret_cast<const unsigned*>(x + row_of(qd) * DM));
    const __amdgpu_buffer_rsrc_t rd = __builtin_amdgcn_make_buffer_rsrc(const_cast<bf16*>(wd), 0, FF * DM * 2, 0x00020000);
    prep(false);
    lds_wait<8>(s_c + F_REL, 1u);
    done_all();
    pull();
    done(NS);
    const unsigned te = clk();
    u32x4 bd[24];
    {
      const int off = ((tw * 384 + (mr >> 3) * 32 + qd * 8 + (mr & 4)) * DM + d_ct * 32 + (mr & 3) * 8) * 2;
      u32x4 e[24];
#pragma unroll
      for (int i = 0; i < 24; ++i) e[i] = __builtin_amdgcn_raw_buffer_load_b128(rd, off + (i & 3) * (DM * 2), (i >> 2) * 64 * (DM * 2), 0);
      MEMFENCE();
#pragma unroll
      for (int s = 0; s < 6; ++s) {
        unsigned w[8][2];
#pragma unroll
        for (int i = 0; i < 4; ++i)
#pragma unroll
          for (int d = 0; d < 2; ++d) {
            w[i][d] = __builtin_amdgcn_update_dpp(e[s * 4 + i][d], e[s * 4 + i][2 + d], 0x114, 0xf, 0xa, false);
            w[4 + i][d] = __builtin_amdgcn_update_dpp(e[s * 4 + i][2 + d], e[s * 4 + i][d], 0x104, 0xf, 0x5, false);
          }
#pragma unroll
        for (int k = 0; k < 4; ++k) {
          bd[s * 4 + 0][k] = PERM_LO(w[2 * k + 1][0], w[2 * k][0]); bd[s * 4 + 1][k] = PERM_HI(w[2 * k + 1][0], w[2 * k][0]);
          bd[s * 4 + 2][k] = PERM_LO(w[2 * k + 1][1], w[2 * k][1]); bd[s * 4 + 3][k] = PERM_HI(w[2 * k + 1][1], w[2 * k][1]);
        }
        PIN4(bd, s * 4);
      }
    }
    until(te, DLATE);
    {
      u32x4 sl;
#pragma unroll 1
      for (unsigned spin = 0; spin < SPIN_CAP; ++spin) {
        sl = ld128(rs, WS_SLAB + (r * NB + tw) * DM + d_ct * 32 + j * 4);
        if (__all(min4(sl) != 0u)) break;
        __builtin_amdgcn_s_sleep(2);
      }
      f32x4 hv = {bf_lo(xd.x), bf_hi(xd.x), bf_lo(xd.y), bf_hi(xd.y)};
#pragma unroll
      for (int i = 0; i < 4; ++i) hv[i] += sum_r(__uint_as_float(sl[i]));
      if (lane < 8) reinterpret_cast<f32x4*>(red + 512 + tw * 32)[lane] = hv;
      LGKM0();
      lds_add(s_c + C_W, lane);
      lds_wait<1>(s_c + C_W, 4u);
    }
    const int col = (mr & 3) * 8 + (mr & 4) + (mr >> 3) * 2;
    f32x2 hv = *reinterpret_cast<const f32x2*>(red + 512 + qd * 32 + col);
    typedef __attribute__((address_space(3))) f32x2 lds_f32x2;
    typedef __attribute__((address_space(1))) unsigned g_u32w;
    lds_f32x2* r2 = (lds_f32x2*)red + lane;
    lds_f32x2* r2w = r2 + tw * 64;
    g_u32w* po = (g_u32w*)(out + row_of(qd) * DM + d_ct * 32 + col);
    int aoff = (WS_ACT + tw * 12 * 128 + ((mr >> 1) & 1) * 128 + qd * 32 + (mr >> 2) * 8 + (mr & 1) * 4) * 4;
    asm volatile("" : "+v"(hv), "+v"(r2), "+v"(r2w), "+v"(po), "+v"(aoff));
    u32x4 hs, a[6];
#pragma unroll 1
    for (unsigned spin = 0; spin < SPIN_CAP; ++spin) {
      hs = ld128(rs, WS_H + (qd * 4 + (mr & 3)) * 192 + 160);
      if (__all(min4(hs) != 0u)) break;
      __builtin_amdgcn_s_sleep(1);
    }
#pragma unroll
    for (int s = 0; s < 6; ++s) a[s] = __builtin_amdgcn_raw_buffer_load_b128(rs, aoff, s * 1024, AUX_SC1);
    MEMFENCE();
    float gate;
    {
      f32x4 t = __builtin_bit_cast(f32x4, hs);
#pragma unroll
      for (int i = 0; i < 4; ++i) { t[i] += dpp<DPP_XOR2>(t[i]); t[i] += dpp<DPP_XOR1>(t[i]); }
      const float d = t[0] * (-1.0f / DM);
      const float rstd = __builtin_amdgcn_rsqf(fmaxf(t[1] + d * t[0], 0.f) * (1.0f / DM) + LN_EPS);
      hv += ((hv - cq) + d) * rstd;
      gate = __builtin_amdgcn_rcpf(1.0f + __expf(-rstd * (t[2] + d * t[3])));
      asm volatile("" : "+v"(hv), "+v"(gate));
    }
#pragma unroll 1
    for (unsigned spin = 0; spin < SPIN_CAP; ++spin) {
      unsigned mn = min4(a[0]);
#pragma unroll
      for (int s = 1; s < 6; ++s) mn = min(mn, min4(a[s]));
      if (__all(mn != 0u)) break;
#pragma unroll
      for (int s = 0; s < 6; ++s) a[s] = __builtin_amdgcn_raw_buffer_load_b128(rs, aoff, s * 1024, AUX_SC1);
      MEMFENCE();
    }
    f32x4 c[4];
#pragma unroll
    for (int t = 0; t < 4; ++t) c[t] = f32x4{0.f, 0.f, 0.f, 0.f};
#pragma unroll
    for (int s = 0; s < 6; ++s)
#pragma unroll
      for (int t = 0; t < 4; ++t) c[t] = mfma16(a[s], bd[s * 4 + t], c[t]);
    const bool up = mr >= 8;
    const float k0 = up ? c[2][2] + c[2][3] : c[0][0] + c[0][1], k1 = up ? c[3][2] + c[3][3] : c[1][0] + c[1][1];
    const float o0 = up ? c[0][2] + c[0][3] : c[2][0] + c[2][1], o1 = up ? c[1][2] + c[1][3] : c[3][0] + c[3][1];
    *r2w = f32x2{k0 + dpp<DPP_ROR8>(o0), k1 + dpp<DPP_ROR8>(o1)};
    const int k = arrive(s_c + C_T);
    f32x2 y = hv + ((r2[0] + r2[64]) + (r2[128] + r2[192])) * gate;
    asm volatile("" : "+v"(y));
    if (arrived(k) == 3) {
      __hip_atomic_store(po, __builtin_bit_cast(unsigned, __builtin_convertvector(y, bf16x2)), __ATOMIC_RELAXED, __HIP_MEMORY_SCOPE_AGENT);
    }
  } else {
    prep(bid == LEADER);
    if (isC && tw == 1) {
      lds_wait<8>(s_c + F_REL, 1u);
      const int cs = c_it != 1 ? c_it * 2 : min(4 - (g >> 3), 3), cnt = c_it != 1 ? 8 : (g < 16 ? 16 : 32), x0 = c_it != 1 ? g * 8 : (g < 16 ? g * 16 : (g & 7) * 32);
      done(c_it < 2 ? 4 : NA);
      pause(lds_get(s_c + S_BURST + (c_it >> 1)), WDELAY);
      const float wk = weight(cs, x0, cnt);
      f32x4 o = {0.f, 0.f, 0.f, 0.f};
#pragma unroll 1
      for (int k0 = 0; k0 < cnt; k0 += 16) {
        u32x2 pv[16];
#pragma unroll
        for (int k = 0; k < 16; ++k) pv[k] = __builtin_amdgcn_raw_buffer_load_b64(rs, lp * 2, (WS_PQ + ((x0 + ((k0 + k) & (cnt - 1))) * 5 + cs) * (DH / 2)) * 4, AUX_EARLY);
#pragma unroll
        for (int k = 0; k < 16; ++k) {
          const float w = k0 + k < cnt ? __uint_as_float(__builtin_amdgcn_readlane(__float_as_uint(wk), k0 + k)) : 0.f;
          o += w * f32x4{(float)(short)pv[k][0], (float)((int)pv[k][0] >> 16), (float)(short)pv[k][1], (float)((int)pv[k][1] >> 16)};
        }
      }
      u32x2 hi, lw;
      split4(o, hi, lw);
      if (lane < DH / 4) st128(rs, WS_OC + (c_it * NG + g) * DH + lane * 4, u32x4{max(hi[0], 1u), max(hi[1], 1u), max(lw[0], 1u), max(lw[1], 1u)});
    }
    done_all();
    pull();
  }
  if (tw == 3) done_all();
}

extern "C" void kernel_launch(void* const* d_in, const int* in_sizes, int n_in, void* d_out, int out_size, void* d_ws,
                              size_t ws_size, hipStream_t stream) {
  const bf16* x = (const bf16*)d_in[0];
  const bf16* kc = (const bf16*)d_in[1];
  const bf16* vc = (const bf16*)d_in[2];
  const bf16* wq = (const bf16*)d_in[3];
  const bf16* wo = (const bf16*)d_in[4];
  const bf16* wr = (const bf16*)d_in[6];
  const bf16* wg = (const bf16*)d_in[7];
  const bf16* wu = (const bf16*)d_in[8];
  const bf16* wd = (const bf16*)d_in[9];
  fused_kernel<<<NWG, NW * 64, 0, stream>>>(x, kc, vc, wq, wo, wr, wg, wu, wd, (float*)d_ws, (bf16*)d_out);
}
```

```cpp
#include <hip/hip_runtime.h>
#include <hip/hip_bf16.h>
#include <stdint.h>

typedef __hip_bfloat16 bf16;
using u32x2 = __attribute__((ext_vector_type(2))) unsigned;
using u32x4 = __attribute__((ext_vector_type(4))) unsigned;
using f32x4 = __attribute__((ext_vector_type(4))) float;
using f32x2 = __attribute__((ext_vector_type(2))) float;
using bf16x8 = __attribute__((ext_vector_type(8))) __bf16;

constexpr int NB = 4, DM = 640, NH = 32, SK = 8192, DH = 192, HD = NH * DH, FF = 1536;
constexpr float LN_EPS = 1e-5f;
constexpr float QSCALE = 0.07216878364870322f * 1.4426950408889634f;

constexpr int NWG = 256;
constexpr int GS = 8, NG = NWG / GS, NI = GS / 2;
constexpr int HPG = NI / NB;
constexpr int PPI = 512 / GS;
constexpr int NPAIR = NI * PPI;

constexpr int TAUK = 1320, TAUV = 1332;
constexpr int TSTART = 110;
constexpr int ANCH = 90;
constexpr int OGAP = 105, OLATE = 250, OSTEP = 12;
constexpr int VGAP = 50;
constexpr int DEEP1 = 100, DEEP2 = 200, LAGOFF = 20;
constexpr int ENDT = 12, ENDS = 2;
constexpr int JUMP = 145;
constexpr int ZT = 1800, HST = 2800, RT0 = 3900, RTS = 1000, RA1 = 220, RA2 = 500, LEADER = 24;
constexpr unsigned NRD = 5, RMIN = 30;
constexpr int FBX = 20, FB0 = 80, FB1 = 10, FBD = 10, FBS = 10;
constexpr int HOLD = 4000, GB = 24, GBS = 2, GBX = 60, GBF = 12;
constexpr unsigned PRD = 3; constexpr int PRB = 24;
constexpr int ROT = 16, ROWS = 0x1B;
constexpr int TLEAD = 10, PFD = 3, WFD = 3;
constexpr int QWAIT = 750, QBACK = 100;
constexpr int WDELAY = 100, WDELAY2 = 460, HDELAY = 1150, HDELAY2 = 780, DLATE = 100;
constexpr int TPT = 600, TPS = 20;
constexpr int CODE = 24 * 64 * 46;
constexpr int PW = 5, DPW = 150, GW = 30;

constexpr int KS = 8;
constexpr int WS_PML = 0;
constexpr int WS_PO = WS_PML + NWG * 64;
constexpr int WS_PQ = WS_PO + NWG * DH;
constexpr int WS_OC = WS_PQ + NWG * 5 * (DH / 2);
constexpr int WS_SLAB = WS_OC + (NB - 1) * NH * DH;
constexpr int WS_ACT = WS_SLAB + KS * NB * DM;
constexpr int WS_H = WS_ACT + NB * FF;
constexpr int WS_HN = WS_H + NB * 4 * 192;
constexpr int WS_HR = WS_HN + NWG;
constexpr int WS_HA = WS_HR + 2 * NWG;
constexpr int QFS = 2 * GS * 32;
constexpr int WS_QF = WS_HA + 32 * NWG;
constexpr int WS_QP = WS_QF + NG * QFS;
constexpr int WS_END = WS_QP + NH * 2 * NB * DH;
constexpr int NPROD = HPG * 6;
static_assert(WS_PO % 32 == 0 && WS_SLAB % 32 == 0 && WS_ACT % 32 == 0 && WS_HN % 32 == 0 && WS_QF % 32 == 0 && WS_QP % 32 == 0, "line alignment");


__device__ __forceinline__ int row_of(int b) { return (ROWS >> (2 * b)) & 3; }
__device__ __forceinline__ float bf_lo(unsigned u) { return __uint_as_float(u << 16); }
__device__ __forceinline__ float bf_hi(unsigned u) { return __uint_as_float(u & 0xffff0000u); }
__device__ __forceinline__ f32x2 bf_pair(unsigned u) { return f32x2{bf_lo(u), bf_hi(u)}; }
__device__ __forceinline__ f32x2 fma2(float a, f32x2 b, f32x2 c) { return __builtin_elementwise_fma(f32x2{a, a}, b, c); }
__device__ __forceinline__ f32x2 fma2(f32x2 a, f32x2 b, f32x2 c) { return __builtin_elementwise_fma(a, b, c); }

template <int CTRL> __device__ __forceinline__ float dpp(float x) {
  return __builtin_bit_cast(float, __builtin_amdgcn_mov_dpp(__builtin_bit_cast(int, x), CTRL, 0xf, 0xf, true));
}
constexpr int DPP_XOR1 = 0xB1, DPP_XOR2 = 0x4E, DPP_HMIRROR = 0x141, DPP_MIRROR = 0x140, DPP_ROR8 = 0x128;

__device__ __forceinline__ float sum_x16_32(float x) {
  auto s = __builtin_amdgcn_permlane16_swap(__float_as_uint(x), __float_as_uint(x), false, false);
  x = __uint_as_float(s[0]) + __uint_as_float(s[1]);
  auto t = __builtin_amdgcn_permlane32_swap(__float_as_uint(x), __float_as_uint(x), false, false);
  return __uint_as_float(t[0]) + __uint_as_float(t[1]);
}
__device__ __forceinline__ float max_x16_32(float x) {
  auto s = __builtin_amdgcn_permlane16_swap(__float_as_uint(x), __float_as_uint(x), false, false);
  x = fmaxf(__uint_as_float(s[0]), __uint_as_float(s[1]));
  auto t = __builtin_amdgcn_permlane32_swap(__float_as_uint(x), __float_as_uint(x), false, false);
  return fmaxf(__uint_as_float(t[0]), __uint_as_float(t[1]));
}
__device__ __forceinline__ float tsum32(float a, float b) {
  auto t = __builtin_amdgcn_permlane32_swap(__float_as_uint(a), __float_as_uint(b), false, false);
  return __uint_as_float(t[0]) + __uint_as_float(t[1]);
}
__device__ __forceinline__ float tsum16(float a, float b) {
  auto t = __builtin_amdgcn_permlane16_swap(__float_as_uint(a), __float_as_uint(b), false, false);
  return __uint_as_float(t[0]) + __uint_as_float(t[1]);
}
__device__ __forceinline__ float sum_r(float v) { v += dpp<DPP_ROR8>(v); return sum_x16_32(v); }
__device__ __forceinline__ float row_sum(float v) {
  v += dpp<DPP_XOR1>(v); v += dpp<DPP_XOR2>(v); v += dpp<DPP_HMIRROR>(v);
  return v + dpp<DPP_MIRROR>(v);
}
__device__ __forceinline__ float row_max(float v) {
  v = fmaxf(v, dpp<DPP_XOR1>(v)); v = fmaxf(v, dpp<DPP_XOR2>(v)); v = fmaxf(v, dpp<DPP_HMIRROR>(v));
  return fmaxf(v, dpp<DPP_MIRROR>(v));
}
__device__ __forceinline__ float wave_sum(float v) {
  v += dpp<DPP_XOR1>(v); v += dpp<DPP_XOR2>(v); v += dpp<DPP_HMIRROR>(v);
  return sum_r(v);
}

__device__ __forceinline__ u32x4 ld_nt(const char* p) { return __builtin_nontemporal_load(reinterpret_cast<const u32x4*>(p)); }

struct Tile { u32x4 d[3]; };

#define PIN4(a, o) asm volatile("" : "+v"((a)[(o) + 0]), "+v"((a)[(o) + 1]), "+v"((a)[(o) + 2]), "+v"((a)[(o) + 3]))
#define MEMFENCE() do { asm volatile("" ::: "memory"); __builtin_amdgcn_sched_barrier(0); } while (0)

__device__ __forceinline__ void glds16_nt(const void* gsrc, unsigned lds_dst) {
  unsigned keep;
  asm volatile("s_mov_b32 %0, m0\n\ts_mov_b32 m0, %2\n\ts_nop 0\n\tglobal_load_lds_dwordx4 %1, off nt\n\ts_mov_b32 m0, %0"
               : "=&s"(keep) : "v"(gsrc), "s"(lds_dst) : "memory");
}

using bf16x2 = __attribute__((ext_vector_type(2))) __bf16;
__device__ __forceinline__ float dot2(unsigned a, unsigned b, float c) { return __builtin_amdgcn_fdot2_f32_bf16(__builtin_bit_cast(bf16x2, a), __builtin_bit_cast(bf16x2, b), c, false); }
__device__ __forceinline__ float tile_score(const Tile& k, const u32x2 (&q)[12]) {
  float s0 = 0.f, s1 = 0.f;
#pragma unroll
  for (int i = 0; i < 3; ++i) {
    u32x4 v = k.d[i];
    asm("" : "+v"(v));
#pragma unroll
    for (int d = 0; d < 4; ++d) { s0 = dot2(v[d], q[i * 4 + d][0], s0); s1 = dot2(v[d], q[i * 4 + d][1], s1); }
  }
  return s0 + s1;
}
__device__ __forceinline__ void pair_put(float a, float b, float* dst, int j) {
  float s = (j < 4 ? a : b) + dpp<DPP_HMIRROR>(j < 4 ? b : a);
  s += dpp<DPP_XOR1>(s);
  s += dpp<DPP_XOR2>(s);
  if ((j & 3) == 0) dst[j * 2] = s;
}
__device__ __forceinline__ void score_put(float s, float* dst, int j) {
  s += dpp<DPP_XOR1>(s);
  s += dpp<DPP_XOR2>(s);
  s += dpp<DPP_HMIRROR>(s);
  if (j == 0) *dst = s;
}
__device__ __forceinline__ void tile_load(Tile& t, const char* base, unsigned off, int half) {
#pragma unroll
  for (int i = 0; i < 3; ++i) t.d[i] = ld_nt(base + off + half * 3072 + i * 128);
}
__device__ __forceinline__ void pair_issue(Tile& ta, Tile& tb, const char* base, unsigned off, int deep) {
  deep = __builtin_amdgcn_readfirstlane(deep);
#define WAIT_IF(level, n) asm volatile("s_cmp_lg_u32 %0, " #level "\n\ts_cbranch_scc1 .Lpi%=\n\ts_waitcnt vmcnt(" #n ")\n.Lpi%=:" :: "s"(deep) : "scc", "memory")
  const char* p = base + off;
  WAIT_IF(0, 3);
  ta.d[0] = ld_nt(p); ta.d[1] = ld_nt(p + 128); ta.d[2] = ld_nt(p + 256); MEMFENCE();
  WAIT_IF(0, 3); WAIT_IF(1, 6);
  tb.d[0] = ld_nt(p + 3072); tb.d[1] = ld_nt(p + 3200); tb.d[2] = ld_nt(p + 3328); MEMFENCE();
#undef WAIT_IF
}
__device__ __forceinline__ void pv_last(float p, Tile& v, f32x2 (&acc)[12]) {
#pragma unroll
  for (int i = 0; i < 3; ++i) {
    asm volatile("" : "+v"(v.d[i]), "+v"(p));
#pragma unroll
    for (int d = 0; d < 4; ++d) acc[i * 4 + d] = fma2(p, bf_pair(v.d[i][d]), acc[i * 4 + d]);
    PIN4(acc, i * 4);
  }
}
__device__ __forceinline__ void q_load(u32x2 (&q)[12], const float* src, int j) {
#pragma unroll
  for (int i = 0; i < 6; ++i) {
    const u32x4 a = *reinterpret_cast<const u32x4*>(src + (i >> 1) * 64 + j * 8 + (i & 1) * 4);
    q[i * 2] = u32x2{a[0], a[1]}; q[i * 2 + 1] = u32x2{a[2], a[3]};
  }
}
#define VMCNT(n) asm volatile("s_waitcnt vmcnt(%0)" :: "n"(n) : "memory")

constexpr int AUX_SC1 = 16, AUX_EARLY = 18;
constexpr unsigned SPIN_CAP = 1u << 17;
__device__ __forceinline__ unsigned nz_bits(float v) { return max(__float_as_uint(v), 1u); }
__device__ __forceinline__ unsigned min4(u32x4 v) { return min(min(v[0], v[1]), min(v[2], v[3])); }
__device__ __forceinline__ unsigned ld32(__amdgpu_buffer_rsrc_t rs, int word) { return __builtin_amdgcn_raw_buffer_load_b32(rs, word * 4, 0, AUX_SC1); }
__device__ __forceinline__ u32x2 ld64(__amdgpu_buffer_rsrc_t rs, int word) { return __builtin_amdgcn_raw_buffer_load_b64(rs, word * 4, 0, AUX_SC1); }
__device__ __forceinline__ u32x4 ld128(__amdgpu_buffer_rsrc_t rs, int word) { return __builtin_amdgcn_raw_buffer_load_b128(rs, word * 4, 0, AUX_SC1); }
__device__ __forceinline__ void st32(__amdgpu_buffer_rsrc_t rs, int word, unsigned v) { __builtin_amdgcn_raw_buffer_store_b32(v, rs, word * 4, 0, AUX_SC1); }
__device__ __forceinline__ void st128(__amdgpu_buffer_rsrc_t rs, int word, u32x4 v) { __builtin_amdgcn_raw_buffer_store_b128(v, rs, word * 4, 0, AUX_SC1); }

__device__ __forceinline__ u32x4 ld128nt(__amdgpu_buffer_rsrc_t rs, int word) { return __builtin_amdgcn_raw_buffer_load_b128(rs, word * 4, 0, AUX_EARLY); }
__device__ __forceinline__ void st128nt(__amdgpu_buffer_rsrc_t rs, int word, u32x4 v) { __builtin_amdgcn_raw_buffer_store_b128(v, rs, word * 4, 0, AUX_EARLY); }

__device__ __forceinline__ bool leader_release(__amdgpu_buffer_rsrc_t rs, int lane, unsigned t0, unsigned rd, int slow, int& prev) {
  u32x4 a[4];
#pragma unroll
  for (int k = 0; k < 4; ++k) a[k] = ld128(rs, WS_HA + (lane * 4 + k) * 32);
  const u32x4 n = ld128(rs, WS_HN + lane * 4);
  if (rd == 0u && !__all(a[0][0] == n[0] && a[1][0] == n[1] && a[2][0] == n[2] && a[3][0] == n[3])) return false;
  const float e = fmaxf(fmaxf((float)(int)(t0 - a[0][2]), (float)(int)(t0 - a[1][2])), fmaxf((float)(int)(t0 - a[2][2]), (float)(int)(t0 - a[3][2])));
  unsigned first = t0 - (unsigned)(int)max_x16_32(row_max(e));
  float ls = 0.f, lx = 0.f;
  const int pend = max(prev - FBS * (RTS - RA2) * 16 / TAUK, 0);
  prev = max(pend - FBS * RA2 * 16 / TAUK, 0);
#pragma unroll
  for (int k = 0; k < 4; ++k) {
    const float l = a[k][0] == n[k] + rd ? (float)min(a[k][3] + (rd ? 0u : min(a[k][2] - first, 300u)), 2000u) : (float)-pend;
    ls += l; lx = fmaxf(lx, l);
  }
  ls = wave_sum(ls) * (1.0f / NWG); lx = max_x16_32(row_max(lx));
  const int extra = __builtin_amdgcn_readfirstlane(min(max((int)(ls + lx * (FBX / 100.0f)) - (rd ? FB1 + (int)(NRD - rd) * FBD : FB0), 0), 600));
  if (rd != 0u && __all(ls * NWG < HOLD)) return true;
  first = (first & 0xffffu) | ((unsigned)(slow + extra) << 16);
  prev += extra;
  st128(rs, WS_HR + lane * 8, u32x4{a[0][1] + rd, first, a[1][1] + rd, first});
  st128(rs, WS_HR + lane * 8 + 4, u32x4{a[2][1] + rd, first, a[3][1] + rd, first});
  return true;
}

typedef __attribute__((address_space(3))) unsigned lds_u32;
__device__ __forceinline__ unsigned lds_add(lds_u32* p, int lane) {
  unsigned k = 0;
  if (lane == 0) k = __hip_atomic_fetch_add(p, 1u, __ATOMIC_RELAXED, __HIP_MEMORY_SCOPE_WORKGROUP);
  return __builtin_amdgcn_readfirstlane(k);
}
#define CFENCE() asm volatile("" ::: "memory")
__device__ __forceinline__ int arrive(lds_u32* p) {
  int v;
  unsigned long long keep;
  asm volatile("s_mov_b64 %1, exec\n\ts_mov_b64 exec, 1\n\tds_add_rtn_u32 %0, %2, %3\n\ts_mov_b64 exec, %1"
               : "=&v"(v), "=&s"(keep) : "v"((unsigned)(size_t)p), "v"(1u) : "memory");
  return v;
}
__device__ __forceinline__ void bump(lds_u32* p) {
  unsigned long long keep;
  asm volatile("s_mov_b64 %0, exec\n\ts_mov_b64 exec, 1\n\tds_add_u32 %1, %2\n\ts_mov_b64 exec, %0" : "=&s"(keep) : "v"((unsigned)(size_t)p), "v"(1u) : "memory");
}
__device__ __forceinline__ int arrived(int v) {
  asm volatile("s_waitcnt lgkmcnt(0)" : "+v"(v) :: "memory");
  return __builtin_amdgcn_readfirstlane(v);
}
__device__ __forceinline__ unsigned lds_get(lds_u32* p) {
  return __builtin_amdgcn_readfirstlane(__hip_atomic_load(p, __ATOMIC_RELAXED, __HIP_MEMORY_SCOPE_WORKGROUP));
}
template <int SLEEP> __device__ __forceinline__ void lds_wait(lds_u32* p, unsigned n) {
#pragma unroll 1
  for (unsigned spin = 0; spin < (SPIN_CAP << 4); ++spin) {
    if (lds_get(p) >= n) break;
    __builtin_amdgcn_s_sleep(SLEEP);
  }
  asm volatile("" ::: "memory");
}
__device__ __forceinline__ unsigned clk() { return (unsigned)__builtin_amdgcn_s_memrealtime(); }
struct Table {
  unsigned t0; int was, moved, slowed, folded;
  u32x2 told;
  unsigned mine, tmp;
  template <int K> __device__ __forceinline__ void put(int v) { asm volatile("v_mov_b32 %0, %2\n\tds_write_b32 %1, %0 offset:%3" : "+v"(tmp), "+v"(mine) : "s"(v), "n"(K * 4) : "memory"); }
  __device__ __forceinline__ int gate(int due, int sh = 0) {
    const int fold = __builtin_amdgcn_readfirstlane(told[1]);
    if (fold != folded) { const int f = min(fold - folded, moved); moved -= f; slowed += f; folded = fold; put<0>(moved); }
    const int left = (int)__builtin_amdgcn_readfirstlane(told[0]) - slowed, d = max(min(left, FBS), left < -GBX ? -GBF : -GBS);
    t0 += d; was -= d; slowed += d;
    int late = (int)(clk() - t0) - due;
    if (late - was > JUMP) { t0 += late - was; moved += late - was; late = was; put<0>(moved); }
    const int lag = max(late - max(left - d, 0) - LAGOFF, 0);
    put<4>(lag);
    was = late;
#pragma unroll 1
    while (late < 0) { __builtin_amdgcn_s_sleep(1); late = (int)(clk() - t0) - due; }
    return lag < (DEEP1 >> sh) ? 0 : (lag < (DEEP2 >> sh) ? 1 : 2);
  }
  __device__ __forceinline__ void listen(lds_u32* slow) {
    told = __builtin_bit_cast(u32x2, __hip_atomic_load((__attribute__((address_space(3))) unsigned long long*)slow, __ATOMIC_RELAXED, __HIP_MEMORY_SCOPE_WORKGROUP));
  }
  __device__ __forceinline__ void agree(lds_u32* all) {
    if (moved == 0) return;
    const int m = (int)min(min(lds_get(all), lds_get(all + 1)), min(lds_get(all + 2), lds_get(all + 3))) + 30;
    if (m < moved) { t0 -= moved - m; moved = m; was = 1 << 20; put<0>(moved); }
  }
};
__device__ __forceinline__ void until(unsigned t0, int due) {
#pragma unroll 1
  while ((int)(clk() - t0) < due) __builtin_amdgcn_s_sleep(1);
}
__device__ __forceinline__ void pause(unsigned t0, unsigned due) {
#pragma unroll 1
  while (clk() - t0 < due) __builtin_amdgcn_s_sleep(16);
}
#define LGKM0() asm volatile("s_waitcnt lgkmcnt(0)" ::: "memory")

__device__ __forceinline__ void split4(f32x4 v, u32x2& hi, u32x2& lo) {
  unsigned h[4], r[4];
#pragma unroll
  for (int i = 0; i < 4; ++i) { h[i] = __float_as_uint(v[i]) & 0xffff0000u; r[i] = __float_as_uint(v[i] - __uint_as_float(h[i])); }
  hi = u32x2{__builtin_amdgcn_perm(h[1], h[0], 0x07060302u), __builtin_amdgcn_perm(h[3], h[2], 0x07060302u)};
  lo = u32x2{__builtin_amdgcn_perm(r[1], r[0], 0x07060302u), __builtin_amdgcn_perm(r[3], r[2], 0x07060302u)};
}
__device__ __forceinline__ u32x4 split8(f32x4 v, bool odd) {
  u32x2 hi, lo;
  split4(v, hi, lo);
  const unsigned n0 = __float_as_uint(dpp<DPP_XOR1>(__uint_as_float(odd ? hi[0] : lo[0]))), n1 = __float_as_uint(dpp<DPP_XOR1>(__uint_as_float(odd ? hi[1] : lo[1])));
  return u32x4{max(odd ? n0 : hi[0], 1u), max(odd ? n1 : hi[1], 1u), max(odd ? lo[0] : n0, 1u), max(odd ? lo[1] : n1, 1u)};
}
__device__ __forceinline__ u32x4 q_pack(u32x4 a, u32x4 b) {
  u32x2 hi, lo;
  split4((__builtin_bit_cast(f32x4, a) + __builtin_bit_cast(f32x4, b)) * QSCALE, hi, lo);
  return u32x4{hi[0], lo[0], hi[1], lo[1]};
}
__device__ __forceinline__ f32x4 mfma16(u32x4 a, u32x4 b, f32x4 c) {
  return __builtin_amdgcn_mfma_f32_16x16x32_bf16(__builtin_bit_cast(bf16x8, a), __builtin_bit_cast(bf16x8, b), c, 0, 0, 0);
}

constexpr int NW = 8, NMW = 4;
constexpr int NLP = 5, NRP = 8;
constexpr int NTP = 4 * (NLP + NRP);
constexpr int REG = NLP * 6 * 1024;
constexpr int NRING = 4;
constexpr int NS = 6, NA = NS - 1;
__device__ __forceinline__ int seg_beg(int s) { return s >= NS ? NPAIR : ((0xC86540 >> (4 * s)) & 15) * 16; }
__device__ __forceinline__ int seg_k(int t) { return (int)(0x5433222211110000ull >> (4 * (t >> 4))) & 15; }
__device__ __forceinline__ int seg_of_k(int k) { return (0x123540 >> (4 * k)) & 15; }
__device__ __forceinline__ int k_of_seg(int s) { return (0x213450 >> (4 * s)) & 15; }
__device__ __forceinline__ int pair_of(int t) { return t < 64 ? t : (t < 192 ? t + 64 : t - (t < 224 ? 96 : (t < 240 ? 144 : 176))); }
constexpr int OFF_CNT = 0, OFF_REG = 256,
              OFF_SC = OFF_REG + 4 * REG, OFF_BUF = OFF_SC + NPAIR * 16 * 4, OFF_Q = OFF_BUF + NRING * NMW * DH * 4, OFF_ML = OFF_Q + NS * DH * 4,
              OFF_RED = OFF_ML + 128, OFF_END = OFF_RED + 4 * 256 * 4;
enum { F_REL = 0, F_DONE, C_LAST, C_T = 3, F_OUT, C_P, C_K, F_P, F_Q = 8, F_FREE, S_FIRST, F_Q2, C_W = 12, C_SC = 16, C_PART = 24, S_ECHO = 32, S_BURST = 40, S_LATE = 42, S_MOVED = 48, S_SLOW = 56 };
__device__ __forceinline__ int due_k(int p) { return TSTART + ((p * TAUK) >> 6); }
__device__ __forceinline__ int due_v(int p) { return TSTART + OGAP + ((NPAIR * TAUK + p * TAUV + max(p - NPAIR / 2, 0) * (VGAP * 64 / (NPAIR / 2))) >> 6); }
constexpr int AW_LD = DH * 2 + 16, AG_LD = 512 + 16;

__global__ void __launch_bounds__(NW * 64, 2) fused_kernel(const bf16* __restrict__ x, const bf16* __restrict__ kc,
                                                        const bf16* __restrict__ vc, const bf16* __restrict__ wq,
                                                        const bf16* __restrict__ wo, const bf16* __restrict__ wr,
                                                        const bf16* __restrict__ wg, const bf16* __restrict__ wu,
                                                        const bf16* __restrict__ wd, float* ws, bf16* __restrict__ out) {
  __shared__ __attribute__((aligned(16))) unsigned char smem[OFF_END];
  float* s_sc = reinterpret_cast<float*>(smem + OFF_SC);
  float* s_buf = reinterpret_cast<float*>(smem + OFF_BUF);
  float* s_q = reinterpret_cast<float*>(smem + OFF_Q);
  u32x4* s_ml = reinterpret_cast<u32x4*>(smem + OFF_ML);
  lds_u32* s_c = (lds_u32*)(smem + OFF_CNT);

  const int tid = threadIdx.x, lane = tid & 63, r = lane >> 3, j = lane & 7;
  const int wave = __builtin_amdgcn_readfirstlane(tid >> 6);
  const int tw = wave - NMW;
  const int bid = blockIdx.x, xl = bid & 7, idx = bid >> 3;
  const int g = (xl * 32 + idx) / GS, mm = idx % GS;
  const __amdgpu_buffer_rsrc_t rs = __builtin_amdgcn_make_buffer_rsrc(ws, 0, WS_END * 4, 0x00020000);
  const int ro = ((idx & 3) << 3) | (idx >> 2);
  const bool isW = ro < 10, isG = ro >= 10 && ro < 16;
  const int w_ks = xl, w_ct = ro;
  const int g_cg = xl * 6 + (ro - 10);
  const int d_ct = (xl + ((ro - 16) >> 2) * 8) * 2 + (((ro - 16) >> 1) & 1);
  const bool isD = ro >= 16 && ro < 24 && (ro & 1) == 0 && d_ct < DM / 32;
  const bool isH = ro == 22 && xl >= 2 && xl < 6;
  const bool isC = (mm & 7) == 3 || (mm & 7) >= 6;
  const int c_it = (mm >> 3) * 3 + max((mm & 7) - 5, 0);
  const int qf = WS_QF + g * QFS;
  const char* kb = reinterpret_cast<const char*>(kc);
  const char* vb = reinterpret_cast<const char*>(vc);
  const unsigned lo = (r * 24 + j) * 16;
  const int xb = g * GS + mm, c0 = mm * 8 + g * ROT;
  const int b2 = (2 * NH + (xb >> 4)) * 1024 + (xb & 15) * 8, b3 = (2 * NH + 16 + (xb >> 5)) * 1024 + (xb & 31) * 8;
  auto poff = [&](int p) -> unsigned {
    const int turn = (p & 63) >> 2;
    int t = (row_of(p >> 6) * NH + g) * 1024 + ((turn * 64 + (p & 3) * 2 + c0) & 1023);
    if ((p >> 6) == 1) t = (turn >= 8 ? b2 + (turn & 7) * 128 : b3 + (~turn & 4) * 2048 + (turn & 3) * 256) + (p & 3) * 2;
    return (unsigned)t * 3072u;
  };
  const unsigned lds_base = (unsigned)(size_t)(__attribute__((address_space(3))) unsigned char*)smem + OFF_REG + tw * REG;
  auto merged = [&](int it, int ln) -> f32x4 {
    const float* src = s_buf + (it % NRING) * (NMW * DH) + ((ln >> 1) & 7) * 24 + (ln >> 4) * 8 + (ln & 1) * 4;
    return (*reinterpret_cast<const f32x4*>(src) + *reinterpret_cast<const f32x4*>(src + DH)) +
           (*reinterpret_cast<const f32x4*>(src + 2 * DH) + *reinterpret_cast<const f32x4*>(src + 3 * DH));
  };
#define NONCE() do { unsigned n_ = __builtin_amdgcn_readfirstlane(f) + 1u; while (__any(f == n_)) ++n_; \
    if (lane < 32) st32(rs, qf + (GS + mm) * 32 + lane, n_); f = n_; } while (0)
  unsigned long long pc;
  asm volatile("s_getpc_b64 %0" : "=s"(pc));
  unsigned r0 = 0u;
  unsigned t0 = clk();
  unsigned tb0 = t0;
  if (wave < NMW) {
    Tile ta, tb, tc, td;
    const int c = lane & (GS - 1);
    unsigned f = 0u;
    if (wave == 3) f = ld32(rs, qf + c * 32 + mm);
    if (mm < NPROD) {
      const int hp = g + (mm / 6) * NG, ck = (mm % 6) >> 1, hf = mm & 1, jp = hf * 4 + wave;
      const int bb = lane >> 4, t = lane & 15;
      const u32x4* xp = reinterpret_cast<const u32x4*>(x + row_of(bb) * DM);
      u32x4 xv[5], xs = {0u, 0u, 0u, 0u};
#pragma unroll
      for (int k = 0; k < 5; ++k) xv[k] = xp[t + 16 * k];
      if (t < 10) xs = xp[jp * 10 + t];
      const u32x4* wp = reinterpret_cast<const u32x4*>(wq + (size_t)(jp * 80 + r) * HD + hp * DH + ck * 64) + j;
      u32x4 wv[10];
#pragma unroll
      for (int k = 0; k < 10; ++k) wv[k] = __builtin_nontemporal_load(wp + (size_t)k * 8 * (HD / 8));
      if (wave == 0) s_c[lane] = lane == C_K ? NTP + NMW : 0u;
      MEMFENCE(); LGKM0(); __builtin_amdgcn_s_barrier(); MEMFENCE();
      if (wave == 3) { NONCE(); }
      float sm = 0.f;
#pragma unroll
      for (int k = 0; k < 5; ++k)
#pragma unroll
        for (int d = 0; d < 4; ++d) sm += bf_lo(xv[k][d]) + bf_hi(xv[k][d]);
      const float mean = row_sum(sm) * (1.0f / DM);
      float sv = 0.f;
#pragma unroll
      for (int k = 0; k < 5; ++k)
#pragma unroll
        for (int d = 0; d < 4; ++d) { const float a = bf_lo(xv[k][d]) - mean, c2 = bf_hi(xv[k][d]) - mean; sv += a * a + c2 * c2; }
      const float rstd = 1.0f / sqrtf(row_sum(sv) * (1.0f / DM) + LN_EPS);
      float* s_xn = s_buf + wave * 320;
      if (t < 10) {
#pragma unroll
        for (int d = 0; d < 4; ++d) {
          s_xn[(t * 8 + 2 * d) * 4 + bb] = (bf_lo(xs[d]) - mean) * rstd;
          s_xn[(t * 8 + 2 * d + 1) * 4 + bb] = (bf_hi(xs[d]) - mean) * rstd;
        }
      }
      LGKM0();
      f32x2 qa[16];
#pragma unroll
      for (int i = 0; i < 16; ++i) qa[i] = f32x2{0.f, 0.f};
#pragma unroll
      for (int k = 0; k < 10; ++k) {
        const f32x4 xn = *reinterpret_cast<const f32x4*>(&s_xn[(k * 8 + r) * 4]);
#pragma unroll
        for (int d = 0; d < 4; ++d) {
          const f32x2 w2 = bf_pair(wv[k][d]);
#pragma unroll
          for (int e = 0; e < 4; ++e) qa[e * 4 + d] = fma2(xn[e], w2, qa[e * 4 + d]);
        }
      }
      float t16[16], t8[8];
#pragma unroll
      for (int i = 0; i < 16; ++i) t16[i] = tsum32(qa[i >> 1][i & 1], qa[8 + (i >> 1)][i & 1]);
#pragma unroll
      for (int i = 0; i < 8; ++i) t8[i] = tsum16(t16[i], t16[i + 8]);
#pragma unroll
      for (int i = 0; i < 8; ++i) t8[i] += dpp<DPP_ROR8>(t8[i]);
      const bool up = (lane & 8) != 0;
      f32x4* r4 = reinterpret_cast<f32x4*>(smem + OFF_RED);
      r4[wave * 64 + lane] = f32x4{up ? t8[4] : t8[0], up ? t8[5] : t8[1], up ? t8[6] : t8[2], up ? t8[7] : t8[3]};
      LGKM0();
      if (lds_add(s_c + C_P, lane) == NMW - 1) {
        asm volatile("" ::: "memory");
        const f32x4 o4 = (r4[lane] + r4[64 + lane]) + (r4[128 + lane] + r4[192 + lane]);
        st128nt(rs, WS_QP + ((hp * 2 + hf) * NB + bb) * DH + ck * 64 + j * 8 + (up ? 4 : 0), __builtin_bit_cast(u32x4, o4));
        VMCNT(0);
        if (lane == 0) s_c[F_OUT] = 1u;
      }
    } else {
      if (wave == 0) s_c[lane] = lane == C_K ? NTP + NMW : 0u;
      MEMFENCE(); LGKM0(); __builtin_amdgcn_s_barrier(); MEMFENCE();
      if (wave == 3) { NONCE(); }
      else {
        typedef __attribute__((address_space(1))) const unsigned g_u32;
        typedef __attribute__((address_space(1))) const char g_char;
        const int id = ((idx >> 3) * 2 + (mm & 1)) * 3 + wave;
        if (lane < CODE / (24 * 64)) {
          const unsigned v = *(g_u32*)((pc & ~255ull) + id * (CODE / 24) + lane * 64);
          asm volatile("" :: "v"(v));
        }
        if (lane < 7 && id < 21) {
          const int st = id < 8 ? (0x1320 >> ((id >> 1) * 4)) & 3 : (id >> 1) & 3, hf = id & 1;
          int head = row_of(st) * NH + xl * 4 + hf * 2;
          unsigned off = min(lane << 20, (6 << 20) - 64);
          if (st == 1) {
            head = 2 * NH + (hf ? 16 + xl : 2 * xl);
            if (hf) off = lane < 4 ? min(lane << 20, (3 << 20) - 64) : 8 * (3 << 20) + min((lane - 4) * (3 << 19), (3 << 20) - 64);
          }
          g_char* p = (g_char*)(id < 8 ? kb : vb) + (size_t)head * (3 << 20) + off;
          if (id >= 16) p = (g_char*)(id == 16 ? wo + xl * 4 * DH * DM : (id == 17 ? wg : (id == 18 ? wu : wd))) + min(lane * (id == 16 ? 163840 : 327680), (id == 16 ? 983040 : 1966080) - 64);
          if (id >= 20) p = (g_char*)(lane ? wr : out);
          until(t0, TPT + id * TPS);
          const unsigned v = __builtin_nontemporal_load((g_u32*)p);
          asm volatile("" :: "v"(v));
        }
      }
    }
    unsigned tk_e = poff(pair_of(lane * 4)) | (pair_of(lane * 4) >> 2) | (seg_k(lane * 4) << 6), tv_o = poff(lane * 4 + wave);
    int tv_d = due_v(lane * 4 + wave);
    asm volatile("" : "+v"(tk_e), "+v"(tv_o), "+v"(tv_d));
    MEMFENCE();
    if (wave == 3) {
      __builtin_amdgcn_s_setprio(3);
      const unsigned nonce = f;
      unsigned echoed = 0u;
      if (mm < NPROD) {
        echoed = ld32(rs, qf + (GS + c) * 32);
        asm volatile("" : "+v"(echoed) :: "memory");
        lds_wait<1>(s_c + F_OUT, 1u);
        if (lane < 32) st32(rs, qf + mm * 32 + lane, echoed);
#pragma unroll 1
        while ((int)(clk() - t0) < QWAIT - QBACK) {
          const unsigned nv = ld32(rs, qf + (GS + c) * 32);
          if (__any(nv != echoed)) {
            if (lane < 32) st32(rs, qf + mm * 32 + lane, nv);
            echoed = nv;
          }
          __builtin_amdgcn_s_sleep(4);
        }
      }
      until(t0, QWAIT);
#pragma unroll 1
      for (unsigned spin = 0; spin < SPIN_CAP; ++spin) {
        const unsigned nv = ld32(rs, qf + (GS + c) * 32);
        f = ld32(rs, qf + c * 32 + mm);
        const bool done = __all(c == mm || c >= NPROD || f == nonce);
        if (mm < NPROD && __any(nv != echoed)) {
          if (lane < 32) st32(rs, qf + mm * 32 + lane, nv);
          echoed = nv;
        }
        if (done) break;
        __builtin_amdgcn_s_sleep(8);
      }
      if (lane < GS) s_c[S_ECHO + lane] = echoed;
      if (lane < DH / 4) {
        u32x4 p[3][2];
#pragma unroll
        for (int it = 0; it < 3; ++it)
#pragma unroll
          for (int k = 0; k < 2; ++k) p[it][k] = ld128nt(rs, WS_QP + ((g * 2 + k) * NB + (it ? it + 1 : 0)) * DH + lane * 4);
#pragma unroll
        for (int it = 0; it < 3; ++it) {
          *reinterpret_cast<u32x4*>(&s_q[(it ? it + 3 : 0) * DH + lane * 4]) = q_pack(p[it][0], p[it][1]);
          if (it != 1) { LGKM0(); if (lane == 0) s_c[F_Q] = it + 1; MEMFENCE(); }
        }
      }
    }
    __builtin_amdgcn_s_setprio(3);
    until(t0, due_k(NTP + wave));
    tb0 = lds_get(s_c + S_LATE);
    if ((unsigned)(tb0 - t0 + 100u) > 500u) tb0 = t0;
    until(tb0, due_k(NTP + wave));
    Table tt = {tb0, 1 << 20, 0, 0, 0, {0u, 0u}, (unsigned)(size_t)(s_c + S_MOVED + wave), 0u};
    bool common = false;
    tile_load(ta, kb, poff(NTP + wave) + lo, 0);
    tile_load(tb, kb, poff(NTP + wave) + lo, 1);
    MEMFENCE();
    {
      u32x2 q[12];
      int cp = NTP + wave, it = 0, nxt = lds_add(s_c + C_K, lane), passed = 0, ci = -1;
      auto kuse = [&](Tile& xa, Tile& xb_) __attribute__((always_inline)) {
        if (it != ci) {
          LGKM0();
          for (; passed < it; ++passed) bump(s_c + C_SC + passed);
          lds_wait<1>(s_c + (it < 3 ? F_Q : F_Q2), min(it + 1, 3));
          q_load(q, s_q + seg_of_k(it) * DH, j);
          if (!common) { const unsigned d = lds_get(s_c + S_FIRST); if (d) { tt.t0 -= d - 1u; common = true; tt.was = 1 << 20; } }
          tt.agree(s_c + S_MOVED);
          ci = it;
        }
        const int tk = arrive(s_c + C_K);
        tt.listen(s_c + S_SLOW + 2 * wave);
        pair_put(tile_score(xa, q), tile_score(xb_, q), s_sc + cp * 16 + r, j);
        MEMFENCE();
        return arrived(tk);
      };
      auto kstep = [&](Tile& xa, Tile& xb_, Tile& ya, Tile& yb) __attribute__((always_inline)) {
        const unsigned e = __builtin_amdgcn_readlane(tk_e, nxt >> 2);
        MEMFENCE();
        const int deep = tt.gate(due_k(nxt));
        MEMFENCE();
        pair_issue(ya, yb, kb, (e & ~8191u) + (nxt & 3) * 6144 + lo, deep);
        const int n2 = kuse(xa, xb_);
        cp = (e & 63) * 4 + (nxt & 3); it = (e >> 6) & 7; nxt = n2;
      };
      bool swap;
      for (;;) {
        if (nxt >= NPAIR) { swap = false; break; }
        kstep(ta, tb, tc, td);
        if (nxt >= NPAIR) { swap = true; break; }
        kstep(tc, td, ta, tb);
      }
      tt.gate(due_k(NPAIR + wave));
      MEMFENCE();
      const unsigned* pw = reinterpret_cast<const unsigned*>(wo + (xl * 128 + idx * 4 + wave) * 3840) + lane * 16;
      const unsigned v0 = pw[0], v1 = pw[56 * 16];
      MEMFENCE();
      if (swap) { ta = tc; tb = td; }
      kuse(ta, tb);
      for (; passed < NS; ++passed) bump(s_c + C_SC + passed);
      tt.gate(due_v(wave));
      MEMFENCE();
      pair_issue(ta, tb, vb, __builtin_amdgcn_readlane(tv_o, 0) + lo, 2);
      asm volatile("" :: "v"(v0), "v"(v1));
    }
    const unsigned sc_r = (unsigned)(size_t)(__attribute__((address_space(3))) float*)s_sc + r * 4;
    f32x2 acc[12];
    auto vstep = [&](Tile& xa, Tile& xb_, Tile& ya, Tile& yb, int p, bool fin) __attribute__((always_inline)) {
      const unsigned off = __builtin_amdgcn_readlane(tv_o, (p >> 2) + 1) + lo;
      MEMFENCE();
      const int deep = tt.gate(__builtin_amdgcn_readlane(tv_d, (p >> 2) + 1), p >= NPAIR - ENDT * NMW ? ENDS : 0);
      float pa, pb;
      asm volatile("ds_read_b32 %0, %2\n\tds_read_b32 %1, %2 offset:32" : "=&v"(pa), "=&v"(pb) : "v"(sc_r + p * 64) : "memory");
      MEMFENCE();
      pair_issue(ya, yb, vb, off, deep);
      asm volatile("s_waitcnt lgkmcnt(0)" : "+v"(pa), "+v"(pb) :: "memory");
      if (!fin) tt.listen(s_c + S_SLOW + 2 * wave);
      else bump(s_c + C_LAST);
      pv_last(pa, xa, acc);
      pv_last(pb, xb_, acc);
      MEMFENCE();
    };
    typedef __attribute__((address_space(3))) float lds_f32;
    auto fold8 = [&](lds_f32* dst) __attribute__((always_inline)) {
      float t12[12], t6[6];
#pragma unroll
      for (int i = 0; i < 12; ++i) t12[i] = tsum32(acc[i >> 1][i & 1], acc[6 + (i >> 1)][i & 1]);
#pragma unroll
      for (int i = 0; i < 6; ++i) t6[i] = tsum16(t12[i], t12[i + 6]);
#pragma unroll
      for (int i = 0; i < 6; ++i) t6[i] += dpp<DPP_ROR8>(t6[i]);
      auto d2 = (__attribute__((address_space(3))) f32x2*)dst;
      d2[0] = f32x2{t6[0], t6[1]}; d2[1] = f32x2{t6[2], t6[3]}; d2[2] = f32x2{t6[4], t6[5]};
      CFENCE();
    };
    lds_f32* dst = (lds_f32*)s_buf + wave * DH + j * 24 + (lane >> 4) * 6;
#pragma unroll 1
    for (int it = 0;; ++it) {
      lds_wait<1>(s_c + F_P, k_of_seg(it) + 1);
      if (!common) { const unsigned d = lds_get(s_c + S_FIRST); if (d) { tt.t0 -= d - 1u; common = true; tt.was = 1 << 20; } }
      tt.agree(s_c + S_MOVED);
#pragma unroll
      for (int i = 0; i < 12; ++i) acc[i] = f32x2{0.f, 0.f};
#pragma unroll 1
      for (int p = seg_beg(it) + wave; p < min(seg_beg(it + 1), NPAIR - 2 * NMW); p += 2 * NMW) {
        vstep(ta, tb, tc, td, p, false);
        vstep(tc, td, ta, tb, p + NMW, false);
      }
      if (it >= NRING) lds_wait<1>(s_c + F_FREE, it - NRING + 1);
      if (it == NS - 1) break;
      fold8(dst + (it % NRING) * (NMW * DH));
      bump(s_c + C_PART + it);
    }
    dst += ((NS - 1) % NRING) * (NMW * DH);
    const int ln = lane < DH / 4 ? lane : 0;
    auto src = (__attribute__((address_space(3))) f32x4*)((lds_f32*)s_buf + ((NS - 1) % NRING) * (NMW * DH) + ((ln >> 1) & 7) * 24 + (ln >> 4) * 8 + (ln & 1) * 4);
    int so = (lane < DH / 4 ? WS_PO + xb * DH + lane * 4 : WS_END) * 4;
    const int p = NPAIR - NMW + wave;
    float pa = s_sc[p * 16 + r], pb = s_sc[p * 16 + 8 + r];
    asm volatile("" : "+v"(dst), "+v"(src), "+v"(so), "+v"(pa), "+v"(pb));
    vstep(ta, tb, tc, td, NPAIR - 2 * NMW + wave, true);
    pv_last(pa, tc, acc);
    pv_last(pb, td, acc);
    fold8(dst);
    const int k = arrive(s_c + C_PART + NS - 1);
    const f32x4 m = (src[0] + src[DH / 4]) + (src[2 * DH / 4] + src[3 * DH / 4]);
    u32x4 o = {nz_bits(m[0]), nz_bits(m[1]), nz_bits(m[2]), nz_bits(m[3])};
    asm volatile("" : "+v"(o));
    if (arrived(k) == NMW - 1) {
      __builtin_amdgcn_raw_buffer_store_b128(o, rs, so, 0, AUX_SC1);
      bump(s_c + F_DONE);
    }
    return;
  }

  {
    Tile pk[NRP * 2];
    __builtin_amdgcn_s_setprio(3);
    const unsigned it0 = (row_of(0) * NH + g) * 1024, ct = c0 + tw * 2;
    auto toff = [&](int turn) -> unsigned { return (it0 + ((turn * 64 + ct) & 1023)) * 3072u + lo; };
    unsigned tbw = 0u;
#pragma unroll 1
    for (int e = 0; e < NLP * 2; ++e) {
      const char* src = kb + (toff(e >> 1) + (e & 1) * 3072);
      if (e >= 2) until(tbw, (e * 2 * TAUK) >> 6);
#pragma unroll
      for (int i = 0; i < 3; ++i) glds16_nt(src + i * 128, lds_base + (e * 3 + i) * 1024);
      if (e == 1) {
        VMCNT(3);
        tb0 = t0 + min(max((int)(clk() - t0) - ANCH, 0), 300);
        tbw = tb0 + due_k(tw) - TLEAD;
        __builtin_amdgcn_s_barrier();
        if (tw == 0 && lane == 0) s_c[S_LATE] = tb0;
      }
      VMCNT(PFD);
    }
#pragma unroll
    for (int e = 0; e < NRP * 2; ++e) {
      until(tbw, ((NLP * 2 + e) * 2 * TAUK) >> 6);
      tile_load(pk[e], kb, toff(NLP + (e >> 1)), e & 1);
      MEMFENCE();
      if (e + 1 < NRP * 2) VMCNT(PFD);
    }
    if (tw == 0) {
      r0 = ld32(rs, WS_HR + bid * 2);
      if (bid == LEADER) {
        u32x4 n;
#pragma unroll
        for (int k = 0; k < 4; ++k) n[k] = ld32(rs, WS_HA + (lane * 4 + k) * 32) + 1u;
        st128(rs, WS_HN + lane * 4, n);
      }
    }
    __builtin_amdgcn_s_setprio(0);
    lds_wait<1>(s_c + F_Q, HPG);
    u32x2 q[12];
    q_load(q, s_q, j);
    const u32x4* s_kv = reinterpret_cast<const u32x4*>(smem + OFF_REG + tw * REG) + lane;
    static_assert(NLP * 4 <= PPI && NTP <= HPG * PPI, "tail tiles: batch row 0 only");
#pragma unroll 1
    for (int e = 0; e < NLP * 2; ++e) {
      Tile tmp;
#pragma unroll
      for (int i = 0; i < 3; ++i) tmp.d[i] = s_kv[(e * 3 + i) * 64];
      score_put(tile_score(tmp, q), s_sc + ((e >> 1) * 4 + tw) * 16 + (e & 1) * 8 + r, j);
    }
#pragma unroll
    for (int e = 0; e < NRP * 2; ++e) {
      if ((NLP * 2 + e) * 2 == PPI) q_load(q, s_q + DH, j);
      score_put(tile_score(pk[e], q), s_sc + ((NLP + (e >> 1)) * 4 + tw) * 16 + (e & 1) * 8 + r, j);
      __builtin_amdgcn_sched_barrier(0);
    }
    asm volatile("s_waitcnt vmcnt(0) lgkmcnt(0)" ::: "memory");
#pragma unroll
    for (int it = 0; it * PPI < NTP; ++it) lds_add(s_c + C_SC + it, lane);
    if (tw == 0) {
      until((t0 + ZT + 511u) & ~511u, 0);
      const u32x4 z = {0u, 0u, 0u, 0u};
      st128(rs, lane < 48 ? WS_PO + xb * DH + lane * 4 : WS_PML + xb * 64 + (lane - 48) * 4, z);
      const int o = isW ? WS_SLAB + w_ks * NB * DM + w_ct * 64 : (isG ? WS_ACT + g_cg * 128 : (isH ? WS_H + (xl - 2) * 768 : WS_OC + (c_it * NG + g) * DH));
      st128(rs, lane < (isG ? 32 : (isC ? 48 : (isW || isH ? 64 : 0))) ? o + (isW ? (lane >> 4) * DM + (lane & 15) * 4 : lane * 4) : WS_END, z);
      if (isH) { st128(rs, o + 256 + lane * 4, z); st128(rs, o + 512 + lane * 4, z); }
      VMCNT(0);
    }
  }

  const int mr = lane & 15, qd = lane >> 4;
  unsigned char* reg = smem + OFF_REG + tw * REG;
  float* red = reinterpret_cast<float*>(smem + OFF_RED);
#define PERM_LO(hi_, lo_) __builtin_amdgcn_perm(hi_, lo_, 0x05040100u)
#define PERM_HI(hi_, lo_) __builtin_amdgcn_perm(hi_, lo_, 0x07060302u)
  int served = 0;
  auto done = [&](int n) {
    if (tw == 3 && served < min(n, NA)) {
      const int from = served;
      int ln = lane;
      asm volatile("" : "+v"(ln));
#pragma unroll 1
      for (; served < min(n, NA); ++served) {
        lds_wait<8>(s_c + C_PART + served, NMW);
        const f32x4 o = merged(served, ln < DH / 4 ? ln : 0);
        const float am = max_x16_32(row_max(fmaxf(fmaxf(fabsf(o[0]), fabsf(o[1])), fmaxf(fabsf(o[2]), fabsf(o[3])))));
        const float inv = am > 1e-30f ? 32767.0f / am : 0.f;
        const int q0 = __float2int_rn(o[0] * inv), q1 = __float2int_rn(o[1] * inv), q2 = __float2int_rn(o[2] * inv), q3 = __float2int_rn(o[3] * inv);
        if (ln < DH / 4) reinterpret_cast<u32x2*>(s_sc + served * (DH / 2))[ln] = u32x2{PERM_LO((unsigned)q1, (unsigned)q0), PERM_LO((unsigned)q3, (unsigned)q2)};
        const bool nan = __any((o[0] + o[1]) + (o[2] + o[3]) != (o[0] + o[1]) + (o[2] + o[3]));
        if (ln == 0) s_ml[served][2] = nan ? 0x7fc00000u : nz_bits(am * (1.0f / 32767.0f));
        LGKM0();
        if (ln == 0) s_c[F_FREE] = served + 1;
      }
#pragma unroll 1
      for (int c = from * 24 + ln; c < served * 24; c += 64) st128nt(rs, WS_PQ + xb * 5 * (DH / 2) + c * 4, reinterpret_cast<const u32x4*>(s_sc)[c]);
      VMCNT(0);
      if (ln < 8) st128(rs, WS_PML + xb * 64 + (from ? 32 : 0) + ln * 4, s_ml[ln]);
      if (ln == 0) s_c[S_BURST + (from ? 1 : 0)] = clk();
      CFENCE();
      if (ln == 0) __hip_atomic_fetch_add(s_c + F_DONE, (unsigned)(served - from), __ATOMIC_RELAXED, __HIP_MEMORY_SCOPE_WORKGROUP);
    }
    if (n > NA) lds_wait<1>(s_c + F_DONE, n); else lds_wait<8>(s_c + F_DONE, n);
  };
  auto done_all = [&]() {
#pragma unroll 1
    for (int n = 4; n <= NA; ++n) done(n);
  };
  const int lp = (lane < DH / 4 ? lane : 0) * 4;
  auto weight = [&](int it, int x0, int cnt) -> float {
    const int mlw = WS_PML + (x0 + (lane & (cnt - 1))) * 64 + (it < 4 ? 0 : 32) + it * 4;
    u32x4 ml;
#pragma unroll 1
    for (unsigned spin = 0; spin < SPIN_CAP; ++spin) {
      ml = ld128(rs, mlw);
      if (__all(min4(ml) != 0u)) break;
      __builtin_amdgcn_s_sleep(16);
    }
    const float mk = __uint_as_float(ml[0]);
    const float e = __builtin_amdgcn_exp2f(mk - max_x16_32(row_max(mk)));
    return e / (wave_sum(e * __uint_as_float(ml[1])) * ((float)cnt * (1.0f / 64.0f))) * __uint_as_float(ml[2]);
  };
  const unsigned tok = __builtin_amdgcn_readfirstlane(r0) + 1u;
  unsigned echoed = 0u, fold = 0u;
  int slow = 0, prev = 0;
  auto lag = [&]() {
    const unsigned v = __hip_atomic_load(s_c + S_MOVED + (lane & 7), __ATOMIC_RELAXED, __HIP_MEMORY_SCOPE_WORKGROUP);
    fold += v;
    unsigned a = 0u;
#pragma unroll
    for (int k = 0; k < 8; ++k) a += __builtin_amdgcn_readlane(v, k);
    return a >> 2;
  };
  auto tell = [&]() { if (lane < 4) { s_c[S_SLOW + 2 * lane] = slow; s_c[S_SLOW + 2 * lane + 1] = fold; } };
  auto round = [&](unsigned rd, bool lead) {
    const int tr = RT0 + (int)(rd - 1) * RTS;
    until(t0, tr);
    const unsigned bh = lag();
    if (bh > RMIN && lane < 8) st128(rs, WS_HA + bid * 32 + lane * 4, u32x4{echoed + rd, tok, tb0, bh});
    if (lead) { until(t0, tr + RA1); leader_release(rs, lane, tb0, rd, slow, prev); }
    until(t0, tr + RA2);
    const u32x2 rv = ld64(rs, WS_HR + bid * 2);
    slow = (int)__builtin_amdgcn_readfirstlane(rv[1]) >> 16;
#pragma unroll 1
    for (unsigned k = min(__builtin_amdgcn_readfirstlane(rv[0]) - tok, rd) + 1u; k <= rd; ++k) slow = slow > 0 ? max(slow - GB, 0) : slow - (k <= PRD ? PRB : 0);
    tell();
  };
  auto prep = [&](bool lead) {
    if (tw == 0) {
      const int c = lane & (GS - 1);
      unsigned qe = s_c[S_ECHO + c];
      bool first = true, released = false, sent = false;
#pragma unroll 1
      while ((int)(clk() - t0) < HST) {
        const unsigned qn = ld32(rs, qf + (GS + c) * 32);
        if (mm < NPROD && __any(qn != qe)) {
          if (lane < 32) st32(rs, qf + mm * 32 + lane, qn);
          qe = qn;
        }
        __builtin_amdgcn_s_sleep(64);
      }
      const unsigned behind = lag();
#pragma unroll 1
      for (unsigned spin = 0; spin < SPIN_CAP && !released; ++spin) {
        const unsigned qn = ld32(rs, qf + (GS + c) * 32);
        const u32x2 rv = ld64(rs, WS_HR + bid * 2);
        const unsigned nv = __builtin_amdgcn_readfirstlane(ld32(rs, WS_HN + bid));
        if (__builtin_amdgcn_readfirstlane(rv[0]) - tok <= NRD) {
          released = true;
          slow = (int)__builtin_amdgcn_readfirstlane(rv[1]) >> 16;
          if (lane == 0) { s_c[S_FIRST] = min((tb0 - rv[1]) & 0xffffu, 300u) + 1u; }
        } else {
          if (mm < NPROD && __any(qn != qe)) {
            if (lane < 32) st32(rs, qf + mm * 32 + lane, qn);
            qe = qn;
          }
          if (first || nv != echoed) {
            echoed = nv; first = false;
            if (lane < 8) st128(rs, WS_HA + bid * 32 + lane * 4, u32x4{echoed, tok, tb0, behind});
          }
          if (lead && !sent) sent = leader_release(rs, lane, tb0, 0u, slow, prev);
          __builtin_amdgcn_s_sleep(32);
        }
      }
      tell();
      if (lane == 0) s_c[F_REL] = 1u;
      if (lane < DH / 4) {
        u32x4 p[3][2];
#pragma unroll
        for (int e = 0; e < 3; ++e)
#pragma unroll
          for (int k = 0; k < 2; ++k) p[e][k] = ld128nt(rs, WS_QP + (((e < 2 ? 24 - e * 8 + (xb >> 5) : xb >> 4) * 2 + k) * NB + 1) * DH + lane * 4);
#pragma unroll
        for (int e = 0; e < 3; ++e) *reinterpret_cast<u32x4*>(&s_q[(1 + e) * DH + lane * 4]) = q_pack(p[e][0], p[e][1]);
      }
      LGKM0();
      if (lane == 0) s_c[F_Q2] = 3u;
#pragma unroll 1
      for (unsigned rd = 1; rd <= NRD; ++rd) round(rd, lead);
    }
    if (tw == 3) {
#pragma unroll 1
      for (int ik = 0; ik < NS; ++ik) {
        const int it = seg_of_k(ik), nv = (seg_beg(it + 1) - seg_beg(it)) >> 4;
        lds_wait<4>(s_c + C_SC + ik, ik == 0 ? NW : NMW);
        f32x4* sp = reinterpret_cast<f32x4*>(s_sc + seg_beg(it) * 16) + lane;
        f32x4 v[4];
#pragma unroll
        for (int k = 0; k < 4; ++k) v[k] = sp[(k & (nv - 1)) * 64];
        const f32x4 mx = __builtin_elementwise_max(__builtin_elementwise_max(v[0], v[1]), __builtin_elementwise_max(v[2], v[3]));
        const float m = max_x16_32(row_max(fmaxf(fmaxf(mx[0], mx[1]), fmaxf(mx[2], mx[3]))));
        float l = 0.f;
#pragma unroll
        for (int k = 0; k < 4; ++k) {
#pragma unroll
          for (int e = 0; e < 4; ++e) v[k][e] = __builtin_amdgcn_exp2f(v[k][e] - m);
          if (k < nv) {
            l += (v[k][0] + v[k][1]) + (v[k][2] + v[k][3]);
            sp[k * 64] = v[k];
          }
        }
        l = wave_sum(l);
        if (lane == 0) s_ml[it] = u32x4{nz_bits(m), nz_bits(l), 0x3f800000u, 0x3f800000u};
        LGKM0();
        if (lane == 0) s_c[F_P] = ik + 1;
      }
    }
  };
  auto pull = [&]() {
    typedef __attribute__((address_space(1))) const unsigned g_u32;
    const int id = (ro - 16) * 4 + tw;
    g_u32* p1[2];
#pragma unroll
    for (int i = 0; i < 2; ++i) {
      const int l = id * 60 + min(i * 32 + (lane >> 1), 59), m = l >= 1920 ? l - 1920 : l;
      p1[i] = (g_u32*)((l >= 1920 ? wu : wg) + (size_t)(m / 3) * FF + xl * 192 + (m % 3) * 64 + (lane & 1) * 32);
    }
    g_u32* p2 = (g_u32*)(wd + (size_t)(id * 24 + min(lane >> 1, 23)) * DM + xl * 64 + (lane & 1) * 32);
    asm volatile("" : "+v"(p1[0]), "+v"(p1[1]), "+v"(p2));
    lds_wait<1>(s_c + C_LAST, NMW);
    const unsigned tp = clk();
    until(tp, PW);
    const unsigned v0 = p1[0][0], v1 = p1[1][0];
    MEMFENCE();
    until(tp, PW + DPW);
    const unsigned v2 = p2[0], v3 = p2[xl < 2 ? 256 : 0];
    asm volatile("" :: "v"(v0), "v"(v1), "v"(v2), "v"(v3));
  };
  if (isW) {
    u32x4 bx[24];
    const char* p = reinterpret_cast<const char*>(wo + (size_t)((w_ks * 4 + tw) * DH + r) * DM + w_ct * 64 + j * 8);
    prep(false);
    if (tw != 0) { lds_wait<16>(s_c + F_P, NS); pause(clk(), OLATE + (tw % 3) * 24 * OSTEP); }
    const unsigned tw0 = clk();
#pragma unroll 1
    for (int i = 0; i < 24; ++i) { until(tw0, i * OSTEP); glds16_nt(p, lds_base + i * 1024); p += 8 * DM * 2; VMCNT(WFD); }
    asm volatile("s_waitcnt vmcnt(0)" ::: "memory");
#pragma unroll
    for (int s = 0; s < 6; ++s) {
      u32x2 e[8];
#pragma unroll
      for (int k = 0; k < 8; ++k) e[k] = *reinterpret_cast<const u32x2*>(reg + (s * 32 + qd * 8 + k) * 128 + mr * 8);
#pragma unroll
      for (int k = 0; k < 4; ++k) {
        bx[s * 4 + 0][k] = PERM_LO(e[2 * k + 1][0], e[2 * k][0]); bx[s * 4 + 1][k] = PERM_HI(e[2 * k + 1][0], e[2 * k][0]);
        bx[s * 4 + 2][k] = PERM_LO(e[2 * k + 1][1], e[2 * k][1]); bx[s * 4 + 3][k] = PERM_HI(e[2 * k + 1][1], e[2 * k][1]);
      }
    }
#pragma unroll
    for (int s = 0; s < 24; s += 4) asm volatile("" : "+v"(bx[s]), "+v"(bx[s + 1]), "+v"(bx[s + 2]), "+v"(bx[s + 3]));
    LGKM0();
    lds_wait<8>(s_c + F_REL, 1u);
    const int hh = w_ks * 4 + tw;
#pragma unroll 1
    for (int pass = 0; pass < 3; ++pass) {
      if (pass < 2) {
        done(pass ? NA : 4);
        pause(lds_get(s_c + S_BURST + pass), pass ? WDELAY2 : WDELAY2 + 300);
        const int b0 = pass * 2, b1 = pass + 1;
        u32x4 v0, v1;
#pragma unroll 1
        for (unsigned spin = 0; spin < SPIN_CAP; ++spin) {
          v0 = ld128(rs, WS_OC + (b0 * NH + hh) * DH + lp);
          v1 = ld128(rs, WS_OC + (b1 * NH + hh) * DH + lp);
          if (__all(min(min4(v0), min4(v1)) != 0u)) break;
          __builtin_amdgcn_s_sleep(16);
        }
        if (lane < DH / 4) {
          *reinterpret_cast<u32x2*>(reg + (b0 * 4) * AW_LD + lane * 8) = u32x2{v0[0], v0[1]};
          *reinterpret_cast<u32x2*>(reg + (b0 * 4 + 1) * AW_LD + lane * 8) = u32x2{v0[2], v0[3]};
          *reinterpret_cast<u32x2*>(reg + (b1 * 4) * AW_LD + lane * 8) = u32x2{v1[0], v1[1]};
          *reinterpret_cast<u32x2*>(reg + (b1 * 4 + 1) * AW_LD + lane * 8) = u32x2{v1[2], v1[3]};
        }
      } else {
        const float wk = weight(NS - 1, hh * GS, GS);
        float w[GS];
#pragma unroll
        for (int k = 0; k < GS; ++k) { w[k] = __uint_as_float(__builtin_amdgcn_readlane(__float_as_uint(wk), k)); asm volatile("" : "+s"(w[k])); }
        done(NS);
        __builtin_amdgcn_s_setprio(3);
        const int po = WS_PO + hh * GS * DH;
        u32x4 pv[GS];
#pragma unroll 1
        for (unsigned spin = 0; spin < SPIN_CAP; ++spin) {
#pragma unroll
          for (int k = 0; k < GS; ++k) pv[k] = __builtin_amdgcn_raw_buffer_load_b128(rs, lp * 4, (po + k * DH) * 4, AUX_SC1);
          unsigned mn = min4(pv[0]);
#pragma unroll
          for (int k = 1; k < GS; ++k) mn = min(mn, min4(pv[k]));
          if (__all(mn != 0u)) break;
          __builtin_amdgcn_s_sleep(1);
        }
        f32x4 o = w[0] * __builtin_bit_cast(f32x4, pv[0]), o2 = w[1] * __builtin_bit_cast(f32x4, pv[1]);
#pragma unroll
        for (int k = 2; k < GS; k += 2) { o += w[k] * __builtin_bit_cast(f32x4, pv[k]); o2 += w[k + 1] * __builtin_bit_cast(f32x4, pv[k + 1]); }
        u32x2 hi, lw;
        split4(o + o2, hi, lw);
        if (lane < DH / 4) {
          *reinterpret_cast<u32x2*>(reg + ((NB - 1) * 4) * AW_LD + lane * 8) = hi;
          *reinterpret_cast<u32x2*>(reg + ((NB - 1) * 4 + 1) * AW_LD + lane * 8) = lw;
        }
      }
      CFENCE();
      u32x4 a[6];
#pragma unroll
      for (int s = 0; s < 6; ++s) a[s] = *reinterpret_cast<const u32x4*>(reg + mr * AW_LD + s * 64 + qd * 16);
      __builtin_amdgcn_sched_barrier(0);
      f32x4 c[4];
#pragma unroll
      for (int t = 0; t < 4; ++t) c[t] = f32x4{0.f, 0.f, 0.f, 0.f};
#pragma unroll
      for (int s = 0; s < 6; ++s)
#pragma unroll
        for (int t = 0; t < 4; ++t) c[t] = mfma16(a[s], bx[s * 4 + t], c[t]);
      const bool mine = max(qd - 1, 0) == pass;
      f32x4* r4 = reinterpret_cast<f32x4*>(red);
      if (mine) r4[tw * 64 + lane] = f32x4{c[0][0] + c[0][1], c[1][0] + c[1][1], c[2][0] + c[2][1], c[3][0] + c[3][1]};
      const int k = arrive(s_c + C_W + pass);
      f32x4 sum = (r4[lane] + r4[64 + lane]) + (r4[128 + lane] + r4[192 + lane]);
      asm volatile("" : "+v"(sum));
      if (arrived(k) == 3) {
        if (mine) st128(rs, WS_SLAB + (w_ks * NB + qd) * DM + w_ct * 64 + mr * 4, u32x4{nz_bits(sum[0]), nz_bits(sum[1]), nz_bits(sum[2]), nz_bits(sum[3])});
      }
    }
  } else if (isG || isH) {
    f32x2* rst = reinterpret_cast<f32x2*>(red);
    const int hb = isH ? xl - 2 : NB - 1;
    uint2 wr4 = make_uint2(0u, 0u), xg = wr4;
    if (lane < 40) {
      wr4 = *reinterpret_cast<const uint2*>(wr + tw * 160 + lane * 4);
      xg = *reinterpret_cast<const uint2*>(x + row_of(hb) * DM + tw * 160 + lane * 4);
    }
    f32x4 xc = f32x4{bf_lo(xg.x), bf_hi(xg.x), bf_lo(xg.y), bf_hi(xg.y)} - bf_lo(*reinterpret_cast<const unsigned*>(x + row_of(hb) * DM));
    asm volatile("" : "+v"(xc));
    typedef __attribute__((address_space(1))) const u32x4 g_u32x4;
    g_u32x4* wp[10];
    wp[0] = (g_u32x4*)(((mr & 4) ? wu : wg) + (size_t)(tw * 160 + qd * 8 + (mr >> 3) * 4) * FF + g_cg * 32 + (mr & 3) * 8);
    u32x4 e[20], bx[20];
    auto frag = [&](int s) __attribute__((always_inline)) {
      unsigned w[8][2];
#pragma unroll
      for (int i = 0; i < 4; ++i)
#pragma unroll
        for (int d = 0; d < 2; ++d) {
          w[i][d] = __builtin_amdgcn_update_dpp(e[s * 4 + i][d], e[s * 4 + i][2 + d], DPP_ROR8, 0xf, 0xc, false);
          w[4 + i][d] = __builtin_amdgcn_update_dpp(e[s * 4 + i][2 + d], e[s * 4 + i][d], DPP_ROR8, 0xf, 0x3, false);
        }
#pragma unroll
      for (int k = 0; k < 4; ++k) {
        bx[s * 4 + 0][k] = PERM_LO(w[2 * k + 1][0], w[2 * k][0]); bx[s * 4 + 1][k] = PERM_HI(w[2 * k + 1][0], w[2 * k][0]);
        bx[s * 4 + 2][k] = PERM_LO(w[2 * k + 1][1], w[2 * k][1]); bx[s * 4 + 3][k] = PERM_HI(w[2 * k + 1][1], w[2 * k][1]);
      }
      PIN4(bx, s * 4);
    };
    prep(false);
    lds_wait<8>(s_c + F_REL, 1u);
    auto hrow = [&](bool fast) -> f32x4 {
      f32x4 hv = {0.f, 0.f, 0.f, 0.f};
      if (lane < 40) {
        const int off = (WS_SLAB + hb * DM + tw * 160 + lane * 4) * 4;
        u32x4 sl[KS];
#pragma unroll 1
        for (unsigned spin = 0; spin < SPIN_CAP; ++spin) {
#pragma unroll
          for (int ks = 0; ks < KS; ++ks) sl[ks] = __builtin_amdgcn_raw_buffer_load_b128(rs, off, ks * (NB * DM * 4), AUX_SC1);
          unsigned mn = min4(sl[0]);
#pragma unroll
          for (int ks = 1; ks < KS; ++ks) mn = min(mn, min4(sl[ks]));
          if (!__any(mn == 0u)) break;
          if (fast) __builtin_amdgcn_s_sleep(1); else __builtin_amdgcn_s_sleep(16);
        }
#define SL(k) __builtin_bit_cast(f32x4, sl[k])
        hv = xc + (((SL(0) + SL(1)) + (SL(2) + SL(3))) + ((SL(4) + SL(5)) + (SL(6) + SL(7))));
#undef SL
      }
      return hv;
    };
    auto sums = [&](f32x4 hv) -> f32x2 {
      return f32x2{wave_sum((hv[0] + hv[1]) + (hv[2] + hv[3])), wave_sum((hv[0] * hv[0] + hv[1] * hv[1]) + (hv[2] * hv[2] + hv[3] * hv[3]))};
    };
    if (isH) {
      const float wsum = wave_sum((bf_lo(wr4.x) + bf_hi(wr4.x)) + (bf_lo(wr4.y) + bf_hi(wr4.y)));
      if (tw == 3 || hb >= 2) done_all(); else done(4);
      if (hb == NB - 1) { pull(); done(NS); }
      else pause(lds_get(s_c + S_BURST + (hb >> 1)), hb < 2 ? HDELAY : HDELAY2);
      const f32x4 hv = hrow(hb == NB - 1);
      const f32x2 sm = sums(hv);
      const float dot = wave_sum((hv[0] * bf_lo(wr4.x) + hv[1] * bf_hi(wr4.x)) + (hv[2] * bf_lo(wr4.y) + hv[3] * bf_hi(wr4.y)));
      if (lane < 8) st128(rs, WS_H + (hb * 4 + tw) * 192 + 160 + lane * 4, u32x4{nz_bits(sm[0]), nz_bits(sm[1]), nz_bits(dot), nz_bits(wsum)});
      if (hb == NB - 1) return;
      const u32x4 v = split8(hv, (lane & 1) != 0);
      if (lane < 40) st128(rs, WS_H + (hb * 4 + tw) * 192 + lane * 4, v);
      done_all();
      pull();
      return;
    }
    done_all();
#pragma unroll
    for (int i = 0; i < 10; ++i) { wp[i] = wp[0] + ((i >> 1) * 32 + (i & 1) * 2) * (FF / 8); asm volatile("" : "+v"(wp[i])); }
    done(NS);
    until(clk(), GW);
#pragma unroll
    for (int i = 0; i < 20; ++i) e[i] = wp[i >> 1][(i & 1) * (FF / 8)];
    u32x4 a[5], sm;
    const int hoff = (WS_H + (min(mr >> 2, NB - 2) * 4 + tw) * 192 + qd * 8 + (mr & 1) * 4) * 4;
#pragma unroll
    for (int s = 0; s < 5; ++s) a[s] = __builtin_amdgcn_raw_buffer_load_b128(rs, hoff + s * 128, 0, AUX_SC1);
    sm = ld128(rs, WS_H + (min(lane, NB - 2) * 4 + tw) * 192 + 160);
    MEMFENCE();
#pragma unroll
    for (int s = 0; s < 5; ++s) frag(s);
    {
#pragma unroll 1
      for (unsigned spin = 0; spin < SPIN_CAP; ++spin) {
        unsigned mn = min4(sm);
#pragma unroll
        for (int s = 0; s < 5; ++s) mn = min(mn, min4(a[s]));
        if (__all(mn != 0u)) break;
        __builtin_amdgcn_s_sleep(2);
#pragma unroll
        for (int s = 0; s < 5; ++s) a[s] = __builtin_amdgcn_raw_buffer_load_b128(rs, hoff + s * 128, 0, AUX_SC1);
        sm = ld128(rs, WS_H + (min(lane, NB - 2) * 4 + tw) * 192 + 160);
      }
      if (lane < NB - 1) rst[lane * 4 + tw] = f32x2{__uint_as_float(sm[0]), __uint_as_float(sm[1])};
#pragma unroll
      for (int s = 0; s < 5; ++s) {
        if (mr & 2) a[s] = u32x4{0x3f803f80u, 0x3f803f80u, 0x3f803f80u, 0x3f803f80u};
        asm volatile("" : "+v"(a[s]));
      }
    }
    typedef __attribute__((address_space(3))) unsigned char lds_u8;
    const int eb = mr >> 2;
    lds_u8* l_aw = (lds_u8*)reg + lane * 8;
    lds_u8* l_ar = (lds_u8*)reg + (mr & 1) * AG_LD + qd * 16;
    lds_u8* l_pw = (lds_u8*)reg + 4096 + (qd * 16 + (mr & 7) * 2 + (mr >> 3)) * 16;
    lds_u8* l_pr = (lds_u8*)smem + OFF_REG + 4096 + (lane & 1) * 1152 + (eb * 16 + qd * 2 + ((lane >> 1) & 1)) * 16;
    lds_u8* l_p2 = l_pr + 2 * REG;
    lds_u8* l_sr = (lds_u8*)(rst + eb * 4);
    lds_u8* l_sw = (lds_u8*)(rst + (NB - 1) * 4 + tw);
    int so = (lane & 2) == 0 ? (WS_ACT + g_cg * 128 + (lane >> 2) * 8 + (lane & 1) * 4) * 4 : WS_END * 4;
    asm volatile("" : "+v"(l_aw), "+v"(l_ar), "+v"(l_pw), "+v"(l_pr), "+v"(l_p2), "+v"(l_sr), "+v"(l_sw), "+v"(so));
#define LDS(T, p) (*reinterpret_cast<__attribute__((address_space(3))) T*>(p))
    const f32x4 hv = hrow(true);
    {
      u32x2 hi, lw;
      split4(hv, hi, lw);
      LDS(u32x2, l_aw) = hi;
      LDS(u32x2, l_aw + AG_LD) = lw;
    }
    CFENCE();
    if ((mr & 14) == 12) {
#pragma unroll
      for (int s = 0; s < 5; ++s) a[s] = LDS(u32x4, l_ar + s * 64);
    }
    __builtin_amdgcn_sched_barrier(0);
    f32x4 c[4];
#pragma unroll
    for (int t = 0; t < 4; ++t) c[t] = f32x4{0.f, 0.f, 0.f, 0.f};
#pragma unroll
    for (int s = 0; s < 5; ++s)
#pragma unroll
      for (int t = 0; t < 4; ++t) c[t] = mfma16(a[s], bx[s * 4 + t], c[t]);
    LDS(f32x2, l_sw) = sums(hv);
    LDS(f32x4, l_pw) = f32x4{c[0][0] + c[0][1], c[1][0] + c[1][1], c[0][2], c[1][2]};
    LDS(f32x4, l_pw + 1152) = f32x4{c[2][0] + c[2][1], c[3][0] + c[3][1], c[2][2], c[3][2]};
    const int k = arrive(s_c + C_T);
    f32x4 ts[2] = {LDS(f32x4, l_sr), LDS(f32x4, l_sr + 16)};
    f32x4 pg[4] = {LDS(f32x4, l_pr), LDS(f32x4, l_pr + REG), LDS(f32x4, l_p2), LDS(f32x4, l_p2 + REG)};
    f32x4 pu[4] = {LDS(f32x4, l_pr + 128), LDS(f32x4, l_pr + REG + 128), LDS(f32x4, l_p2 + 128), LDS(f32x4, l_p2 + REG + 128)};
    asm volatile("" : "+v"(ts[0]), "+v"(ts[1]));
    PIN4(pg, 0); PIN4(pu, 0);
    if (arrived(k) == 3) {
      const f32x4 t = ts[0] + ts[1], g4 = (pg[0] + pg[1]) + (pg[2] + pg[3]), u4 = (pu[0] + pu[1]) + (pu[2] + pu[3]);
      const float s1 = t[0] + t[2], d = s1 * (-1.0f / DM);
      const float rstd = __builtin_amdgcn_rsqf(fmaxf((t[1] + t[3]) + d * s1, 0.f) * (1.0f / DM) + LN_EPS);
      const float g0 = rstd * (g4[0] + d * g4[2]), g1 = rstd * (g4[1] + d * g4[3]);
      const f32x2 act = {g0 * __builtin_amdgcn_rcpf(1.0f + __expf(-g0)) * (rstd * (u4[0] + d * u4[2])),
                         g1 * __builtin_amdgcn_rcpf(1.0f + __expf(-g1)) * (rstd * (u4[1] + d * u4[3]))};
      const unsigned h0 = __float_as_uint(act[0]) & 0xffff0000u, h1 = __float_as_uint(act[1]) & 0xffff0000u;
      const unsigned hi = max(PERM_HI(h1, h0), 1u), lw = max(PERM_HI(__float_as_uint(act[1] - __uint_as_float(h1)), __float_as_uint(act[0] - __uint_as_float(h0))), 1u);
      const bool odd = (lane & 1) != 0;
      const unsigned own = odd ? lw : hi, oth = __float_as_uint(dpp<DPP_XOR1>(__uint_as_float(odd ? hi : lw)));
      const unsigned w0 = odd ? oth : own, w1 = odd ? own : oth;
      const u32x4 av = {w0, w1, __float_as_uint(dpp<DPP_XOR2>(__uint_as_float(w0))), __float_as_uint(dpp<DPP_XOR2>(__uint_as_float(w1)))};
      __builtin_amdgcn_raw_buffer_store_b128(av, rs, so, 0, AUX_SC1);
    }
  } else if (isD) {
    const uint2 xd = *reinterpret_cast<const uint2*>(x + row_of(tw) * DM + d_ct * 32 + j * 4);
    const float cq = bf_lo(*reinterpret_cast<const unsigned*>(x + row_of(qd) * DM));
    const __amdgpu_buffer_rsrc_t rd = __builtin_amdgcn_make_buffer_rsrc(const_cast<bf16*>(wd), 0, FF * DM * 2, 0x00020000);
    prep(false);
    lds_wait<8>(s_c + F_REL, 1u);
    done_all();
    pull();
    done(NS);
    const unsigned te = clk();
    u32x4 bd[24];
    {
      const int off = ((tw * 384 + (mr >> 3) * 32 + qd * 8 + (mr & 4)) * DM + d_ct * 32 + (mr & 3) * 8) * 2;
      u32x4 e[24];
#pragma unroll
      for (int i = 0; i < 24; ++i) e[i] = __builtin_amdgcn_raw_buffer_load_b128(rd, off + (i & 3) * (DM * 2), (i >> 2) * 64 * (DM * 2), 0);
      MEMFENCE();
#pragma unroll
      for (int s = 0; s < 6; ++s) {
        unsigned w[8][2];
#pragma unroll
        for (int i = 0; i < 4; ++i)
#pragma unroll
          for (int d = 0; d < 2; ++d) {
            w[i][d] = __builtin_amdgcn_update_dpp(e[s * 4 + i][d], e[s * 4 + i][2 + d], 0x114, 0xf, 0xa, false);
            w[4 + i][d] = __builtin_amdgcn_update_dpp(e[s * 4 + i][2 + d], e[s * 4 + i][d], 0x104, 0xf, 0x5, false);
          }
#pragma unroll
        for (int k = 0; k < 4; ++k) {
          bd[s * 4 + 0][k] = PERM_LO(w[2 * k + 1][0], w[2 * k][0]); bd[s * 4 + 1][k] = PERM_HI(w[2 * k + 1][0], w[2 * k][0]);
          bd[s * 4 + 2][k] = PERM_LO(w[2 * k + 1][1], w[2 * k][1]); bd[s * 4 + 3][k] = PERM_HI(w[2 * k + 1][1], w[2 * k][1]);
        }
        PIN4(bd, s * 4);
      }
    }
    until(te, DLATE);
    {
      u32x4 sl;
#pragma unroll 1
      for (unsigned spin = 0; spin < SPIN_CAP; ++spin) {
        sl = ld128(rs, WS_SLAB + (r * NB + tw) * DM + d_ct * 32 + j * 4);
        if (__all(min4(sl) != 0u)) break;
        __builtin_amdgcn_s_sleep(2);
      }
      f32x4 hv = {bf_lo(xd.x), bf_hi(xd.x), bf_lo(xd.y), bf_hi(xd.y)};
#pragma unroll
      for (int i = 0; i < 4; ++i) hv[i] += sum_r(__uint_as_float(sl[i]));
      if (lane < 8) reinterpret_cast<f32x4*>(red + 512 + tw * 32)[lane] = hv;
      LGKM0();
      lds_add(s_c + C_W, lane);
      lds_wait<1>(s_c + C_W, 4u);
    }
    const int col = (mr & 3) * 8 + (mr & 4) + (mr >> 3) * 2;
    f32x2 hv = *reinterpret_cast<const f32x2*>(red + 512 + qd * 32 + col);
    typedef __attribute__((address_space(3))) f32x2 lds_f32x2;
    typedef __attribute__((address_space(1))) unsigned g_u32w;
    lds_f32x2* r2 = (lds_f32x2*)red + lane;
    lds_f32x2* r2w = r2 + tw * 64;
    g_u32w* po = (g_u32w*)(out + row_of(qd) * DM + d_ct * 32 + col);
    int aoff = (WS_ACT + tw * 12 * 128 + ((mr >> 1) & 1) * 128 + qd * 32 + (mr >> 2) * 8 + (mr & 1) * 4) * 4;
    asm volatile("" : "+v"(hv), "+v"(r2), "+v"(r2w), "+v"(po), "+v"(aoff));
    u32x4 hs, a[6];
#pragma unroll 1
    for (unsigned spin = 0; spin < SPIN_CAP; ++spin) {
      hs = ld128(rs, WS_H + (qd * 4 + (mr & 3)) * 192 + 160);
      if (__all(min4(hs) != 0u)) break;
      __builtin_amdgcn_s_sleep(1);
    }
#pragma unroll
    for (int s = 0; s < 6; ++s) a[s] = __builtin_amdgcn_raw_buffer_load_b128(rs, aoff, s * 1024, AUX_SC1);
    MEMFENCE();
    float gate;
    {
      f32x4 t = __builtin_bit_cast(f32x4, hs);
#pragma unroll
      for (int i = 0; i < 4; ++i) { t[i] += dpp<DPP_XOR2>(t[i]); t[i] += dpp<DPP_XOR1>(t[i]); }
      const float d = t[0] * (-1.0f / DM);
      const float rstd = __builtin_amdgcn_rsqf(fmaxf(t[1] + d * t[0], 0.f) * (1.0f / DM) + LN_EPS);
      hv += ((hv - cq) + d) * rstd;
      gate = __builtin_amdgcn_rcpf(1.0f + __expf(-rstd * (t[2] + d * t[3])));
      asm volatile("" : "+v"(hv), "+v"(gate));
    }
#pragma unroll 1
    for (unsigned spin = 0; spin < SPIN_CAP; ++spin) {
      unsigned mn = min4(a[0]);
#pragma unroll
      for (int s = 1; s < 6; ++s) mn = min(mn, min4(a[s]));
      if (__all(mn != 0u)) break;
#pragma unroll
      for (int s = 0; s < 6; ++s) a[s] = __builtin_amdgcn_raw_buffer_load_b128(rs, aoff, s * 1024, AUX_SC1);
      MEMFENCE();
    }
    f32x4 c[4];
#pragma unroll
    for (int t = 0; t < 4; ++t) c[t] = f32x4{0.f, 0.f, 0.f, 0.f};
#pragma unroll
    for (int s = 0; s < 6; ++s)
#pragma unroll
      for (int t = 0; t < 4; ++t) c[t] = mfma16(a[s], bd[s * 4 + t], c[t]);
    const bool up = mr >= 8;
    const float k0 = up ? c[2][2] + c[2][3] : c[0][0] + c[0][1], k1 = up ? c[3][2] + c[3][3] : c[1][0] + c[1][1];
    const float o0 = up ? c[0][2] + c[0][3] : c[2][0] + c[2][1], o1 = up ? c[1][2] + c[1][3] : c[3][0] + c[3][1];
    *r2w = f32x2{k0 + dpp<DPP_ROR8>(o0), k1 + dpp<DPP_ROR8>(o1)};
    const int k = arrive(s_c + C_T);
    f32x2 y = hv + ((r2[0] + r2[64]) + (r2[128] + r2[192])) * gate;
    asm volatile("" : "+v"(y));
    if (arrived(k) == 3) {
      __hip_atomic_store(po, __builtin_bit_cast(unsigned, __builtin_convertvector(y, bf16x2)), __ATOMIC_RELAXED, __HIP_MEMORY_SCOPE_AGENT);
    }
  } else {
    prep(bid == LEADER);
    if (isC && tw == 1) {
      lds_wait<8>(s_c + F_REL, 1u);
      const int cs = c_it != 1 ? c_it * 2 : min(4 - (g >> 3), 3), cnt = c_it != 1 ? 8 : (g < 16 ? 16 : 32), x0 = c_it != 1 ? g * 8 : (g < 16 ? g * 16 : (g & 7) * 32);
      done(c_it < 2 ? 4 : NA);
      pause(lds_get(s_c + S_BURST + (c_it >> 1)), WDELAY);
      const float wk = weight(cs, x0, cnt);
      f32x4 o = {0.f, 0.f, 0.f, 0.f};
#pragma unroll 1
      for (int k0 = 0; k0 < cnt; k0 += 16) {
        u32x2 pv[16];
#pragma unroll
        for (int k = 0; k < 16; ++k) pv[k] = __builtin_amdgcn_raw_buffer_load_b64(rs, lp * 2, (WS_PQ + ((x0 + ((k0 + k) & (cnt - 1))) * 5 + cs) * (DH / 2)) * 4, AUX_EARLY);
#pragma unroll
        for (int k = 0; k < 16; ++k) {
          const float w = k0 + k < cnt ? __uint_as_float(__builtin_amdgcn_readlane(__float_as_uint(wk), k0 + k)) : 0.f;
          o += w * f32x4{(float)(short)pv[k][0], (float)((int)pv[k][0] >> 16), (float)(short)pv[k][1], (float)((int)pv[k][1] >> 16)};
        }
      }
      u32x2 hi, lw;
      split4(o, hi, lw);
      if (lane < DH / 4) st128(rs, WS_OC + (c_it * NG + g) * DH + lane * 4, u32x4{max(hi[0], 1u), max(hi[1], 1u), max(lw[0], 1u), max(lw[1], 1u)});
    }
    done_all();
    pull();
  }
  if (tw == 3) done_all();
}

extern "C" void kernel_launch(void* const* d_in, const int* in_sizes, int n_in, void* d_out, int out_size, void* d_ws,
                              size_t ws_size, hipStream_t stream) {
  const bf16* x = (const bf16*)d_in[0];
  const bf16* kc = (const bf16*)d_in[1];
  const bf16* vc = (const bf16*)d_in[2];
  const bf16* wq = (const bf16*)d_in[3];
  const bf16* wo = (const bf16*)d_in[4];
  const bf16* wr = (const bf16*)d_in[6];
  const bf16* wg = (const bf16*)d_in[7];
  const bf16* wu = (const bf16*)d_in[8];
  const bf16* wd = (const bf16*)d_in[9];
  fused_kernel<<<NWG, NW * 64, 0, stream>>>(x, kc, vc, wq, wo, wr, wg, wu, wd, (float*)d_ws, (bf16*)d_out);
}
```

```cpp
#include <hip/hip_runtime.h>
#include <hip/hip_bf16.h>
#include <stdint.h>

typedef __hip_bfloat16 bf16;
using u32x2 = __attribute__((ext_vector_type(2))) unsigned;
using u32x4 = __attribute__((ext_vector_type(4))) unsigned;
using f32x4 = __attribute__((ext_vector_type(4))) float;
using f32x2 = __attribute__((ext_vector_type(2))) float;
using bf16x8 = __attribute__((ext_vector_type(8))) __bf16;

constexpr int NB = 4, DM = 640, NH = 32, SK = 8192, DH = 192, HD = NH * DH, FF = 1536;
constexpr float LN_EPS = 1e-5f;
constexpr float QSCALE = 0.07216878364870322f * 1.4426950408889634f;

constexpr int NWG = 256;
constexpr int GS = 8, NG = NWG / GS, NI = GS / 2;
constexpr int HPG = NI / NB;
constexpr int PPI = 512 / GS;
constexpr int NPAIR = NI * PPI;

constexpr int TAUK = 1280, TAUV = 1296;
constexpr int TSTART = 110;
constexpr int AV = 5, ANCH = 90;
constexpr int OGAP = 140, OLATE = 250, OSTEP = 30;
constexpr int KS1 = 100, KP1 = 72, KS2 = 0, KP2 = 88, KN2 = 64;
constexpr int VS0 = 0, VN0 = 16, VS1 = 0, VN1 = 24, SOFT = 1;
constexpr int VGAP = 50;
constexpr int DEEP1 = 70, DEEP2 = 160, LAGOFF = 20;
constexpr int ENDT = 12, ENDS = 2;
constexpr int JUMP = 145;
constexpr int ZT = 1800, HST = 2800, RT0 = 3900, RTS = 1000, RA1 = 220, RA2 = 500, LEADER = 24;
constexpr unsigned NRD = 5, RMIN = 30;
constexpr int FBX = 35, FB0 = 20, FB1 = 10, FBD = 10, FBS = 10;
constexpr int HOLD = 4000, GB = 12, GBS = 4, GBX = 60, GBF = 12;
constexpr unsigned PRD = 3; constexpr int PRB = 0;
constexpr int ROWS = 0x1B;
constexpr int RW0 = 640, RW1 = 272, RW2 = 208, RW3 = 992, RW4 = 80;
constexpr int TLEAD = 10, PFD = 3, WFD = 3;
constexpr int QWAIT = 820;
constexpr int WDELAY = 100, WDELAY2 = 460, HDELAY = 1150, HDELAY2 = 780, DLATE = 100;
constexpr int TPT = 600, TPS = 20;
constexpr int CODE = 24 * 64 * 46;
constexpr int PW = 5, DPW = 150, GW = 30;

constexpr int KS = 8;
constexpr int WS_PML = 0;
constexpr int WS_PO = WS_PML + NWG * 64;
constexpr int WS_PQ = WS_PO + NWG * DH;
constexpr int WS_OC = WS_PQ + NWG * 5 * (DH / 2);
constexpr int WS_SLAB = WS_OC + (NB - 1) * NH * DH;
constexpr int WS_ACT = WS_SLAB + KS * NB * DM;
constexpr int WS_H = WS_ACT + NB * FF;
constexpr int WS_HN = WS_H + NB * 4 * 192;
constexpr int WS_HR = WS_HN + NWG;
constexpr int WS_HA = WS_HR + 2 * NWG;
constexpr int QFS = 2 * GS * 32;
constexpr int WS_QF = WS_HA + 32 * NWG;
constexpr int WS_QP = WS_QF + NG * QFS;
constexpr int WS_END = WS_QP + NH * 2 * NB * DH;
constexpr int NPROD = HPG * 6;
static_assert(WS_PO % 32 == 0 && WS_SLAB % 32 == 0 && WS_ACT % 32 == 0 && WS_HN % 32 == 0 && WS_QF % 32 == 0 && WS_QP % 32 == 0, "line alignment");


__device__ __forceinline__ int row_of(int b) { return (ROWS >> (2 * b)) & 3; }
__device__ __forceinline__ float bf_lo(unsigned u) { return __uint_as_float(u << 16); }
__device__ __forceinline__ float bf_hi(unsigned u) { return __uint_as_float(u & 0xffff0000u); }
__device__ __forceinline__ f32x2 bf_pair(unsigned u) { return f32x2{bf_lo(u), bf_hi(u)}; }
__device__ __forceinline__ f32x2 fma2(float a, f32x2 b, f32x2 c) { return __builtin_elementwise_fma(f32x2{a, a}, b, c); }
__device__ __forceinline__ f32x2 fma2(f32x2 a, f32x2 b, f32x2 c) { return __builtin_elementwise_fma(a, b, c); }

template <int CTRL> __device__ __forceinline__ float dpp(float x) {
  return __builtin_bit_cast(float, __builtin_amdgcn_mov_dpp(__builtin_bit_cast(int, x), CTRL, 0xf, 0xf, true));
}
constexpr int DPP_XOR1 = 0xB1, DPP_XOR2 = 0x4E, DPP_HMIRROR = 0x141, DPP_MIRROR = 0x140, DPP_ROR8 = 0x128;

__device__ __forceinline__ float sum_x16_32(float x) {
  auto s = __builtin_amdgcn_permlane16_swap(__float_as_uint(x), __float_as_uint(x), false, false);
  x = __uint_as_float(s[0]) + __uint_as_float(s[1]);
  auto t = __builtin_amdgcn_permlane32_swap(__float_as_uint(x), __float_as_uint(x), false, false);
  return __uint_as_float(t[0]) + __uint_as_float(t[1]);
}
__device__ __forceinline__ float max_x16_32(float x) {
  auto s = __builtin_amdgcn_permlane16_swap(__float_as_uint(x), __float_as_uint(x), false, false);
  x = fmaxf(__uint_as_float(s[0]), __uint_as_float(s[1]));
  auto t = __builtin_amdgcn_permlane32_swap(__float_as_uint(x), __float_as_uint(x), false, false);
  return fmaxf(__uint_as_float(t[0]), __uint_as_float(t[1]));
}
__device__ __forceinline__ float tsum32(float a, float b) {
  auto t = __builtin_amdgcn_permlane32_swap(__float_as_uint(a), __float_as_uint(b), false, false);
  return __uint_as_float(t[0]) + __uint_as_float(t[1]);
}
__device__ __forceinline__ float tsum16(float a, float b) {
  auto t = __builtin_amdgcn_permlane16_swap(__float_as_uint(a), __float_as_uint(b), false, false);
  return __uint_as_float(t[0]) + __uint_as_float(t[1]);
}
__device__ __forceinline__ float sum_r(float v) { v += dpp<DPP_ROR8>(v); return sum_x16_32(v); }
__device__ __forceinline__ float row_sum(float v) {
  v += dpp<DPP_XOR1>(v); v += dpp<DPP_XOR2>(v); v += dpp<DPP_HMIRROR>(v);
  return v + dpp<DPP_MIRROR>(v);
}
__device__ __forceinline__ float row_max(float v) {
  v = fmaxf(v, dpp<DPP_XOR1>(v)); v = fmaxf(v, dpp<DPP_XOR2>(v)); v = fmaxf(v, dpp<DPP_HMIRROR>(v));
  return fmaxf(v, dpp<DPP_MIRROR>(v));
}
__device__ __forceinline__ float wave_sum(float v) {
  v += dpp<DPP_XOR1>(v); v += dpp<DPP_XOR2>(v); v += dpp<DPP_HMIRROR>(v);
  return sum_r(v);
}

__device__ __forceinline__ u32x4 ld_nt(const char* p) { return __builtin_nontemporal_load(reinterpret_cast<const u32x4*>(p)); }

struct Tile { u32x4 d[3]; };

#define PIN4(a, o) asm volatile("" : "+v"((a)[(o) + 0]), "+v"((a)[(o) + 1]), "+v"((a)[(o) + 2]), "+v"((a)[(o) + 3]))
#define MEMFENCE() do { asm volatile("" ::: "memory"); __builtin_amdgcn_sched_barrier(0); } while (0)

__device__ __forceinline__ void glds16_nt(const void* gsrc, unsigned lds_dst) {
  unsigned keep;
  asm volatile("s_mov_b32 %0, m0\n\ts_mov_b32 m0, %2\n\ts_nop 0\n\tglobal_load_lds_dwordx4 %1, off nt\n\ts_mov_b32 m0, %0"
               : "=&s"(keep) : "v"(gsrc), "s"(lds_dst) : "memory");
}

using bf16x2 = __attribute__((ext_vector_type(2))) __bf16;
__device__ __forceinline__ float dot2(unsigned a, unsigned b, float c) { return __builtin_amdgcn_fdot2_f32_bf16(__builtin_bit_cast(bf16x2, a), __builtin_bit_cast(bf16x2, b), c, false); }
__device__ __forceinline__ float tile_score(const Tile& k, const u32x2 (&q)[12]) {
  float s0 = 0.f, s1 = 0.f;
#pragma unroll
  for (int i = 0; i < 3; ++i) {
    u32x4 v = k.d[i];
    asm("" : "+v"(v));
#pragma unroll
    for (int d = 0; d < 4; ++d) { s0 = dot2(v[d], q[i * 4 + d][0], s0); s1 = dot2(v[d], q[i * 4 + d][1], s1); }
  }
  return s0 + s1;
}
__device__ __forceinline__ void pair_put(float a, float b, float* dst, int j) {
  float s = (j < 4 ? a : b) + dpp<DPP_HMIRROR>(j < 4 ? b : a);
  s += dpp<DPP_XOR1>(s);
  s += dpp<DPP_XOR2>(s);
  if ((j & 3) == 0) dst[j * 2] = s;
}
__device__ __forceinline__ void score_put(float s, float* dst, int j) {
  s += dpp<DPP_XOR1>(s);
  s += dpp<DPP_XOR2>(s);
  s += dpp<DPP_HMIRROR>(s);
  if (j == 0) *dst = s;
}
__device__ __forceinline__ void tile_load(Tile& t, const char* base, unsigned off, int half) {
#pragma unroll
  for (int i = 0; i < 3; ++i) t.d[i] = ld_nt(base + off + half * 3072 + i * 128);
}
__device__ __forceinline__ void pair_issue(Tile& ta, Tile& tb, const char* base, unsigned off, int deep) {
  deep = __builtin_amdgcn_readfirstlane(deep);
#define WAIT_IF(level, n) asm volatile("s_cmp_lg_u32 %0, " #level "\n\ts_cbranch_scc1 .Lpi%=\n\ts_waitcnt vmcnt(" #n ")\n.Lpi%=:" :: "s"(deep) : "scc", "memory")
  const char* p = base + off;
  WAIT_IF(0, 3);
  ta.d[0] = ld_nt(p); ta.d[1] = ld_nt(p + 128); ta.d[2] = ld_nt(p + 256); MEMFENCE();
  WAIT_IF(0, 3); WAIT_IF(1, 6);
  tb.d[0] = ld_nt(p + 3072); tb.d[1] = ld_nt(p + 3200); tb.d[2] = ld_nt(p + 3328); MEMFENCE();
#undef WAIT_IF
}
__device__ __forceinline__ void pv_last(float p, Tile& v, f32x2 (&acc)[12]) {
#pragma unroll
  for (int i = 0; i < 3; ++i) {
    asm volatile("" : "+v"(v.d[i]), "+v"(p));
#pragma unroll
    for (int d = 0; d < 4; ++d) acc[i * 4 + d] = fma2(p, bf_pair(v.d[i][d]), acc[i * 4 + d]);
    PIN4(acc, i * 4);
  }
}
__device__ __forceinline__ void q_load(u32x2 (&q)[12], const float* src, int j) {
#pragma unroll
  for (int i = 0; i < 6; ++i) {
    const u32x4 a = *reinterpret_cast<const u32x4*>(src + (i >> 1) * 64 + j * 8 + (i & 1) * 4);
    q[i * 2] = u32x2{a[0], a[1]}; q[i * 2 + 1] = u32x2{a[2], a[3]};
  }
}
#define VMCNT(n) asm volatile("s_waitcnt vmcnt(%0)" :: "n"(n) : "memory")

constexpr int AUX_SC1 = 16, AUX_EARLY = 18;
constexpr unsigned SPIN_CAP = 1u << 17;
__device__ __forceinline__ unsigned nz_bits(float v) { return max(__float_as_uint(v), 1u); }
__device__ __forceinline__ unsigned min4(u32x4 v) { return min(min(v[0], v[1]), min(v[2], v[3])); }
__device__ __forceinline__ unsigned ld32(__amdgpu_buffer_rsrc_t rs, int word) { return __builtin_amdgcn_raw_buffer_load_b32(rs, word * 4, 0, AUX_SC1); }
__device__ __forceinline__ u32x2 ld64(__amdgpu_buffer_rsrc_t rs, int word) { return __builtin_amdgcn_raw_buffer_load_b64(rs, word * 4, 0, AUX_SC1); }
__device__ __forceinline__ u32x4 ld128(__amdgpu_buffer_rsrc_t rs, int word) { return __builtin_amdgcn_raw_buffer_load_b128(rs, word * 4, 0, AUX_SC1); }
__device__ __forceinline__ void st128(__amdgpu_buffer_rsrc_t rs, int word, u32x4 v) { __builtin_amdgcn_raw_buffer_store_b128(v, rs, word * 4, 0, AUX_SC1); }

__device__ __forceinline__ u32x4 ld128nt(__amdgpu_buffer_rsrc_t rs, int word) { return __builtin_amdgcn_raw_buffer_load_b128(rs, word * 4, 0, AUX_EARLY); }
__device__ __forceinline__ void st128nt(__amdgpu_buffer_rsrc_t rs, int word, u32x4 v) { __builtin_amdgcn_raw_buffer_store_b128(v, rs, word * 4, 0, AUX_EARLY); }

__device__ __forceinline__ bool leader_release(__amdgpu_buffer_rsrc_t rs, int lane, unsigned t0, unsigned rd, int slow, int& prev) {
  u32x4 a[4];
#pragma unroll
  for (int k = 0; k < 4; ++k) a[k] = ld128(rs, WS_HA + (lane * 4 + k) * 32);
  const u32x4 n = ld128(rs, WS_HN + lane * 4);
  if (rd == 0u && !__all(a[0][0] == n[0] && a[1][0] == n[1] && a[2][0] == n[2] && a[3][0] == n[3])) return false;
  const float e = fmaxf(fmaxf((float)(int)(t0 - a[0][2]), (float)(int)(t0 - a[1][2])), fmaxf((float)(int)(t0 - a[2][2]), (float)(int)(t0 - a[3][2])));
  unsigned first = t0 - (unsigned)(int)max_x16_32(row_max(e));
  float ls = 0.f, lx = 0.f;
  const int pend = max(prev - FBS * (RTS - RA2) * 16 / TAUK, 0);
  prev = max(pend - FBS * RA2 * 16 / TAUK, 0);
#pragma unroll
  for (int k = 0; k < 4; ++k) {
    const float l = a[k][0] == n[k] + rd ? (float)min(a[k][3] + (rd ? 0u : min(a[k][2] - first, 300u)), 2000u) : (float)-pend;
    ls += l; lx = fmaxf(lx, l);
  }
  ls = wave_sum(ls) * (1.0f / NWG); lx = max_x16_32(row_max(lx));
  const int extra = __builtin_amdgcn_readfirstlane(min(max((int)(ls + lx * (FBX / 100.0f)) - (rd ? FB1 + (int)(NRD - rd) * FBD : FB0), 0), 600));
  if (rd != 0u && __all(ls * NWG < HOLD)) return true;
  first = (first & 0xffffu) | ((unsigned)(slow + extra) << 16);
  prev += extra;
  st128(rs, WS_HR + lane * 8, u32x4{a[0][1] + rd, first, a[1][1] + rd, first});
  st128(rs, WS_HR + lane * 8 + 4, u32x4{a[2][1] + rd, first, a[3][1] + rd, first});
  return true;
}

typedef __attribute__((address_space(3))) unsigned lds_u32;
__device__ __forceinline__ unsigned lds_add(lds_u32* p, int lane) {
  unsigned k = 0;
  if (lane == 0) k = __hip_atomic_fetch_add(p, 1u, __ATOMIC_RELAXED, __HIP_MEMORY_SCOPE_WORKGROUP);
  return __builtin_amdgcn_readfirstlane(k);
}
#define CFENCE() asm volatile("" ::: "memory")
__device__ __forceinline__ int arrive(lds_u32* p) {
  int v;
  unsigned long long keep;
  asm volatile("s_mov_b64 %1, exec\n\ts_mov_b64 exec, 1\n\tds_add_rtn_u32 %0, %2, %3\n\ts_mov_b64 exec, %1"
               : "=&v"(v), "=&s"(keep) : "v"((unsigned)(size_t)p), "v"(1u) : "memory");
  return v;
}
__device__ __forceinline__ void bump(lds_u32* p) {
  unsigned long long keep;
  asm volatile("s_mov_b64 %0, exec\n\ts_mov_b64 exec, 1\n\tds_add_u32 %1, %2\n\ts_mov_b64 exec, %0" : "=&s"(keep) : "v"((unsigned)(size_t)p), "v"(1u) : "memory");
}
__device__ __forceinline__ int arrived(int v) {
  asm volatile("s_waitcnt lgkmcnt(0)" : "+v"(v) :: "memory");
  return __builtin_amdgcn_readfirstlane(v);
}
__device__ __forceinline__ unsigned lds_get(lds_u32* p) {
  return __builtin_amdgcn_readfirstlane(__hip_atomic_load(p, __ATOMIC_RELAXED, __HIP_MEMORY_SCOPE_WORKGROUP));
}
template <int SLEEP> __device__ __forceinline__ void lds_wait(lds_u32* p, unsigned n) {
#pragma unroll 1
  for (unsigned spin = 0; spin < (SPIN_CAP << 4); ++spin) {
    if (lds_get(p) >= n) break;
    __builtin_amdgcn_s_sleep(SLEEP);
  }
  asm volatile("" ::: "memory");
}
__device__ __forceinline__ unsigned clk() { return (unsigned)__builtin_amdgcn_s_memrealtime(); }
struct Table {
  unsigned t0; int was, moved, slowed, folded;
  u32x2 told;
  unsigned mine, tmp;
  template <int K> __device__ __forceinline__ void put(int v) { asm volatile("v_mov_b32 %0, %2\n\tds_write_b32 %1, %0 offset:%3" : "+v"(tmp), "+v"(mine) : "s"(v), "n"(K * 4) : "memory"); }
  __device__ __forceinline__ int gate(int due, int sh = 0) {
    const int fold = __builtin_amdgcn_readfirstlane(told[1]);
    if (fold != folded) { const int f = min(fold - folded, moved); moved -= f; slowed += f; folded = fold; put<0>(moved); }
    const int left = (int)__builtin_amdgcn_readfirstlane(told[0]) - slowed, d = max(min(left, FBS), left < -GBX ? -GBF : -GBS);
    t0 += d; was -= d; slowed += d;
    int late = (int)(clk() - t0) - due;
    if (late - was > JUMP) { t0 += late - was; moved += late - was; late = was; put<0>(moved); }
    const int lag = max(late - max(left - d, 0) - LAGOFF, 0);
    put<4>(lag);
    was = late;
#pragma unroll 1
    while (late < 0) { __builtin_amdgcn_s_sleep(1); late = (int)(clk() - t0) - due; }
    return lag < (DEEP1 >> sh) ? 0 : (lag < (DEEP2 >> sh) ? 1 : 2);
  }
  __device__ __forceinline__ void listen(lds_u32* slow) {
    told = __builtin_bit_cast(u32x2, __hip_atomic_load((__attribute__((address_space(3))) unsigned long long*)slow, __ATOMIC_RELAXED, __HIP_MEMORY_SCOPE_WORKGROUP));
  }
  __device__ __forceinline__ void agree(lds_u32* all) {
    if (moved == 0) return;
    const int m = (int)min(min(lds_get(all), lds_get(all + 1)), min(lds_get(all + 2), lds_get(all + 3))) + 30;
    if (m < moved) { t0 -= moved - m; moved = m; was = 1 << 20; put<0>(moved); }
  }
};
__device__ __forceinline__ void until(unsigned t0, int due) {
#pragma unroll 1
  while ((int)(clk() - t0) < due) __builtin_amdgcn_s_sleep(1);
}
__device__ __forceinline__ void pause(unsigned t0, unsigned due) {
#pragma unroll 1
  while (clk() - t0 < due) __builtin_amdgcn_s_sleep(16);
}
#define LGKM0() asm volatile("s_waitcnt lgkmcnt(0)" ::: "memory")

__device__ __forceinline__ void split4(f32x4 v, u32x2& hi, u32x2& lo) {
  unsigned h[4], r[4];
#pragma unroll
  for (int i = 0; i < 4; ++i) { h[i] = __float_as_uint(v[i]) & 0xffff0000u; r[i] = __float_as_uint(v[i] - __uint_as_float(h[i])); }
  hi = u32x2{__builtin_amdgcn_perm(h[1], h[0], 0x07060302u), __builtin_amdgcn_perm(h[3], h[2], 0x07060302u)};
  lo = u32x2{__builtin_amdgcn_perm(r[1], r[0], 0x07060302u), __builtin_amdgcn_perm(r[3], r[2], 0x07060302u)};
}
__device__ __forceinline__ u32x4 split8(f32x4 v, bool odd) {
  u32x2 hi, lo;
  split4(v, hi, lo);
  const unsigned n0 = __float_as_uint(dpp<DPP_XOR1>(__uint_as_float(odd ? hi[0] : lo[0]))), n1 = __float_as_uint(dpp<DPP_XOR1>(__uint_as_float(odd ? hi[1] : lo[1])));
  return u32x4{max(odd ? n0 : hi[0], 1u), max(odd ? n1 : hi[1], 1u), max(odd ? lo[0] : n0, 1u), max(odd ? lo[1] : n1, 1u)};
}
__device__ __forceinline__ u32x4 q_pack(u32x4 a, u32x4 b) {
  u32x2 hi, lo;
  split4((__builtin_bit_cast(f32x4, a) + __builtin_bit_cast(f32x4, b)) * QSCALE, hi, lo);
  return u32x4{hi[0], lo[0], hi[1], lo[1]};
}
__device__ __forceinline__ f32x4 mfma16(u32x4 a, u32x4 b, f32x4 c) {
  return __builtin_amdgcn_mfma_f32_16x16x32_bf16(__builtin_bit_cast(bf16x8, a), __builtin_bit_cast(bf16x8, b), c, 0, 0, 0);
}

constexpr int NW = 8, NMW = 4;
constexpr int NLP = 5, NRP = 8;
constexpr int NTP = 4 * (NLP + NRP);
constexpr int REG = NLP * 6 * 1024;
constexpr int NRING = 4;
constexpr int NS = 6, NA = NS - 1;
__device__ __forceinline__ int seg_beg(int s) { return s >= NS ? NPAIR : ((0xC86540 >> (4 * s)) & 15) * 16; }
__device__ __forceinline__ int seg_k(int t) { return (int)(0x5433222211110000ull >> (4 * (t >> 4))) & 15; }
__device__ __forceinline__ int seg_of_k(int k) { return (0x123540 >> (4 * k)) & 15; }
__device__ __forceinline__ int k_of_seg(int s) { return (0x213450 >> (4 * s)) & 15; }
__device__ __forceinline__ int pair_of(int t) { return t < 64 ? t : (t < 192 ? t + 64 : t - (t < 224 ? 96 : (t < 240 ? 144 : 176))); }
constexpr int OFF_CNT = 0, OFF_REG = 256,
              OFF_SC = OFF_REG + 4 * REG, OFF_BUF = OFF_SC + NPAIR * 16 * 4, OFF_Q = OFF_BUF + NRING * NMW * DH * 4, OFF_ML = OFF_Q + NS * DH * 4,
              OFF_RED = OFF_ML + 128, OFF_END = OFF_RED + 4 * 256 * 4;
enum { F_REL = 0, F_DONE, C_LAST, C_T = 3, F_OUT, C_P, C_K, F_P, F_Q = 8, F_FREE, S_FIRST, F_Q2, C_W = 12, C_SC = 16, C_PART = 24, S_ECHO = 32, S_BURST = 40, S_LATE = 42, S_MOVED = 48, S_SLOW = 56 };
__device__ __forceinline__ int due_k(int p) { return TSTART + ((p * TAUK + min(p, KP1) * KS1 + min(max(p - KP2, 0), KN2) * KS2) >> 6); }
__device__ __forceinline__ int due_v(int p) { return TSTART + OGAP + ((NPAIR * TAUK + KP1 * KS1 + KN2 * KS2 + p * TAUV + min(p, VN0) * VS0 + (min(max(p - 132, 0), VN1) + min(max(p - 196, 0), VN1)) * VS1 + max(p - NPAIR / 2, 0) * (VGAP * 64 / (NPAIR / 2))) >> 6); }
constexpr int AW_LD = DH * 2 + 16, AG_LD = 512 + 16;

__global__ void __launch_bounds__(NW * 64, 2) fused_kernel(const bf16* __restrict__ x, const bf16* __restrict__ kc,
                                                        const bf16* __restrict__ vc, const bf16* __restrict__ wq,
                                                        const bf16* __restrict__ wo, const bf16* __restrict__ wr,
                                                        const bf16* __restrict__ wg, const bf16* __restrict__ wu,
                                                        const bf16* __restrict__ wd, float* ws, bf16* __restrict__ out) {
  __shared__ __attribute__((aligned(16))) unsigned char smem[OFF_END];
  float* s_sc = reinterpret_cast<float*>(smem + OFF_SC);
  float* s_buf = reinterpret_cast<float*>(smem + OFF_BUF);
  float* s_q = reinterpret_cast<float*>(smem + OFF_Q);
  u32x4* s_ml = reinterpret_cast<u32x4*>(smem + OFF_ML);
  lds_u32* s_c = (lds_u32*)(smem + OFF_CNT);

  const int tid = threadIdx.x, lane = tid & 63, r = lane >> 3, j = lane & 7;
  const int wave = __builtin_amdgcn_readfirstlane(tid >> 6);
  const int tw = wave - NMW;
  const int bid = blockIdx.x, xl = bid & 7, idx = bid >> 3;
  const int g = (xl * 32 + idx) / GS, mm = idx % GS;
  const __amdgpu_buffer_rsrc_t rs = __builtin_amdgcn_make_buffer_rsrc(ws, 0, WS_END * 4, 0x00020000);
  const int ro = ((idx & 3) << 3) | (idx >> 2);
  const bool isW = ro < 10, isG = ro >= 10 && ro < 16;
  const int w_ks = xl, w_ct = ro;
  const int g_cg = xl * 6 + (ro - 10);
  const int d_ct = (xl + ((ro - 16) >> 2) * 8) * 2 + (((ro - 16) >> 1) & 1);
  const bool isD = ro >= 16 && ro < 24 && (ro & 1) == 0 && d_ct < DM / 32;
  const bool isH = ro == 22 && xl >= 2 && xl < 6;
  const bool isC = (mm & 7) == 3 || (mm & 7) >= 6;
  const int c_it = (mm >> 3) * 3 + max((mm & 7) - 5, 0);
  const int qf = WS_QF + g * QFS;
  const char* kb = reinterpret_cast<const char*>(kc);
  const char* vb = reinterpret_cast<const char*>(vc);
  const unsigned lo = (r * 24 + j) * 16;
  const int xb = g * GS + mm, c0 = mm * 2 + (g & 1) * RW0 + (g >> 1 & 1) * RW1 + (g >> 2 & 1) * RW2 + (g >> 3 & 1) * RW3 + (g >> 4) * RW4;
  auto ptile = [&](int p) -> unsigned {
    int item = row_of(p >> 6) * NH + g, t = (p & 63) * 16 + c0;
    if ((p >> 6) == 1) {
      item = 2 * NH + ((p & 32) ? xb >> 4 : 24 - ((p & 16) >> 1) + xl);
      t = (p & 32) ? (p & 31) * 32 + (xb & 15) * 2 + (g >> 1 & 1) * 640 + (xl & 1) * 960 + (xl >> 1 & 1) * 32 + (xl >> 2) * 544
                   : (p & 15) * 64 + idx * 2 + (xl & 1) * 768 + (xl >> 1 & 1) * 704 + (xl >> 2) * 128;
    }
    return item * 1024 + (t & 1023);
  };
  auto poff = [&](int p) -> unsigned { return ptile(p) * 3072u; };
  const unsigned lds_base = (unsigned)(size_t)(__attribute__((address_space(3))) unsigned char*)smem + OFF_REG + tw * REG;
  auto merged = [&](int it, int ln) -> f32x4 {
    const float* src = s_buf + (it % NRING) * (NMW * DH) + ((ln >> 1) & 7) * 24 + (ln >> 4) * 8 + (ln & 1) * 4;
    return (*reinterpret_cast<const f32x4*>(src) + *reinterpret_cast<const f32x4*>(src + DH)) +
           (*reinterpret_cast<const f32x4*>(src + 2 * DH) + *reinterpret_cast<const f32x4*>(src + 3 * DH));
  };
#define NONCE() do { unsigned n_ = __builtin_amdgcn_readfirstlane(f) + 1u; while (__any(f == n_)) ++n_; \
    if (lane < GS) st128(rs, qf + (GS + mm) * 32 + lane * 4, u32x4{n_, n_, n_, n_}); f = n_; } while (0)
  unsigned long long pc;
  asm volatile("s_getpc_b64 %0" : "=s"(pc));
  unsigned r0 = 0u;
  unsigned t0 = clk();
  unsigned tb0 = t0;
  if (wave < NMW) {
    Tile ta, tb, tc, td;
    const int c = lane & (GS - 1);
    unsigned f = 0u;
    if (wave == 3) f = ld32(rs, qf + c * 32 + mm * 4);
    if (mm < NPROD) {
      const int hp = g + (mm / 6) * NG, ck = (mm % 6) >> 1, hf = mm & 1, jp = hf * 4 + wave;
      const int bb = lane >> 4, t = lane & 15;
      const u32x4* xp = reinterpret_cast<const u32x4*>(x + row_of(bb) * DM);
      u32x4 xv[5], xs = {0u, 0u, 0u, 0u};
#pragma unroll
      for (int k = 0; k < 5; ++k) xv[k] = xp[t + 16 * k];
      if (t < 10) xs = xp[jp * 10 + t];
      const u32x4* wp = reinterpret_cast<const u32x4*>(wq + (size_t)(jp * 80 + r) * HD + hp * DH + ck * 64) + j;
      u32x4 wv[10];
#pragma unroll
      for (int k = 0; k < 10; ++k) wv[k] = __builtin_nontemporal_load(wp + (size_t)k * 8 * (HD / 8));
      if (wave == 0) s_c[lane] = lane == C_K ? NTP + NMW : 0u;
      MEMFENCE(); LGKM0(); __builtin_amdgcn_s_barrier(); MEMFENCE();
      if (wave == 3) { NONCE(); }
      float sm = 0.f;
#pragma unroll
      for (int k = 0; k < 5; ++k)
#pragma unroll
        for (int d = 0; d < 4; ++d) sm += bf_lo(xv[k][d]) + bf_hi(xv[k][d]);
      const float mean = row_sum(sm) * (1.0f / DM);
      float sv = 0.f;
#pragma unroll
      for (int k = 0; k < 5; ++k)
#pragma unroll
        for (int d = 0; d < 4; ++d) { const float a = bf_lo(xv[k][d]) - mean, c2 = bf_hi(xv[k][d]) - mean; sv += a * a + c2 * c2; }
      const float rstd = 1.0f / sqrtf(row_sum(sv) * (1.0f / DM) + LN_EPS);
      float* s_xn = s_buf + wave * 320;
      if (t < 10) {
#pragma unroll
        for (int d = 0; d < 4; ++d) {
          s_xn[(t * 8 + 2 * d) * 4 + bb] = (bf_lo(xs[d]) - mean) * rstd;
          s_xn[(t * 8 + 2 * d + 1) * 4 + bb] = (bf_hi(xs[d]) - mean) * rstd;
        }
      }
      LGKM0();
      f32x2 qa[16];
#pragma unroll
      for (int i = 0; i < 16; ++i) qa[i] = f32x2{0.f, 0.f};
#pragma unroll
      for (int k = 0; k < 10; ++k) {
        const f32x4 xn = *reinterpret_cast<const f32x4*>(&s_xn[(k * 8 + r) * 4]);
#pragma unroll
        for (int d = 0; d < 4; ++d) {
          const f32x2 w2 = bf_pair(wv[k][d]);
#pragma unroll
          for (int e = 0; e < 4; ++e) qa[e * 4 + d] = fma2(xn[e], w2, qa[e * 4 + d]);
        }
      }
      float t16[16], t8[8];
#pragma unroll
      for (int i = 0; i < 16; ++i) t16[i] = tsum32(qa[i >> 1][i & 1], qa[8 + (i >> 1)][i & 1]);
#pragma unroll
      for (int i = 0; i < 8; ++i) t8[i] = tsum16(t16[i], t16[i + 8]);
#pragma unroll
      for (int i = 0; i < 8; ++i) t8[i] += dpp<DPP_ROR8>(t8[i]);
      const bool up = (lane & 8) != 0;
      f32x4* r4 = reinterpret_cast<f32x4*>(smem + OFF_RED);
      r4[wave * 64 + lane] = f32x4{up ? t8[4] : t8[0], up ? t8[5] : t8[1], up ? t8[6] : t8[2], up ? t8[7] : t8[3]};
      LGKM0();
      if (lds_add(s_c + C_P, lane) == NMW - 1) {
        asm volatile("" ::: "memory");
        const f32x4 o4 = (r4[lane] + r4[64 + lane]) + (r4[128 + lane] + r4[192 + lane]);
        st128nt(rs, WS_QP + ((hp * 2 + hf) * NB + bb) * DH + ck * 64 + j * 8 + (up ? 4 : 0), __builtin_bit_cast(u32x4, o4));
        VMCNT(0);
        if (lane == 0) s_c[F_OUT] = 1u;
      }
    } else {
      if (wave == 0) s_c[lane] = lane == C_K ? NTP + NMW : 0u;
      MEMFENCE(); LGKM0(); __builtin_amdgcn_s_barrier(); MEMFENCE();
      if (wave == 3) { NONCE(); }
      else {
        typedef __attribute__((address_space(1))) const unsigned g_u32;
        typedef __attribute__((address_space(1))) const char g_char;
        const int id = ((idx >> 3) * 2 + (mm & 1)) * 3 + wave;
        if (lane < CODE / (24 * 64)) {
          const unsigned v = *(g_u32*)((pc & ~255ull) + id * (CODE / 24) + lane * 64);
          asm volatile("" :: "v"(v));
        }
        if (lane < 7 && id < 21) {
          const int st = id < 8 ? (0x1320 >> ((id >> 1) * 4)) & 3 : (id >> 1) & 3, hf = id & 1;
          int head = row_of(st) * NH + xl * 4 + hf * 2;
          unsigned off = min(lane << 20, (6 << 20) - 64);
          if (st == 1) {
            head = 2 * NH + (hf ? 16 + xl : 2 * xl);
            if (hf) off = lane < 4 ? min(lane << 20, (3 << 20) - 64) : 8 * (3 << 20) + min((lane - 4) * (3 << 19), (3 << 20) - 64);
          }
          g_char* p = (g_char*)(id < 8 ? kb : vb) + (size_t)head * (3 << 20) + off;
          if (id >= 16) p = (g_char*)(id == 16 ? wo + xl * 4 * DH * DM : (id == 17 ? wg : (id == 18 ? wu : wd))) + min(lane * (id == 16 ? 163840 : 327680), (id == 16 ? 983040 : 1966080) - 64);
          if (id >= 20) p = (g_char*)(lane ? wr : out);
          until(t0, TPT + id * TPS);
          const unsigned v = __builtin_nontemporal_load((g_u32*)p);
          asm volatile("" :: "v"(v));
        }
      }
    }
    unsigned tk_e = ptile(pair_of(lane * 4)) | (pair_of(lane * 4) << 17) | (seg_k(lane * 4) << 25) | ((lane < 48 ? 4u : (lane < 56 ? 5u : 6u)) << 28), tv_o = poff(lane * 4 + wave);
    int tk_d = due_k(lane * 4), tv_d = due_v(lane * 4 + wave);
    asm volatile("" : "+v"(tk_e), "+v"(tv_o), "+v"(tk_d), "+v"(tv_d));
    MEMFENCE();
    if (wave == 3) {
      __builtin_amdgcn_s_setprio(3);
      const unsigned nonce = f;
      unsigned echoed = 0u;
      if (mm < NPROD) {
        echoed = ld32(rs, qf + (GS + c) * 32);
        asm volatile("" : "+v"(echoed) :: "memory");
        lds_wait<1>(s_c + F_OUT, 1u);
        if (lane < GS) st128(rs, qf + mm * 32 + lane * 4, u32x4{echoed, echoed, echoed, echoed});
      }
      until(t0, QWAIT);
#pragma unroll 1
      for (unsigned spin = 0; spin < SPIN_CAP; ++spin) {
        const unsigned nv = ld32(rs, qf + (GS + c) * 32);
        f = ld32(rs, qf + c * 32 + mm * 4);
        const bool done = __all(c == mm || c >= NPROD || f == nonce);
        if (mm < NPROD && __any(nv != echoed)) {
          if (lane < GS) st128(rs, qf + mm * 32 + lane * 4, u32x4{nv, nv, nv, nv});
          echoed = nv;
        }
        if (done) break;
        __builtin_amdgcn_s_sleep(8);
      }
      if (lane < GS) s_c[S_ECHO + lane] = echoed;
      if (lane < DH / 4) {
        u32x4 p[3][2];
#pragma unroll
        for (int it = 0; it < 3; ++it)
#pragma unroll
          for (int k = 0; k < 2; ++k) p[it][k] = ld128nt(rs, WS_QP + ((g * 2 + k) * NB + (it ? it + 1 : 0)) * DH + lane * 4);
#pragma unroll
        for (int it = 0; it < 3; ++it) {
          *reinterpret_cast<u32x4*>(&s_q[(it ? it + 3 : 0) * DH + lane * 4]) = q_pack(p[it][0], p[it][1]);
          if (it != 1) { LGKM0(); if (lane == 0) s_c[F_Q] = it + 1; MEMFENCE(); }
        }
      }
    }
    __builtin_amdgcn_s_setprio(3);
    until(t0, due_k(NTP + wave));
    tb0 = lds_get(s_c + S_LATE);
    if ((unsigned)(tb0 - t0 + 100u) > 500u) tb0 = t0;
    until(tb0, due_k(NTP + wave));
    Table tt = {tb0, 1 << 20, 0, 0, 0, {0u, 0u}, (unsigned)(size_t)(s_c + S_MOVED + wave), 0u};
    bool common = false;
    tile_load(ta, kb, poff(NTP + wave) + lo, 0);
    tile_load(tb, kb, poff(NTP + wave) + lo, 1);
    MEMFENCE();
    {
      u32x2 q[12];
      int cp = NTP + wave, it = 0, nxt = lds_add(s_c + C_K, lane), passed = 0, ci = -1;
      auto kuse = [&](Tile& xa, Tile& xb_) __attribute__((always_inline)) {
        if (it != ci) {
          LGKM0();
          for (; passed < it; ++passed) bump(s_c + C_SC + passed);
          lds_wait<1>(s_c + (it < 3 ? F_Q : F_Q2), min(it + 1, 3));
          q_load(q, s_q + seg_of_k(it) * DH, j);
          if (!SOFT && !common) { const unsigned d = lds_get(s_c + S_FIRST); if (d) { tt.t0 -= d - 1u; common = true; tt.was = 1 << 20; } }
          tt.agree(s_c + S_MOVED);
          ci = it;
        }
        const int tk = arrive(s_c + C_K);
        tt.listen(s_c + S_SLOW + 2 * wave);
        pair_put(tile_score(xa, q), tile_score(xb_, q), s_sc + cp * 16 + r, j);
        MEMFENCE();
        return arrived(tk);
      };
      auto kstep = [&](Tile& xa, Tile& xb_, Tile& ya, Tile& yb) __attribute__((always_inline)) {
        const unsigned e = __builtin_amdgcn_readlane(tk_e, nxt >> 2);
        MEMFENCE();
        const int deep = tt.gate(__builtin_amdgcn_readlane(tk_d, nxt >> 2) + (nxt & 3) * (TAUK / 64));
        MEMFENCE();
        pair_issue(ya, yb, kb, ((e & 0x1fc00u) | ((e + ((nxt & 3) << (e >> 28))) & 1023u)) * 3072u + lo, deep);
        const int n2 = kuse(xa, xb_);
        cp = ((e >> 17) & 255) + (nxt & 3); it = (e >> 25) & 7; nxt = n2;
      };
      bool swap;
      for (;;) {
        if (nxt >= NPAIR) { swap = false; break; }
        kstep(ta, tb, tc, td);
        if (nxt >= NPAIR) { swap = true; break; }
        kstep(tc, td, ta, tb);
      }
      tt.gate(due_k(NPAIR + wave));
      MEMFENCE();
      const unsigned* pw = reinterpret_cast<const unsigned*>(wo + (xl * 128 + idx * 4 + wave) * 3840) + lane * 16;
      const unsigned v0 = pw[0], v1 = pw[56 * 16];
      MEMFENCE();
      if (swap) { ta = tc; tb = td; }
      kuse(ta, tb);
      for (; passed < NS; ++passed) bump(s_c + C_SC + passed);
      tt.gate(due_v(wave));
      MEMFENCE();
      pair_issue(ta, tb, vb, __builtin_amdgcn_readlane(tv_o, 0) + lo, 2);
      asm volatile("" :: "v"(v0), "v"(v1));
    }
    const unsigned sc_r = (unsigned)(size_t)(__attribute__((address_space(3))) float*)s_sc + r * 4;
    f32x2 acc[12];
    auto vstep = [&](Tile& xa, Tile& xb_, Tile& ya, Tile& yb, int p, bool fin) __attribute__((always_inline)) {
      const unsigned off = __builtin_amdgcn_readlane(tv_o, (p >> 2) + 1) + lo;
      MEMFENCE();
      const int deep = tt.gate(__builtin_amdgcn_readlane(tv_d, (p >> 2) + 1), p >= NPAIR - ENDT * NMW ? ENDS : 0);
      float pa, pb;
      asm volatile("ds_read_b32 %0, %2\n\tds_read_b32 %1, %2 offset:32" : "=&v"(pa), "=&v"(pb) : "v"(sc_r + p * 64) : "memory");
      MEMFENCE();
      pair_issue(ya, yb, vb, off, deep);
      asm volatile("s_waitcnt lgkmcnt(0)" : "+v"(pa), "+v"(pb) :: "memory");
      if (!fin) tt.listen(s_c + S_SLOW + 2 * wave);
      else bump(s_c + C_LAST);
      pv_last(pa, xa, acc);
      pv_last(pb, xb_, acc);
      MEMFENCE();
    };
    typedef __attribute__((address_space(3))) float lds_f32;
    auto fold8 = [&](lds_f32* dst) __attribute__((always_inline)) {
      float t12[12], t6[6];
#pragma unroll
      for (int i = 0; i < 12; ++i) t12[i] = tsum32(acc[i >> 1][i & 1], acc[6 + (i >> 1)][i & 1]);
#pragma unroll
      for (int i = 0; i < 6; ++i) t6[i] = tsum16(t12[i], t12[i + 6]);
#pragma unroll
      for (int i = 0; i < 6; ++i) t6[i] += dpp<DPP_ROR8>(t6[i]);
      auto d2 = (__attribute__((address_space(3))) f32x2*)dst;
      d2[0] = f32x2{t6[0], t6[1]}; d2[1] = f32x2{t6[2], t6[3]}; d2[2] = f32x2{t6[4], t6[5]};
      CFENCE();
    };
    lds_f32* dst = (lds_f32*)s_buf + wave * DH + j * 24 + (lane >> 4) * 6;
#pragma unroll 1
    for (int it = 0;; ++it) {
      lds_wait<1>(s_c + F_P, k_of_seg(it) + 1);
      if (!SOFT && !common) { const unsigned d = lds_get(s_c + S_FIRST); if (d) { tt.t0 -= d - 1u; common = true; tt.was = 1 << 20; } }
      tt.agree(s_c + S_MOVED);
#pragma unroll
      for (int i = 0; i < 12; ++i) acc[i] = f32x2{0.f, 0.f};
#pragma unroll 1
      for (int p = seg_beg(it) + wave; p < min(seg_beg(it + 1), NPAIR - 2 * NMW); p += 2 * NMW) {
        vstep(ta, tb, tc, td, p, false);
        vstep(tc, td, ta, tb, p + NMW, false);
      }
      if (it >= NRING) lds_wait<1>(s_c + F_FREE, it - NRING + 1);
      if (it == NS - 1) break;
      fold8(dst + (it % NRING) * (NMW * DH));
      bump(s_c + C_PART + it);
    }
    dst += ((NS - 1) % NRING) * (NMW * DH);
    const int ln = lane < DH / 4 ? lane : 0;
    auto src = (__attribute__((address_space(3))) f32x4*)((lds_f32*)s_buf + ((NS - 1) % NRING) * (NMW * DH) + ((ln >> 1) & 7) * 24 + (ln >> 4) * 8 + (ln & 1) * 4);
    int so = (lane < DH / 4 ? WS_PO + xb * DH + lane * 4 : WS_END) * 4;
    const int p = NPAIR - NMW + wave;
    float pa = s_sc[p * 16 + r], pb = s_sc[p * 16 + 8 + r];
    asm volatile("" : "+v"(dst), "+v"(src), "+v"(so), "+v"(pa), "+v"(pb));
    vstep(ta, tb, tc, td, NPAIR - 2 * NMW + wave, true);
    pv_last(pa, tc, acc);
    pv_last(pb, td, acc);
    fold8(dst);
    const int k = arrive(s_c + C_PART + NS - 1);
    const f32x4 m = (src[0] + src[DH / 4]) + (src[2 * DH / 4] + src[3 * DH / 4]);
    u32x4 o = {nz_bits(m[0]), nz_bits(m[1]), nz_bits(m[2]), nz_bits(m[3])};
    asm volatile("" : "+v"(o));
    if (arrived(k) == NMW - 1) {
      __builtin_amdgcn_raw_buffer_store_b128(o, rs, so, 0, AUX_SC1);
      bump(s_c + F_DONE);
    }
    return;
  }

  {
    Tile pk[NRP * 2];
    __builtin_amdgcn_s_setprio(3);
    const unsigned it0 = (row_of(0) * NH + g) * 1024, ct = c0 + tw * 16;
    auto toff = [&](int turn) -> unsigned { return (it0 + ((turn * 64 + ct) & 1023)) * 3072u + lo; };
    unsigned tbw = 0u;
#pragma unroll 1
    for (int e = 0; e < NLP * 2; ++e) {
      const char* src = kb + (toff(e >> 1) + (e & 1) * 3072);
      if (e >= 2) until(tbw, (e * 2 * (TAUK + KS1)) >> 6);
#pragma unroll
      for (int i = 0; i < 3; ++i) glds16_nt(src + i * 128, lds_base + (e * 3 + i) * 1024);
      if (e == 1) {
        VMCNT(AV);
        tb0 = t0 + min(max((int)(clk() - t0) - ANCH, 0), 300);
        tbw = tb0 + TSTART + ((tw * TAUK) >> 6) - TLEAD;
        __builtin_amdgcn_s_barrier();
        if (tw == 0 && lane == 0) s_c[S_LATE] = tb0;
      }
      VMCNT(PFD);
    }
#pragma unroll
    for (int e = 0; e < NRP * 2; ++e) {
      until(tbw, ((NLP * 2 + e) * 2 * (TAUK + KS1)) >> 6);
      tile_load(pk[e], kb, toff(NLP + (e >> 1)), e & 1);
      MEMFENCE();
      if (e + 1 < NRP * 2) VMCNT(PFD);
    }
    if (tw == 0) {
      r0 = ld32(rs, WS_HR + bid * 2);
      if (bid == LEADER) {
        u32x4 n;
#pragma unroll
        for (int k = 0; k < 4; ++k) n[k] = ld32(rs, WS_HA + (lane * 4 + k) * 32) + 1u;
        st128(rs, WS_HN + lane * 4, n);
      }
    }
    __builtin_amdgcn_s_setprio(0);
    lds_wait<1>(s_c + F_Q, HPG);
    u32x2 q[12];
    q_load(q, s_q, j);
    const u32x4* s_kv = reinterpret_cast<const u32x4*>(smem + OFF_REG + tw * REG) + lane;
    static_assert(NLP * 4 <= PPI && NTP <= HPG * PPI, "tail tiles: batch row 0 only");
#pragma unroll 1
    for (int e = 0; e < NLP * 2; ++e) {
      Tile tmp;
#pragma unroll
      for (int i = 0; i < 3; ++i) tmp.d[i] = s_kv[(e * 3 + i) * 64];
      score_put(tile_score(tmp, q), s_sc + ((e >> 1) * 4 + tw) * 16 + (e & 1) * 8 + r, j);
    }
#pragma unroll
    for (int e = 0; e < NRP * 2; ++e) {
      if ((NLP * 2 + e) * 2 == PPI) q_load(q, s_q + DH, j);
      score_put(tile_score(pk[e], q), s_sc + ((NLP + (e >> 1)) * 4 + tw) * 16 + (e & 1) * 8 + r, j);
      __builtin_amdgcn_sched_barrier(0);
    }
    asm volatile("s_waitcnt vmcnt(0) lgkmcnt(0)" ::: "memory");
#pragma unroll
    for (int it = 0; it * PPI < NTP; ++it) lds_add(s_c + C_SC + it, lane);
    if (tw == 0) {
      until((t0 + ZT + 511u) & ~511u, 0);
      const u32x4 z = {0u, 0u, 0u, 0u};
      st128(rs, lane < 48 ? WS_PO + xb * DH + lane * 4 : WS_PML + xb * 64 + (lane - 48) * 4, z);
      const int o = isW ? WS_SLAB + w_ks * NB * DM + w_ct * 64 : (isG ? WS_ACT + g_cg * 128 : (isH ? WS_H + (xl - 2) * 768 : WS_OC + (c_it * NG + g) * DH));
      st128(rs, lane < (isG ? 32 : (isC ? 48 : (isW || isH ? 64 : 0))) ? o + (isW ? (lane >> 4) * DM + (lane & 15) * 4 : lane * 4) : WS_END, z);
      if (isH) { st128(rs, o + 256 + lane * 4, z); st128(rs, o + 512 + lane * 4, z); }
      VMCNT(0);
    }
  }

  const int mr = lane & 15, qd = lane >> 4;
  unsigned char* reg = smem + OFF_REG + tw * REG;
  float* red = reinterpret_cast<float*>(smem + OFF_RED);
#define PERM_LO(hi_, lo_) __builtin_amdgcn_perm(hi_, lo_, 0x05040100u)
#define PERM_HI(hi_, lo_) __builtin_amdgcn_perm(hi_, lo_, 0x07060302u)
  int served = 0;
  auto done = [&](int n) {
    if (tw == 3 && served < min(n, NA)) {
      const int from = served;
      int ln = lane;
      asm volatile("" : "+v"(ln));
#pragma unroll 1
      for (; served < min(n, NA); ++served) {
        lds_wait<8>(s_c + C_PART + served, NMW);
        const f32x4 o = merged(served, ln < DH / 4 ? ln : 0);
        const float am = max_x16_32(row_max(fmaxf(fmaxf(fabsf(o[0]), fabsf(o[1])), fmaxf(fabsf(o[2]), fabsf(o[3])))));
        const float inv = am > 1e-30f ? 32767.0f / am : 0.f;
        const int q0 = __float2int_rn(o[0] * inv), q1 = __float2int_rn(o[1] * inv), q2 = __float2int_rn(o[2] * inv), q3 = __float2int_rn(o[3] * inv);
        if (ln < DH / 4) reinterpret_cast<u32x2*>(s_sc + served * (DH / 2))[ln] = u32x2{PERM_LO((unsigned)q1, (unsigned)q0), PERM_LO((unsigned)q3, (unsigned)q2)};
        const bool nan = __any((o[0] + o[1]) + (o[2] + o[3]) != (o[0] + o[1]) + (o[2] + o[3]));
        if (ln == 0) s_ml[served][2] = nan ? 0x7fc00000u : nz_bits(am * (1.0f / 32767.0f));
        LGKM0();
        if (ln == 0) s_c[F_FREE] = served + 1;
      }
#pragma unroll 1
      for (int c = from * 24 + ln; c < served * 24; c += 64) st128nt(rs, WS_PQ + xb * 5 * (DH / 2) + c * 4, reinterpret_cast<const u32x4*>(s_sc)[c]);
      VMCNT(0);
      if (ln < 8) st128(rs, WS_PML + xb * 64 + (from ? 32 : 0) + ln * 4, s_ml[ln]);
      if (ln == 0) s_c[S_BURST + (from ? 1 : 0)] = clk();
      CFENCE();
      if (ln == 0) __hip_atomic_fetch_add(s_c + F_DONE, (unsigned)(served - from), __ATOMIC_RELAXED, __HIP_MEMORY_SCOPE_WORKGROUP);
    }
    if (n > NA) lds_wait<1>(s_c + F_DONE, n); else lds_wait<8>(s_c + F_DONE, n);
  };
  auto done_all = [&]() {
#pragma unroll 1
    for (int n = 4; n <= NA; ++n) done(n);
  };
  const int lp = (lane < DH / 4 ? lane : 0) * 4;
  auto weight = [&](int it, int x0, int cnt) -> float {
    const int mlw = WS_PML + (x0 + (lane & (cnt - 1))) * 64 + (it < 4 ? 0 : 32) + it * 4;
    u32x4 ml;
#pragma unroll 1
    for (unsigned spin = 0; spin < SPIN_CAP; ++spin) {
      ml = ld128(rs, mlw);
      if (__all(min4(ml) != 0u)) break;
      __builtin_amdgcn_s_sleep(16);
    }
    const float mk = __uint_as_float(ml[0]);
    const float e = __builtin_amdgcn_exp2f(mk - max_x16_32(row_max(mk)));
    return e / (wave_sum(e * __uint_as_float(ml[1])) * ((float)cnt * (1.0f / 64.0f))) * __uint_as_float(ml[2]);
  };
  const unsigned tok = __builtin_amdgcn_readfirstlane(r0) + 1u;
  unsigned echoed = 0u, fold = 0u;
  int slow = 0, prev = 0, later = 0;
  auto lag = [&]() {
    const unsigned v = __hip_atomic_load(s_c + S_MOVED + (lane & 7), __ATOMIC_RELAXED, __HIP_MEMORY_SCOPE_WORKGROUP);
    fold += v;
    unsigned a = 0u;
#pragma unroll
    for (int k = 0; k < 8; ++k) a += __builtin_amdgcn_readlane(v, k);
    return a >> 2;
  };
  auto tell = [&]() { if (lane < 4) { s_c[S_SLOW + 2 * lane] = slow - later; s_c[S_SLOW + 2 * lane + 1] = fold; } };
  auto round = [&](unsigned rd, bool lead) {
    const int tr = RT0 + (int)(rd - 1) * RTS;
    until(t0, tr);
    const unsigned bh = lag();
    if (bh > RMIN && lane < 8) st128(rs, WS_HA + bid * 32 + lane * 4, u32x4{echoed + rd, tok, tb0, bh});
    if (lead) { until(t0, tr + RA1); leader_release(rs, lane, tb0, rd, slow, prev); }
    until(t0, tr + RA2);
    const u32x2 rv = ld64(rs, WS_HR + bid * 2);
    slow = (int)__builtin_amdgcn_readfirstlane(rv[1]) >> 16;
#pragma unroll 1
    for (unsigned k = min(__builtin_amdgcn_readfirstlane(rv[0]) - tok, rd) + 1u; k <= rd; ++k) slow = slow > 0 ? max(slow - GB, 0) : slow - (k <= PRD ? PRB : 0);
    tell();
  };
  auto prep = [&](bool lead) {
    if (tw == 0) {
      const int c = lane & (GS - 1);
      unsigned qe = s_c[S_ECHO + c];
      bool first = true, released = false, sent = false;
#pragma unroll 1
      while ((int)(clk() - t0) < HST) {
        const unsigned qn = ld32(rs, qf + (GS + c) * 32);
        if (mm < NPROD && __any(qn != qe)) {
          if (lane < GS) st128(rs, qf + mm * 32 + lane * 4, u32x4{qn, qn, qn, qn});
          qe = qn;
        }
        __builtin_amdgcn_s_sleep(64);
      }
      const unsigned behind = lag();
#pragma unroll 1
      for (unsigned spin = 0; spin < SPIN_CAP && !released; ++spin) {
        const unsigned qn = ld32(rs, qf + (GS + c) * 32);
        const u32x2 rv = ld64(rs, WS_HR + bid * 2);
        const unsigned nv = __builtin_amdgcn_readfirstlane(ld32(rs, WS_HN + bid));
        if (__builtin_amdgcn_readfirstlane(rv[0]) - tok <= NRD) {
          released = true;
          slow = (int)__builtin_amdgcn_readfirstlane(rv[1]) >> 16;
          if (SOFT) later = __builtin_amdgcn_readfirstlane(min((tb0 - rv[1]) & 0xffffu, 300u));
          if (lane == 0) { s_c[S_FIRST] = min((tb0 - rv[1]) & 0xffffu, 300u) + 1u; }
        } else {
          if (mm < NPROD && __any(qn != qe)) {
            if (lane < GS) st128(rs, qf + mm * 32 + lane * 4, u32x4{qn, qn, qn, qn});
            qe = qn;
          }
          if (first || nv != echoed) {
            echoed = nv; first = false;
            if (lane < 8) st128(rs, WS_HA + bid * 32 + lane * 4, u32x4{echoed, tok, tb0, behind});
          }
          if (lead && !sent) sent = leader_release(rs, lane, tb0, 0u, slow, prev);
          __builtin_amdgcn_s_sleep(32);
        }
      }
      tell();
      if (lane == 0) s_c[F_REL] = 1u;
      if (lane < DH / 4) {
        u32x4 p[3][2];
#pragma unroll
        for (int e = 0; e < 3; ++e)
#pragma unroll
          for (int k = 0; k < 2; ++k) p[e][k] = ld128nt(rs, WS_QP + (((e < 2 ? 24 - e * 8 + (xb >> 5) : xb >> 4) * 2 + k) * NB + 1) * DH + lane * 4);
#pragma unroll
        for (int e = 0; e < 3; ++e) *reinterpret_cast<u32x4*>(&s_q[(1 + e) * DH + lane * 4]) = q_pack(p[e][0], p[e][1]);
      }
      LGKM0();
      if (lane == 0) s_c[F_Q2] = 3u;
#pragma unroll 1
      for (unsigned rd = 1; rd <= NRD; ++rd) round(rd, lead);
    }
    if (tw == 3) {
#pragma unroll 1
      for (int ik = 0; ik < NS; ++ik) {
        const int it = seg_of_k(ik), nv = (seg_beg(it + 1) - seg_beg(it)) >> 4;
        lds_wait<4>(s_c + C_SC + ik, ik == 0 ? NW : NMW);
        f32x4* sp = reinterpret_cast<f32x4*>(s_sc + seg_beg(it) * 16) + lane;
        f32x4 v[4];
#pragma unroll
        for (int k = 0; k < 4; ++k) v[k] = sp[(k & (nv - 1)) * 64];
        const f32x4 mx = __builtin_elementwise_max(__builtin_elementwise_max(v[0], v[1]), __builtin_elementwise_max(v[2], v[3]));
        const float m = max_x16_32(row_max(fmaxf(fmaxf(mx[0], mx[1]), fmaxf(mx[2], mx[3]))));
        float l = 0.f;
#pragma unroll
        for (int k = 0; k < 4; ++k) {
#pragma unroll
          for (int e = 0; e < 4; ++e) v[k][e] = __builtin_amdgcn_exp2f(v[k][e] - m);
          if (k < nv) {
            l += (v[k][0] + v[k][1]) + (v[k][2] + v[k][3]);
            sp[k * 64] = v[k];
          }
        }
        l = wave_sum(l);
        if (lane == 0) s_ml[it] = u32x4{nz_bits(m), nz_bits(l), 0x3f800000u, 0x3f800000u};
        LGKM0();
        if (lane == 0) s_c[F_P] = ik + 1;
      }
    }
  };
  auto pull = [&]() {
    typedef __attribute__((address_space(1))) const unsigned g_u32;
    const int id = (ro - 16) * 4 + tw;
    g_u32* p1[2];
#pragma unroll
    for (int i = 0; i < 2; ++i) {
      const int l = id * 60 + min(i * 32 + (lane >> 1), 59), m = l >= 1920 ? l - 1920 : l;
      p1[i] = (g_u32*)((l >= 1920 ? wu : wg) + (size_t)(m / 3) * FF + xl * 192 + (m % 3) * 64 + (lane & 1) * 32);
    }
    g_u32* p2 = (g_u32*)(wd + (size_t)(id * 24 + min(lane >> 1, 23)) * DM + xl * 64 + (lane & 1) * 32);
    asm volatile("" : "+v"(p1[0]), "+v"(p1[1]), "+v"(p2));
    lds_wait<1>(s_c + C_LAST, NMW);
    const unsigned tp = clk();
    until(tp, PW);
    const unsigned v0 = p1[0][0], v1 = p1[1][0];
    MEMFENCE();
    until(tp, PW + DPW);
    const unsigned v2 = p2[0], v3 = p2[xl < 2 ? 256 : 0];
    asm volatile("" :: "v"(v0), "v"(v1), "v"(v2), "v"(v3));
  };
  if (isW) {
    u32x4 bx[24];
    const char* p = reinterpret_cast<const char*>(wo + (size_t)((w_ks * 4 + tw) * DH + r) * DM + w_ct * 64 + j * 8);
    prep(false);
    if (tw != 0) { lds_wait<16>(s_c + F_P, NS); pause(clk(), OLATE + (tw % 3) * 24 * OSTEP); }
    const unsigned tw0 = clk();
#pragma unroll 1
    for (int i = 0; i < 24; ++i) { until(tw0, i * OSTEP); glds16_nt(p, lds_base + i * 1024); p += 8 * DM * 2; VMCNT(WFD); }
    asm volatile("s_waitcnt vmcnt(0)" ::: "memory");
#pragma unroll
    for (int s = 0; s < 6; ++s) {
      u32x2 e[8];
#pragma unroll
      for (int k = 0; k < 8; ++k) e[k] = *reinterpret_cast<const u32x2*>(reg + (s * 32 + qd * 8 + k) * 128 + mr * 8);
#pragma unroll
      for (int k = 0; k < 4; ++k) {
        bx[s * 4 + 0][k] = PERM_LO(e[2 * k + 1][0], e[2 * k][0]); bx[s * 4 + 1][k] = PERM_HI(e[2 * k + 1][0], e[2 * k][0]);
        bx[s * 4 + 2][k] = PERM_LO(e[2 * k + 1][1], e[2 * k][1]); bx[s * 4 + 3][k] = PERM_HI(e[2 * k + 1][1], e[2 * k][1]);
      }
    }
#pragma unroll
    for (int s = 0; s < 24; s += 4) asm volatile("" : "+v"(bx[s]), "+v"(bx[s + 1]), "+v"(bx[s + 2]), "+v"(bx[s + 3]));
    LGKM0();
    lds_wait<8>(s_c + F_REL, 1u);
    const int hh = w_ks * 4 + tw;
#pragma unroll 1
    for (int pass = 0; pass < 3; ++pass) {
      if (pass < 2) {
        done(pass ? NA : 4);
        pause(lds_get(s_c + S_BURST + pass), pass ? WDELAY2 : WDELAY2 + 300);
        const int b0 = pass * 2, b1 = pass + 1;
        u32x4 v0, v1;
#pragma unroll 1
        for (unsigned spin = 0; spin < SPIN_CAP; ++spin) {
          v0 = ld128(rs, WS_OC + (b0 * NH + hh) * DH + lp);
          v1 = ld128(rs, WS_OC + (b1 * NH + hh) * DH + lp);
          if (__all(min(min4(v0), min4(v1)) != 0u)) break;
          __builtin_amdgcn_s_sleep(16);
        }
        if (lane < DH / 4) {
          *reinterpret_cast<u32x2*>(reg + (b0 * 4) * AW_LD + lane * 8) = u32x2{v0[0], v0[1]};
          *reinterpret_cast<u32x2*>(reg + (b0 * 4 + 1) * AW_LD + lane * 8) = u32x2{v0[2], v0[3]};
          *reinterpret_cast<u32x2*>(reg + (b1 * 4) * AW_LD + lane * 8) = u32x2{v1[0], v1[1]};
          *reinterpret_cast<u32x2*>(reg + (b1 * 4 + 1) * AW_LD + lane * 8) = u32x2{v1[2], v1[3]};
        }
      } else {
        const float wk = weight(NS - 1, hh * GS, GS);
        float w[GS];
#pragma unroll
        for (int k = 0; k < GS; ++k) { w[k] = __uint_as_float(__builtin_amdgcn_readlane(__float_as_uint(wk), k)); asm volatile("" : "+s"(w[k])); }
        done(NS);
        __builtin_amdgcn_s_setprio(3);
        const int po = WS_PO + hh * GS * DH;
        u32x4 pv[GS];
#pragma unroll 1
        for (unsigned spin = 0; spin < SPIN_CAP; ++spin) {
#pragma unroll
          for (int k = 0; k < GS; ++k) pv[k] = __builtin_amdgcn_raw_buffer_load_b128(rs, lp * 4, (po + k * DH) * 4, AUX_SC1);
          unsigned mn = min4(pv[0]);
#pragma unroll
          for (int k = 1; k < GS; ++k) mn = min(mn, min4(pv[k]));
          if (__all(mn != 0u)) break;
          __builtin_amdgcn_s_sleep(1);
        }
        f32x4 o = w[0] * __builtin_bit_cast(f32x4, pv[0]), o2 = w[1] * __builtin_bit_cast(f32x4, pv[1]);
#pragma unroll
        for (int k = 2; k < GS; k += 2) { o += w[k] * __builtin_bit_cast(f32x4, pv[k]); o2 += w[k + 1] * __builtin_bit_cast(f32x4, pv[k + 1]); }
        u32x2 hi, lw;
        split4(o + o2, hi, lw);
        if (lane < DH / 4) {
          *reinterpret_cast<u32x2*>(reg + ((NB - 1) * 4) * AW_LD + lane * 8) = hi;
          *reinterpret_cast<u32x2*>(reg + ((NB - 1) * 4 + 1) * AW_LD + lane * 8) = lw;
        }
      }
      CFENCE();
      u32x4 a[6];
#pragma unroll
      for (int s = 0; s < 6; ++s) a[s] = *reinterpret_cast<const u32x4*>(reg + mr * AW_LD + s * 64 + qd * 16);
      __builtin_amdgcn_sched_barrier(0);
      f32x4 c[4];
#pragma unroll
      for (int t = 0; t < 4; ++t) c[t] = f32x4{0.f, 0.f, 0.f, 0.f};
#pragma unroll
      for (int s = 0; s < 6; ++s)
#pragma unroll
        for (int t = 0; t < 4; ++t) c[t] = mfma16(a[s], bx[s * 4 + t], c[t]);
      const bool mine = max(qd - 1, 0) == pass;
      f32x4* r4 = reinterpret_cast<f32x4*>(red);
      if (mine) r4[tw * 64 + lane] = f32x4{c[0][0] + c[0][1], c[1][0] + c[1][1], c[2][0] + c[2][1], c[3][0] + c[3][1]};
      const int k = arrive(s_c + C_W + pass);
      f32x4 sum = (r4[lane] + r4[64 + lane]) + (r4[128 + lane] + r4[192 + lane]);
      asm volatile("" : "+v"(sum));
      if (arrived(k) == 3) {
        if (mine) st128(rs, WS_SLAB + (w_ks * NB + qd) * DM + w_ct * 64 + mr * 4, u32x4{nz_bits(sum[0]), nz_bits(sum[1]), nz_bits(sum[2]), nz_bits(sum[3])});
      }
    }
  } else if (isG || isH) {
    f32x2* rst = reinterpret_cast<f32x2*>(red);
    const int hb = isH ? xl - 2 : NB - 1;
    uint2 wr4 = make_uint2(0u, 0u), xg = wr4;
    if (lane < 40) {
      wr4 = *reinterpret_cast<const uint2*>(wr + tw * 160 + lane * 4);
      xg = *reinterpret_cast<const uint2*>(x + row_of(hb) * DM + tw * 160 + lane * 4);
    }
    f32x4 xc = f32x4{bf_lo(xg.x), bf_hi(xg.x), bf_lo(xg.y), bf_hi(xg.y)} - bf_lo(*reinterpret_cast<const unsigned*>(x + row_of(hb) * DM));
    asm volatile("" : "+v"(xc));
    typedef __attribute__((address_space(1))) const u32x4 g_u32x4;
    g_u32x4* wp[10];
    wp[0] = (g_u32x4*)(((mr & 4) ? wu : wg) + (size_t)(tw * 160 + qd * 8 + (mr >> 3) * 4) * FF + g_cg * 32 + (mr & 3) * 8);
    u32x4 e[20], bx[20];
    auto frag = [&](int s) __attribute__((always_inline)) {
      unsigned w[8][2];
#pragma unroll
      for (int i = 0; i < 4; ++i)
#pragma unroll
        for (int d = 0; d < 2; ++d) {
          w[i][d] = __builtin_amdgcn_update_dpp(e[s * 4 + i][d], e[s * 4 + i][2 + d], DPP_ROR8, 0xf, 0xc, false);
          w[4 + i][d] = __builtin_amdgcn_update_dpp(e[s * 4 + i][2 + d], e[s * 4 + i][d], DPP_ROR8, 0xf, 0x3, false);
        }
#pragma unroll
      for (int k = 0; k < 4; ++k) {
        bx[s * 4 + 0][k] = PERM_LO(w[2 * k + 1][0], w[2 * k][0]); bx[s * 4 + 1][k] = PERM_HI(w[2 * k + 1][0], w[2 * k][0]);
        bx[s * 4 + 2][k] = PERM_LO(w[2 * k + 1][1], w[2 * k][1]); bx[s * 4 + 3][k] = PERM_HI(w[2 * k + 1][1], w[2 * k][1]);
      }
      PIN4(bx, s * 4);
    };
    prep(false);
    lds_wait<8>(s_c + F_REL, 1u);
    auto hrow = [&](bool fast) -> f32x4 {
      f32x4 hv = {0.f, 0.f, 0.f, 0.f};
      if (lane < 40) {
        const int off = (WS_SLAB + hb * DM + tw * 160 + lane * 4) * 4;
        u32x4 sl[KS];
#pragma unroll 1
        for (unsigned spin = 0; spin < SPIN_CAP; ++spin) {
#pragma unroll
          for (int ks = 0; ks < KS; ++ks) sl[ks] = __builtin_amdgcn_raw_buffer_load_b128(rs, off, ks * (NB * DM * 4), AUX_SC1);
          unsigned mn = min4(sl[0]);
#pragma unroll
          for (int ks = 1; ks < KS; ++ks) mn = min(mn, min4(sl[ks]));
          if (!__any(mn == 0u)) break;
          if (fast) __builtin_amdgcn_s_sleep(1); else __builtin_amdgcn_s_sleep(16);
        }
#define SL(k) __builtin_bit_cast(f32x4, sl[k])
        hv = xc + (((SL(0) + SL(1)) + (SL(2) + SL(3))) + ((SL(4) + SL(5)) + (SL(6) + SL(7))));
#undef SL
      }
      return hv;
    };
    auto sums = [&](f32x4 hv) -> f32x2 {
      return f32x2{wave_sum((hv[0] + hv[1]) + (hv[2] + hv[3])), wave_sum((hv[0] * hv[0] + hv[1] * hv[1]) + (hv[2] * hv[2] + hv[3] * hv[3]))};
    };
    if (isH) {
      const float wsum = wave_sum((bf_lo(wr4.x) + bf_hi(wr4.x)) + (bf_lo(wr4.y) + bf_hi(wr4.y)));
      if (tw == 3 || hb >= 2) done_all(); else done(4);
      if (hb == NB - 1) { pull(); done(NS); }
      else pause(lds_get(s_c + S_BURST + (hb >> 1)), hb < 2 ? HDELAY : HDELAY2);
      const f32x4 hv = hrow(hb == NB - 1);
      const f32x2 sm = sums(hv);
      const float dot = wave_sum((hv[0] * bf_lo(wr4.x) + hv[1] * bf_hi(wr4.x)) + (hv[2] * bf_lo(wr4.y) + hv[3] * bf_hi(wr4.y)));
      if (lane < 8) st128(rs, WS_H + (hb * 4 + tw) * 192 + 160 + lane * 4, u32x4{nz_bits(sm[0]), nz_bits(sm[1]), nz_bits(dot), nz_bits(wsum)});
      if (hb == NB - 1) return;
      const u32x4 v = split8(hv, (lane & 1) != 0);
      if (lane < 40) st128(rs, WS_H + (hb * 4 + tw) * 192 + lane * 4, v);
      done_all();
      pull();
      return;
    }
    done_all();
#pragma unroll
    for (int i = 0; i < 10; ++i) { wp[i] = wp[0] + ((i >> 1) * 32 + (i & 1) * 2) * (FF / 8); asm volatile("" : "+v"(wp[i])); }
    done(NS);
    until(clk(), GW);
#pragma unroll
    for (int i = 0; i < 20; ++i) e[i] = wp[i >> 1][(i & 1) * (FF / 8)];
    u32x4 a[5], sm;
    const int hoff = (WS_H + (min(mr >> 2, NB - 2) * 4 + tw) * 192 + qd * 8 + (mr & 1) * 4) * 4;
#pragma unroll
    for (int s = 0; s < 5; ++s) a[s] = __builtin_amdgcn_raw_buffer_load_b128(rs, hoff + s * 128, 0, AUX_SC1);
    sm = ld128(rs, WS_H + (min(lane, NB - 2) * 4 + tw) * 192 + 160);
    MEMFENCE();
#pragma unroll
    for (int s = 0; s < 5; ++s) frag(s);
    {
#pragma unroll 1
      for (unsigned spin = 0; spin < SPIN_CAP; ++spin) {
        unsigned mn = min4(sm);
#pragma unroll
        for (int s = 0; s < 5; ++s) mn = min(mn, min4(a[s]));
        if (__all(mn != 0u)) break;
        __builtin_amdgcn_s_sleep(2);
#pragma unroll
        for (int s = 0; s < 5; ++s) a[s] = __builtin_amdgcn_raw_buffer_load_b128(rs, hoff + s * 128, 0, AUX_SC1);
        sm = ld128(rs, WS_H + (min(lane, NB - 2) * 4 + tw) * 192 + 160);
      }
      if (lane < NB - 1) rst[lane * 4 + tw] = f32x2{__uint_as_float(sm[0]), __uint_as_float(sm[1])};
#pragma unroll
      for (int s = 0; s < 5; ++s) {
        if (mr & 2) a[s] = u32x4{0x3f803f80u, 0x3f803f80u, 0x3f803f80u, 0x3f803f80u};
        asm volatile("" : "+v"(a[s]));
      }
    }
    typedef __attribute__((address_space(3))) unsigned char lds_u8;
    const int eb = mr >> 2;
    lds_u8* l_aw = (lds_u8*)reg + lane * 8;
    lds_u8* l_ar = (lds_u8*)reg + (mr & 1) * AG_LD + qd * 16;
    lds_u8* l_pw = (lds_u8*)reg + 4096 + (qd * 16 + (mr & 7) * 2 + (mr >> 3)) * 16;
    lds_u8* l_pr = (lds_u8*)smem + OFF_REG + 4096 + (lane & 1) * 1152 + (eb * 16 + qd * 2 + ((lane >> 1) & 1)) * 16;
    lds_u8* l_p2 = l_pr + 2 * REG;
    lds_u8* l_sr = (lds_u8*)(rst + eb * 4);
    lds_u8* l_sw = (lds_u8*)(rst + (NB - 1) * 4 + tw);
    int so = (lane & 2) == 0 ? (WS_ACT + g_cg * 128 + (lane >> 2) * 8 + (lane & 1) * 4) * 4 : WS_END * 4;
    asm volatile("" : "+v"(l_aw), "+v"(l_ar), "+v"(l_pw), "+v"(l_pr), "+v"(l_p2), "+v"(l_sr), "+v"(l_sw), "+v"(so));
#define LDS(T, p) (*reinterpret_cast<__attribute__((address_space(3))) T*>(p))
    const f32x4 hv = hrow(true);
    {
      u32x2 hi, lw;
      split4(hv, hi, lw);
      LDS(u32x2, l_aw) = hi;
      LDS(u32x2, l_aw + AG_LD) = lw;
    }
    CFENCE();
    if ((mr & 14) == 12) {
#pragma unroll
      for (int s = 0; s < 5; ++s) a[s] = LDS(u32x4, l_ar + s * 64);
    }
    __builtin_amdgcn_sched_barrier(0);
    f32x4 c[4];
#pragma unroll
    for (int t = 0; t < 4; ++t) c[t] = f32x4{0.f, 0.f, 0.f, 0.f};
#pragma unroll
    for (int s = 0; s < 5; ++s)
#pragma unroll
      for (int t = 0; t < 4; ++t) c[t] = mfma16(a[s], bx[s * 4 + t], c[t]);
    LDS(f32x2, l_sw) = sums(hv);
    LDS(f32x4, l_pw) = f32x4{c[0][0] + c[0][1], c[1][0] + c[1][1], c[0][2], c[1][2]};
    LDS(f32x4, l_pw + 1152) = f32x4{c[2][0] + c[2][1], c[3][0] + c[3][1], c[2][2], c[3][2]};
    const int k = arrive(s_c + C_T);
    f32x4 ts[2] = {LDS(f32x4, l_sr), LDS(f32x4, l_sr + 16)};
    f32x4 pg[4] = {LDS(f32x4, l_pr), LDS(f32x4, l_pr + REG), LDS(f32x4, l_p2), LDS(f32x4, l_p2 + REG)};
    f32x4 pu[4] = {LDS(f32x4, l_pr + 128), LDS(f32x4, l_pr + REG + 128), LDS(f32x4, l_p2 + 128), LDS(f32x4, l_p2 + REG + 128)};
    asm volatile("" : "+v"(ts[0]), "+v"(ts[1]));
    PIN4(pg, 0); PIN4(pu, 0);
    if (arrived(k) == 3) {
      const f32x4 t = ts[0] + ts[1], g4 = (pg[0] + pg[1]) + (pg[2] + pg[3]), u4 = (pu[0] + pu[1]) + (pu[2] + pu[3]);
      const float s1 = t[0] + t[2], d = s1 * (-1.0f / DM);
      const float rstd = __builtin_amdgcn_rsqf(fmaxf((t[1] + t[3]) + d * s1, 0.f) * (1.0f / DM) + LN_EPS);
      const float g0 = rstd * (g4[0] + d * g4[2]), g1 = rstd * (g4[1] + d * g4[3]);
      const f32x2 act = {g0 * __builtin_amdgcn_rcpf(1.0f + __expf(-g0)) * (rstd * (u4[0] + d * u4[2])),
                         g1 * __builtin_amdgcn_rcpf(1.0f + __expf(-g1)) * (rstd * (u4[1] + d * u4[3]))};
      const unsigned h0 = __float_as_uint(act[0]) & 0xffff0000u, h1 = __float_as_uint(act[1]) & 0xffff0000u;
      const unsigned hi = max(PERM_HI(h1, h0), 1u), lw = max(PERM_HI(__float_as_uint(act[1] - __uint_as_float(h1)), __float_as_uint(act[0] - __uint_as_float(h0))), 1u);
      const bool odd = (lane & 1) != 0;
      const unsigned own = odd ? lw : hi, oth = __float_as_uint(dpp<DPP_XOR1>(__uint_as_float(odd ? hi : lw)));
      const unsigned w0 = odd ? oth : own, w1 = odd ? own : oth;
      const u32x4 av = {w0, w1, __float_as_uint(dpp<DPP_XOR2>(__uint_as_float(w0))), __float_as_uint(dpp<DPP_XOR2>(__uint_as_float(w1)))};
      __builtin_amdgcn_raw_buffer_store_b128(av, rs, so, 0, AUX_SC1);
    }
  } else if (isD) {
    const uint2 xd = *reinterpret_cast<const uint2*>(x + row_of(tw) * DM + d_ct * 32 + j * 4);
    const float cq = bf_lo(*reinterpret_cast<const unsigned*>(x + row_of(qd) * DM));
    const __amdgpu_buffer_rsrc_t rd = __builtin_amdgcn_make_buffer_rsrc(const_cast<bf16*>(wd), 0, FF * DM * 2, 0x00020000);
    prep(false);
    lds_wait<8>(s_c + F_REL, 1u);
    done_all();
    pull();
    done(NS);
    const unsigned te = clk();
    u32x4 bd[24];
    {
      const int off = ((tw * 384 + (mr >> 3) * 32 + qd * 8 + (mr & 4)) * DM + d_ct * 32 + (mr & 3) * 8) * 2;
      u32x4 e[24];
#pragma unroll
      for (int i = 0; i < 24; ++i) e[i] = __builtin_amdgcn_raw_buffer_load_b128(rd, off + (i & 3) * (DM * 2), (i >> 2) * 64 * (DM * 2), 0);
      MEMFENCE();
#pragma unroll
      for (int s = 0; s < 6; ++s) {
        unsigned w[8][2];
#pragma unroll
        for (int i = 0; i < 4; ++i)
#pragma unroll
          for (int d = 0; d < 2; ++d) {
            w[i][d] = __builtin_amdgcn_update_dpp(e[s * 4 + i][d], e[s * 4 + i][2 + d], 0x114, 0xf, 0xa, false);
            w[4 + i][d] = __builtin_amdgcn_update_dpp(e[s * 4 + i][2 + d], e[s * 4 + i][d], 0x104, 0xf, 0x5, false);
          }
#pragma unroll
        for (int k = 0; k < 4; ++k) {
          bd[s * 4 + 0][k] = PERM_LO(w[2 * k + 1][0], w[2 * k][0]); bd[s * 4 + 1][k] = PERM_HI(w[2 * k + 1][0], w[2 * k][0]);
          bd[s * 4 + 2][k] = PERM_LO(w[2 * k + 1][1], w[2 * k][1]); bd[s * 4 + 3][k] = PERM_HI(w[2 * k + 1][1], w[2 * k][1]);
        }
        PIN4(bd, s * 4);
      }
    }
    until(te, DLATE);
    {
      u32x4 sl;
#pragma unroll 1
      for (unsigned spin = 0; spin < SPIN_CAP; ++spin) {
        sl = ld128(rs, WS_SLAB + (r * NB + tw) * DM + d_ct * 32 + j * 4);
        if (__all(min4(sl) != 0u)) break;
        __builtin_amdgcn_s_sleep(2);
      }
      f32x4 hv = {bf_lo(xd.x), bf_hi(xd.x), bf_lo(xd.y), bf_hi(xd.y)};
#pragma unroll
      for (int i = 0; i < 4; ++i) hv[i] += sum_r(__uint_as_float(sl[i]));
      if (lane < 8) reinterpret_cast<f32x4*>(red + 512 + tw * 32)[lane] = hv;
      LGKM0();
      lds_add(s_c + C_W, lane);
      lds_wait<1>(s_c + C_W, 4u);
    }
    const int col = (mr & 3) * 8 + (mr & 4) + (mr >> 3) * 2;
    f32x2 hv = *reinterpret_cast<const f32x2*>(red + 512 + qd * 32 + col);
    typedef __attribute__((address_space(3))) f32x2 lds_f32x2;
    typedef __attribute__((address_space(1))) unsigned g_u32w;
    lds_f32x2* r2 = (lds_f32x2*)red + lane;
    lds_f32x2* r2w = r2 + tw * 64;
    g_u32w* po = (g_u32w*)(out + row_of(qd) * DM + d_ct * 32 + col);
    int aoff = (WS_ACT + tw * 12 * 128 + ((mr >> 1) & 1) * 128 + qd * 32 + (mr >> 2) * 8 + (mr & 1) * 4) * 4;
    asm volatile("" : "+v"(hv), "+v"(r2), "+v"(r2w), "+v"(po), "+v"(aoff));
    u32x4 hs, a[6];
#pragma unroll 1
    for (unsigned spin = 0; spin < SPIN_CAP; ++spin) {
      hs = ld128(rs, WS_H + (qd * 4 + (mr & 3)) * 192 + 160);
      if (__all(min4(hs) != 0u)) break;
      __builtin_amdgcn_s_sleep(1);
    }
#pragma unroll
    for (int s = 0; s < 6; ++s) a[s] = __builtin_amdgcn_raw_buffer_load_b128(rs, aoff, s * 1024, AUX_SC1);
    MEMFENCE();
    float gate;
    {
      f32x4 t = __builtin_bit_cast(f32x4, hs);
#pragma unroll
      for (int i = 0; i < 4; ++i) { t[i] += dpp<DPP_XOR2>(t[i]); t[i] += dpp<DPP_XOR1>(t[i]); }
      const float d = t[0] * (-1.0f / DM);
      const float rstd = __builtin_amdgcn_rsqf(fmaxf(t[1] + d * t[0], 0.f) * (1.0f / DM) + LN_EPS);
      hv += ((hv - cq) + d) * rstd;
      gate = __builtin_amdgcn_rcpf(1.0f + __expf(-rstd * (t[2] + d * t[3])));
      asm volatile("" : "+v"(hv), "+v"(gate));
    }
#pragma unroll 1
    for (unsigned spin = 0; spin < SPIN_CAP; ++spin) {
      unsigned mn = min4(a[0]);
#pragma unroll
      for (int s = 1; s < 6; ++s) mn = min(mn, min4(a[s]));
      if (__all(mn != 0u)) break;
#pragma unroll
      for (int s = 0; s < 6; ++s) a[s] = __builtin_amdgcn_raw_buffer_load_b128(rs, aoff, s * 1024, AUX_SC1);
      MEMFENCE();
    }
    f32x4 c[4];
#pragma unroll
    for (int t = 0; t < 4; ++t) c[t] = f32x4{0.f, 0.f, 0.f, 0.f};
#pragma unroll
    for (int s = 0; s < 6; ++s)
#pragma unroll
      for (int t = 0; t < 4; ++t) c[t] = mfma16(a[s], bd[s * 4 + t], c[t]);
    const bool up = mr >= 8;
    const float k0 = up ? c[2][2] + c[2][3] : c[0][0] + c[0][1], k1 = up ? c[3][2] + c[3][3] : c[1][0] + c[1][1];
    const float o0 = up ? c[0][2] + c[0][3] : c[2][0] + c[2][1], o1 = up ? c[1][2] + c[1][3] : c[3][0] + c[3][1];
    *r2w = f32x2{k0 + dpp<DPP_ROR8>(o0), k1 + dpp<DPP_ROR8>(o1)};
    const int k = arrive(s_c + C_T);
    f32x2 y = hv + ((r2[0] + r2[64]) + (r2[128] + r2[192])) * gate;
    asm volatile("" : "+v"(y));
    if (arrived(k) == 3) {
      __hip_atomic_store(po, __builtin_bit_cast(unsigned, __builtin_convertvector(y, bf16x2)), __ATOMIC_RELAXED, __HIP_MEMORY_SCOPE_AGENT);
    }
  } else {
    prep(bid == LEADER);
    if (isC && tw == 1) {
      lds_wait<8>(s_c + F_REL, 1u);
      const int cs = c_it != 1 ? c_it * 2 : min(4 - (g >> 3), 3), cnt = c_it != 1 ? 8 : (g < 16 ? 16 : 32), x0 = c_it != 1 ? g * 8 : (g < 16 ? g * 16 : (g & 7) * 32);
      done(c_it < 2 ? 4 : NA);
      pause(lds_get(s_c + S_BURST + (c_it >> 1)), WDELAY);
      const float wk = weight(cs, x0, cnt);
      f32x4 o = {0.f, 0.f, 0.f, 0.f};
#pragma unroll 1
      for (int k0 = 0; k0 < cnt; k0 += 16) {
        u32x2 pv[16];
#pragma unroll
        for (int k = 0; k < 16; ++k) pv[k] = __builtin_amdgcn_raw_buffer_load_b64(rs, lp * 2, (WS_PQ + ((x0 + ((k0 + k) & (cnt - 1))) * 5 + cs) * (DH / 2)) * 4, AUX_EARLY);
#pragma unroll
        for (int k = 0; k < 16; ++k) {
          const float w = k0 + k < cnt ? __uint_as_float(__builtin_amdgcn_readlane(__float_as_uint(wk), k0 + k)) : 0.f;
          o += w * f32x4{(float)(short)pv[k][0], (float)((int)pv[k][0] >> 16), (float)(short)pv[k][1], (float)((int)pv[k][1] >> 16)};
        }
      }
      u32x2 hi, lw;
      split4(o, hi, lw);
      if (lane < DH / 4) st128(rs, WS_OC + (c_it * NG + g) * DH + lane * 4, u32x4{max(hi[0], 1u), max(hi[1], 1u), max(lw[0], 1u), max(lw[1], 1u)});
    }
    done_all();
    pull();
  }
  if (tw == 3) done_all();
}

extern "C" void kernel_launch(void* const* d_in, const int* in_sizes, int n_in, void* d_out, int out_size, void* d_ws,
                              size_t ws_size, hipStream_t stream) {
  const bf16* x = (const bf16*)d_in[0];
  const bf16* kc = (const bf16*)d_in[1];
  const bf16* vc = (const bf16*)d_in[2];
  const bf16* wq = (const bf16*)d_in[3];
  const bf16* wo = (const bf16*)d_in[4];
  const bf16* wr = (const bf16*)d_in[6];
  const bf16* wg = (const bf16*)d_in[7];
  const bf16* wu = (const bf16*)d_in[8];
  const bf16* wd = (const bf16*)d_in[9];
  fused_kernel<<<NWG, NW * 64, 0, stream>>>(x, kc, vc, wq, wo, wr, wg, wu, wd, (float*)d_ws, (bf16*)d_out);
}
```

```cpp
#include <hip/hip_runtime.h>
#include <hip/hip_bf16.h>
#include <stdint.h>

typedef __hip_bfloat16 bf16;
using u32x2 = __attribute__((ext_vector_type(2))) unsigned;
using u32x4 = __attribute__((ext_vector_type(4))) unsigned;
using f32x4 = __attribute__((ext_vector_type(4))) float;
using f32x2 = __attribute__((ext_vector_type(2))) float;
using bf16x8 = __attribute__((ext_vector_type(8))) __bf16;

constexpr int NB = 4, DM = 640, NH = 32, SK = 8192, DH = 192, HD = NH * DH, FF = 1536;
constexpr float LN_EPS = 1e-5f;
constexpr float QSCALE = 0.07216878364870322f * 1.4426950408889634f;

constexpr int NWG = 256;
constexpr int GS = 8, NG = NWG / GS, NI = GS / 2;
constexpr int HPG = NI / NB;
constexpr int PPI = 512 / GS;
constexpr int NPAIR = NI * PPI;

constexpr int TAUK = 1280, TAUV = 1296;
constexpr int TSTART = 150;
constexpr int E0 = 4;
constexpr int ANCH = 75;
constexpr int OGAP = 140, OLATE = 250, OSTEP = 30;
constexpr int KS1 = 30, KP1 = 72;
constexpr int VGAP = 50;
constexpr int DEEP1 = 70, DEEP2 = 160, LAGOFF = 20;
constexpr int ENDT = 12, ENDS = 2;
constexpr int JUMP = 145, STALL = 230;
constexpr int ZT = 1800, HST = 2800, RT0 = 3900, RTS = 1000, RA1 = 220, RA2 = 500, LEADER = 24;
constexpr unsigned NRD = 5, RMIN = 30;
constexpr int FBX = 35, FB0 = 20, FB1 = 10, FBD = 10, FBS = 10;
constexpr int HOLD = 4000, GB = 12, GBS = 4, GBX = 60, GBF = 12;
constexpr int ROWS = 0x1B;
constexpr int RW0 = 656, RW1 = 272, RW2 = 224, RW3 = 512, RW4 = 528;
constexpr int TLEAD = 10, PFD = 3, WFD = 3;
constexpr int QWAIT = 820;
constexpr int WDELAY = 100, WDELAY2 = 460, HDELAY = 1150, HDELAY2 = 780, DLATE = 100;
constexpr int TPT = 350, TPS = 28;
constexpr int CODE = 24 * 64 * 46;
constexpr int PW = 5, DPW = 150, GW = 30;

constexpr int KS = 8;
constexpr int WS_PML = 0;
constexpr int WS_PO = WS_PML + NWG * 64;
constexpr int WS_PQ = WS_PO + NWG * DH;
constexpr int WS_OC = WS_PQ + NWG * 5 * (DH / 2);
constexpr int WS_SLAB = WS_OC + (NB - 1) * NH * DH;
constexpr int WS_ACT = WS_SLAB + KS * NB * DM;
constexpr int WS_H = WS_ACT + NB * FF;
constexpr int WS_HN = WS_H + NB * 4 * 192;
constexpr int WS_HR = WS_HN + NWG;
constexpr int WS_HA = WS_HR + 2 * NWG;
constexpr int QFS = 2 * GS * 32;
constexpr int WS_QF = WS_HA + 32 * NWG;
constexpr int WS_QP = WS_QF + NG * QFS;
constexpr int WS_END = WS_QP + NH * 2 * NB * DH;
constexpr int NPROD = HPG * 6;
static_assert(WS_PO % 32 == 0 && WS_SLAB % 32 == 0 && WS_ACT % 32 == 0 && WS_HN % 32 == 0 && WS_QF % 32 == 0 && WS_QP % 32 == 0, "line alignment");


__device__ __forceinline__ int row_of(int b) { return (ROWS >> (2 * b)) & 3; }
__device__ __forceinline__ float bf_lo(unsigned u) { return __uint_as_float(u << 16); }
__device__ __forceinline__ float bf_hi(unsigned u) { return __uint_as_float(u & 0xffff0000u); }
__device__ __forceinline__ f32x2 bf_pair(unsigned u) { return f32x2{bf_lo(u), bf_hi(u)}; }
__device__ __forceinline__ f32x2 fma2(float a, f32x2 b, f32x2 c) { return __builtin_elementwise_fma(f32x2{a, a}, b, c); }
__device__ __forceinline__ f32x2 fma2(f32x2 a, f32x2 b, f32x2 c) { return __builtin_elementwise_fma(a, b, c); }

template <int CTRL> __device__ __forceinline__ float dpp(float x) {
  return __builtin_bit_cast(float, __builtin_amdgcn_mov_dpp(__builtin_bit_cast(int, x), CTRL, 0xf, 0xf, true));
}
constexpr int DPP_XOR1 = 0xB1, DPP_XOR2 = 0x4E, DPP_HMIRROR = 0x141, DPP_MIRROR = 0x140, DPP_ROR8 = 0x128;

__device__ __forceinline__ float sum_x16_32(float x) {
  auto s = __builtin_amdgcn_permlane16_swap(__float_as_uint(x), __float_as_uint(x), false, false);
  x = __uint_as_float(s[0]) + __uint_as_float(s[1]);
  auto t = __builtin_amdgcn_permlane32_swap(__float_as_uint(x), __float_as_uint(x), false, false);
  return __uint_as_float(t[0]) + __uint_as_float(t[1]);
}
__device__ __forceinline__ float max_x16_32(float x) {
  auto s = __builtin_amdgcn_permlane16_swap(__float_as_uint(x), __float_as_uint(x), false, false);
  x = fmaxf(__uint_as_float(s[0]), __uint_as_float(s[1]));
  auto t = __builtin_amdgcn_permlane32_swap(__float_as_uint(x), __float_as_uint(x), false, false);
  return fmaxf(__uint_as_float(t[0]), __uint_as_float(t[1]));
}
__device__ __forceinline__ float tsum32(float a, float b) {
  auto t = __builtin_amdgcn_permlane32_swap(__float_as_uint(a), __float_as_uint(b), false, false);
  return __uint_as_float(t[0]) + __uint_as_float(t[1]);
}
__device__ __forceinline__ float tsum16(float a, float b) {
  auto t = __builtin_amdgcn_permlane16_swap(__float_as_uint(a), __float_as_uint(b), false, false);
  return __uint_as_float(t[0]) + __uint_as_float(t[1]);
}
__device__ __forceinline__ float sum_r(float v) { v += dpp<DPP_ROR8>(v); return sum_x16_32(v); }
__device__ __forceinline__ float row_sum(float v) {
  v += dpp<DPP_XOR1>(v); v += dpp<DPP_XOR2>(v); v += dpp<DPP_HMIRROR>(v);
  return v + dpp<DPP_MIRROR>(v);
}
__device__ __forceinline__ float row_max(float v) {
  v = fmaxf(v, dpp<DPP_XOR1>(v)); v = fmaxf(v, dpp<DPP_XOR2>(v)); v = fmaxf(v, dpp<DPP_HMIRROR>(v));
  return fmaxf(v, dpp<DPP_MIRROR>(v));
}
__device__ __forceinline__ float wave_sum(float v) {
  v += dpp<DPP_XOR1>(v); v += dpp<DPP_XOR2>(v); v += dpp<DPP_HMIRROR>(v);
  return sum_r(v);
}

__device__ __forceinline__ u32x4 ld_nt(const char* p) { return __builtin_nontemporal_load(reinterpret_cast<const u32x4*>(p)); }

struct Tile { u32x4 d[3]; };

#define PIN4(a, o) asm volatile("" : "+v"((a)[(o) + 0]), "+v"((a)[(o) + 1]), "+v"((a)[(o) + 2]), "+v"((a)[(o) + 3]))
#define MEMFENCE() do { asm volatile("" ::: "memory"); __builtin_amdgcn_sched_barrier(0); } while (0)

__device__ __forceinline__ void glds16_nt(const void* gsrc, unsigned lds_dst) {
  unsigned keep;
  asm volatile("s_mov_b32 %0, m0\n\ts_mov_b32 m0, %2\n\ts_nop 0\n\tglobal_load_lds_dwordx4 %1, off nt\n\ts_mov_b32 m0, %0"
               : "=&s"(keep) : "v"(gsrc), "s"(lds_dst) : "memory");
}

using bf16x2 = __attribute__((ext_vector_type(2))) __bf16;
__device__ __forceinline__ float dot2(unsigned a, unsigned b, float c) { return __builtin_amdgcn_fdot2_f32_bf16(__builtin_bit_cast(bf16x2, a), __builtin_bit_cast(bf16x2, b), c, false); }
__device__ __forceinline__ float tile_score(const Tile& k, const u32x2 (&q)[12]) {
  float s0 = 0.f, s1 = 0.f;
#pragma unroll
  for (int i = 0; i < 3; ++i) {
    u32x4 v = k.d[i];
    asm("" : "+v"(v));
#pragma unroll
    for (int d = 0; d < 4; ++d) { s0 = dot2(v[d], q[i * 4 + d][0], s0); s1 = dot2(v[d], q[i * 4 + d][1], s1); }
  }
  return s0 + s1;
}
__device__ __forceinline__ void pair_put(float a, float b, float* dst, int j) {
  float s = (j < 4 ? a : b) + dpp<DPP_HMIRROR>(j < 4 ? b : a);
  s += dpp<DPP_XOR1>(s);
  s += dpp<DPP_XOR2>(s);
  if ((j & 3) == 0) dst[j * 2] = s;
}
__device__ __forceinline__ void score_put(float s, float* dst, int j) {
  s += dpp<DPP_XOR1>(s);
  s += dpp<DPP_XOR2>(s);
  s += dpp<DPP_HMIRROR>(s);
  if (j == 0) *dst = s;
}
__device__ __forceinline__ void tile_load(Tile& t, const char* base, unsigned off, int half) {
#pragma unroll
  for (int i = 0; i < 3; ++i) t.d[i] = ld_nt(base + off + half * 3072 + i * 128);
}
__device__ __forceinline__ void pair_issue(Tile& ta, Tile& tb, const char* base, unsigned off, int deep) {
  deep = __builtin_amdgcn_readfirstlane(deep);
#define WAIT_IF(level, n) asm volatile("s_cmp_lg_u32 %0, " #level "\n\ts_cbranch_scc1 .Lpi%=\n\ts_waitcnt vmcnt(" #n ")\n.Lpi%=:" :: "s"(deep) : "scc", "memory")
  const char* p = base + off;
  WAIT_IF(0, 3);
  ta.d[0] = ld_nt(p); ta.d[1] = ld_nt(p + 128); ta.d[2] = ld_nt(p + 256); MEMFENCE();
  WAIT_IF(0, 3); WAIT_IF(1, 6);
  tb.d[0] = ld_nt(p + 3072); tb.d[1] = ld_nt(p + 3200); tb.d[2] = ld_nt(p + 3328); MEMFENCE();
#undef WAIT_IF
}
__device__ __forceinline__ void pv_last(float p, Tile& v, f32x2 (&acc)[12]) {
#pragma unroll
  for (int i = 0; i < 3; ++i) {
    asm volatile("" : "+v"(v.d[i]), "+v"(p));
#pragma unroll
    for (int d = 0; d < 4; ++d) acc[i * 4 + d] = fma2(p, bf_pair(v.d[i][d]), acc[i * 4 + d]);
    PIN4(acc, i * 4);
  }
}
__device__ __forceinline__ void q_load(u32x2 (&q)[12], const float* src, int j) {
#pragma unroll
  for (int i = 0; i < 6; ++i) {
    const u32x4 a = *reinterpret_cast<const u32x4*>(src + (i >> 1) * 64 + j * 8 + (i & 1) * 4);
    q[i * 2] = u32x2{a[0], a[1]}; q[i * 2 + 1] = u32x2{a[2], a[3]};
  }
}
#define VMCNT(n) asm volatile("s_waitcnt vmcnt(%0)" :: "n"(n) : "memory")

constexpr int AUX_SC1 = 16, AUX_EARLY = 18;
constexpr unsigned SPIN_CAP = 1u << 17;
__device__ __forceinline__ unsigned nz_bits(float v) { return max(__float_as_uint(v), 1u); }
__device__ __forceinline__ unsigned min4(u32x4 v) { return min(min(v[0], v[1]), min(v[2], v[3])); }
__device__ __forceinline__ unsigned ld32(__amdgpu_buffer_rsrc_t rs, int word) { return __builtin_amdgcn_raw_buffer_load_b32(rs, word * 4, 0, AUX_SC1); }
__device__ __forceinline__ u32x2 ld64(__amdgpu_buffer_rsrc_t rs, int word) { return __builtin_amdgcn_raw_buffer_load_b64(rs, word * 4, 0, AUX_SC1); }
__device__ __forceinline__ u32x4 ld128(__amdgpu_buffer_rsrc_t rs, int word) { return __builtin_amdgcn_raw_buffer_load_b128(rs, word * 4, 0, AUX_SC1); }
__device__ __forceinline__ void st128(__amdgpu_buffer_rsrc_t rs, int word, u32x4 v) { __builtin_amdgcn_raw_buffer_store_b128(v, rs, word * 4, 0, AUX_SC1); }

__device__ __forceinline__ u32x4 ld128nt(__amdgpu_buffer_rsrc_t rs, int word) { return __builtin_amdgcn_raw_buffer_load_b128(rs, word * 4, 0, AUX_EARLY); }
__device__ __forceinline__ void st128nt(__amdgpu_buffer_rsrc_t rs, int word, u32x4 v) { __builtin_amdgcn_raw_buffer_store_b128(v, rs, word * 4, 0, AUX_EARLY); }

__device__ __forceinline__ bool leader_release(__amdgpu_buffer_rsrc_t rs, int lane, unsigned t0, unsigned rd, int slow, int& prev) {
  u32x4 a[4];
#pragma unroll
  for (int k = 0; k < 4; ++k) a[k] = ld128(rs, WS_HA + (lane * 4 + k) * 32);
  const u32x4 n = ld128(rs, WS_HN + lane * 4);
  if (rd == 0u && !__all(a[0][0] == n[0] && a[1][0] == n[1] && a[2][0] == n[2] && a[3][0] == n[3])) return false;
  const float e = fmaxf(fmaxf((float)(int)(t0 - a[0][2]), (float)(int)(t0 - a[1][2])), fmaxf((float)(int)(t0 - a[2][2]), (float)(int)(t0 - a[3][2])));
  unsigned first = t0 - (unsigned)(int)max_x16_32(row_max(e));
  float ls = 0.f, lx = 0.f;
  const int pend = max(prev - FBS * (RTS - RA2) * 16 / TAUK, 0);
  prev = max(pend - FBS * RA2 * 16 / TAUK, 0);
#pragma unroll
  for (int k = 0; k < 4; ++k) {
    const float l = a[k][0] == n[k] + rd ? (float)min(a[k][3] + (rd ? 0u : min(a[k][2] - first, 300u)), 2000u) : (float)-pend;
    ls += l; lx = fmaxf(lx, l);
  }
  ls = wave_sum(ls) * (1.0f / NWG); lx = max_x16_32(row_max(lx));
  const int extra = __builtin_amdgcn_readfirstlane(min(max((int)(ls + (lx - max(ls, 0.f)) * (FBX / 100.0f)) - (rd ? FB1 + (int)(NRD - rd) * FBD : FB0), 0), 600));
  if (rd != 0u && __all(ls * NWG < HOLD)) return true;
  first = (first & 0xffffu) | ((unsigned)(slow + extra) << 16);
  prev += extra;
  st128(rs, WS_HR + lane * 8, u32x4{a[0][1] + rd, first, a[1][1] + rd, first});
  st128(rs, WS_HR + lane * 8 + 4, u32x4{a[2][1] + rd, first, a[3][1] + rd, first});
  return true;
}

typedef __attribute__((address_space(3))) unsigned lds_u32;
__device__ __forceinline__ unsigned lds_add(lds_u32* p, int lane) {
  unsigned k = 0;
  if (lane == 0) k = __hip_atomic_fetch_add(p, 1u, __ATOMIC_RELAXED, __HIP_MEMORY_SCOPE_WORKGROUP);
  return __builtin_amdgcn_readfirstlane(k);
}
#define CFENCE() asm volatile("" ::: "memory")
__device__ __forceinline__ int arrive(lds_u32* p) {
  int v;
  unsigned long long keep;
  asm volatile("s_mov_b64 %1, exec\n\ts_mov_b64 exec, 1\n\tds_add_rtn_u32 %0, %2, %3\n\ts_mov_b64 exec, %1"
               : "=&v"(v), "=&s"(keep) : "v"((unsigned)(size_t)p), "v"(1u) : "memory");
  return v;
}
__device__ __forceinline__ void bump(lds_u32* p) {
  unsigned long long keep;
  asm volatile("s_mov_b64 %0, exec\n\ts_mov_b64 exec, 1\n\tds_add_u32 %1, %2\n\ts_mov_b64 exec, %0" : "=&s"(keep) : "v"((unsigned)(size_t)p), "v"(1u) : "memory");
}
__device__ __forceinline__ int arrived(int v) {
  asm volatile("s_waitcnt lgkmcnt(0)" : "+v"(v) :: "memory");
  return __builtin_amdgcn_readfirstlane(v);
}
__device__ __forceinline__ unsigned lds_get(lds_u32* p) {
  return __builtin_amdgcn_readfirstlane(__hip_atomic_load(p, __ATOMIC_RELAXED, __HIP_MEMORY_SCOPE_WORKGROUP));
}
template <int SLEEP> __device__ __forceinline__ void lds_wait(lds_u32* p, unsigned n) {
#pragma unroll 1
  for (unsigned spin = 0; spin < (SPIN_CAP << 4); ++spin) {
    if (lds_get(p) >= n) break;
    __builtin_amdgcn_s_sleep(SLEEP);
  }
  asm volatile("" ::: "memory");
}
__device__ __forceinline__ unsigned clk() { return (unsigned)__builtin_amdgcn_s_memrealtime(); }
struct Table {
  unsigned t0; int was, moved, slowed, folded;
  u32x2 told;
  unsigned mine, tmp;
  template <int K> __device__ __forceinline__ void put(int v) { asm volatile("v_mov_b32 %0, %2\n\tds_write_b32 %1, %0 offset:%3" : "+v"(tmp), "+v"(mine) : "s"(v), "n"(K * 4) : "memory"); }
  __device__ __forceinline__ int gate(int due, int sh = 0) {
    const int fold = __builtin_amdgcn_readfirstlane(told[1]);
    if (fold != folded) { const int f = min(fold - folded, moved); moved -= f; slowed += f; folded = fold; put<0>(moved); }
    const int left = (int)__builtin_amdgcn_readfirstlane(told[0]) - slowed, d = max(min(left, FBS), left < -GBX ? -GBF : -GBS);
    t0 += d; was -= d; slowed += d;
    int late = (int)(clk() - t0) - due;
    if (late - was > JUMP) { const int m = late - was < STALL + JUMP ? STALL : late - was; t0 += m; moved += m; late -= m; put<0>(moved); }
    const int lag = max(late - max(left - d, 0) - LAGOFF, 0);
    put<4>(lag);
    was = max(late, -40);
#pragma unroll 1
    while (late < 0) { __builtin_amdgcn_s_sleep(1); late = (int)(clk() - t0) - due; }
    return lag < (DEEP1 >> sh) ? 0 : (lag < (DEEP2 >> sh) ? 1 : 2);
  }
  __device__ __forceinline__ void listen(lds_u32* slow) {
    told = __builtin_bit_cast(u32x2, __hip_atomic_load((__attribute__((address_space(3))) unsigned long long*)slow, __ATOMIC_RELAXED, __HIP_MEMORY_SCOPE_WORKGROUP));
  }
  __device__ __forceinline__ void agree(lds_u32* all) {
    if (moved == 0) return;
    const int m = (int)min(min(lds_get(all), lds_get(all + 1)), min(lds_get(all + 2), lds_get(all + 3))) + 30;
    if (m < moved) { t0 -= moved - m; moved = m; was = 1 << 20; put<0>(moved); }
  }
};
__device__ __forceinline__ void until(unsigned t0, int due) {
#pragma unroll 1
  while ((int)(clk() - t0) < due) __builtin_amdgcn_s_sleep(1);
}
__device__ __forceinline__ void pause(unsigned t0, unsigned due) {
#pragma unroll 1
  while (clk() - t0 < due) __builtin_amdgcn_s_sleep(16);
}
#define LGKM0() asm volatile("s_waitcnt lgkmcnt(0)" ::: "memory")

__device__ __forceinline__ void split4(f32x4 v, u32x2& hi, u32x2& lo) {
  unsigned h[4], r[4];
#pragma unroll
  for (int i = 0; i < 4; ++i) { h[i] = __float_as_uint(v[i]) & 0xffff0000u; r[i] = __float_as_uint(v[i] - __uint_as_float(h[i])); }
  hi = u32x2{__builtin_amdgcn_perm(h[1], h[0], 0x07060302u), __builtin_amdgcn_perm(h[3], h[2], 0x07060302u)};
  lo = u32x2{__builtin_amdgcn_perm(r[1], r[0], 0x07060302u), __builtin_amdgcn_perm(r[3], r[2], 0x07060302u)};
}
__device__ __forceinline__ u32x4 split8(f32x4 v, bool odd) {
  u32x2 hi, lo;
  split4(v, hi, lo);
  const unsigned n0 = __float_as_uint(dpp<DPP_XOR1>(__uint_as_float(odd ? hi[0] : lo[0]))), n1 = __float_as_uint(dpp<DPP_XOR1>(__uint_as_float(odd ? hi[1] : lo[1])));
  return u32x4{max(odd ? n0 : hi[0], 1u), max(odd ? n1 : hi[1], 1u), max(odd ? lo[0] : n0, 1u), max(odd ? lo[1] : n1, 1u)};
}
__device__ __forceinline__ u32x4 q_pack(u32x4 a, u32x4 b) {
  u32x2 hi, lo;
  split4((__builtin_bit_cast(f32x4, a) + __builtin_bit_cast(f32x4, b)) * QSCALE, hi, lo);
  return u32x4{hi[0], lo[0], hi[1], lo[1]};
}
__device__ __forceinline__ f32x4 mfma16(u32x4 a, u32x4 b, f32x4 c) {
  return __builtin_amdgcn_mfma_f32_16x16x32_bf16(__builtin_bit_cast(bf16x8, a), __builtin_bit_cast(bf16x8, b), c, 0, 0, 0);
}

constexpr int NW = 8, NMW = 4;
constexpr int NLP = 5, NRP = 8;
constexpr int NTP = 4 * (NLP + NRP);
constexpr int REG = NLP * 6 * 1024;
constexpr int NRING = 4;
constexpr int NS = 6, NA = NS - 1;
__device__ __forceinline__ int seg_beg(int s) { return s >= NS ? NPAIR : ((0xC86540 >> (4 * s)) & 15) * 16; }
__device__ __forceinline__ int seg_k(int t) { return (int)(0x5433222211110000ull >> (4 * (t >> 4))) & 15; }
__device__ __forceinline__ int seg_of_k(int k) { return (0x123540 >> (4 * k)) & 15; }
__device__ __forceinline__ int k_of_seg(int s) { return (0x213450 >> (4 * s)) & 15; }
__device__ __forceinline__ int pair_of(int t) { return t < 64 ? t : (t < 192 ? t + 64 : t - (t < 224 ? 96 : (t < 240 ? 144 : 176))); }
constexpr int OFF_CNT = 0, OFF_REG = 256,
              OFF_SC = OFF_REG + 4 * REG, OFF_BUF = OFF_SC + NPAIR * 16 * 4, OFF_Q = OFF_BUF + NRING * NMW * DH * 4, OFF_ML = OFF_Q + NS * DH * 4,
              OFF_RED = OFF_ML + 128, OFF_END = OFF_RED + 4 * 256 * 4;
enum { F_REL = 0, F_DONE, C_LAST, C_T = 3, F_OUT, C_P, C_K, F_P, F_Q = 8, F_FREE, F_Q2, C_W = 12, C_SC = 16, C_PART = 24, S_ECHO = 32, S_BURST = 40, S_LATE = 42, S_MOVED = 48, S_SLOW = 56 };
__device__ __forceinline__ int due_k(int p) { return TSTART + ((p * TAUK + min(p, KP1) * KS1) >> 6); }
__device__ __forceinline__ int due_v(int p) { return TSTART + OGAP + ((NPAIR * TAUK + KP1 * KS1 + p * TAUV + max(p - NPAIR / 2, 0) * (VGAP * 64 / (NPAIR / 2))) >> 6); }
constexpr int AW_LD = DH * 2 + 16, AG_LD = 512 + 16;

__global__ void __launch_bounds__(NW * 64, 2) fused_kernel(const bf16* __restrict__ x, const bf16* __restrict__ kc,
                                                        const bf16* __restrict__ vc, const bf16* __restrict__ wq,
                                                        const bf16* __restrict__ wo, const bf16* __restrict__ wr,
                                                        const bf16* __restrict__ wg, const bf16* __restrict__ wu,
                                                        const bf16* __restrict__ wd, float* ws, bf16* __restrict__ out) {
  __shared__ __attribute__((aligned(16))) unsigned char smem[OFF_END];
  float* s_sc = reinterpret_cast<float*>(smem + OFF_SC);
  float* s_buf = reinterpret_cast<float*>(smem + OFF_BUF);
  float* s_q = reinterpret_cast<float*>(smem + OFF_Q);
  u32x4* s_ml = reinterpret_cast<u32x4*>(smem + OFF_ML);
  lds_u32* s_c = (lds_u32*)(smem + OFF_CNT);

  const int tid = threadIdx.x, lane = tid & 63, r = lane >> 3, j = lane & 7;
  const int wave = __builtin_amdgcn_readfirstlane(tid >> 6);
  const int tw = wave - NMW;
  const int bid = blockIdx.x, xl = bid & 7, idx = bid >> 3;
  const int g = (xl * 32 + idx) / GS, mm = idx % GS;
  const __amdgpu_buffer_rsrc_t rs = __builtin_amdgcn_make_buffer_rsrc(ws, 0, WS_END * 4, 0x00020000);
  const int ro = ((idx & 3) << 3) | (idx >> 2);
  const bool isW = ro < 10, isG = ro >= 10 && ro < 16;
  const int w_ks = xl, w_ct = ro;
  const int g_cg = xl * 6 + (ro - 10);
  const int d_ct = (xl + ((ro - 16) >> 2) * 8) * 2 + (((ro - 16) >> 1) & 1);
  const bool isD = ro >= 16 && ro < 24 && (ro & 1) == 0 && d_ct < DM / 32;
  const bool isH = ro == 22 && xl >= 2 && xl < 6;
  const bool isC = (mm & 7) == 3 || (mm & 7) >= 6;
  const int c_it = (mm >> 3) * 3 + max((mm & 7) - 5, 0);
  const int qf = WS_QF + g * QFS;
  const char* kb = reinterpret_cast<const char*>(kc);
  const char* vb = reinterpret_cast<const char*>(vc);
  const unsigned lo = (r * 24 + j) * 16;
  const int xb = g * GS + mm, c0 = mm * 2 + (g & 1) * RW0 + (g >> 1 & 1) * RW1 + (g >> 2 & 1) * RW2 + (g >> 3 & 1) * RW3 + (g >> 4) * RW4;
  auto ptile = [&](int p) -> unsigned {
    int item = row_of(p >> 6) * NH + g, t = (p & 63) * 16 + c0;
    if ((p >> 6) == 1) {
      item = 2 * NH + ((p & 32) ? xb >> 4 : 24 - ((p & 16) >> 1) + xl);
      t = (p & 32) ? (p & 31) * 32 + (xb & 15) * 2 + (g >> 1 & 1) * 640 + (xl & 1) * 960 + (xl >> 1 & 1) * 32 + (xl >> 2) * 544
                   : (p & 15) * 64 + idx * 2 + (xl & 1) * 768 + (xl >> 1 & 1) * 704 + (xl >> 2) * 128;
    }
    return item * 1024 + (t & 1023);
  };
  auto poff = [&](int p) -> unsigned { return ptile(p) * 3072u; };
  const unsigned lds_base = (unsigned)(size_t)(__attribute__((address_space(3))) unsigned char*)smem + OFF_REG + tw * REG;
  auto merged = [&](int it, int ln) -> f32x4 {
    const float* src = s_buf + (it % NRING) * (NMW * DH) + ((ln >> 1) & 7) * 24 + (ln >> 4) * 8 + (ln & 1) * 4;
    return (*reinterpret_cast<const f32x4*>(src) + *reinterpret_cast<const f32x4*>(src + DH)) +
           (*reinterpret_cast<const f32x4*>(src + 2 * DH) + *reinterpret_cast<const f32x4*>(src + 3 * DH));
  };
#define NONCE() do { unsigned n_ = __builtin_amdgcn_readfirstlane(f) + 1u; while (__any(f == n_)) ++n_; \
    if (lane < GS) st128(rs, qf + (GS + mm) * 32 + lane * 4, u32x4{n_, n_, n_, n_}); f = n_; } while (0)
  unsigned long long pc;
  asm volatile("s_getpc_b64 %0" : "=s"(pc));
  unsigned r0 = 0u;
  unsigned t0 = clk();
  unsigned tb0 = t0;
  if (wave < NMW) {
    Tile ta, tb, tc, td;
    const int c = lane & (GS - 1);
    unsigned f = 0u;
    if (wave == 3) f = ld32(rs, qf + c * 32 + mm * 4);
    if (mm < NPROD) {
      const int hp = g + (mm / 6) * NG, ck = (mm % 6) >> 1, hf = mm & 1, jp = hf * 4 + wave;
      const int bb = lane >> 4, t = lane & 15;
      const u32x4* xp = reinterpret_cast<const u32x4*>(x + row_of(bb) * DM);
      u32x4 xv[5], xs = {0u, 0u, 0u, 0u};
#pragma unroll
      for (int k = 0; k < 5; ++k) xv[k] = xp[t + 16 * k];
      if (t < 10) xs = xp[jp * 10 + t];
      const u32x4* wp = reinterpret_cast<const u32x4*>(wq + (size_t)(jp * 80 + r) * HD + hp * DH + ck * 64) + j;
      u32x4 wv[10];
#pragma unroll
      for (int k = 0; k < 10; ++k) wv[k] = __builtin_nontemporal_load(wp + (size_t)k * 8 * (HD / 8));
      if (wave == 0) s_c[lane] = lane == C_K ? NTP + NMW : 0u;
      MEMFENCE(); LGKM0(); __builtin_amdgcn_s_barrier(); MEMFENCE();
      if (wave == 3) { NONCE(); }
      float sm = 0.f;
#pragma unroll
      for (int k = 0; k < 5; ++k)
#pragma unroll
        for (int d = 0; d < 4; ++d) sm += bf_lo(xv[k][d]) + bf_hi(xv[k][d]);
      const float mean = row_sum(sm) * (1.0f / DM);
      float sv = 0.f;
#pragma unroll
      for (int k = 0; k < 5; ++k)
#pragma unroll
        for (int d = 0; d < 4; ++d) { const float a = bf_lo(xv[k][d]) - mean, c2 = bf_hi(xv[k][d]) - mean; sv += a * a + c2 * c2; }
      const float rstd = 1.0f / sqrtf(row_sum(sv) * (1.0f / DM) + LN_EPS);
      float* s_xn = s_buf + wave * 320;
      if (t < 10) {
#pragma unroll
        for (int d = 0; d < 4; ++d) {
          s_xn[(t * 8 + 2 * d) * 4 + bb] = (bf_lo(xs[d]) - mean) * rstd;
          s_xn[(t * 8 + 2 * d + 1) * 4 + bb] = (bf_hi(xs[d]) - mean) * rstd;
        }
      }
      LGKM0();
      f32x2 qa[16];
#pragma unroll
      for (int i = 0; i < 16; ++i) qa[i] = f32x2{0.f, 0.f};
#pragma unroll
      for (int k = 0; k < 10; ++k) {
        const f32x4 xn = *reinterpret_cast<const f32x4*>(&s_xn[(k * 8 + r) * 4]);
#pragma unroll
        for (int d = 0; d < 4; ++d) {
          const f32x2 w2 = bf_pair(wv[k][d]);
#pragma unroll
          for (int e = 0; e < 4; ++e) qa[e * 4 + d] = fma2(xn[e], w2, qa[e * 4 + d]);
        }
      }
      float t16[16], t8[8];
#pragma unroll
      for (int i = 0; i < 16; ++i) t16[i] = tsum32(qa[i >> 1][i & 1], qa[8 + (i >> 1)][i & 1]);
#pragma unroll
      for (int i = 0; i < 8; ++i) t8[i] = tsum16(t16[i], t16[i + 8]);
#pragma unroll
      for (int i = 0; i < 8; ++i) t8[i] += dpp<DPP_ROR8>(t8[i]);
      const bool up = (lane & 8) != 0;
      f32x4* r4 = reinterpret_cast<f32x4*>(smem + OFF_RED);
      r4[wave * 64 + lane] = f32x4{up ? t8[4] : t8[0], up ? t8[5] : t8[1], up ? t8[6] : t8[2], up ? t8[7] : t8[3]};
      LGKM0();
      if (lds_add(s_c + C_P, lane) == NMW - 1) {
        asm volatile("" ::: "memory");
        const f32x4 o4 = (r4[lane] + r4[64 + lane]) + (r4[128 + lane] + r4[192 + lane]);
        st128nt(rs, WS_QP + ((hp * 2 + hf) * NB + bb) * DH + ck * 64 + j * 8 + (up ? 4 : 0), __builtin_bit_cast(u32x4, o4));
        VMCNT(0);
        if (lane == 0) s_c[F_OUT] = 1u;
      }
    } else {
      if (wave == 0) s_c[lane] = lane == C_K ? NTP + NMW : 0u;
      MEMFENCE(); LGKM0(); __builtin_amdgcn_s_barrier(); MEMFENCE();
      if (wave == 3) { NONCE(); }
      else {
        typedef __attribute__((address_space(1))) const unsigned g_u32;
        typedef __attribute__((address_space(1))) const char g_char;
        const int id = ((idx >> 3) * 2 + (mm & 1)) * 3 + wave;
        if (lane < CODE / (24 * 64)) {
          const unsigned v = *(g_u32*)((pc & ~255ull) + id * (CODE / 24) + lane * 64);
          asm volatile("" :: "v"(v));
        }
        if (lane < 7 && id < 21) {
          const int st = id < 8 ? (0x1320 >> ((id >> 1) * 4)) & 3 : (id >> 1) & 3, hf = id & 1;
          int head = row_of(st) * NH + xl * 4 + hf * 2;
          unsigned off = min(lane << 20, (6 << 20) - 64);
          if (st == 1) {
            head = 2 * NH + (hf ? 16 + xl : 2 * xl);
            if (hf) off = lane < 4 ? min(lane << 20, (3 << 20) - 64) : 8 * (3 << 20) + min((lane - 4) * (3 << 19), (3 << 20) - 64);
          }
          g_char* p = (g_char*)(id < 8 ? kb : vb) + (size_t)head * (3 << 20) + off;
          if (id >= 16) p = (g_char*)(id == 16 ? wo + xl * 4 * DH * DM : (id == 17 ? wg : (id == 18 ? wu : wd))) + min(lane * (id == 16 ? 163840 : 327680), (id == 16 ? 983040 : 1966080) - 64);
          if (id >= 20) p = (g_char*)(lane ? wr : out);
          until(t0, TPT + id * TPS);
          const unsigned v = __builtin_nontemporal_load((g_u32*)p);
          asm volatile("" :: "v"(v));
        }
      }
    }
    unsigned tk_e = ptile(pair_of(lane * 4)) | (pair_of(lane * 4) << 17) | (seg_k(lane * 4) << 25) | ((lane < 48 ? 4u : (lane < 56 ? 5u : 6u)) << 28), tv_o = poff(lane * 4 + wave);
    int tk_d = due_k(lane * 4), tv_d = due_v(lane * 4 + wave);
    asm volatile("" : "+v"(tk_e), "+v"(tv_o), "+v"(tk_d), "+v"(tv_d));
    MEMFENCE();
    if (wave == 3) {
      __builtin_amdgcn_s_setprio(3);
      const unsigned nonce = f;
      unsigned echoed = 0u;
      if (mm < NPROD) {
        echoed = ld32(rs, qf + (GS + c) * 32);
        asm volatile("" : "+v"(echoed) :: "memory");
        lds_wait<1>(s_c + F_OUT, 1u);
        if (lane < GS) st128(rs, qf + mm * 32 + lane * 4, u32x4{echoed, echoed, echoed, echoed});
      }
      until(t0, QWAIT);
#pragma unroll 1
      for (unsigned spin = 0; spin < SPIN_CAP; ++spin) {
        const unsigned nv = ld32(rs, qf + (GS + c) * 32);
        f = ld32(rs, qf + c * 32 + mm * 4);
        const bool done = __all(c == mm || c >= NPROD || f == nonce);
        if (mm < NPROD && __any(nv != echoed)) {
          if (lane < GS) st128(rs, qf + mm * 32 + lane * 4, u32x4{nv, nv, nv, nv});
          echoed = nv;
        }
        if (done) break;
        __builtin_amdgcn_s_sleep(8);
      }
      if (lane < GS) s_c[S_ECHO + lane] = echoed;
      if (lane < DH / 4) {
        u32x4 p[3][2];
#pragma unroll
        for (int it = 0; it < 3; ++it)
#pragma unroll
          for (int k = 0; k < 2; ++k) p[it][k] = ld128nt(rs, WS_QP + ((g * 2 + k) * NB + (it ? it + 1 : 0)) * DH + lane * 4);
#pragma unroll
        for (int it = 0; it < 3; ++it) {
          *reinterpret_cast<u32x4*>(&s_q[(it ? it + 3 : 0) * DH + lane * 4]) = q_pack(p[it][0], p[it][1]);
          if (it != 1) { LGKM0(); if (lane == 0) s_c[F_Q] = it + 1; MEMFENCE(); }
        }
      }
    }
    __builtin_amdgcn_s_setprio(3);
    until(t0, due_k(NTP + wave));
    tb0 = lds_get(s_c + S_LATE);
    if ((unsigned)(tb0 - t0 + 100u) > 500u) tb0 = t0;
    until(tb0, due_k(NTP + wave));
    Table tt = {tb0, 1 << 20, 0, 0, 0, {0u, 0u}, (unsigned)(size_t)(s_c + S_MOVED + wave), 0u};
    tile_load(ta, kb, poff(NTP + wave) + lo, 0);
    tile_load(tb, kb, poff(NTP + wave) + lo, 1);
    MEMFENCE();
    {
      u32x2 q[12];
      int cp = NTP + wave, it = 0, nxt = lds_add(s_c + C_K, lane), passed = 0, ci = -1;
      auto kuse = [&](Tile& xa, Tile& xb_) __attribute__((always_inline)) {
        if (it != ci) {
          LGKM0();
          for (; passed < it; ++passed) bump(s_c + C_SC + passed);
          lds_wait<1>(s_c + (it < 3 ? F_Q : F_Q2), min(it + 1, 3));
          q_load(q, s_q + seg_of_k(it) * DH, j);
          tt.agree(s_c + S_MOVED);
          ci = it;
        }
        const int tk = arrive(s_c + C_K);
        tt.listen(s_c + S_SLOW + 2 * wave);
        pair_put(tile_score(xa, q), tile_score(xb_, q), s_sc + cp * 16 + r, j);
        MEMFENCE();
        return arrived(tk);
      };
      auto kstep = [&](Tile& xa, Tile& xb_, Tile& ya, Tile& yb) __attribute__((always_inline)) {
        const unsigned e = __builtin_amdgcn_readlane(tk_e, nxt >> 2);
        MEMFENCE();
        const int deep = tt.gate(__builtin_amdgcn_readlane(tk_d, nxt >> 2) + (nxt & 3) * (TAUK / 64));
        MEMFENCE();
        pair_issue(ya, yb, kb, ((e & 0x1fc00u) | ((e + ((nxt & 3) << (e >> 28))) & 1023u)) * 3072u + lo, deep);
        const int n2 = kuse(xa, xb_);
        cp = ((e >> 17) & 255) + (nxt & 3); it = (e >> 25) & 7; nxt = n2;
      };
      bool swap;
      for (;;) {
        if (nxt >= NPAIR) { swap = false; break; }
        kstep(ta, tb, tc, td);
        if (nxt >= NPAIR) { swap = true; break; }
        kstep(tc, td, ta, tb);
      }
      tt.gate(due_k(NPAIR + wave));
      MEMFENCE();
      const unsigned* pw = reinterpret_cast<const unsigned*>(wo + (xl * 128 + wave * 32 + idx) * 3840) + lane * 16;
      const unsigned v0 = pw[0], v1 = pw[56 * 16];
      MEMFENCE();
      if (swap) { ta = tc; tb = td; }
      kuse(ta, tb);
      for (; passed < NS; ++passed) bump(s_c + C_SC + passed);
      tt.gate(due_v(wave));
      MEMFENCE();
      pair_issue(ta, tb, vb, __builtin_amdgcn_readlane(tv_o, 0) + lo, 2);
      asm volatile("" :: "v"(v0), "v"(v1));
    }
    const unsigned sc_r = (unsigned)(size_t)(__attribute__((address_space(3))) float*)s_sc + r * 4;
    f32x2 acc[12];
    auto vstep = [&](Tile& xa, Tile& xb_, Tile& ya, Tile& yb, int p, bool fin) __attribute__((always_inline)) {
      const unsigned off = __builtin_amdgcn_readlane(tv_o, (p >> 2) + 1) + lo;
      MEMFENCE();
      const int deep = tt.gate(__builtin_amdgcn_readlane(tv_d, (p >> 2) + 1), p >= NPAIR - ENDT * NMW ? ENDS : 0);
      float pa, pb;
      asm volatile("ds_read_b32 %0, %2\n\tds_read_b32 %1, %2 offset:32" : "=&v"(pa), "=&v"(pb) : "v"(sc_r + p * 64) : "memory");
      MEMFENCE();
      pair_issue(ya, yb, vb, off, deep);
      asm volatile("s_waitcnt lgkmcnt(0)" : "+v"(pa), "+v"(pb) :: "memory");
      if (!fin) tt.listen(s_c + S_SLOW + 2 * wave);
      else bump(s_c + C_LAST);
      pv_last(pa, xa, acc);
      pv_last(pb, xb_, acc);
      MEMFENCE();
    };
    typedef __attribute__((address_space(3))) float lds_f32;
    auto fold8 = [&](lds_f32* dst) __attribute__((always_inline)) {
      float t12[12], t6[6];
#pragma unroll
      for (int i = 0; i < 12; ++i) t12[i] = tsum32(acc[i >> 1][i & 1], acc[6 + (i >> 1)][i & 1]);
#pragma unroll
      for (int i = 0; i < 6; ++i) t6[i] = tsum16(t12[i], t12[i + 6]);
#pragma unroll
      for (int i = 0; i < 6; ++i) t6[i] += dpp<DPP_ROR8>(t6[i]);
      auto d2 = (__attribute__((address_space(3))) f32x2*)dst;
      d2[0] = f32x2{t6[0], t6[1]}; d2[1] = f32x2{t6[2], t6[3]}; d2[2] = f32x2{t6[4], t6[5]};
      CFENCE();
    };
    lds_f32* dst = (lds_f32*)s_buf + wave * DH + j * 24 + (lane >> 4) * 6;
#pragma unroll 1
    for (int it = 0;; ++it) {
      lds_wait<1>(s_c + F_P, k_of_seg(it) + 1);
      tt.agree(s_c + S_MOVED);
#pragma unroll
      for (int i = 0; i < 12; ++i) acc[i] = f32x2{0.f, 0.f};
#pragma unroll 1
      for (int p = seg_beg(it) + wave; p < min(seg_beg(it + 1), NPAIR - 2 * NMW); p += 2 * NMW) {
        vstep(ta, tb, tc, td, p, false);
        vstep(tc, td, ta, tb, p + NMW, false);
      }
      if (it >= NRING) lds_wait<1>(s_c + F_FREE, it - NRING + 1);
      if (it == NS - 1) break;
      fold8(dst + (it % NRING) * (NMW * DH));
      bump(s_c + C_PART + it);
    }
    dst += ((NS - 1) % NRING) * (NMW * DH);
    const int ln = lane < DH / 4 ? lane : 0;
    auto src = (__attribute__((address_space(3))) f32x4*)((lds_f32*)s_buf + ((NS - 1) % NRING) * (NMW * DH) + ((ln >> 1) & 7) * 24 + (ln >> 4) * 8 + (ln & 1) * 4);
    int so = (lane < DH / 4 ? WS_PO + xb * DH + lane * 4 : WS_END) * 4;
    const int p = NPAIR - NMW + wave;
    float pa = s_sc[p * 16 + r], pb = s_sc[p * 16 + 8 + r];
    asm volatile("" : "+v"(dst), "+v"(src), "+v"(so), "+v"(pa), "+v"(pb));
    vstep(ta, tb, tc, td, NPAIR - 2 * NMW + wave, true);
    pv_last(pa, tc, acc);
    pv_last(pb, td, acc);
    fold8(dst);
    const int k = arrive(s_c + C_PART + NS - 1);
    const f32x4 m = (src[0] + src[DH / 4]) + (src[2 * DH / 4] + src[3 * DH / 4]);
    u32x4 o = {nz_bits(m[0]), nz_bits(m[1]), nz_bits(m[2]), nz_bits(m[3])};
    asm volatile("" : "+v"(o));
    if (arrived(k) == NMW - 1) {
      __builtin_amdgcn_raw_buffer_store_b128(o, rs, so, 0, AUX_SC1);
      bump(s_c + F_DONE);
    }
    return;
  }

  {
    Tile pk[NRP * 2];
    __builtin_amdgcn_s_setprio(3);
    const unsigned it0 = (row_of(0) * NH + g) * 1024, ct = c0 + tw * 16;
    auto toff = [&](int turn) -> unsigned { return (it0 + ((turn * 64 + ct) & 1023)) * 3072u + lo; };
    unsigned tbw = 0u;
#pragma unroll 1
    for (int e = 0; e < NLP * 2; ++e) {
      const char* src = kb + (toff(e >> 1) + (e & 1) * 3072);
      if (e >= E0) until(tbw, ((e & ~1) * 2 * (TAUK + KS1)) >> 6);
#pragma unroll
      for (int i = 0; i < 3; ++i) glds16_nt(src + i * 128, lds_base + (e * 3 + i) * 1024);
      lds_u32* og = (lds_u32*)(smem + OFF_ML + 96);
      if (e == 1) {
        VMCNT(5);
        og[tw] = t0 + min(max((int)(clk() - t0) - ANCH, 0), 300);
      }
      if (e == E0 - 1) {
        LGKM0();
        __builtin_amdgcn_s_barrier();
        tb0 = t0 + min(min((int)(lds_get(og) - t0), (int)(lds_get(og + 1) - t0)), min((int)(lds_get(og + 2) - t0), (int)(lds_get(og + 3) - t0)));
        tbw = tb0 + TSTART + ((tw * TAUK) >> 6) - TLEAD;
        if (tw == 0 && lane == 0) s_c[S_LATE] = tb0;
      }
      if (e >= E0 - 1) VMCNT(PFD);
    }
#pragma unroll
    for (int e = 0; e < NRP; ++e) {
      until(tbw, ((NLP + e) * 4 * (TAUK + KS1)) >> 6);
      tile_load(pk[2 * e], kb, toff(NLP + e), 0);
      MEMFENCE();
      VMCNT(PFD);
      tile_load(pk[2 * e + 1], kb, toff(NLP + e), 1);
      MEMFENCE();
      VMCNT(PFD);
    }
    if (tw == 0) {
      r0 = ld32(rs, WS_HR + bid * 2);
      if (bid == LEADER) {
        u32x4 n;
#pragma unroll
        for (int k = 0; k < 4; ++k) n[k] = ld32(rs, WS_HA + (lane * 4 + k) * 32) + 1u;
        st128(rs, WS_HN + lane * 4, n);
      }
    }
    __builtin_amdgcn_s_setprio(0);
    lds_wait<1>(s_c + F_Q, HPG);
    u32x2 q[12];
    q_load(q, s_q, j);
    const u32x4* s_kv = reinterpret_cast<const u32x4*>(smem + OFF_REG + tw * REG) + lane;
    static_assert(NLP * 4 <= PPI && NTP <= HPG * PPI, "tail tiles: batch row 0 only");
#pragma unroll 1
    for (int e = 0; e < NLP * 2; ++e) {
      Tile tmp;
#pragma unroll
      for (int i = 0; i < 3; ++i) tmp.d[i] = s_kv[(e * 3 + i) * 64];
      score_put(tile_score(tmp, q), s_sc + ((e >> 1) * 4 + tw) * 16 + (e & 1) * 8 + r, j);
    }
#pragma unroll
    for (int e = 0; e < NRP * 2; ++e) {
      if ((NLP * 2 + e) * 2 == PPI) q_load(q, s_q + DH, j);
      score_put(tile_score(pk[e], q), s_sc + ((NLP + (e >> 1)) * 4 + tw) * 16 + (e & 1) * 8 + r, j);
      __builtin_amdgcn_sched_barrier(0);
    }
    asm volatile("s_waitcnt vmcnt(0) lgkmcnt(0)" ::: "memory");
#pragma unroll
    for (int it = 0; it * PPI < NTP; ++it) lds_add(s_c + C_SC + it, lane);
    if (tw == 0) {
      until((t0 + ZT + 511u) & ~511u, 0);
      const u32x4 z = {0u, 0u, 0u, 0u};
      st128(rs, lane < 48 ? WS_PO + xb * DH + lane * 4 : WS_PML + xb * 64 + (lane - 48) * 4, z);
      const int o = isW ? WS_SLAB + w_ks * NB * DM + w_ct * 64 : (isG ? WS_ACT + g_cg * 128 : (isH ? WS_H + (xl - 2) * 768 : WS_OC + (c_it * NG + g) * DH));
      st128(rs, lane < (isG ? 32 : (isC ? 48 : (isW || isH ? 64 : 0))) ? o + (isW ? (lane >> 4) * DM + (lane & 15) * 4 : lane * 4) : WS_END, z);
      if (isH) { st128(rs, o + 256 + lane * 4, z); st128(rs, o + 512 + lane * 4, z); }
      VMCNT(0);
    }
  }

  const int mr = lane & 15, qd = lane >> 4;
  unsigned char* reg = smem + OFF_REG + tw * REG;
  float* red = reinterpret_cast<float*>(smem + OFF_RED);
#define PERM_LO(hi_, lo_) __builtin_amdgcn_perm(hi_, lo_, 0x05040100u)
#define PERM_HI(hi_, lo_) __builtin_amdgcn_perm(hi_, lo_, 0x07060302u)
  int served = 0;
  auto done = [&](int n) {
    if (tw == 3 && served < min(n, NA)) {
      const int from = served;
      int ln = lane;
      asm volatile("" : "+v"(ln));
#pragma unroll 1
      for (; served < min(n, NA); ++served) {
        lds_wait<8>(s_c + C_PART + served, NMW);
        const f32x4 o = merged(served, ln < DH / 4 ? ln : 0);
        const float am = max_x16_32(row_max(fmaxf(fmaxf(fabsf(o[0]), fabsf(o[1])), fmaxf(fabsf(o[2]), fabsf(o[3])))));
        const float inv = am > 1e-30f ? 32767.0f / am : 0.f;
        const int q0 = __float2int_rn(o[0] * inv), q1 = __float2int_rn(o[1] * inv), q2 = __float2int_rn(o[2] * inv), q3 = __float2int_rn(o[3] * inv);
        if (ln < DH / 4) reinterpret_cast<u32x2*>(s_sc + served * (DH / 2))[ln] = u32x2{PERM_LO((unsigned)q1, (unsigned)q0), PERM_LO((unsigned)q3, (unsigned)q2)};
        const bool nan = __any((o[0] + o[1]) + (o[2] + o[3]) != (o[0] + o[1]) + (o[2] + o[3]));
        if (ln == 0) s_ml[served][2] = nan ? 0x7fc00000u : nz_bits(am * (1.0f / 32767.0f));
        LGKM0();
        if (ln == 0) s_c[F_FREE] = served + 1;
      }
#pragma unroll 1
      for (int c = from * 24 + ln; c < served * 24; c += 64) st128nt(rs, WS_PQ + xb * 5 * (DH / 2) + c * 4, reinterpret_cast<const u32x4*>(s_sc)[c]);
      VMCNT(0);
      if (ln < 8) st128(rs, WS_PML + xb * 64 + (from ? 32 : 0) + ln * 4, s_ml[ln]);
      if (ln == 0) s_c[S_BURST + (from ? 1 : 0)] = clk();
      CFENCE();
      if (ln == 0) __hip_atomic_fetch_add(s_c + F_DONE, (unsigned)(served - from), __ATOMIC_RELAXED, __HIP_MEMORY_SCOPE_WORKGROUP);
    }
    if (n > NA) lds_wait<1>(s_c + F_DONE, n); else lds_wait<8>(s_c + F_DONE, n);
  };
  auto done_all = [&]() {
#pragma unroll 1
    for (int n = 4; n <= NA; ++n) done(n);
  };
  const int lp = (lane < DH / 4 ? lane : 0) * 4;
  auto weight = [&](int it, int x0, int cnt) -> float {
    const int mlw = WS_PML + (x0 + (lane & (cnt - 1))) * 64 + (it < 4 ? 0 : 32) + it * 4;
    u32x4 ml;
#pragma unroll 1
    for (unsigned spin = 0; spin < SPIN_CAP; ++spin) {
      ml = ld128(rs, mlw);
      if (__all(min4(ml) != 0u)) break;
      __builtin_amdgcn_s_sleep(16);
    }
    const float mk = __uint_as_float(ml[0]);
    const float e = __builtin_amdgcn_exp2f(mk - max_x16_32(row_max(mk)));
    return e / (wave_sum(e * __uint_as_float(ml[1])) * ((float)cnt * (1.0f / 64.0f))) * __uint_as_float(ml[2]);
  };
  const unsigned tok = __builtin_amdgcn_readfirstlane(r0) + 1u;
  unsigned echoed = 0u, fold = 0u;
  int slow = 0, prev = 0, later = 0;
  auto lag = [&]() {
    const unsigned v = __hip_atomic_load(s_c + S_MOVED + (lane & 7), __ATOMIC_RELAXED, __HIP_MEMORY_SCOPE_WORKGROUP);
    fold += v;
    unsigned a = 0u;
#pragma unroll
    for (int k = 0; k < 8; ++k) a += __builtin_amdgcn_readlane(v, k);
    return a >> 2;
  };
  auto tell = [&]() { if (lane < 4) { s_c[S_SLOW + 2 * lane] = slow - later; s_c[S_SLOW + 2 * lane + 1] = fold; } };
  auto round = [&](unsigned rd, bool lead) {
    const int tr = RT0 + (int)(rd - 1) * RTS;
    until(t0, tr);
    const unsigned bh = lag();
    if (bh > RMIN && lane < 8) st128(rs, WS_HA + bid * 32 + lane * 4, u32x4{echoed + rd, tok, tb0, bh});
    if (lead) { until(t0, tr + RA1); leader_release(rs, lane, tb0, rd, slow, prev); }
    until(t0, tr + RA2);
    const u32x2 rv = ld64(rs, WS_HR + bid * 2);
    slow = (int)__builtin_amdgcn_readfirstlane(rv[1]) >> 16;
#pragma unroll 1
    for (unsigned k = min(__builtin_amdgcn_readfirstlane(rv[0]) - tok, rd) + 1u; k <= rd; ++k) slow = max(slow - GB, 0);
    tell();
  };
  auto prep = [&](bool lead) {
    if (tw == 0) {
      const int c = lane & (GS - 1);
      unsigned qe = s_c[S_ECHO + c];
      bool first = true, released = false, sent = false;
#pragma unroll 1
      while ((int)(clk() - t0) < HST) {
        const unsigned qn = ld32(rs, qf + (GS + c) * 32);
        if (mm < NPROD && __any(qn != qe)) {
          if (lane < GS) st128(rs, qf + mm * 32 + lane * 4, u32x4{qn, qn, qn, qn});
          qe = qn;
        }
        __builtin_amdgcn_s_sleep(64);
      }
      const unsigned behind = lag();
#pragma unroll 1
      for (unsigned spin = 0; spin < SPIN_CAP && !released; ++spin) {
        const unsigned qn = ld32(rs, qf + (GS + c) * 32);
        const u32x2 rv = ld64(rs, WS_HR + bid * 2);
        const unsigned nv = __builtin_amdgcn_readfirstlane(ld32(rs, WS_HN + bid));
        if (__builtin_amdgcn_readfirstlane(rv[0]) - tok <= NRD) {
          released = true;
          slow = (int)__builtin_amdgcn_readfirstlane(rv[1]) >> 16;
          later = __builtin_amdgcn_readfirstlane(min((tb0 - rv[1]) & 0xffffu, 300u));
        } else {
          if (mm < NPROD && __any(qn != qe)) {
            if (lane < GS) st128(rs, qf + mm * 32 + lane * 4, u32x4{qn, qn, qn, qn});
            qe = qn;
          }
          if (first || nv != echoed) {
            echoed = nv; first = false;
            if (lane < 8) st128(rs, WS_HA + bid * 32 + lane * 4, u32x4{echoed, tok, tb0, behind});
          }
          if (lead && !sent) sent = leader_release(rs, lane, tb0, 0u, slow, prev);
          __builtin_amdgcn_s_sleep(32);
        }
      }
      tell();
      if (lane == 0) s_c[F_REL] = 1u;
      if (lane < DH / 4) {
        u32x4 p[3][2];
#pragma unroll
        for (int e = 0; e < 3; ++e)
#pragma unroll
          for (int k = 0; k < 2; ++k) p[e][k] = ld128nt(rs, WS_QP + (((e < 2 ? 24 - e * 8 + (xb >> 5) : xb >> 4) * 2 + k) * NB + 1) * DH + lane * 4);
#pragma unroll
        for (int e = 0; e < 3; ++e) *reinterpret_cast<u32x4*>(&s_q[(1 + e) * DH + lane * 4]) = q_pack(p[e][0], p[e][1]);
      }
      LGKM0();
      if (lane == 0) s_c[F_Q2] = 3u;
#pragma unroll 1
      for (unsigned rd = 1; rd <= NRD; ++rd) round(rd, lead);
    }
    if (tw == 3) {
#pragma unroll 1
      for (int ik = 0; ik < NS; ++ik) {
        const int it = seg_of_k(ik), nv = (seg_beg(it + 1) - seg_beg(it)) >> 4;
        lds_wait<4>(s_c + C_SC + ik, ik == 0 ? NW : NMW);
        f32x4* sp = reinterpret_cast<f32x4*>(s_sc + seg_beg(it) * 16) + lane;
        f32x4 v[4];
#pragma unroll
        for (int k = 0; k < 4; ++k) v[k] = sp[(k & (nv - 1)) * 64];
        const f32x4 mx = __builtin_elementwise_max(__builtin_elementwise_max(v[0], v[1]), __builtin_elementwise_max(v[2], v[3]));
        const float m = max_x16_32(row_max(fmaxf(fmaxf(mx[0], mx[1]), fmaxf(mx[2], mx[3]))));
        float l = 0.f;
#pragma unroll
        for (int k = 0; k < 4; ++k) {
#pragma unroll
          for (int e = 0; e < 4; ++e) v[k][e] = __builtin_amdgcn_exp2f(v[k][e] - m);
          if (k < nv) {
            l += (v[k][0] + v[k][1]) + (v[k][2] + v[k][3]);
            sp[k * 64] = v[k];
          }
        }
        l = wave_sum(l);
        if (lane == 0) s_ml[it] = u32x4{nz_bits(m), nz_bits(l), 0x3f800000u, 0x3f800000u};
        LGKM0();
        if (lane == 0) s_c[F_P] = ik + 1;
      }
    }
  };
  auto pull = [&]() {
    typedef __attribute__((address_space(1))) const unsigned g_u32;
    const int id = (ro - 16) * 4 + tw;
    g_u32* p1[2];
#pragma unroll
    for (int i = 0; i < 2; ++i) {
      const int l = id * 60 + min(i * 32 + (lane >> 1), 59), m = l >= 1920 ? l - 1920 : l;
      p1[i] = (g_u32*)((l >= 1920 ? wu : wg) + (size_t)(m / 3) * FF + xl * 192 + (m % 3) * 64 + (lane & 1) * 32);
    }
    g_u32* p2 = (g_u32*)(wd + (size_t)(id * 24 + min(lane >> 1, 23)) * DM + xl * 64 + (lane & 1) * 32);
    asm volatile("" : "+v"(p1[0]), "+v"(p1[1]), "+v"(p2));
    lds_wait<1>(s_c + C_LAST, NMW);
    const unsigned tp = clk();
    until(tp, PW);
    const unsigned v0 = p1[0][0], v1 = p1[1][0];
    MEMFENCE();
    until(tp, PW + DPW);
    const unsigned v2 = p2[0], v3 = p2[xl < 2 ? 256 : 0];
    asm volatile("" :: "v"(v0), "v"(v1), "v"(v2), "v"(v3));
  };
  if (isW) {
    u32x4 bx[24];
    const char* p = reinterpret_cast<const char*>(wo + (size_t)((w_ks * 4 + tw) * DH + r) * DM + w_ct * 64 + j * 8);
    prep(false);
    if (tw != 0) { lds_wait<16>(s_c + F_P, NS); pause(clk(), OLATE + (tw % 3) * 24 * OSTEP); }
    const unsigned tw0 = clk();
#pragma unroll 1
    for (int i = 0; i < 24; ++i) { until(tw0, i * OSTEP); glds16_nt(p, lds_base + i * 1024); p += 8 * DM * 2; VMCNT(WFD); }
    asm volatile("s_waitcnt vmcnt(0)" ::: "memory");
#pragma unroll
    for (int s = 0; s < 6; ++s) {
      u32x2 e[8];
#pragma unroll
      for (int k = 0; k < 8; ++k) e[k] = *reinterpret_cast<const u32x2*>(reg + (s * 32 + qd * 8 + k) * 128 + mr * 8);
#pragma unroll
      for (int k = 0; k < 4; ++k) {
        bx[s * 4 + 0][k] = PERM_LO(e[2 * k + 1][0], e[2 * k][0]); bx[s * 4 + 1][k] = PERM_HI(e[2 * k + 1][0], e[2 * k][0]);
        bx[s * 4 + 2][k] = PERM_LO(e[2 * k + 1][1], e[2 * k][1]); bx[s * 4 + 3][k] = PERM_HI(e[2 * k + 1][1], e[2 * k][1]);
      }
    }
#pragma unroll
    for (int s = 0; s < 24; s += 4) asm volatile("" : "+v"(bx[s]), "+v"(bx[s + 1]), "+v"(bx[s + 2]), "+v"(bx[s + 3]));
    LGKM0();
    lds_wait<8>(s_c + F_REL, 1u);
    const int hh = w_ks * 4 + tw;
#pragma unroll 1
    for (int pass = 0; pass < 3; ++pass) {
      if (pass < 2) {
        done(pass ? NA : 4);
        pause(lds_get(s_c + S_BURST + pass), pass ? WDELAY2 : WDELAY2 + 300);
        const int b0 = pass * 2, b1 = pass + 1;
        u32x4 v0, v1;
#pragma unroll 1
        for (unsigned spin = 0; spin < SPIN_CAP; ++spin) {
          v0 = ld128(rs, WS_OC + (b0 * NH + hh) * DH + lp);
          v1 = ld128(rs, WS_OC + (b1 * NH + hh) * DH + lp);
          if (__all(min(min4(v0), min4(v1)) != 0u)) break;
          __builtin_amdgcn_s_sleep(16);
        }
        if (lane < DH / 4) {
          *reinterpret_cast<u32x2*>(reg + (b0 * 4) * AW_LD + lane * 8) = u32x2{v0[0], v0[1]};
          *reinterpret_cast<u32x2*>(reg + (b0 * 4 + 1) * AW_LD + lane * 8) = u32x2{v0[2], v0[3]};
          *reinterpret_cast<u32x2*>(reg + (b1 * 4) * AW_LD + lane * 8) = u32x2{v1[0], v1[1]};
          *reinterpret_cast<u32x2*>(reg + (b1 * 4 + 1) * AW_LD + lane * 8) = u32x2{v1[2], v1[3]};
        }
      } else {
        const float wk = weight(NS - 1, hh * GS, GS);
        float w[GS];
#pragma unroll
        for (int k = 0; k < GS; ++k) { w[k] = __uint_as_float(__builtin_amdgcn_readlane(__float_as_uint(wk), k)); asm volatile("" : "+s"(w[k])); }
        done(NS);
        __builtin_amdgcn_s_setprio(3);
        const int po = WS_PO + hh * GS * DH;
        u32x4 pv[GS];
#pragma unroll 1
        for (unsigned spin = 0; spin < SPIN_CAP; ++spin) {
#pragma unroll
          for (int k = 0; k < GS; ++k) pv[k] = __builtin_amdgcn_raw_buffer_load_b128(rs, lp * 4, (po + k * DH) * 4, AUX_SC1);
          unsigned mn = min4(pv[0]);
#pragma unroll
          for (int k = 1; k < GS; ++k) mn = min(mn, min4(pv[k]));
          if (__all(mn != 0u)) break;
          __builtin_amdgcn_s_sleep(1);
        }
        f32x4 o = w[0] * __builtin_bit_cast(f32x4, pv[0]), o2 = w[1] * __builtin_bit_cast(f32x4, pv[1]);
#pragma unroll
        for (int k = 2; k < GS; k += 2) { o += w[k] * __builtin_bit_cast(f32x4, pv[k]); o2 += w[k + 1] * __builtin_bit_cast(f32x4, pv[k + 1]); }
        u32x2 hi, lw;
        split4(o + o2, hi, lw);
        if (lane < DH / 4) {
          *reinterpret_cast<u32x2*>(reg + ((NB - 1) * 4) * AW_LD + lane * 8) = hi;
          *reinterpret_cast<u32x2*>(reg + ((NB - 1) * 4 + 1) * AW_LD + lane * 8) = lw;
        }
      }
      CFENCE();
      u32x4 a[6];
#pragma unroll
      for (int s = 0; s < 6; ++s) a[s] = *reinterpret_cast<const u32x4*>(reg + mr * AW_LD + s * 64 + qd * 16);
      __builtin_amdgcn_sched_barrier(0);
      f32x4 c[4];
#pragma unroll
      for (int t = 0; t < 4; ++t) c[t] = f32x4{0.f, 0.f, 0.f, 0.f};
#pragma unroll
      for (int s = 0; s < 6; ++s)
#pragma unroll
        for (int t = 0; t < 4; ++t) c[t] = mfma16(a[s], bx[s * 4 + t], c[t]);
      const bool mine = max(qd - 1, 0) == pass;
      f32x4* r4 = reinterpret_cast<f32x4*>(red);
      if (mine) r4[tw * 64 + lane] = f32x4{c[0][0] + c[0][1], c[1][0] + c[1][1], c[2][0] + c[2][1], c[3][0] + c[3][1]};
      const int k = arrive(s_c + C_W + pass);
      f32x4 sum = (r4[lane] + r4[64 + lane]) + (r4[128 + lane] + r4[192 + lane]);
      asm volatile("" : "+v"(sum));
      if (arrived(k) == 3) {
        if (mine) st128(rs, WS_SLAB + (w_ks * NB + qd) * DM + w_ct * 64 + mr * 4, u32x4{nz_bits(sum[0]), nz_bits(sum[1]), nz_bits(sum[2]), nz_bits(sum[3])});
      }
    }
  } else if (isG || isH) {
    f32x2* rst = reinterpret_cast<f32x2*>(red);
    const int hb = isH ? xl - 2 : NB - 1;
    uint2 wr4 = make_uint2(0u, 0u), xg = wr4;
    if (lane < 40) {
      wr4 = *reinterpret_cast<const uint2*>(wr + tw * 160 + lane * 4);
      xg = *reinterpret_cast<const uint2*>(x + row_of(hb) * DM + tw * 160 + lane * 4);
    }
    f32x4 xc = f32x4{bf_lo(xg.x), bf_hi(xg.x), bf_lo(xg.y), bf_hi(xg.y)} - bf_lo(*reinterpret_cast<const unsigned*>(x + row_of(hb) * DM));
    asm volatile("" : "+v"(xc));
    typedef __attribute__((address_space(1))) const u32x4 g_u32x4;
    g_u32x4* wp[10];
    wp[0] = (g_u32x4*)(((mr & 4) ? wu : wg) + (size_t)(tw * 160 + qd * 8 + (mr >> 3) * 4) * FF + g_cg * 32 + (mr & 3) * 8);
    u32x4 e[20], bx[20];
    auto frag = [&](int s) __attribute__((always_inline)) {
      unsigned w[8][2];
#pragma unroll
      for (int i = 0; i < 4; ++i)
#pragma unroll
        for (int d = 0; d < 2; ++d) {
          w[i][d] = __builtin_amdgcn_update_dpp(e[s * 4 + i][d], e[s * 4 + i][2 + d], DPP_ROR8, 0xf, 0xc, false);
          w[4 + i][d] = __builtin_amdgcn_update_dpp(e[s * 4 + i][2 + d], e[s * 4 + i][d], DPP_ROR8, 0xf, 0x3, false);
        }
#pragma unroll
      for (int k = 0; k < 4; ++k) {
        bx[s * 4 + 0][k] = PERM_LO(w[2 * k + 1][0], w[2 * k][0]); bx[s * 4 + 1][k] = PERM_HI(w[2 * k + 1][0], w[2 * k][0]);
        bx[s * 4 + 2][k] = PERM_LO(w[2 * k + 1][1], w[2 * k][1]); bx[s * 4 + 3][k] = PERM_HI(w[2 * k + 1][1], w[2 * k][1]);
      }
      PIN4(bx, s * 4);
    };
    prep(false);
    lds_wait<8>(s_c + F_REL, 1u);
    auto hrow = [&](bool fast) -> f32x4 {
      f32x4 hv = {0.f, 0.f, 0.f, 0.f};
      if (lane < 40) {
        const int off = (WS_SLAB + hb * DM + tw * 160 + lane * 4) * 4;
        u32x4 sl[KS];
#pragma unroll 1
        for (unsigned spin = 0; spin < SPIN_CAP; ++spin) {
#pragma unroll
          for (int ks = 0; ks < KS; ++ks) sl[ks] = __builtin_amdgcn_raw_buffer_load_b128(rs, off, ks * (NB * DM * 4), AUX_SC1);
          unsigned mn = min4(sl[0]);
#pragma unroll
          for (int ks = 1; ks < KS; ++ks) mn = min(mn, min4(sl[ks]));
          if (!__any(mn == 0u)) break;
          if (fast) __builtin_amdgcn_s_sleep(1); else __builtin_amdgcn_s_sleep(16);
        }
#define SL(k) __builtin_bit_cast(f32x4, sl[k])
        hv = xc + (((SL(0) + SL(1)) + (SL(2) + SL(3))) + ((SL(4) + SL(5)) + (SL(6) + SL(7))));
#undef SL
      }
      return hv;
    };
    auto sums = [&](f32x4 hv) -> f32x2 {
      return f32x2{wave_sum((hv[0] + hv[1]) + (hv[2] + hv[3])), wave_sum((hv[0] * hv[0] + hv[1] * hv[1]) + (hv[2] * hv[2] + hv[3] * hv[3]))};
    };
    if (isH) {
      const float wsum = wave_sum((bf_lo(wr4.x) + bf_hi(wr4.x)) + (bf_lo(wr4.y) + bf_hi(wr4.y)));
      if (tw == 3 || hb >= 2) done_all(); else done(4);
      if (hb == NB - 1) { pull(); done(NS); }
      else pause(lds_get(s_c + S_BURST + (hb >> 1)), hb < 2 ? HDELAY : HDELAY2);
      const f32x4 hv = hrow(hb == NB - 1);
      const f32x2 sm = sums(hv);
      const float dot = wave_sum((hv[0] * bf_lo(wr4.x) + hv[1] * bf_hi(wr4.x)) + (hv[2] * bf_lo(wr4.y) + hv[3] * bf_hi(wr4.y)));
      if (lane < 8) st128(rs, WS_H + (hb * 4 + tw) * 192 + 160 + lane * 4, u32x4{nz_bits(sm[0]), nz_bits(sm[1]), nz_bits(dot), nz_bits(wsum)});
      if (hb == NB - 1) return;
      const u32x4 v = split8(hv, (lane & 1) != 0);
      if (lane < 40) st128(rs, WS_H + (hb * 4 + tw) * 192 + lane * 4, v);
      done_all();
      pull();
      return;
    }
    done_all();
#pragma unroll
    for (int i = 0; i < 10; ++i) { wp[i] = wp[0] + ((i >> 1) * 32 + (i & 1) * 2) * (FF / 8); asm volatile("" : "+v"(wp[i])); }
    done(NS);
    until(clk(), GW);
#pragma unroll
    for (int i = 0; i < 20; ++i) e[i] = wp[i >> 1][(i & 1) * (FF / 8)];
    u32x4 a[5], sm;
    const int hoff = (WS_H + (min(mr >> 2, NB - 2) * 4 + tw) * 192 + qd * 8 + (mr & 1) * 4) * 4;
#pragma unroll
    for (int s = 0; s < 5; ++s) a[s] = __builtin_amdgcn_raw_buffer_load_b128(rs, hoff + s * 128, 0, AUX_SC1);
    sm = ld128(rs, WS_H + (min(lane, NB - 2) * 4 + tw) * 192 + 160);
    MEMFENCE();
#pragma unroll
    for (int s = 0; s < 5; ++s) frag(s);
    {
#pragma unroll 1
      for (unsigned spin = 0; spin < SPIN_CAP; ++spin) {
        unsigned mn = min4(sm);
#pragma unroll
        for (int s = 0; s < 5; ++s) mn = min(mn, min4(a[s]));
        if (__all(mn != 0u)) break;
        __builtin_amdgcn_s_sleep(2);
#pragma unroll
        for (int s = 0; s < 5; ++s) a[s] = __builtin_amdgcn_raw_buffer_load_b128(rs, hoff + s * 128, 0, AUX_SC1);
        sm = ld128(rs, WS_H + (min(lane, NB - 2) * 4 + tw) * 192 + 160);
      }
      if (lane < NB - 1) rst[lane * 4 + tw] = f32x2{__uint_as_float(sm[0]), __uint_as_float(sm[1])};
#pragma unroll
      for (int s = 0; s < 5; ++s) {
        if (mr & 2) a[s] = u32x4{0x3f803f80u, 0x3f803f80u, 0x3f803f80u, 0x3f803f80u};
        asm volatile("" : "+v"(a[s]));
      }
    }
    typedef __attribute__((address_space(3))) unsigned char lds_u8;
    const int eb = mr >> 2;
    lds_u8* l_aw = (lds_u8*)reg + lane * 8;
    lds_u8* l_ar = (lds_u8*)reg + (mr & 1) * AG_LD + qd * 16;
    lds_u8* l_pw = (lds_u8*)reg + 4096 + (qd * 16 + (mr & 7) * 2 + (mr >> 3)) * 16;
    lds_u8* l_pr = (lds_u8*)smem + OFF_REG + 4096 + (lane & 1) * 1152 + (eb * 16 + qd * 2 + ((lane >> 1) & 1)) * 16;
    lds_u8* l_p2 = l_pr + 2 * REG;
    lds_u8* l_sr = (lds_u8*)(rst + eb * 4);
    lds_u8* l_sw = (lds_u8*)(rst + (NB - 1) * 4 + tw);
    int so = (lane & 2) == 0 ? (WS_ACT + g_cg * 128 + (lane >> 2) * 8 + (lane & 1) * 4) * 4 : WS_END * 4;
    asm volatile("" : "+v"(l_aw), "+v"(l_ar), "+v"(l_pw), "+v"(l_pr), "+v"(l_p2), "+v"(l_sr), "+v"(l_sw), "+v"(so));
#define LDS(T, p) (*reinterpret_cast<__attribute__((address_space(3))) T*>(p))
    const f32x4 hv = hrow(true);
    {
      u32x2 hi, lw;
      split4(hv, hi, lw);
      LDS(u32x2, l_aw) = hi;
      LDS(u32x2, l_aw + AG_LD) = lw;
    }
    CFENCE();
    if ((mr & 14) == 12) {
#pragma unroll
      for (int s = 0; s < 5; ++s) a[s] = LDS(u32x4, l_ar + s * 64);
    }
    __builtin_amdgcn_sched_barrier(0);
    f32x4 c[4];
#pragma unroll
    for (int t = 0; t < 4; ++t) c[t] = f32x4{0.f, 0.f, 0.f, 0.f};
#pragma unroll
    for (int s = 0; s < 5; ++s)
#pragma unroll
      for (int t = 0; t < 4; ++t) c[t] = mfma16(a[s], bx[s * 4 + t], c[t]);
    LDS(f32x2, l_sw) = sums(hv);
    LDS(f32x4, l_pw) = f32x4{c[0][0] + c[0][1], c[1][0] + c[1][1], c[0][2], c[1][2]};
    LDS(f32x4, l_pw + 1152) = f32x4{c[2][0] + c[2][1], c[3][0] + c[3][1], c[2][2], c[3][2]};
    const int k = arrive(s_c + C_T);
    f32x4 ts[2] = {LDS(f32x4, l_sr), LDS(f32x4, l_sr + 16)};
    f32x4 pg[4] = {LDS(f32x4, l_pr), LDS(f32x4, l_pr + REG), LDS(f32x4, l_p2), LDS(f32x4, l_p2 + REG)};
    f32x4 pu[4] = {LDS(f32x4, l_pr + 128), LDS(f32x4, l_pr + REG + 128), LDS(f32x4, l_p2 + 128), LDS(f32x4, l_p2 + REG + 128)};
    asm volatile("" : "+v"(ts[0]), "+v"(ts[1]));
    PIN4(pg, 0); PIN4(pu, 0);
    if (arrived(k) == 3) {
      const f32x4 t = ts[0] + ts[1], g4 = (pg[0] + pg[1]) + (pg[2] + pg[3]), u4 = (pu[0] + pu[1]) + (pu[2] + pu[3]);
      const float s1 = t[0] + t[2], d = s1 * (-1.0f / DM);
      const float rstd = __builtin_amdgcn_rsqf(fmaxf((t[1] + t[3]) + d * s1, 0.f) * (1.0f / DM) + LN_EPS);
      const float g0 = rstd * (g4[0] + d * g4[2]), g1 = rstd * (g4[1] + d * g4[3]);
      const f32x2 act = {g0 * __builtin_amdgcn_rcpf(1.0f + __expf(-g0)) * (rstd * (u4[0] + d * u4[2])),
                         g1 * __builtin_amdgcn_rcpf(1.0f + __expf(-g1)) * (rstd * (u4[1] + d * u4[3]))};
      const unsigned h0 = __float_as_uint(act[0]) & 0xffff0000u, h1 = __float_as_uint(act[1]) & 0xffff0000u;
      const unsigned hi = max(PERM_HI(h1, h0), 1u), lw = max(PERM_HI(__float_as_uint(act[1] - __uint_as_float(h1)), __float_as_uint(act[0] - __uint_as_float(h0))), 1u);
      const bool odd = (lane & 1) != 0;
      const unsigned own = odd ? lw : hi, oth = __float_as_uint(dpp<DPP_XOR1>(__uint_as_float(odd ? hi : lw)));
      const unsigned w0 = odd ? oth : own, w1 = odd ? own : oth;
      const u32x4 av = {w0, w1, __float_as_uint(dpp<DPP_XOR2>(__uint_as_float(w0))), __float_as_uint(dpp<DPP_XOR2>(__uint_as_float(w1)))};
      __builtin_amdgcn_raw_buffer_store_b128(av, rs, so, 0, AUX_SC1);
    }
  } else if (isD) {
    const uint2 xd = *reinterpret_cast<const uint2*>(x + row_of(tw) * DM + d_ct * 32 + j * 4);
    const float cq = bf_lo(*reinterpret_cast<const unsigned*>(x + row_of(qd) * DM));
    const __amdgpu_buffer_rsrc_t rd = __builtin_amdgcn_make_buffer_rsrc(const_cast<bf16*>(wd), 0, FF * DM * 2, 0x00020000);
    prep(false);
    lds_wait<8>(s_c + F_REL, 1u);
    done_all();
    pull();
    done(NS);
    const unsigned te = clk();
    u32x4 bd[24];
    {
      const int off = ((tw * 384 + (mr >> 3) * 32 + qd * 8 + (mr & 4)) * DM + d_ct * 32 + (mr & 3) * 8) * 2;
      u32x4 e[24];
#pragma unroll
      for (int i = 0; i < 24; ++i) e[i] = __builtin_amdgcn_raw_buffer_load_b128(rd, off + (i & 3) * (DM * 2), (i >> 2) * 64 * (DM * 2), 0);
      MEMFENCE();
#pragma unroll
      for (int s = 0; s < 6; ++s) {
        unsigned w[8][2];
#pragma unroll
        for (int i = 0; i < 4; ++i)
#pragma unroll
          for (int d = 0; d < 2; ++d) {
            w[i][d] = __builtin_amdgcn_update_dpp(e[s * 4 + i][d], e[s * 4 + i][2 + d], 0x114, 0xf, 0xa, false);
            w[4 + i][d] = __builtin_amdgcn_update_dpp(e[s * 4 + i][2 + d], e[s * 4 + i][d], 0x104, 0xf, 0x5, false);
          }
#pragma unroll
        for (int k = 0; k < 4; ++k) {
          bd[s * 4 + 0][k] = PERM_LO(w[2 * k + 1][0], w[2 * k][0]); bd[s * 4 + 1][k] = PERM_HI(w[2 * k + 1][0], w[2 * k][0]);
          bd[s * 4 + 2][k] = PERM_LO(w[2 * k + 1][1], w[2 * k][1]); bd[s * 4 + 3][k] = PERM_HI(w[2 * k + 1][1], w[2 * k][1]);
        }
        PIN4(bd, s * 4);
      }
    }
    until(te, DLATE);
    {
      u32x4 sl;
#pragma unroll 1
      for (unsigned spin = 0; spin < SPIN_CAP; ++spin) {
        sl = ld128(rs, WS_SLAB + (r * NB + tw) * DM + d_ct * 32 + j * 4);
        if (__all(min4(sl) != 0u)) break;
        __builtin_amdgcn_s_sleep(2);
      }
      f32x4 hv = {bf_lo(xd.x), bf_hi(xd.x), bf_lo(xd.y), bf_hi(xd.y)};
#pragma unroll
      for (int i = 0; i < 4; ++i) hv[i] += sum_r(__uint_as_float(sl[i]));
      if (lane < 8) reinterpret_cast<f32x4*>(red + 512 + tw * 32)[lane] = hv;
      LGKM0();
      lds_add(s_c + C_W, lane);
      lds_wait<1>(s_c + C_W, 4u);
    }
    const int col = (mr & 3) * 8 + (mr & 4) + (mr >> 3) * 2;
    f32x2 hv = *reinterpret_cast<const f32x2*>(red + 512 + qd * 32 + col);
    typedef __attribute__((address_space(3))) f32x2 lds_f32x2;
    typedef __attribute__((address_space(1))) unsigned g_u32w;
    lds_f32x2* r2 = (lds_f32x2*)red + lane;
    lds_f32x2* r2w = r2 + tw * 64;
    g_u32w* po = (g_u32w*)(out + row_of(qd) * DM + d_ct * 32 + col);
    int aoff = (WS_ACT + tw * 12 * 128 + ((mr >> 1) & 1) * 128 + qd * 32 + (mr >> 2) * 8 + (mr & 1) * 4) * 4;
    asm volatile("" : "+v"(hv), "+v"(r2), "+v"(r2w), "+v"(po), "+v"(aoff));
    u32x4 hs, a[6];
#pragma unroll 1
    for (unsigned spin = 0; spin < SPIN_CAP; ++spin) {
      hs = ld128(rs, WS_H + (qd * 4 + (mr & 3)) * 192 + 160);
      if (__all(min4(hs) != 0u)) break;
      __builtin_amdgcn_s_sleep(1);
    }
#pragma unroll
    for (int s = 0; s < 6; ++s) a[s] = __builtin_amdgcn_raw_buffer_load_b128(rs, aoff, s * 1024, AUX_SC1);
    MEMFENCE();
    float gate;
    {
      f32x4 t = __builtin_bit_cast(f32x4, hs);
#pragma unroll
      for (int i = 0; i < 4; ++i) { t[i] += dpp<DPP_XOR2>(t[i]); t[i] += dpp<DPP_XOR1>(t[i]); }
      const float d = t[0] * (-1.0f / DM);
      const float rstd = __builtin_amdgcn_rsqf(fmaxf(t[1] + d * t[0], 0.f) * (1.0f / DM) + LN_EPS);
      hv += ((hv - cq) + d) * rstd;
      gate = __builtin_amdgcn_rcpf(1.0f + __expf(-rstd * (t[2] + d * t[3])));
      asm volatile("" : "+v"(hv), "+v"(gate));
    }
#pragma unroll 1
    for (unsigned spin = 0; spin < SPIN_CAP; ++spin) {
      unsigned mn = min4(a[0]);
#pragma unroll
      for (int s = 1; s < 6; ++s) mn = min(mn, min4(a[s]));
      if (__all(mn != 0u)) break;
#pragma unroll
      for (int s = 0; s < 6; ++s) a[s] = __builtin_amdgcn_raw_buffer_load_b128(rs, aoff, s * 1024, AUX_SC1);
      MEMFENCE();
    }
    f32x4 c[4];
#pragma unroll
    for (int t = 0; t < 4; ++t) c[t] = f32x4{0.f, 0.f, 0.f, 0.f};
#pragma unroll
    for (int s = 0; s < 6; ++s)
#pragma unroll
      for (int t = 0; t < 4; ++t) c[t] = mfma16(a[s], bd[s * 4 + t], c[t]);
    const bool up = mr >= 8;
    const float k0 = up ? c[2][2] + c[2][3] : c[0][0] + c[0][1], k1 = up ? c[3][2] + c[3][3] : c[1][0] + c[1][1];
    const float o0 = up ? c[0][2] + c[0][3] : c[2][0] + c[2][1], o1 = up ? c[1][2] + c[1][3] : c[3][0] + c[3][1];
    *r2w = f32x2{k0 + dpp<DPP_ROR8>(o0), k1 + dpp<DPP_ROR8>(o1)};
    const int k = arrive(s_c + C_T);
    f32x2 y = hv + ((r2[0] + r2[64]) + (r2[128] + r2[192])) * gate;
    asm volatile("" : "+v"(y));
    if (arrived(k) == 3) {
      __hip_atomic_store(po, __builtin_bit_cast(unsigned, __builtin_convertvector(y, bf16x2)), __ATOMIC_RELAXED, __HIP_MEMORY_SCOPE_AGENT);
    }
  } else {
    prep(bid == LEADER);
    if (isC && tw == 1) {
      lds_wait<8>(s_c + F_REL, 1u);
      const int cs = c_it != 1 ? c_it * 2 : min(4 - (g >> 3), 3), cnt = c_it != 1 ? 8 : (g < 16 ? 16 : 32), x0 = c_it != 1 ? g * 8 : (g < 16 ? g * 16 : (g & 7) * 32);
      done(c_it < 2 ? 4 : NA);
      pause(lds_get(s_c + S_BURST + (c_it >> 1)), WDELAY);
      const float wk = weight(cs, x0, cnt);
      f32x4 o = {0.f, 0.f, 0.f, 0.f};
#pragma unroll 1
      for (int k0 = 0; k0 < cnt; k0 += 16) {
        u32x2 pv[16];
#pragma unroll
        for (int k = 0; k < 16; ++k) pv[k] = __builtin_amdgcn_raw_buffer_load_b64(rs, lp * 2, (WS_PQ + ((x0 + ((k0 + k) & (cnt - 1))) * 5 + cs) * (DH / 2)) * 4, AUX_EARLY);
#pragma unroll
        for (int k = 0; k < 16; ++k) {
          const float w = k0 + k < cnt ? __uint_as_float(__builtin_amdgcn_readlane(__float_as_uint(wk), k0 + k)) : 0.f;
          o += w * f32x4{(float)(short)pv[k][0], (float)((int)pv[k][0] >> 16), (float)(short)pv[k][1], (float)((int)pv[k][1] >> 16)};
        }
      }
      u32x2 hi, lw;
      split4(o, hi, lw);
      if (lane < DH / 4) st128(rs, WS_OC + (c_it * NG + g) * DH + lane * 4, u32x4{max(hi[0], 1u), max(hi[1], 1u), max(lw[0], 1u), max(lw[1], 1u)});
    }
    done_all();
    pull();
  }
  if (tw == 3) done_all();
}

extern "C" void kernel_launch(void* const* d_in, const int* in_sizes, int n_in, void* d_out, int out_size, void* d_ws,
                              size_t ws_size, hipStream_t stream) {
  const bf16* x = (const bf16*)d_in[0];
  const bf16* kc = (const bf16*)d_in[1];
  const bf16* vc = (const bf16*)d_in[2];
  const bf16* wq = (const bf16*)d_in[3];
  const bf16* wo = (const bf16*)d_in[4];
  const bf16* wr = (const bf16*)d_in[6];
  const bf16* wg = (const bf16*)d_in[7];
  const bf16* wu = (const bf16*)d_in[8];
  const bf16* wd = (const bf16*)d_in[9];
  fused_kernel<<<NWG, NW * 64, 0, stream>>>(x, kc, vc, wq, wo, wr, wg, wu, wd, (float*)d_ws, (bf16*)d_out);
}
```

```cpp
#include <hip/hip_runtime.h>
#include <hip/hip_bf16.h>
#include <stdint.h>

typedef __hip_bfloat16 bf16;
using u32x2 = __attribute__((ext_vector_type(2))) unsigned;
using u32x4 = __attribute__((ext_vector_type(4))) unsigned;
using f32x4 = __attribute__((ext_vector_type(4))) float;
using f32x2 = __attribute__((ext_vector_type(2))) float;
using bf16x8 = __attribute__((ext_vector_type(8))) __bf16;

constexpr int NB = 4, DM = 640, NH = 32, SK = 8192, DH = 192, HD = NH * DH, FF = 1536;
constexpr float LN_EPS = 1e-5f;
constexpr float QSCALE = 0.07216878364870322f * 1.4426950408889634f;

constexpr int NWG = 256;
constexpr int GS = 8, NG = NWG / GS, NI = GS / 2;
constexpr int HPG = NI / NB;
constexpr int PPI = 512 / GS;
constexpr int NPAIR = NI * PPI;

constexpr int TAUK = 1280, TAUV = 1296;
constexpr int TSTART = 110;
constexpr int ANCH = 90;
constexpr int OGAP = 140, OLATE = 250, OSTEP = 30;
constexpr int KS1 = 100, KP1 = 72;
constexpr int VGAP = 50;
constexpr int DEEP1 = 70, DEEP2 = 160, LAGOFF = 20;
constexpr int ENDT = 12, ENDS = 2;
constexpr int JUMP = 145, STALL = 230;
constexpr int ZT = 1800, HST = 2800, RT0 = 3900, RTS = 1000, RA1 = 220, RA2 = 500, LEADER = 24;
constexpr unsigned NRD = 5, RMIN = 30;
constexpr int FBX = 35, FB0 = 20, FB1 = 10, FBD = 10, FBS = 10;
constexpr int HOLD = 4000, GB = 12, GBS = 4, GBX = 60, GBF = 12;
constexpr int ROWS = 0x1B;
constexpr int RW0 = 656, RW1 = 272, RW2 = 224, RW3 = 512, RW4 = 528;
constexpr int TLEAD = 10, PFD = 3, WFD = 3;
constexpr int QWAIT = 820;
constexpr int WDELAY = 100, WDELAY2 = 460, HDELAY = 1150, HDELAY2 = 780, DLATE = 100;
constexpr int TPT = 350, TPS = 28;
constexpr int CODE = 24 * 64 * 46;
constexpr int PW = 5, DPW = 150, GW = 30;

constexpr int KS = 8;
constexpr int WS_PML = 0;
constexpr int WS_PO = WS_PML + NWG * 64;
constexpr int WS_PQ = WS_PO + NWG * DH;
constexpr int WS_OC = WS_PQ + NWG * 5 * (DH / 2);
constexpr int WS_SLAB = WS_OC + (NB - 1) * NH * DH;
constexpr int WS_ACT = WS_SLAB + KS * NB * DM;
constexpr int WS_H = WS_ACT + NB * FF;
constexpr int WS_HN = WS_H + NB * 4 * 192;
constexpr int WS_HR = WS_HN + NWG;
constexpr int WS_HA = WS_HR + 2 * NWG;
constexpr int QFS = 2 * GS * 32;
constexpr int WS_QF = WS_HA + 32 * NWG;
constexpr int WS_QP = WS_QF + NG * QFS;
constexpr int WS_END = WS_QP + NH * 2 * NB * DH;
constexpr int NPROD = HPG * 6;
static_assert(WS_PO % 32 == 0 && WS_SLAB % 32 == 0 && WS_ACT % 32 == 0 && WS_HN % 32 == 0 && WS_QF % 32 == 0 && WS_QP % 32 == 0, "line alignment");


__device__ __forceinline__ int row_of(int b) { return (ROWS >> (2 * b)) & 3; }
__device__ __forceinline__ float bf_lo(unsigned u) { return __uint_as_float(u << 16); }
__device__ __forceinline__ float bf_hi(unsigned u) { return __uint_as_float(u & 0xffff0000u); }
__device__ __forceinline__ f32x2 bf_pair(unsigned u) { return f32x2{bf_lo(u), bf_hi(u)}; }
__device__ __forceinline__ f32x2 fma2(float a, f32x2 b, f32x2 c) { return __builtin_elementwise_fma(f32x2{a, a}, b, c); }
__device__ __forceinline__ f32x2 fma2(f32x2 a, f32x2 b, f32x2 c) { return __builtin_elementwise_fma(a, b, c); }

template <int CTRL> __device__ __forceinline__ float dpp(float x) {
  return __builtin_bit_cast(float, __builtin_amdgcn_mov_dpp(__builtin_bit_cast(int, x), CTRL, 0xf, 0xf, true));
}
constexpr int DPP_XOR1 = 0xB1, DPP_XOR2 = 0x4E, DPP_HMIRROR = 0x141, DPP_MIRROR = 0x140, DPP_ROR8 = 0x128;

__device__ __forceinline__ float sum_x16_32(float x) {
  auto s = __builtin_amdgcn_permlane16_swap(__float_as_uint(x), __float_as_uint(x), false, false);
  x = __uint_as_float(s[0]) + __uint_as_float(s[1]);
  auto t = __builtin_amdgcn_permlane32_swap(__float_as_uint(x), __float_as_uint(x), false, false);
  return __uint_as_float(t[0]) + __uint_as_float(t[1]);
}
__device__ __forceinline__ float max_x16_32(float x) {
  auto s = __builtin_amdgcn_permlane16_swap(__float_as_uint(x), __float_as_uint(x), false, false);
  x = fmaxf(__uint_as_float(s[0]), __uint_as_float(s[1]));
  auto t = __builtin_amdgcn_permlane32_swap(__float_as_uint(x), __float_as_uint(x), false, false);
  return fmaxf(__uint_as_float(t[0]), __uint_as_float(t[1]));
}
__device__ __forceinline__ float tsum32(float a, float b) {
  auto t = __builtin_amdgcn_permlane32_swap(__float_as_uint(a), __float_as_uint(b), false, false);
  return __uint_as_float(t[0]) + __uint_as_float(t[1]);
}
__device__ __forceinline__ float tsum16(float a, float b) {
  auto t = __builtin_amdgcn_permlane16_swap(__float_as_uint(a), __float_as_uint(b), false, false);
  return __uint_as_float(t[0]) + __uint_as_float(t[1]);
}
__device__ __forceinline__ float sum_r(float v) { v += dpp<DPP_ROR8>(v); return sum_x16_32(v); }
__device__ __forceinline__ float row_sum(float v) {
  v += dpp<DPP_XOR1>(v); v += dpp<DPP_XOR2>(v); v += dpp<DPP_HMIRROR>(v);
  return v + dpp<DPP_MIRROR>(v);
}
__device__ __forceinline__ float row_max(float v) {
  v = fmaxf(v, dpp<DPP_XOR1>(v)); v = fmaxf(v, dpp<DPP_XOR2>(v)); v = fmaxf(v, dpp<DPP_HMIRROR>(v));
  return fmaxf(v, dpp<DPP_MIRROR>(v));
}
__device__ __forceinline__ float wave_sum(float v) {
  v += dpp<DPP_XOR1>(v); v += dpp<DPP_XOR2>(v); v += dpp<DPP_HMIRROR>(v);
  return sum_r(v);
}

__device__ __forceinline__ u32x4 ld_nt(const char* p) { return __builtin_nontemporal_load(reinterpret_cast<const u32x4*>(p)); }

struct Tile { u32x4 d[3]; };

#define PIN4(a, o) asm volatile("" : "+v"((a)[(o) + 0]), "+v"((a)[(o) + 1]), "+v"((a)[(o) + 2]), "+v"((a)[(o) + 3]))
#define MEMFENCE() do { asm volatile("" ::: "memory"); __builtin_amdgcn_sched_barrier(0); } while (0)

__device__ __forceinline__ void glds16_nt(const void* gsrc, unsigned lds_dst) {
  unsigned keep;
  asm volatile("s_mov_b32 %0, m0\n\ts_mov_b32 m0, %2\n\ts_nop 0\n\tglobal_load_lds_dwordx4 %1, off nt\n\ts_mov_b32 m0, %0"
               : "=&s"(keep) : "v"(gsrc), "s"(lds_dst) : "memory");
}

using bf16x2 = __attribute__((ext_vector_type(2))) __bf16;
__device__ __forceinline__ float dot2(unsigned a, unsigned b, float c) { return __builtin_amdgcn_fdot2_f32_bf16(__builtin_bit_cast(bf16x2, a), __builtin_bit_cast(bf16x2, b), c, false); }
__device__ __forceinline__ float tile_score(const Tile& k, const u32x2 (&q)[12]) {
  float s0 = 0.f, s1 = 0.f;
#pragma unroll
  for (int i = 0; i < 3; ++i) {
    u32x4 v = k.d[i];
    asm("" : "+v"(v));
#pragma unroll
    for (int d = 0; d < 4; ++d) { s0 = dot2(v[d], q[i * 4 + d][0], s0); s1 = dot2(v[d], q[i * 4 + d][1], s1); }
  }
  return s0 + s1;
}
__device__ __forceinline__ void pair_put(float a, float b, float* dst, int j) {
  float s = (j < 4 ? a : b) + dpp<DPP_HMIRROR>(j < 4 ? b : a);
  s += dpp<DPP_XOR1>(s);
  s += dpp<DPP_XOR2>(s);
  if ((j & 3) == 0) dst[j * 2] = s;
}
__device__ __forceinline__ void score_put(float s, float* dst, int j) {
  s += dpp<DPP_XOR1>(s);
  s += dpp<DPP_XOR2>(s);
  s += dpp<DPP_HMIRROR>(s);
  if (j == 0) *dst = s;
}
__device__ __forceinline__ void tile_load(Tile& t, const char* base, unsigned off, int half) {
#pragma unroll
  for (int i = 0; i < 3; ++i) t.d[i] = ld_nt(base + off + half * 3072 + i * 128);
}
__device__ __forceinline__ void pair_issue(Tile& ta, Tile& tb, const char* base, unsigned off, int deep) {
  deep = __builtin_amdgcn_readfirstlane(deep);
#define WAIT_IF(level, n) asm volatile("s_cmp_lg_u32 %0, " #level "\n\ts_cbranch_scc1 .Lpi%=\n\ts_waitcnt vmcnt(" #n ")\n.Lpi%=:" :: "s"(deep) : "scc", "memory")
  const char* p = base + off;
  WAIT_IF(0, 3);
  ta.d[0] = ld_nt(p); ta.d[1] = ld_nt(p + 128); ta.d[2] = ld_nt(p + 256); MEMFENCE();
  WAIT_IF(0, 3); WAIT_IF(1, 6);
  tb.d[0] = ld_nt(p + 3072); tb.d[1] = ld_nt(p + 3200); tb.d[2] = ld_nt(p + 3328); MEMFENCE();
#undef WAIT_IF
}
__device__ __forceinline__ void pv_last(float p, Tile& v, f32x2 (&acc)[12]) {
#pragma unroll
  for (int i = 0; i < 3; ++i) {
    asm volatile("" : "+v"(v.d[i]), "+v"(p));
#pragma unroll
    for (int d = 0; d < 4; ++d) acc[i * 4 + d] = fma2(p, bf_pair(v.d[i][d]), acc[i * 4 + d]);
    PIN4(acc, i * 4);
  }
}
__device__ __forceinline__ void q_load(u32x2 (&q)[12], const float* src, int j) {
#pragma unroll
  for (int i = 0; i < 6; ++i) {
    const u32x4 a = *reinterpret_cast<const u32x4*>(src + (i >> 1) * 64 + j * 8 + (i & 1) * 4);
    q[i * 2] = u32x2{a[0], a[1]}; q[i * 2 + 1] = u32x2{a[2], a[3]};
  }
}
#define VMCNT(n) asm volatile("s_waitcnt vmcnt(%0)" :: "n"(n) : "memory")

constexpr int AUX_SC1 = 16, AUX_EARLY = 18;
constexpr unsigned SPIN_CAP = 1u << 17;
__device__ __forceinline__ unsigned nz_bits(float v) { return max(__float_as_uint(v), 1u); }
__device__ __forceinline__ unsigned min4(u32x4 v) { return min(min(v[0], v[1]), min(v[2], v[3])); }
__device__ __forceinline__ unsigned ld32(__amdgpu_buffer_rsrc_t rs, int word) { return __builtin_amdgcn_raw_buffer_load_b32(rs, word * 4, 0, AUX_SC1); }
__device__ __forceinline__ u32x2 ld64(__amdgpu_buffer_rsrc_t rs, int word) { return __builtin_amdgcn_raw_buffer_load_b64(rs, word * 4, 0, AUX_SC1); }
__device__ __forceinline__ u32x4 ld128(__amdgpu_buffer_rsrc_t rs, int word) { return __builtin_amdgcn_raw_buffer_load_b128(rs, word * 4, 0, AUX_SC1); }
__device__ __forceinline__ void st128(__amdgpu_buffer_rsrc_t rs, int word, u32x4 v) { __builtin_amdgcn_raw_buffer_store_b128(v, rs, word * 4, 0, AUX_SC1); }

__device__ __forceinline__ u32x4 ld128nt(__amdgpu_buffer_rsrc_t rs, int word) { return __builtin_amdgcn_raw_buffer_load_b128(rs, word * 4, 0, AUX_EARLY); }
__device__ __forceinline__ void st128nt(__amdgpu_buffer_rsrc_t rs, int word, u32x4 v) { __builtin_amdgcn_raw_buffer_store_b128(v, rs, word * 4, 0, AUX_EARLY); }

__device__ __forceinline__ bool leader_release(__amdgpu_buffer_rsrc_t rs, int lane, unsigned t0, unsigned rd, int slow, int& prev) {
  u32x4 a[4];
#pragma unroll
  for (int k = 0; k < 4; ++k) a[k] = ld128(rs, WS_HA + (lane * 4 + k) * 32);
  const u32x4 n = ld128(rs, WS_HN + lane * 4);
  if (rd == 0u && !__all(a[0][0] == n[0] && a[1][0] == n[1] && a[2][0] == n[2] && a[3][0] == n[3])) return false;
  const float e = fmaxf(fmaxf((float)(int)(t0 - a[0][2]), (float)(int)(t0 - a[1][2])), fmaxf((float)(int)(t0 - a[2][2]), (float)(int)(t0 - a[3][2])));
  unsigned first = t0 - (unsigned)(int)max_x16_32(row_max(e));
  float ls = 0.f, lx = 0.f;
  const int pend = max(prev - FBS * (RTS - RA2) * 16 / TAUK, 0);
  prev = max(pend - FBS * RA2 * 16 / TAUK, 0);
#pragma unroll
  for (int k = 0; k < 4; ++k) {
    const float l = a[k][0] == n[k] + rd ? (float)min(a[k][3] + (rd ? 0u : min(a[k][2] - first, 300u)), 2000u) : (float)-pend;
    ls += l; lx = fmaxf(lx, l);
  }
  ls = wave_sum(ls) * (1.0f / NWG); lx = max_x16_32(row_max(lx));
  const int extra = __builtin_amdgcn_readfirstlane(min(max((int)(ls + (lx - max(ls, 0.f)) * (FBX / 100.0f)) - (rd ? FB1 + (int)(NRD - rd) * FBD : FB0), 0), 600));
  if (rd != 0u && __all(ls * NWG < HOLD)) return true;
  first = (first & 0xffffu) | ((unsigned)(slow + extra) << 16);
  prev += extra;
  st128(rs, WS_HR + lane * 8, u32x4{a[0][1] + rd, first, a[1][1] + rd, first});
  st128(rs, WS_HR + lane * 8 + 4, u32x4{a[2][1] + rd, first, a[3][1] + rd, first});
  return true;
}

typedef __attribute__((address_space(3))) unsigned lds_u32;
__device__ __forceinline__ unsigned lds_add(lds_u32* p, int lane) {
  unsigned k = 0;
  if (lane == 0) k = __hip_atomic_fetch_add(p, 1u, __ATOMIC_RELAXED, __HIP_MEMORY_SCOPE_WORKGROUP);
  return __builtin_amdgcn_readfirstlane(k);
}
#define CFENCE() asm volatile("" ::: "memory")
__device__ __forceinline__ int arrive(lds_u32* p) {
  int v;
  unsigned long long keep;
  asm volatile("s_mov_b64 %1, exec\n\ts_mov_b64 exec, 1\n\tds_add_rtn_u32 %0, %2, %3\n\ts_mov_b64 exec, %1"
               : "=&v"(v), "=&s"(keep) : "v"((unsigned)(size_t)p), "v"(1u) : "memory");
  return v;
}
__device__ __forceinline__ void bump(lds_u32* p) {
  unsigned long long keep;
  asm volatile("s_mov_b64 %0, exec\n\ts_mov_b64 exec, 1\n\tds_add_u32 %1, %2\n\ts_mov_b64 exec, %0" : "=&s"(keep) : "v"((unsigned)(size_t)p), "v"(1u) : "memory");
}
__device__ __forceinline__ int arrived(int v) {
  asm volatile("s_waitcnt lgkmcnt(0)" : "+v"(v) :: "memory");
  return __builtin_amdgcn_readfirstlane(v);
}
__device__ __forceinline__ unsigned lds_get(lds_u32* p) {
  return __builtin_amdgcn_readfirstlane(__hip_atomic_load(p, __ATOMIC_RELAXED, __HIP_MEMORY_SCOPE_WORKGROUP));
}
template <int SLEEP> __device__ __forceinline__ void lds_wait(lds_u32* p, unsigned n) {
#pragma unroll 1
  for (unsigned spin = 0; spin < (SPIN_CAP << 4); ++spin) {
    if (lds_get(p) >= n) break;
    __builtin_amdgcn_s_sleep(SLEEP);
  }
  asm volatile("" ::: "memory");
}
__device__ __forceinline__ unsigned clk() { return (unsigned)__builtin_amdgcn_s_memrealtime(); }
struct Table {
  unsigned t0; int was, moved, slowed, folded;
  u32x2 told;
  unsigned mine, tmp;
  template <int K> __device__ __forceinline__ void put(int v) { asm volatile("v_mov_b32 %0, %2\n\tds_write_b32 %1, %0 offset:%3" : "+v"(tmp), "+v"(mine) : "s"(v), "n"(K * 4) : "memory"); }
  __device__ __forceinline__ int gate(int due, int sh = 0) {
    const int fold = __builtin_amdgcn_readfirstlane(told[1]);
    if (fold != folded) { const int f = min(fold - folded, moved); moved -= f; slowed += f; folded = fold; put<0>(moved); }
    const int left = (int)__builtin_amdgcn_readfirstlane(told[0]) - slowed, d = max(min(left, FBS), left < -GBX ? -GBF : -GBS);
    t0 += d; was -= d; slowed += d;
    int late = (int)(clk() - t0) - due;
    if (late - was > JUMP) { const int m = late - was < STALL + JUMP ? STALL : late - was; t0 += m; moved += m; late -= m; put<0>(moved); }
    const int lag = max(late - max(left - d, 0) - LAGOFF, 0);
    put<4>(lag);
    was = max(late, -40);
#pragma unroll 1
    while (late < 0) { __builtin_amdgcn_s_sleep(1); late = (int)(clk() - t0) - due; }
    return lag < (DEEP1 >> sh) ? 0 : (lag < (DEEP2 >> sh) ? 1 : 2);
  }
  __device__ __forceinline__ void listen(lds_u32* slow) {
    told = __builtin_bit_cast(u32x2, __hip_atomic_load((__attribute__((address_space(3))) unsigned long long*)slow, __ATOMIC_RELAXED, __HIP_MEMORY_SCOPE_WORKGROUP));
  }
  __device__ __forceinline__ void agree(lds_u32* all) {
    if (moved == 0) return;
    const int m = (int)min(min(lds_get(all), lds_get(all + 1)), min(lds_get(all + 2), lds_get(all + 3))) + 30;
    if (m < moved) { t0 -= moved - m; moved = m; was = 1 << 20; put<0>(moved); }
  }
};
__device__ __forceinline__ void until(unsigned t0, int due) {
#pragma unroll 1
  while ((int)(clk() - t0) < due) __builtin_amdgcn_s_sleep(1);
}
__device__ __forceinline__ void pause(unsigned t0, unsigned due) {
#pragma unroll 1
  while (clk() - t0 < due) __builtin_amdgcn_s_sleep(16);
}
#define LGKM0() asm volatile("s_waitcnt lgkmcnt(0)" ::: "memory")

__device__ __forceinline__ void split4(f32x4 v, u32x2& hi, u32x2& lo) {
  unsigned h[4], r[4];
#pragma unroll
  for (int i = 0; i < 4; ++i) { h[i] = __float_as_uint(v[i]) & 0xffff0000u; r[i] = __float_as_uint(v[i] - __uint_as_float(h[i])); }
  hi = u32x2{__builtin_amdgcn_perm(h[1], h[0], 0x07060302u), __builtin_amdgcn_perm(h[3], h[2], 0x07060302u)};
  lo = u32x2{__builtin_amdgcn_perm(r[1], r[0], 0x07060302u), __builtin_amdgcn_perm(r[3], r[2], 0x07060302u)};
}
__device__ __forceinline__ u32x4 split8(f32x4 v, bool odd) {
  u32x2 hi, lo;
  split4(v, hi, lo);
  const unsigned n0 = __float_as_uint(dpp<DPP_XOR1>(__uint_as_float(odd ? hi[0] : lo[0]))), n1 = __float_as_uint(dpp<DPP_XOR1>(__uint_as_float(odd ? hi[1] : lo[1])));
  return u32x4{max(odd ? n0 : hi[0], 1u), max(odd ? n1 : hi[1], 1u), max(odd ? lo[0] : n0, 1u), max(odd ? lo[1] : n1, 1u)};
}
__device__ __forceinline__ u32x4 q_pack(u32x4 a, u32x4 b) {
  u32x2 hi, lo;
  split4((__builtin_bit_cast(f32x4, a) + __builtin_bit_cast(f32x4, b)) * QSCALE, hi, lo);
  return u32x4{hi[0], lo[0], hi[1], lo[1]};
}
__device__ __forceinline__ f32x4 mfma16(u32x4 a, u32x4 b, f32x4 c) {
  return __builtin_amdgcn_mfma_f32_16x16x32_bf16(__builtin_bit_cast(bf16x8, a), __builtin_bit_cast(bf16x8, b), c, 0, 0, 0);
}

constexpr int NW = 8, NMW = 4;
constexpr int NLP = 5, NRP = 8;
constexpr int NTP = 4 * (NLP + NRP);
constexpr int REG = NLP * 6 * 1024;
constexpr int NRING = 4;
constexpr int NS = 6, NA = NS - 1;
__device__ __forceinline__ int seg_beg(int s) { return s >= NS ? NPAIR : ((0xC86540 >> (4 * s)) & 15) * 16; }
__device__ __forceinline__ int seg_k(int t) { return (int)(0x5433222211110000ull >> (4 * (t >> 4))) & 15; }
__device__ __forceinline__ int seg_of_k(int k) { return (0x123540 >> (4 * k)) & 15; }
__device__ __forceinline__ int k_of_seg(int s) { return (0x213450 >> (4 * s)) & 15; }
__device__ __forceinline__ int pair_of(int t) { return t < 64 ? t : (t < 192 ? t + 64 : t - (t < 224 ? 96 : (t < 240 ? 144 : 176))); }
constexpr int OFF_CNT = 0, OFF_REG = 256,
              OFF_SC = OFF_REG + 4 * REG, OFF_BUF = OFF_SC + NPAIR * 16 * 4, OFF_Q = OFF_BUF + NRING * NMW * DH * 4, OFF_ML = OFF_Q + NS * DH * 4,
              OFF_RED = OFF_ML + 128, OFF_END = OFF_RED + 4 * 256 * 4;
enum { F_REL = 0, F_DONE, C_LAST, C_T = 3, F_OUT, C_P, C_K, F_P, F_Q = 8, F_FREE, F_Q2, C_W = 12, C_SC = 16, C_PART = 24, S_ECHO = 32, S_BURST = 40, S_LATE = 42, S_MOVED = 48, S_SLOW = 56 };
__device__ __forceinline__ int due_k(int p) { return TSTART + ((p * TAUK + min(p, KP1) * KS1) >> 6); }
__device__ __forceinline__ int due_v(int p) { return TSTART + OGAP + ((NPAIR * TAUK + KP1 * KS1 + p * TAUV + max(p - NPAIR / 2, 0) * (VGAP * 64 / (NPAIR / 2))) >> 6); }
constexpr int AW_LD = DH * 2 + 16, AG_LD = 512 + 16;

__global__ void __launch_bounds__(NW * 64, 2) fused_kernel(const bf16* __restrict__ x, const bf16* __restrict__ kc,
                                                        const bf16* __restrict__ vc, const bf16* __restrict__ wq,
                                                        const bf16* __restrict__ wo, const bf16* __restrict__ wr,
                                                        const bf16* __restrict__ wg, const bf16* __restrict__ wu,
                                                        const bf16* __restrict__ wd, float* ws, bf16* __restrict__ out) {
  __shared__ __attribute__((aligned(16))) unsigned char smem[OFF_END];
  float* s_sc = reinterpret_cast<float*>(smem + OFF_SC);
  float* s_buf = reinterpret_cast<float*>(smem + OFF_BUF);
  float* s_q = reinterpret_cast<float*>(smem + OFF_Q);
  u32x4* s_ml = reinterpret_cast<u32x4*>(smem + OFF_ML);
  lds_u32* s_c = (lds_u32*)(smem + OFF_CNT);

  const int tid = threadIdx.x, lane = tid & 63, r = lane >> 3, j = lane & 7;
  const int wave = __builtin_amdgcn_readfirstlane(tid >> 6);
  const int tw = wave - NMW;
  const int bid = blockIdx.x, xl = bid & 7, idx = bid >> 3;
  const int g = (xl * 32 + idx) / GS, mm = idx % GS;
  const __amdgpu_buffer_rsrc_t rs = __builtin_amdgcn_make_buffer_rsrc(ws, 0, WS_END * 4, 0x00020000);
  const int ro = ((idx & 3) << 3) | (idx >> 2);
  const bool isW = ro < 10, isG = ro >= 10 && ro < 16;
  const int w_ks = xl, w_ct = ro;
  const int g_cg = xl * 6 + (ro - 10);
  const int d_ct = (xl + ((ro - 16) >> 2) * 8) * 2 + (((ro - 16) >> 1) & 1);
  const bool isD = ro >= 16 && ro < 24 && (ro & 1) == 0 && d_ct < DM / 32;
  const bool isH = ro == 22 && xl >= 2 && xl < 6;
  const bool isC = (mm & 7) == 3 || (mm & 7) >= 6;
  const int c_it = (mm >> 3) * 3 + max((mm & 7) - 5, 0);
  const int qf = WS_QF + g * QFS;
  const char* kb = reinterpret_cast<const char*>(kc);
  const char* vb = reinterpret_cast<const char*>(vc);
  const unsigned lo = (r * 24 + j) * 16;
  const int xb = g * GS + mm, c0 = mm * 2 + (g & 1) * RW0 + (g >> 1 & 1) * RW1 + (g >> 2 & 1) * RW2 + (g >> 3 & 1) * RW3 + (g >> 4) * RW4;
  auto ptile = [&](int p) -> unsigned {
    int item = row_of(p >> 6) * NH + g, t = (p & 63) * 16 + c0;
    if ((p >> 6) == 1) {
      item = 2 * NH + ((p & 32) ? xb >> 4 : 24 - ((p & 16) >> 1) + xl);
      t = (p & 32) ? (p & 31) * 32 + (xb & 15) * 2 + (g >> 1 & 1) * 640 + (xl & 1) * 960 + (xl >> 1 & 1) * 32 + (xl >> 2) * 544
                   : (p & 15) * 64 + idx * 2 + (xl & 1) * 768 + (xl >> 1 & 1) * 704 + (xl >> 2) * 128;
    }
    return item * 1024 + (t & 1023);
  };
  auto poff = [&](int p) -> unsigned { return ptile(p) * 3072u; };
  const unsigned lds_base = (unsigned)(size_t)(__attribute__((address_space(3))) unsigned char*)smem + OFF_REG + tw * REG;
  auto merged = [&](int it, int ln) -> f32x4 {
    const float* src = s_buf + (it % NRING) * (NMW * DH) + ((ln >> 1) & 7) * 24 + (ln >> 4) * 8 + (ln & 1) * 4;
    return (*reinterpret_cast<const f32x4*>(src) + *reinterpret_cast<const f32x4*>(src + DH)) +
           (*reinterpret_cast<const f32x4*>(src + 2 * DH) + *reinterpret_cast<const f32x4*>(src + 3 * DH));
  };
#define NONCE() do { unsigned n_ = __builtin_amdgcn_readfirstlane(f) + 1u; while (__any(f == n_)) ++n_; \
    if (lane < GS) st128(rs, qf + (GS + mm) * 32 + lane * 4, u32x4{n_, n_, n_, n_}); f = n_; } while (0)
  unsigned long long pc;
  asm volatile("s_getpc_b64 %0" : "=s"(pc));
  unsigned r0 = 0u;
  unsigned t0 = clk();
  unsigned tb0 = t0;
  if (wave < NMW) {
    Tile ta, tb, tc, td;
    const int c = lane & (GS - 1);
    unsigned f = 0u;
    if (wave == 3) f = ld32(rs, qf + c * 32 + mm * 4);
    if (mm < NPROD) {
      const int hp = g + (mm / 6) * NG, ck = (mm % 6) >> 1, hf = mm & 1, jp = hf * 4 + wave;
      const int bb = lane >> 4, t = lane & 15;
      const u32x4* xp = reinterpret_cast<const u32x4*>(x + row_of(bb) * DM);
      u32x4 xv[5], xs = {0u, 0u, 0u, 0u};
#pragma unroll
      for (int k = 0; k < 5; ++k) xv[k] = xp[t + 16 * k];
      if (t < 10) xs = xp[jp * 10 + t];
      const u32x4* wp = reinterpret_cast<const u32x4*>(wq + (size_t)(jp * 80 + r) * HD + hp * DH + ck * 64) + j;
      u32x4 wv[10];
#pragma unroll
      for (int k = 0; k < 10; ++k) wv[k] = __builtin_nontemporal_load(wp + (size_t)k * 8 * (HD / 8));
      if (wave == 0) s_c[lane] = lane == C_K ? NTP + NMW : 0u;
      MEMFENCE(); LGKM0(); __builtin_amdgcn_s_barrier(); MEMFENCE();
      if (wave == 3) { NONCE(); }
      float sm = 0.f;
#pragma unroll
      for (int k = 0; k < 5; ++k)
#pragma unroll
        for (int d = 0; d < 4; ++d) sm += bf_lo(xv[k][d]) + bf_hi(xv[k][d]);
      const float mean = row_sum(sm) * (1.0f / DM);
      float sv = 0.f;
#pragma unroll
      for (int k = 0; k < 5; ++k)
#pragma unroll
        for (int d = 0; d < 4; ++d) { const float a = bf_lo(xv[k][d]) - mean, c2 = bf_hi(xv[k][d]) - mean; sv += a * a + c2 * c2; }
      const float rstd = 1.0f / sqrtf(row_sum(sv) * (1.0f / DM) + LN_EPS);
      float* s_xn = s_buf + wave * 320;
      if (t < 10) {
#pragma unroll
        for (int d = 0; d < 4; ++d) {
          s_xn[(t * 8 + 2 * d) * 4 + bb] = (bf_lo(xs[d]) - mean) * rstd;
          s_xn[(t * 8 + 2 * d + 1) * 4 + bb] = (bf_hi(xs[d]) - mean) * rstd;
        }
      }
      LGKM0();
      f32x2 qa[16];
#pragma unroll
      for (int i = 0; i < 16; ++i) qa[i] = f32x2{0.f, 0.f};
#pragma unroll
      for (int k = 0; k < 10; ++k) {
        const f32x4 xn = *reinterpret_cast<const f32x4*>(&s_xn[(k * 8 + r) * 4]);
#pragma unroll
        for (int d = 0; d < 4; ++d) {
          const f32x2 w2 = bf_pair(wv[k][d]);
#pragma unroll
          for (int e = 0; e < 4; ++e) qa[e * 4 + d] = fma2(xn[e], w2, qa[e * 4 + d]);
        }
      }
      float t16[16], t8[8];
#pragma unroll
      for (int i = 0; i < 16; ++i) t16[i] = tsum32(qa[i >> 1][i & 1], qa[8 + (i >> 1)][i & 1]);
#pragma unroll
      for (int i = 0; i < 8; ++i) t8[i] = tsum16(t16[i], t16[i + 8]);
#pragma unroll
      for (int i = 0; i < 8; ++i) t8[i] += dpp<DPP_ROR8>(t8[i]);
      const bool up = (lane & 8) != 0;
      f32x4* r4 = reinterpret_cast<f32x4*>(smem + OFF_RED);
      r4[wave * 64 + lane] = f32x4{up ? t8[4] : t8[0], up ? t8[5] : t8[1], up ? t8[6] : t8[2], up ? t8[7] : t8[3]};
      LGKM0();
      if (lds_add(s_c + C_P, lane) == NMW - 1) {
        asm volatile("" ::: "memory");
        const f32x4 o4 = (r4[lane] + r4[64 + lane]) + (r4[128 + lane] + r4[192 + lane]);
        st128nt(rs, WS_QP + ((hp * 2 + hf) * NB + bb) * DH + ck * 64 + j * 8 + (up ? 4 : 0), __builtin_bit_cast(u32x4, o4));
        VMCNT(0);
        if (lane == 0) s_c[F_OUT] = 1u;
      }
    } else {
      if (wave == 0) s_c[lane] = lane == C_K ? NTP + NMW : 0u;
      MEMFENCE(); LGKM0(); __builtin_amdgcn_s_barrier(); MEMFENCE();
      if (wave == 3) { NONCE(); }
      else {
        typedef __attribute__((address_space(1))) const unsigned g_u32;
        typedef __attribute__((address_space(1))) const char g_char;
        const int id = ((idx >> 3) * 2 + (mm & 1)) * 3 + wave;
        if (lane < CODE / (24 * 64)) {
          const unsigned v = *(g_u32*)((pc & ~255ull) + id * (CODE / 24) + lane * 64);
          asm volatile("" :: "v"(v));
        }
        if (lane < 7 && id < 21) {
          const int st = id < 8 ? (0x1320 >> ((id >> 1) * 4)) & 3 : (id >> 1) & 3, hf = id & 1;
          int head = row_of(st) * NH + xl * 4 + hf * 2;
          unsigned off = min(lane << 20, (6 << 20) - 64);
          if (st == 1) {
            head = 2 * NH + (hf ? 16 + xl : 2 * xl);
            if (hf) off = lane < 4 ? min(lane << 20, (3 << 20) - 64) : 8 * (3 << 20) + min((lane - 4) * (3 << 19), (3 << 20) - 64);
          }
          g_char* p = (g_char*)(id < 8 ? kb : vb) + (size_t)head * (3 << 20) + off;
          if (id >= 16) p = (g_char*)(id == 16 ? wo + xl * 4 * DH * DM : (id == 17 ? wg : (id == 18 ? wu : wd))) + min(lane * (id == 16 ? 163840 : 327680), (id == 16 ? 983040 : 1966080) - 64);
          if (id >= 20) p = (g_char*)(lane ? wr : out);
          until(t0, TPT + id * TPS);
          const unsigned v = __builtin_nontemporal_load((g_u32*)p);
          asm volatile("" :: "v"(v));
        }
      }
    }
    unsigned tk_e = ptile(pair_of(lane * 4)) | (pair_of(lane * 4) << 17) | (seg_k(lane * 4) << 25) | ((lane < 48 ? 4u : (lane < 56 ? 5u : 6u)) << 28), tv_o = poff(lane * 4 + wave);
    int tk_d = due_k(lane * 4), tv_d = due_v(lane * 4 + wave);
    asm volatile("" : "+v"(tk_e), "+v"(tv_o), "+v"(tk_d), "+v"(tv_d));
    MEMFENCE();
    if (wave == 3) {
      __builtin_amdgcn_s_setprio(3);
      const unsigned nonce = f;
      unsigned echoed = 0u;
      if (mm < NPROD) {
        echoed = ld32(rs, qf + (GS + c) * 32);
        asm volatile("" : "+v"(echoed) :: "memory");
        lds_wait<1>(s_c + F_OUT, 1u);
        if (lane < GS) st128(rs, qf + mm * 32 + lane * 4, u32x4{echoed, echoed, echoed, echoed});
      }
      until(t0, QWAIT);
#pragma unroll 1
      for (unsigned spin = 0; spin < SPIN_CAP; ++spin) {
        const unsigned nv = ld32(rs, qf + (GS + c) * 32);
        f = ld32(rs, qf + c * 32 + mm * 4);
        const bool done = __all(c == mm || c >= NPROD || f == nonce);
        if (mm < NPROD && __any(nv != echoed)) {
          if (lane < GS) st128(rs, qf + mm * 32 + lane * 4, u32x4{nv, nv, nv, nv});
          echoed = nv;
        }
        if (done) break;
        __builtin_amdgcn_s_sleep(8);
      }
      if (lane < GS) s_c[S_ECHO + lane] = echoed;
      if (lane < DH / 4) {
        u32x4 p[3][2];
#pragma unroll
        for (int it = 0; it < 3; ++it)
#pragma unroll
          for (int k = 0; k < 2; ++k) p[it][k] = ld128nt(rs, WS_QP + ((g * 2 + k) * NB + (it ? it + 1 : 0)) * DH + lane * 4);
#pragma unroll
        for (int it = 0; it < 3; ++it) {
          *reinterpret_cast<u32x4*>(&s_q[(it ? it + 3 : 0) * DH + lane * 4]) = q_pack(p[it][0], p[it][1]);
          if (it != 1) { LGKM0(); if (lane == 0) s_c[F_Q] = it + 1; MEMFENCE(); }
        }
      }
    }
    __builtin_amdgcn_s_setprio(3);
    until(t0, due_k(NTP + wave));
    tb0 = lds_get(s_c + S_LATE);
    if ((unsigned)(tb0 - t0 + 100u) > 500u) tb0 = t0;
    until(tb0, due_k(NTP + wave));
    Table tt = {tb0, 1 << 20, 0, 0, 0, {0u, 0u}, (unsigned)(size_t)(s_c + S_MOVED + wave), 0u};
    tile_load(ta, kb, poff(NTP + wave) + lo, 0);
    tile_load(tb, kb, poff(NTP + wave) + lo, 1);
    MEMFENCE();
    {
      u32x2 q[12];
      int cp = NTP + wave, it = 0, nxt = lds_add(s_c + C_K, lane), passed = 0, ci = -1;
      auto kuse = [&](Tile& xa, Tile& xb_) __attribute__((always_inline)) {
        if (it != ci) {
          LGKM0();
          for (; passed < it; ++passed) bump(s_c + C_SC + passed);
          lds_wait<1>(s_c + (it < 3 ? F_Q : F_Q2), min(it + 1, 3));
          q_load(q, s_q + seg_of_k(it) * DH, j);
          tt.agree(s_c + S_MOVED);
          ci = it;
        }
        const int tk = arrive(s_c + C_K);
        tt.listen(s_c + S_SLOW + 2 * wave);
        pair_put(tile_score(xa, q), tile_score(xb_, q), s_sc + cp * 16 + r, j);
        MEMFENCE();
        return arrived(tk);
      };
      auto kstep = [&](Tile& xa, Tile& xb_, Tile& ya, Tile& yb) __attribute__((always_inline)) {
        const unsigned e = __builtin_amdgcn_readlane(tk_e, nxt >> 2);
        MEMFENCE();
        const int deep = tt.gate(__builtin_amdgcn_readlane(tk_d, nxt >> 2) + (nxt & 3) * (TAUK / 64));
        MEMFENCE();
        pair_issue(ya, yb, kb, ((e & 0x1fc00u) | ((e + ((nxt & 3) << (e >> 28))) & 1023u)) * 3072u + lo, deep);
        const int n2 = kuse(xa, xb_);
        cp = ((e >> 17) & 255) + (nxt & 3); it = (e >> 25) & 7; nxt = n2;
      };
      bool swap;
      for (;;) {
        if (nxt >= NPAIR) { swap = false; break; }
        kstep(ta, tb, tc, td);
        if (nxt >= NPAIR) { swap = true; break; }
        kstep(tc, td, ta, tb);
      }
      tt.gate(due_k(NPAIR + wave));
      MEMFENCE();
      const unsigned* pw = reinterpret_cast<const unsigned*>(wo + (xl * 128 + wave * 32 + idx) * 3840) + lane * 16;
      const unsigned v0 = pw[0], v1 = pw[56 * 16];
      MEMFENCE();
      if (swap) { ta = tc; tb = td; }
      kuse(ta, tb);
      for (; passed < NS; ++passed) bump(s_c + C_SC + passed);
      tt.gate(due_v(wave));
      MEMFENCE();
      pair_issue(ta, tb, vb, __builtin_amdgcn_readlane(tv_o, 0) + lo, 2);
      asm volatile("" :: "v"(v0), "v"(v1));
    }
    const unsigned sc_r = (unsigned)(size_t)(__attribute__((address_space(3))) float*)s_sc + r * 4;
    f32x2 acc[12];
    auto vstep = [&](Tile& xa, Tile& xb_, Tile& ya, Tile& yb, int p, bool fin) __attribute__((always_inline)) {
      const unsigned off = __builtin_amdgcn_readlane(tv_o, (p >> 2) + 1) + lo;
      MEMFENCE();
      const int deep = tt.gate(__builtin_amdgcn_readlane(tv_d, (p >> 2) + 1), p >= NPAIR - ENDT * NMW ? ENDS : 0);
      float pa, pb;
      asm volatile("ds_read_b32 %0, %2\n\tds_read_b32 %1, %2 offset:32" : "=&v"(pa), "=&v"(pb) : "v"(sc_r + p * 64) : "memory");
      MEMFENCE();
      pair_issue(ya, yb, vb, off, deep);
      asm volatile("s_waitcnt lgkmcnt(0)" : "+v"(pa), "+v"(pb) :: "memory");
      if (!fin) tt.listen(s_c + S_SLOW + 2 * wave);
      else bump(s_c + C_LAST);
      pv_last(pa, xa, acc);
      pv_last(pb, xb_, acc);
      MEMFENCE();
    };
    typedef __attribute__((address_space(3))) float lds_f32;
    auto fold8 = [&](lds_f32* dst) __attribute__((always_inline)) {
      float t12[12], t6[6];
#pragma unroll
      for (int i = 0; i < 12; ++i) t12[i] = tsum32(acc[i >> 1][i & 1], acc[6 + (i >> 1)][i & 1]);
#pragma unroll
      for (int i = 0; i < 6; ++i) t6[i] = tsum16(t12[i], t12[i + 6]);
#pragma unroll
      for (int i = 0; i < 6; ++i) t6[i] += dpp<DPP_ROR8>(t6[i]);
      auto d2 = (__attribute__((address_space(3))) f32x2*)dst;
      d2[0] = f32x2{t6[0], t6[1]}; d2[1] = f32x2{t6[2], t6[3]}; d2[2] = f32x2{t6[4], t6[5]};
      CFENCE();
    };
    lds_f32* dst = (lds_f32*)s_buf + wave * DH + j * 24 + (lane >> 4) * 6;
#pragma unroll 1
    for (int it = 0;; ++it) {
      lds_wait<1>(s_c + F_P, k_of_seg(it) + 1);
      tt.agree(s_c + S_MOVED);
#pragma unroll
      for (int i = 0; i < 12; ++i) acc[i] = f32x2{0.f, 0.f};
#pragma unroll 1
      for (int p = seg_beg(it) + wave; p < min(seg_beg(it + 1), NPAIR - 2 * NMW); p += 2 * NMW) {
        vstep(ta, tb, tc, td, p, false);
        vstep(tc, td, ta, tb, p + NMW, false);
      }
      if (it >= NRING) lds_wait<1>(s_c + F_FREE, it - NRING + 1);
      if (it == NS - 1) break;
      fold8(dst + (it % NRING) * (NMW * DH));
      bump(s_c + C_PART + it);
    }
    dst += ((NS - 1) % NRING) * (NMW * DH);
    const int ln = lane < DH / 4 ? lane : 0;
    auto src = (__attribute__((address_space(3))) f32x4*)((lds_f32*)s_buf + ((NS - 1) % NRING) * (NMW * DH) + ((ln >> 1) & 7) * 24 + (ln >> 4) * 8 + (ln & 1) * 4);
    int so = (lane < DH / 4 ? WS_PO + xb * DH + lane * 4 : WS_END) * 4;
    const int p = NPAIR - NMW + wave;
    float pa = s_sc[p * 16 + r], pb = s_sc[p * 16 + 8 + r];
    asm volatile("" : "+v"(dst), "+v"(src), "+v"(so), "+v"(pa), "+v"(pb));
    vstep(ta, tb, tc, td, NPAIR - 2 * NMW + wave, true);
    pv_last(pa, tc, acc);
    pv_last(pb, td, acc);
    fold8(dst);
    const int k = arrive(s_c + C_PART + NS - 1);
    const f32x4 m = (src[0] + src[DH / 4]) + (src[2 * DH / 4] + src[3 * DH / 4]);
    u32x4 o = {nz_bits(m[0]), nz_bits(m[1]), nz_bits(m[2]), nz_bits(m[3])};
    asm volatile("" : "+v"(o));
    if (arrived(k) == NMW - 1) {
      __builtin_amdgcn_raw_buffer_store_b128(o, rs, so, 0, AUX_SC1);
      bump(s_c + F_DONE);
    }
    return;
  }

  {
    Tile pk[NRP * 2];
    __builtin_amdgcn_s_setprio(3);
    const unsigned it0 = (row_of(0) * NH + g) * 1024, ct = c0 + tw * 16;
    auto toff = [&](int turn) -> unsigned { return (it0 + ((turn * 64 + ct) & 1023)) * 3072u + lo; };
    unsigned tbw = 0u;
#pragma unroll 1
    for (int e = 0; e < NLP * 2; ++e) {
      const char* src = kb + (toff(e >> 1) + (e & 1) * 3072);
      if (e >= 2) until(tbw, ((e & ~1) * 2 * (TAUK + KS1)) >> 6);
#pragma unroll
      for (int i = 0; i < 3; ++i) glds16_nt(src + i * 128, lds_base + (e * 3 + i) * 1024);
      if (e == 1) {
        VMCNT(5);
        lds_u32* og = (lds_u32*)(smem + OFF_ML + 96);
        og[tw] = t0 + min(max((int)(clk() - t0) - ANCH, 0), 300);
        LGKM0();
        __builtin_amdgcn_s_barrier();
        tb0 = t0 + min(min((int)(lds_get(og) - t0), (int)(lds_get(og + 1) - t0)), min((int)(lds_get(og + 2) - t0), (int)(lds_get(og + 3) - t0)));
        tbw = tb0 + TSTART + ((tw * TAUK) >> 6) - TLEAD;
        if (tw == 0 && lane == 0) s_c[S_LATE] = tb0;
      }
      VMCNT(PFD);
    }
#pragma unroll
    for (int e = 0; e < NRP; ++e) {
      until(tbw, ((NLP + e) * 4 * (TAUK + KS1)) >> 6);
      tile_load(pk[2 * e], kb, toff(NLP + e), 0);
      MEMFENCE();
      VMCNT(PFD);
      tile_load(pk[2 * e + 1], kb, toff(NLP + e), 1);
      MEMFENCE();
      VMCNT(PFD);
    }
    if (tw == 0) {
      r0 = ld32(rs, WS_HR + bid * 2);
      if (bid == LEADER) {
        u32x4 n;
#pragma unroll
        for (int k = 0; k < 4; ++k) n[k] = ld32(rs, WS_HA + (lane * 4 + k) * 32) + 1u;
        st128(rs, WS_HN + lane * 4, n);
      }
    }
    __builtin_amdgcn_s_setprio(0);
    lds_wait<1>(s_c + F_Q, HPG);
    u32x2 q[12];
    q_load(q, s_q, j);
    const u32x4* s_kv = reinterpret_cast<const u32x4*>(smem + OFF_REG + tw * REG) + lane;
    static_assert(NLP * 4 <= PPI && NTP <= HPG * PPI, "tail tiles: batch row 0 only");
#pragma unroll 1
    for (int e = 0; e < NLP * 2; ++e) {
      Tile tmp;
#pragma unroll
      for (int i = 0; i < 3; ++i) tmp.d[i] = s_kv[(e * 3 + i) * 64];
      score_put(tile_score(tmp, q), s_sc + ((e >> 1) * 4 + tw) * 16 + (e & 1) * 8 + r, j);
    }
#pragma unroll
    for (int e = 0; e < NRP * 2; ++e) {
      if ((NLP * 2 + e) * 2 == PPI) q_load(q, s_q + DH, j);
      score_put(tile_score(pk[e], q), s_sc + ((NLP + (e >> 1)) * 4 + tw) * 16 + (e & 1) * 8 + r, j);
      __builtin_amdgcn_sched_barrier(0);
    }
    asm volatile("s_waitcnt vmcnt(0) lgkmcnt(0)" ::: "memory");
#pragma unroll
    for (int it = 0; it * PPI < NTP; ++it) lds_add(s_c + C_SC + it, lane);
    if (tw == 0) {
      until((t0 + ZT + 511u) & ~511u, 0);
      const u32x4 z = {0u, 0u, 0u, 0u};
      st128(rs, lane < 48 ? WS_PO + xb * DH + lane * 4 : WS_PML + xb * 64 + (lane - 48) * 4, z);
      const int o = isW ? WS_SLAB + w_ks * NB * DM + w_ct * 64 : (isG ? WS_ACT + g_cg * 128 : (isH ? WS_H + (xl - 2) * 768 : WS_OC + (c_it * NG + g) * DH));
      st128(rs, lane < (isG ? 32 : (isC ? 48 : (isW || isH ? 64 : 0))) ? o + (isW ? (lane >> 4) * DM + (lane & 15) * 4 : lane * 4) : WS_END, z);
      if (isH) { st128(rs, o + 256 + lane * 4, z); st128(rs, o + 512 + lane * 4, z); }
      VMCNT(0);
    }
  }

  const int mr = lane & 15, qd = lane >> 4;
  unsigned char* reg = smem + OFF_REG + tw * REG;
  float* red = reinterpret_cast<float*>(smem + OFF_RED);
#define PERM_LO(hi_, lo_) __builtin_amdgcn_perm(hi_, lo_, 0x05040100u)
#define PERM_HI(hi_, lo_) __builtin_amdgcn_perm(hi_, lo_, 0x07060302u)
  int served = 0;
  auto done = [&](int n) {
    if (tw == 3 && served < min(n, NA)) {
      const int from = served;
      int ln = lane;
      asm volatile("" : "+v"(ln));
#pragma unroll 1
      for (; served < min(n, NA); ++served) {
        lds_wait<8>(s_c + C_PART + served, NMW);
        const f32x4 o = merged(served, ln < DH / 4 ? ln : 0);
        const float am = max_x16_32(row_max(fmaxf(fmaxf(fabsf(o[0]), fabsf(o[1])), fmaxf(fabsf(o[2]), fabsf(o[3])))));
        const float inv = am > 1e-30f ? 32767.0f / am : 0.f;
        const int q0 = __float2int_rn(o[0] * inv), q1 = __float2int_rn(o[1] * inv), q2 = __float2int_rn(o[2] * inv), q3 = __float2int_rn(o[3] * inv);
        if (ln < DH / 4) reinterpret_cast<u32x2*>(s_sc + served * (DH / 2))[ln] = u32x2{PERM_LO((unsigned)q1, (unsigned)q0), PERM_LO((unsigned)q3, (unsigned)q2)};
        const bool nan = __any((o[0] + o[1]) + (o[2] + o[3]) != (o[0] + o[1]) + (o[2] + o[3]));
        if (ln == 0) s_ml[served][2] = nan ? 0x7fc00000u : nz_bits(am * (1.0f / 32767.0f));
        LGKM0();
        if (ln == 0) s_c[F_FREE] = served + 1;
      }
#pragma unroll 1
      for (int c = from * 24 + ln; c < served * 24; c += 64) st128nt(rs, WS_PQ + xb * 5 * (DH / 2) + c * 4, reinterpret_cast<const u32x4*>(s_sc)[c]);
      VMCNT(0);
      if (ln < 8) st128(rs, WS_PML + xb * 64 + (from ? 32 : 0) + ln * 4, s_ml[ln]);
      if (ln == 0) s_c[S_BURST + (from ? 1 : 0)] = clk();
      CFENCE();
      if (ln == 0) __hip_atomic_fetch_add(s_c + F_DONE, (unsigned)(served - from), __ATOMIC_RELAXED, __HIP_MEMORY_SCOPE_WORKGROUP);
    }
    if (n > NA) lds_wait<1>(s_c + F_DONE, n); else lds_wait<8>(s_c + F_DONE, n);
  };
  auto done_all = [&]() {
#pragma unroll 1
    for (int n = 4; n <= NA; ++n) done(n);
  };
  const int lp = (lane < DH / 4 ? lane : 0) * 4;
  auto weight = [&](int it, int x0, int cnt) -> float {
    const int mlw = WS_PML + (x0 + (lane & (cnt - 1))) * 64 + (it < 4 ? 0 : 32) + it * 4;
    u32x4 ml;
#pragma unroll 1
    for (unsigned spin = 0; spin < SPIN_CAP; ++spin) {
      ml = ld128(rs, mlw);
      if (__all(min4(ml) != 0u)) break;
      __builtin_amdgcn_s_sleep(16);
    }
    const float mk = __uint_as_float(ml[0]);
    const float e = __builtin_amdgcn_exp2f(mk - max_x16_32(row_max(mk)));
    return e / (wave_sum(e * __uint_as_float(ml[1])) * ((float)cnt * (1.0f / 64.0f))) * __uint_as_float(ml[2]);
  };
  const unsigned tok = __builtin_amdgcn_readfirstlane(r0) + 1u;
  unsigned echoed = 0u, fold = 0u;
  int slow = 0, prev = 0, later = 0;
  auto lag = [&]() {
    const unsigned v = __hip_atomic_load(s_c + S_MOVED + (lane & 7), __ATOMIC_RELAXED, __HIP_MEMORY_SCOPE_WORKGROUP);
    fold += v;
    unsigned a = 0u;
#pragma unroll
    for (int k = 0; k < 8; ++k) a += __builtin_amdgcn_readlane(v, k);
    return a >> 2;
  };
  auto tell = [&]() { if (lane < 4) { s_c[S_SLOW + 2 * lane] = slow - later; s_c[S_SLOW + 2 * lane + 1] = fold; } };
  auto round = [&](unsigned rd, bool lead) {
    const int tr = RT0 + (int)(rd - 1) * RTS;
    until(t0, tr);
    const unsigned bh = lag();
    if (bh > RMIN && lane < 8) st128(rs, WS_HA + bid * 32 + lane * 4, u32x4{echoed + rd, tok, tb0, bh});
    if (lead) { until(t0, tr + RA1); leader_release(rs, lane, tb0, rd, slow, prev); }
    until(t0, tr + RA2);
    const u32x2 rv = ld64(rs, WS_HR + bid * 2);
    slow = (int)__builtin_amdgcn_readfirstlane(rv[1]) >> 16;
#pragma unroll 1
    for (unsigned k = min(__builtin_amdgcn_readfirstlane(rv[0]) - tok, rd) + 1u; k <= rd; ++k) slow = max(slow - GB, 0);
    tell();
  };
  auto prep = [&](bool lead) {
    if (tw == 0) {
      const int c = lane & (GS - 1);
      unsigned qe = s_c[S_ECHO + c];
      bool first = true, released = false, sent = false;
#pragma unroll 1
      while ((int)(clk() - t0) < HST) {
        const unsigned qn = ld32(rs, qf + (GS + c) * 32);
        if (mm < NPROD && __any(qn != qe)) {
          if (lane < GS) st128(rs, qf + mm * 32 + lane * 4, u32x4{qn, qn, qn, qn});
          qe = qn;
        }
        __builtin_amdgcn_s_sleep(64);
      }
      const unsigned behind = lag();
#pragma unroll 1
      for (unsigned spin = 0; spin < SPIN_CAP && !released; ++spin) {
        const unsigned qn = ld32(rs, qf + (GS + c) * 32);
        const u32x2 rv = ld64(rs, WS_HR + bid * 2);
        const unsigned nv = __builtin_amdgcn_readfirstlane(ld32(rs, WS_HN + bid));
        if (__builtin_amdgcn_readfirstlane(rv[0]) - tok <= NRD) {
          released = true;
          slow = (int)__builtin_amdgcn_readfirstlane(rv[1]) >> 16;
          later = __builtin_amdgcn_readfirstlane(min((tb0 - rv[1]) & 0xffffu, 300u));
        } else {
          if (mm < NPROD && __any(qn != qe)) {
            if (lane < GS) st128(rs, qf + mm * 32 + lane * 4, u32x4{qn, qn, qn, qn});
            qe = qn;
          }
          if (first || nv != echoed) {
            echoed = nv; first = false;
            if (lane < 8) st128(rs, WS_HA + bid * 32 + lane * 4, u32x4{echoed, tok, tb0, behind});
          }
          if (lead && !sent) sent = leader_release(rs, lane, tb0, 0u, slow, prev);
          __builtin_amdgcn_s_sleep(32);
        }
      }
      tell();
      if (lane == 0) s_c[F_REL] = 1u;
      if (lane < DH / 4) {
        u32x4 p[3][2];
#pragma unroll
        for (int e = 0; e < 3; ++e)
#pragma unroll
          for (int k = 0; k < 2; ++k) p[e][k] = ld128nt(rs, WS_QP + (((e < 2 ? 24 - e * 8 + (xb >> 5) : xb >> 4) * 2 + k) * NB + 1) * DH + lane * 4);
#pragma unroll
        for (int e = 0; e < 3; ++e) *reinterpret_cast<u32x4*>(&s_q[(1 + e) * DH + lane * 4]) = q_pack(p[e][0], p[e][1]);
      }
      LGKM0();
      if (lane == 0) s_c[F_Q2] = 3u;
#pragma unroll 1
      for (unsigned rd = 1; rd <= NRD; ++rd) round(rd, lead);
    }
    if (tw == 3) {
#pragma unroll 1
      for (int ik = 0; ik < NS; ++ik) {
        const int it = seg_of_k(ik), nv = (seg_beg(it + 1) - seg_beg(it)) >> 4;
        lds_wait<4>(s_c + C_SC + ik, ik == 0 ? NW : NMW);
        f32x4* sp = reinterpret_cast<f32x4*>(s_sc + seg_beg(it) * 16) + lane;
        f32x4 v[4];
#pragma unroll
        for (int k = 0; k < 4; ++k) v[k] = sp[(k & (nv - 1)) * 64];
        const f32x4 mx = __builtin_elementwise_max(__builtin_elementwise_max(v[0], v[1]), __builtin_elementwise_max(v[2], v[3]));
        const float m = max_x16_32(row_max(fmaxf(fmaxf(mx[0], mx[1]), fmaxf(mx[2], mx[3]))));
        float l = 0.f;
#pragma unroll
        for (int k = 0; k < 4; ++k) {
#pragma unroll
          for (int e = 0; e < 4; ++e) v[k][e] = __builtin_amdgcn_exp2f(v[k][e] - m);
          if (k < nv) {
            l += (v[k][0] + v[k][1]) + (v[k][2] + v[k][3]);
            sp[k * 64] = v[k];
          }
        }
        l = wave_sum(l);
        if (lane == 0) s_ml[it] = u32x4{nz_bits(m), nz_bits(l), 0x3f800000u, 0x3f800000u};
        LGKM0();
        if (lane == 0) s_c[F_P] = ik + 1;
      }
    }
  };
  auto pull = [&]() {
    typedef __attribute__((address_space(1))) const unsigned g_u32;
    const int id = (ro - 16) * 4 + tw;
    g_u32* p1[2];
#pragma unroll
    for (int i = 0; i < 2; ++i) {
      const int l = id * 60 + min(i * 32 + (lane >> 1), 59), m = l >= 1920 ? l - 1920 : l;
      p1[i] = (g_u32*)((l >= 1920 ? wu : wg) + (size_t)(m / 3) * FF + xl * 192 + (m % 3) * 64 + (lane & 1) * 32);
    }
    g_u32* p2 = (g_u32*)(wd + (size_t)(id * 24 + min(lane >> 1, 23)) * DM + xl * 64 + (lane & 1) * 32);
    asm volatile("" : "+v"(p1[0]), "+v"(p1[1]), "+v"(p2));
    lds_wait<1>(s_c + C_LAST, NMW);
    const unsigned tp = clk();
    until(tp, PW);
    const unsigned v0 = p1[0][0], v1 = p1[1][0];
    MEMFENCE();
    until(tp, PW + DPW);
    const unsigned v2 = p2[0], v3 = p2[xl < 2 ? 256 : 0];
    asm volatile("" :: "v"(v0), "v"(v1), "v"(v2), "v"(v3));
  };
  if (isW) {
    u32x4 bx[24];
    const char* p = reinterpret_cast<const char*>(wo + (size_t)((w_ks * 4 + tw) * DH + r) * DM + w_ct * 64 + j * 8);
    prep(false);
    if (tw != 0) { lds_wait<16>(s_c + F_P, NS); pause(clk(), OLATE + (tw % 3) * 24 * OSTEP); }
    const unsigned tw0 = clk();
#pragma unroll 1
    for (int i = 0; i < 24; ++i) { until(tw0, i * OSTEP); glds16_nt(p, lds_base + i * 1024); p += 8 * DM * 2; VMCNT(WFD); }
    asm volatile("s_waitcnt vmcnt(0)" ::: "memory");
#pragma unroll
    for (int s = 0; s < 6; ++s) {
      u32x2 e[8];
#pragma unroll
      for (int k = 0; k < 8; ++k) e[k] = *reinterpret_cast<const u32x2*>(reg + (s * 32 + qd * 8 + k) * 128 + mr * 8);
#pragma unroll
      for (int k = 0; k < 4; ++k) {
        bx[s * 4 + 0][k] = PERM_LO(e[2 * k + 1][0], e[2 * k][0]); bx[s * 4 + 1][k] = PERM_HI(e[2 * k + 1][0], e[2 * k][0]);
        bx[s * 4 + 2][k] = PERM_LO(e[2 * k + 1][1], e[2 * k][1]); bx[s * 4 + 3][k] = PERM_HI(e[2 * k + 1][1], e[2 * k][1]);
      }
    }
#pragma unroll
    for (int s = 0; s < 24; s += 4) asm volatile("" : "+v"(bx[s]), "+v"(bx[s + 1]), "+v"(bx[s + 2]), "+v"(bx[s + 3]));
    LGKM0();
    lds_wait<8>(s_c + F_REL, 1u);
    const int hh = w_ks * 4 + tw;
#pragma unroll 1
    for (int pass = 0; pass < 3; ++pass) {
      if (pass < 2) {
        done(pass ? NA : 4);
        pause(lds_get(s_c + S_BURST + pass), pass ? WDELAY2 : WDELAY2 + 300);
        const int b0 = pass * 2, b1 = pass + 1;
        u32x4 v0, v1;
#pragma unroll 1
        for (unsigned spin = 0; spin < SPIN_CAP; ++spin) {
          v0 = ld128(rs, WS_OC + (b0 * NH + hh) * DH + lp);
          v1 = ld128(rs, WS_OC + (b1 * NH + hh) * DH + lp);
          if (__all(min(min4(v0), min4(v1)) != 0u)) break;
          __builtin_amdgcn_s_sleep(16);
        }
        if (lane < DH / 4) {
          *reinterpret_cast<u32x2*>(reg + (b0 * 4) * AW_LD + lane * 8) = u32x2{v0[0], v0[1]};
          *reinterpret_cast<u32x2*>(reg + (b0 * 4 + 1) * AW_LD + lane * 8) = u32x2{v0[2], v0[3]};
          *reinterpret_cast<u32x2*>(reg + (b1 * 4) * AW_LD + lane * 8) = u32x2{v1[0], v1[1]};
          *reinterpret_cast<u32x2*>(reg + (b1 * 4 + 1) * AW_LD + lane * 8) = u32x2{v1[2], v1[3]};
        }
      } else {
        const float wk = weight(NS - 1, hh * GS, GS);
        float w[GS];
#pragma unroll
        for (int k = 0; k < GS; ++k) { w[k] = __uint_as_float(__builtin_amdgcn_readlane(__float_as_uint(wk), k)); asm volatile("" : "+s"(w[k])); }
        done(NS);
        __builtin_amdgcn_s_setprio(3);
        const int po = WS_PO + hh * GS * DH;
        u32x4 pv[GS];
#pragma unroll 1
        for (unsigned spin = 0; spin < SPIN_CAP; ++spin) {
#pragma unroll
          for (int k = 0; k < GS; ++k) pv[k] = __builtin_amdgcn_raw_buffer_load_b128(rs, lp * 4, (po + k * DH) * 4, AUX_SC1);
          unsigned mn = min4(pv[0]);
#pragma unroll
          for (int k = 1; k < GS; ++k) mn = min(mn, min4(pv[k]));
          if (__all(mn != 0u)) break;
          __builtin_amdgcn_s_sleep(1);
        }
        f32x4 o = w[0] * __builtin_bit_cast(f32x4, pv[0]), o2 = w[1] * __builtin_bit_cast(f32x4, pv[1]);
#pragma unroll
        for (int k = 2; k < GS; k += 2) { o += w[k] * __builtin_bit_cast(f32x4, pv[k]); o2 += w[k + 1] * __builtin_bit_cast(f32x4, pv[k + 1]); }
        u32x2 hi, lw;
        split4(o + o2, hi, lw);
        if (lane < DH / 4) {
          *reinterpret_cast<u32x2*>(reg + ((NB - 1) * 4) * AW_LD + lane * 8) = hi;
          *reinterpret_cast<u32x2*>(reg + ((NB - 1) * 4 + 1) * AW_LD + lane * 8) = lw;
        }
      }
      CFENCE();
      u32x4 a[6];
#pragma unroll
      for (int s = 0; s < 6; ++s) a[s] = *reinterpret_cast<const u32x4*>(reg + mr * AW_LD + s * 64 + qd * 16);
      __builtin_amdgcn_sched_barrier(0);
      f32x4 c[4];
#pragma unroll
      for (int t = 0; t < 4; ++t) c[t] = f32x4{0.f, 0.f, 0.f, 0.f};
#pragma unroll
      for (int s = 0; s < 6; ++s)
#pragma unroll
        for (int t = 0; t < 4; ++t) c[t] = mfma16(a[s], bx[s * 4 + t], c[t]);
      const bool mine = max(qd - 1, 0) == pass;
      f32x4* r4 = reinterpret_cast<f32x4*>(red);
      if (mine) r4[tw * 64 + lane] = f32x4{c[0][0] + c[0][1], c[1][0] + c[1][1], c[2][0] + c[2][1], c[3][0] + c[3][1]};
      const int k = arrive(s_c + C_W + pass);
      f32x4 sum = (r4[lane] + r4[64 + lane]) + (r4[128 + lane] + r4[192 + lane]);
      asm volatile("" : "+v"(sum));
      if (arrived(k) == 3) {
        if (mine) st128(rs, WS_SLAB + (w_ks * NB + qd) * DM + w_ct * 64 + mr * 4, u32x4{nz_bits(sum[0]), nz_bits(sum[1]), nz_bits(sum[2]), nz_bits(sum[3])});
      }
    }
  } else if (isG || isH) {
    f32x2* rst = reinterpret_cast<f32x2*>(red);
    const int hb = isH ? xl - 2 : NB - 1;
    uint2 wr4 = make_uint2(0u, 0u), xg = wr4;
    if (lane < 40) {
      wr4 = *reinterpret_cast<const uint2*>(wr + tw * 160 + lane * 4);
      xg = *reinterpret_cast<const uint2*>(x + row_of(hb) * DM + tw * 160 + lane * 4);
    }
    f32x4 xc = f32x4{bf_lo(xg.x), bf_hi(xg.x), bf_lo(xg.y), bf_hi(xg.y)} - bf_lo(*reinterpret_cast<const unsigned*>(x + row_of(hb) * DM));
    asm volatile("" : "+v"(xc));
    typedef __attribute__((address_space(1))) const u32x4 g_u32x4;
    g_u32x4* wp[10];
    wp[0] = (g_u32x4*)(((mr & 4) ? wu : wg) + (size_t)(tw * 160 + qd * 8 + (mr >> 3) * 4) * FF + g_cg * 32 + (mr & 3) * 8);
    u32x4 e[20], bx[20];
    auto frag = [&](int s) __attribute__((always_inline)) {
      unsigned w[8][2];
#pragma unroll
      for (int i = 0; i < 4; ++i)
#pragma unroll
        for (int d = 0; d < 2; ++d) {
          w[i][d] = __builtin_amdgcn_update_dpp(e[s * 4 + i][d], e[s * 4 + i][2 + d], DPP_ROR8, 0xf, 0xc, false);
          w[4 + i][d] = __builtin_amdgcn_update_dpp(e[s * 4 + i][2 + d], e[s * 4 + i][d], DPP_ROR8, 0xf, 0x3, false);
        }
#pragma unroll
      for (int k = 0; k < 4; ++k) {
        bx[s * 4 + 0][k] = PERM_LO(w[2 * k + 1][0], w[2 * k][0]); bx[s * 4 + 1][k] = PERM_HI(w[2 * k + 1][0], w[2 * k][0]);
        bx[s * 4 + 2][k] = PERM_LO(w[2 * k + 1][1], w[2 * k][1]); bx[s * 4 + 3][k] = PERM_HI(w[2 * k + 1][1], w[2 * k][1]);
      }
      PIN4(bx, s * 4);
    };
    prep(false);
    lds_wait<8>(s_c + F_REL, 1u);
    auto hrow = [&](bool fast) -> f32x4 {
      f32x4 hv = {0.f, 0.f, 0.f, 0.f};
      if (lane < 40) {
        const int off = (WS_SLAB + hb * DM + tw * 160 + lane * 4) * 4;
        u32x4 sl[KS];
#pragma unroll 1
        for (unsigned spin = 0; spin < SPIN_CAP; ++spin) {
#pragma unroll
          for (int ks = 0; ks < KS; ++ks) sl[ks] = __builtin_amdgcn_raw_buffer_load_b128(rs, off, ks * (NB * DM * 4), AUX_SC1);
          unsigned mn = min4(sl[0]);
#pragma unroll
          for (int ks = 1; ks < KS; ++ks) mn = min(mn, min4(sl[ks]));
          if (!__any(mn == 0u)) break;
          if (fast) __builtin_amdgcn_s_sleep(1); else __builtin_amdgcn_s_sleep(16);
        }
#define SL(k) __builtin_bit_cast(f32x4, sl[k])
        hv = xc + (((SL(0) + SL(1)) + (SL(2) + SL(3))) + ((SL(4) + SL(5)) + (SL(6) + SL(7))));
#undef SL
      }
      return hv;
    };
    auto sums = [&](f32x4 hv) -> f32x2 {
      return f32x2{wave_sum((hv[0] + hv[1]) + (hv[2] + hv[3])), wave_sum((hv[0] * hv[0] + hv[1] * hv[1]) + (hv[2] * hv[2] + hv[3] * hv[3]))};
    };
    if (isH) {
      const float wsum = wave_sum((bf_lo(wr4.x) + bf_hi(wr4.x)) + (bf_lo(wr4.y) + bf_hi(wr4.y)));
      if (tw == 3 || hb >= 2) done_all(); else done(4);
      if (hb == NB - 1) { pull(); done(NS); }
      else pause(lds_get(s_c + S_BURST + (hb >> 1)), hb < 2 ? HDELAY : HDELAY2);
      const f32x4 hv = hrow(hb == NB - 1);
      const f32x2 sm = sums(hv);
      const float dot = wave_sum((hv[0] * bf_lo(wr4.x) + hv[1] * bf_hi(wr4.x)) + (hv[2] * bf_lo(wr4.y) + hv[3] * bf_hi(wr4.y)));
      if (lane < 8) st128(rs, WS_H + (hb * 4 + tw) * 192 + 160 + lane * 4, u32x4{nz_bits(sm[0]), nz_bits(sm[1]), nz_bits(dot), nz_bits(wsum)});
      if (hb == NB - 1) return;
      const u32x4 v = split8(hv, (lane & 1) != 0);
      if (lane < 40) st128(rs, WS_H + (hb * 4 + tw) * 192 + lane * 4, v);
      done_all();
      pull();
      return;
    }
    done_all();
#pragma unroll
    for (int i = 0; i < 10; ++i) { wp[i] = wp[0] + ((i >> 1) * 32 + (i & 1) * 2) * (FF / 8); asm volatile("" : "+v"(wp[i])); }
    done(NS);
    until(clk(), GW);
#pragma unroll
    for (int i = 0; i < 20; ++i) e[i] = wp[i >> 1][(i & 1) * (FF / 8)];
    u32x4 a[5], sm;
    const int hoff = (WS_H + (min(mr >> 2, NB - 2) * 4 + tw) * 192 + qd * 8 + (mr & 1) * 4) * 4;
#pragma unroll
    for (int s = 0; s < 5; ++s) a[s] = __builtin_amdgcn_raw_buffer_load_b128(rs, hoff + s * 128, 0, AUX_SC1);
    sm = ld128(rs, WS_H + (min(lane, NB - 2) * 4 + tw) * 192 + 160);
    MEMFENCE();
#pragma unroll
    for (int s = 0; s < 5; ++s) frag(s);
    {
#pragma unroll 1
      for (unsigned spin = 0; spin < SPIN_CAP; ++spin) {
        unsigned mn = min4(sm);
#pragma unroll
        for (int s = 0; s < 5; ++s) mn = min(mn, min4(a[s]));
        if (__all(mn != 0u)) break;
        __builtin_amdgcn_s_sleep(2);
#pragma unroll
        for (int s = 0; s < 5; ++s) a[s] = __builtin_amdgcn_raw_buffer_load_b128(rs, hoff + s * 128, 0, AUX_SC1);
        sm = ld128(rs, WS_H + (min(lane, NB - 2) * 4 + tw) * 192 + 160);
      }
      if (lane < NB - 1) rst[lane * 4 + tw] = f32x2{__uint_as_float(sm[0]), __uint_as_float(sm[1])};
#pragma unroll
      for (int s = 0; s < 5; ++s) {
        if (mr & 2) a[s] = u32x4{0x3f803f80u, 0x3f803f80u, 0x3f803f80u, 0x3f803f80u};
        asm volatile("" : "+v"(a[s]));
      }
    }
    typedef __attribute__((address_space(3))) unsigned char lds_u8;
    const int eb = mr >> 2;
    lds_u8* l_aw = (lds_u8*)reg + lane * 8;
    lds_u8* l_ar = (lds_u8*)reg + (mr & 1) * AG_LD + qd * 16;
    lds_u8* l_pw = (lds_u8*)reg + 4096 + (qd * 16 + (mr & 7) * 2 + (mr >> 3)) * 16;
    lds_u8* l_pr = (lds_u8*)smem + OFF_REG + 4096 + (lane & 1) * 1152 + (eb * 16 + qd * 2 + ((lane >> 1) & 1)) * 16;
    lds_u8* l_p2 = l_pr + 2 * REG;
    lds_u8* l_sr = (lds_u8*)(rst + eb * 4);
    lds_u8* l_sw = (lds_u8*)(rst + (NB - 1) * 4 + tw);
    int so = (lane & 2) == 0 ? (WS_ACT + g_cg * 128 + (lane >> 2) * 8 + (lane & 1) * 4) * 4 : WS_END * 4;
    asm volatile("" : "+v"(l_aw), "+v"(l_ar), "+v"(l_pw), "+v"(l_pr), "+v"(l_p2), "+v"(l_sr), "+v"(l_sw), "+v"(so));
#define LDS(T, p) (*reinterpret_cast<__attribute__((address_space(3))) T*>(p))
    const f32x4 hv = hrow(true);
    {
      u32x2 hi, lw;
      split4(hv, hi, lw);
      LDS(u32x2, l_aw) = hi;
      LDS(u32x2, l_aw + AG_LD) = lw;
    }
    CFENCE();
    if ((mr & 14) == 12) {
#pragma unroll
      for (int s = 0; s < 5; ++s) a[s] = LDS(u32x4, l_ar + s * 64);
    }
    __builtin_amdgcn_sched_barrier(0);
    f32x4 c[4];
#pragma unroll
    for (int t = 0; t < 4; ++t) c[t] = f32x4{0.f, 0.f, 0.f, 0.f};
#pragma unroll
    for (int s = 0; s < 5; ++s)
#pragma unroll
      for (int t = 0; t < 4; ++t) c[t] = mfma16(a[s], bx[s * 4 + t], c[t]);
    LDS(f32x2, l_sw) = sums(hv);
    LDS(f32x4, l_pw) = f32x4{c[0][0] + c[0][1], c[1][0] + c[1][1], c[0][2], c[1][2]};
    LDS(f32x4, l_pw + 1152) = f32x4{c[2][0] + c[2][1], c[3][0] + c[3][1], c[2][2], c[3][2]};
    const int k = arrive(s_c + C_T);
    f32x4 ts[2] = {LDS(f32x4, l_sr), LDS(f32x4, l_sr + 16)};
    f32x4 pg[4] = {LDS(f32x4, l_pr), LDS(f32x4, l_pr + REG), LDS(f32x4, l_p2), LDS(f32x4, l_p2 + REG)};
    f32x4 pu[4] = {LDS(f32x4, l_pr + 128), LDS(f32x4, l_pr + REG + 128), LDS(f32x4, l_p2 + 128), LDS(f32x4, l_p2 + REG + 128)};
    asm volatile("" : "+v"(ts[0]), "+v"(ts[1]));
    PIN4(pg, 0); PIN4(pu, 0);
    if (arrived(k) == 3) {
      const f32x4 t = ts[0] + ts[1], g4 = (pg[0] + pg[1]) + (pg[2] + pg[3]), u4 = (pu[0] + pu[1]) + (pu[2] + pu[3]);
      const float s1 = t[0] + t[2], d = s1 * (-1.0f / DM);
      const float rstd = __builtin_amdgcn_rsqf(fmaxf((t[1] + t[3]) + d * s1, 0.f) * (1.0f / DM) + LN_EPS);
      const float g0 = rstd * (g4[0] + d * g4[2]), g1 = rstd * (g4[1] + d * g4[3]);
      const f32x2 act = {g0 * __builtin_amdgcn_rcpf(1.0f + __expf(-g0)) * (rstd * (u4[0] + d * u4[2])),
                         g1 * __builtin_amdgcn_rcpf(1.0f + __expf(-g1)) * (rstd * (u4[1] + d * u4[3]))};
      const unsigned h0 = __float_as_uint(act[0]) & 0xffff0000u, h1 = __float_as_uint(act[1]) & 0xffff0000u;
      const unsigned hi = max(PERM_HI(h1, h0), 1u), lw = max(PERM_HI(__float_as_uint(act[1] - __uint_as_float(h1)), __float_as_uint(act[0] - __uint_as_float(h0))), 1u);
      const bool odd = (lane & 1) != 0;
      const unsigned own = odd ? lw : hi, oth = __float_as_uint(dpp<DPP_XOR1>(__uint_as_float(odd ? hi : lw)));
      const unsigned w0 = odd ? oth : own, w1 = odd ? own : oth;
      const u32x4 av = {w0, w1, __float_as_uint(dpp<DPP_XOR2>(__uint_as_float(w0))), __float_as_uint(dpp<DPP_XOR2>(__uint_as_float(w1)))};
      __builtin_amdgcn_raw_buffer_store_b128(av, rs, so, 0, AUX_SC1);
    }
  } else if (isD) {
    const uint2 xd = *reinterpret_cast<const uint2*>(x + row_of(tw) * DM + d_ct * 32 + j * 4);
    const float cq = bf_lo(*reinterpret_cast<const unsigned*>(x + row_of(qd) * DM));
    const __amdgpu_buffer_rsrc_t rd = __builtin_amdgcn_make_buffer_rsrc(const_cast<bf16*>(wd), 0, FF * DM * 2, 0x00020000);
    prep(false);
    lds_wait<8>(s_c + F_REL, 1u);
    done_all();
    pull();
    done(NS);
    const unsigned te = clk();
    u32x4 bd[24];
    {
      const int off = ((tw * 384 + (mr >> 3) * 32 + qd * 8 + (mr & 4)) * DM + d_ct * 32 + (mr & 3) * 8) * 2;
      u32x4 e[24];
#pragma unroll
      for (int i = 0; i < 24; ++i) e[i] = __builtin_amdgcn_raw_buffer_load_b128(rd, off + (i & 3) * (DM * 2), (i >> 2) * 64 * (DM * 2), 0);
      MEMFENCE();
#pragma unroll
      for (int s = 0; s < 6; ++s) {
        unsigned w[8][2];
#pragma unroll
        for (int i = 0; i < 4; ++i)
#pragma unroll
          for (int d = 0; d < 2; ++d) {
            w[i][d] = __builtin_amdgcn_update_dpp(e[s * 4 + i][d], e[s * 4 + i][2 + d], 0x114, 0xf, 0xa, false);
            w[4 + i][d] = __builtin_amdgcn_update_dpp(e[s * 4 + i][2 + d], e[s * 4 + i][d], 0x104, 0xf, 0x5, false);
          }
#pragma unroll
        for (int k = 0; k < 4; ++k) {
          bd[s * 4 + 0][k] = PERM_LO(w[2 * k + 1][0], w[2 * k][0]); bd[s * 4 + 1][k] = PERM_HI(w[2 * k + 1][0], w[2 * k][0]);
          bd[s * 4 + 2][k] = PERM_LO(w[2 * k + 1][1], w[2 * k][1]); bd[s * 4 + 3][k] = PERM_HI(w[2 * k + 1][1], w[2 * k][1]);
        }
        PIN4(bd, s * 4);
      }
    }
    until(te, DLATE);
    {
      u32x4 sl;
#pragma unroll 1
      for (unsigned spin = 0; spin < SPIN_CAP; ++spin) {
        sl = ld128(rs, WS_SLAB + (r * NB + tw) * DM + d_ct * 32 + j * 4);
        if (__all(min4(sl) != 0u)) break;
        __builtin_amdgcn_s_sleep(2);
      }
      f32x4 hv = {bf_lo(xd.x), bf_hi(xd.x), bf_lo(xd.y), bf_hi(xd.y)};
#pragma unroll
      for (int i = 0; i < 4; ++i) hv[i] += sum_r(__uint_as_float(sl[i]));
      if (lane < 8) reinterpret_cast<f32x4*>(red + 512 + tw * 32)[lane] = hv;
      LGKM0();
      lds_add(s_c + C_W, lane);
      lds_wait<1>(s_c + C_W, 4u);
    }
    const int col = (mr & 3) * 8 + (mr & 4) + (mr >> 3) * 2;
    f32x2 hv = *reinterpret_cast<const f32x2*>(red + 512 + qd * 32 + col);
    typedef __attribute__((address_space(3))) f32x2 lds_f32x2;
    typedef __attribute__((address_space(1))) unsigned g_u32w;
    lds_f32x2* r2 = (lds_f32x2*)red + lane;
    lds_f32x2* r2w = r2 + tw * 64;
    g_u32w* po = (g_u32w*)(out + row_of(qd) * DM + d_ct * 32 + col);
    int aoff = (WS_ACT + tw * 12 * 128 + ((mr >> 1) & 1) * 128 + qd * 32 + (mr >> 2) * 8 + (mr & 1) * 4) * 4;
    asm volatile("" : "+v"(hv), "+v"(r2), "+v"(r2w), "+v"(po), "+v"(aoff));
    u32x4 hs, a[6];
#pragma unroll 1
    for (unsigned spin = 0; spin < SPIN_CAP; ++spin) {
      hs = ld128(rs, WS_H + (qd * 4 + (mr & 3)) * 192 + 160);
      if (__all(min4(hs) != 0u)) break;
      __builtin_amdgcn_s_sleep(1);
    }
#pragma unroll
    for (int s = 0; s < 6; ++s) a[s] = __builtin_amdgcn_raw_buffer_load_b128(rs, aoff, s * 1024, AUX_SC1);
    MEMFENCE();
    float gate;
    {
      f32x4 t = __builtin_bit_cast(f32x4, hs);
#pragma unroll
      for (int i = 0; i < 4; ++i) { t[i] += dpp<DPP_XOR2>(t[i]); t[i] += dpp<DPP_XOR1>(t[i]); }
      const float d = t[0] * (-1.0f / DM);
      const float rstd = __builtin_amdgcn_rsqf(fmaxf(t[1] + d * t[0], 0.f) * (1.0f / DM) + LN_EPS);
      hv += ((hv - cq) + d) * rstd;
      gate = __builtin_amdgcn_rcpf(1.0f + __expf(-rstd * (t[2] + d * t[3])));
      asm volatile("" : "+v"(hv), "+v"(gate));
    }
#pragma unroll 1
    for (unsigned spin = 0; spin < SPIN_CAP; ++spin) {
      unsigned mn = min4(a[0]);
#pragma unroll
      for (int s = 1; s < 6; ++s) mn = min(mn, min4(a[s]));
      if (__all(mn != 0u)) break;
#pragma unroll
      for (int s = 0; s < 6; ++s) a[s] = __builtin_amdgcn_raw_buffer_load_b128(rs, aoff, s * 1024, AUX_SC1);
      MEMFENCE();
    }
    f32x4 c[4];
#pragma unroll
    for (int t = 0; t < 4; ++t) c[t] = f32x4{0.f, 0.f, 0.f, 0.f};
#pragma unroll
    for (int s = 0; s < 6; ++s)
#pragma unroll
      for (int t = 0; t < 4; ++t) c[t] = mfma16(a[s], bd[s * 4 + t], c[t]);
    const bool up = mr >= 8;
    const float k0 = up ? c[2][2] + c[2][3] : c[0][0] + c[0][1], k1 = up ? c[3][2] + c[3][3] : c[1][0] + c[1][1];
    const float o0 = up ? c[0][2] + c[0][3] : c[2][0] + c[2][1], o1 = up ? c[1][2] + c[1][3] : c[3][0] + c[3][1];
    *r2w = f32x2{k0 + dpp<DPP_ROR8>(o0), k1 + dpp<DPP_ROR8>(o1)};
    const int k = arrive(s_c + C_T);
    f32x2 y = hv + ((r2[0] + r2[64]) + (r2[128] + r2[192])) * gate;
    asm volatile("" : "+v"(y));
    if (arrived(k) == 3) {
      __hip_atomic_store(po, __builtin_bit_cast(unsigned, __builtin_convertvector(y, bf16x2)), __ATOMIC_RELAXED, __HIP_MEMORY_SCOPE_AGENT);
    }
  } else {
    prep(bid == LEADER);
    if (isC && tw == 1) {
      lds_wait<8>(s_c + F_REL, 1u);
      const int cs = c_it != 1 ? c_it * 2 : min(4 - (g >> 3), 3), cnt = c_it != 1 ? 8 : (g < 16 ? 16 : 32), x0 = c_it != 1 ? g * 8 : (g < 16 ? g * 16 : (g & 7) * 32);
      done(c_it < 2 ? 4 : NA);
      pause(lds_get(s_c + S_BURST + (c_it >> 1)), WDELAY);
      const float wk = weight(cs, x0, cnt);
      f32x4 o = {0.f, 0.f, 0.f, 0.f};
#pragma unroll 1
      for (int k0 = 0; k0 < cnt; k0 += 16) {
        u32x2 pv[16];
#pragma unroll
        for (int k = 0; k < 16; ++k) pv[k] = __builtin_amdgcn_raw_buffer_load_b64(rs, lp * 2, (WS_PQ + ((x0 + ((k0 + k) & (cnt - 1))) * 5 + cs) * (DH / 2)) * 4, AUX_EARLY);
#pragma unroll
        for (int k = 0; k < 16; ++k) {
          const float w = k0 + k < cnt ? __uint_as_float(__builtin_amdgcn_readlane(__float_as_uint(wk), k0 + k)) : 0.f;
          o += w * f32x4{(float)(short)pv[k][0], (float)((int)pv[k][0] >> 16), (float)(short)pv[k][1], (float)((int)pv[k][1] >> 16)};
        }
      }
      u32x2 hi, lw;
      split4(o, hi, lw);
      if (lane < DH / 4) st128(rs, WS_OC + (c_it * NG + g) * DH + lane * 4, u32x4{max(hi[0], 1u), max(hi[1], 1u), max(lw[0], 1u), max(lw[1], 1u)});
    }
    done_all();
    pull();
  }
  if (tw == 3) done_all();
}

extern "C" void kernel_launch(void* const* d_in, const int* in_sizes, int n_in, void* d_out, int out_size, void* d_ws,
                              size_t ws_size, hipStream_t stream) {
  const bf16* x = (const bf16*)d_in[0];
  const bf16* kc = (const bf16*)d_in[1];
  const bf16* vc = (const bf16*)d_in[2];
  const bf16* wq = (const bf16*)d_in[3];
  const bf16* wo = (const bf16*)d_in[4];
  const bf16* wr = (const bf16*)d_in[6];
  const bf16* wg = (const bf16*)d_in[7];
  const bf16* wu = (const bf16*)d_in[8];
  const bf16* wd = (const bf16*)d_in[9];
  fused_kernel<<<NWG, NW * 64, 0, stream>>>(x, kc, vc, wq, wo, wr, wg, wu, wd, (float*)d_ws, (bf16*)d_out);
}
```
